```python
import torch

def _softmax(x, axis=-1):
    return torch.softmax(x, dim=axis)
def _gelu(x):
    return torch.nn.functional.gelu(x, approximate="tanh")
def _silu(x):
    return torch.nn.functional.silu(x)
def _sigmoid(x):
    return torch.sigmoid(x)
def _layernorm(x, axis=-1, eps=1e-5):
    m = x.mean(dim=axis, keepdim=True)
    v = x.var(dim=axis, keepdim=True, unbiased=False)
    return (x - m) / torch.sqrt(v + eps)
def _scatter_add(tgt, idx, src, axis=0):
    return tgt.clone().index_add_(axis, idx, src)
def _segment_sum(src, seg, n):
    out = torch.zeros((n,) + src.shape[1:], dtype=src.dtype)
    return out.index_add_(0, seg, src)
def _pad(x, pw):
    flat = tuple(v for pair in reversed(pw) for v in pair)
    return torch.nn.functional.pad(x, flat)

kernel_name = 'interleave_layers__sliding_local_dilated__mixer_token_mlp__prompt'

def setup_inputs():

    _w = (lambda a: (lambda t: t.to(torch.bfloat16) if t.is_floating_point() else t)(torch.as_tensor(a)))
    def _A_setup():
        B, S, D = 1, 2048, 1024
        return {
            "x": _w((lambda *s: torch.randn(*s, dtype=torch.bfloat16) * 0.05)(B, S, D)),
            "wqkv": _w((lambda *s: torch.randn(*s, dtype=torch.bfloat16) * 0.05)(D, 3 * D)),
            "wo": _w((lambda *s: torch.randn(*s, dtype=torch.bfloat16) * 0.05)(D, D)),
        }
    _A_in = _A_setup()

    def _B_setup():
        B, S, D, F = 1, 2048, 1024, 3584
        return {
            "x": _w((lambda *s: torch.randn(*s, dtype=torch.bfloat16) * 0.05)(B, S, D)),
            "w1": _w((lambda *s: torch.randn(*s, dtype=torch.bfloat16) * 1.6)(S, F) * (S ** -0.5)),
            "b1": _w((lambda *s: torch.randn(*s, dtype=torch.bfloat16) * 1.6)(F) * 0.02),
            "w2": _w((lambda *s: torch.randn(*s, dtype=torch.bfloat16) * 1.6)(F, S) * (F ** -0.5)),
            "b2": _w((lambda *s: torch.randn(*s, dtype=torch.bfloat16) * 1.6)(S) * 0.02),
        }
    _B_in = _B_setup()

    return {
        "A_x": _A_in["x"],
        "A_wqkv": _A_in["wqkv"],
        "A_wo": _A_in["wo"],
        "B_x": _B_in["x"],
        "B_w1": _B_in["w1"],
        "B_b1": _B_in["b1"],
        "B_w2": _B_in["w2"],
        "B_b2": _B_in["b2"],
    }

def reference(A_x, A_wqkv, A_wo, B_x, B_w1, B_b1, B_w2, B_b2):
    def _A_ref(x, wqkv, wo):
        B, S, D = x.shape
        H = 32; Dh = D // H
        W, dil = 8, 2
        qkv = (lambda x, *d: x.permute(*d))((x @ wqkv).reshape(B, S, 3, H, Dh), 2, 0, 3, 1, 4)
        q, k, v = qkv[0], qkv[1], qkv[2]
        scale = Dh ** -0.5
        pos = torch.arange(S)
        off = torch.arange(W) * dil
        idx = pos[:, None] - off[None, :]
        mask = idx >= 0
        idx_c = torch.where(mask, idx, 0).reshape(-1)
        kg = k[:, :, idx_c, :].reshape(B, H, S, W, Dh)
        vg = v[:, :, idx_c, :].reshape(B, H, S, W, Dh)
        logits = torch.einsum("bhsd,bhswd->bhsw", q, kg) * scale
        logits = torch.where(mask[None, None, :, :], logits, -1e9)
        attn = _softmax(logits, axis=-1)
        o = torch.einsum("bhsw,bhswd->bhsd", attn, vg)
        return (lambda x, *d: x.permute(*d))(o, 0, 2, 1, 3).reshape(B, S, D) @ wo

    def _B_ref(x, w1, b1, w2, b2):
        B, S, D = x.shape
        h = _layernorm(x, axis=-1)
        u = torch.einsum("bsd,sf->bdf", h, w1) + b1
        u = _gelu(u)
        y = torch.einsum("bdf,fs->bds", u, w2) + b2
        return (lambda x, *d: x.permute(*d))(y, 0, 2, 1)

    x = A_x
    x = x + _A_ref(_layernorm(x, axis=-1), A_wqkv, A_wo)
    x = x + _B_ref(_layernorm(x, axis=-1), B_w1, B_b1, B_w2, B_b2)
    x = x + _A_ref(_layernorm(x, axis=-1), A_wqkv, A_wo)
    x = x + _B_ref(_layernorm(x, axis=-1), B_w1, B_b1, B_w2, B_b2)
    return x
```

```cpp
#include <hip/hip_runtime.h>
#include <hip/hip_bf16.h>
#include <stdint.h>

typedef unsigned short u16;
typedef unsigned long long u64;
typedef short bf16x8 __attribute__((ext_vector_type(8)));
typedef short bf16x4 __attribute__((ext_vector_type(4)));
typedef float f32x4 __attribute__((ext_vector_type(4)));
typedef float f32x2 __attribute__((ext_vector_type(2)));
typedef unsigned u32x4 __attribute__((ext_vector_type(4)));
typedef unsigned u32x2 __attribute__((ext_vector_type(2)));
typedef __bf16 bf16v2 __attribute__((ext_vector_type(2)));
#define LAS __attribute__((address_space(3)))
#define GAS __attribute__((address_space(1)))
#define DEVI __device__ __forceinline__
typedef __attribute__((address_space(1))) unsigned gu32;
typedef __attribute__((address_space(1))) unsigned long long gu64;

constexpr int S = 2048, D = 1024, F = 3584;
constexpr float EPS = 1e-5f;

DEVI float bf2f(u16 v) { return __uint_as_float(((unsigned)v) << 16); }
DEVI float bflo(unsigned v) { return __uint_as_float(v << 16); }
DEVI float bfhi(unsigned v) { return __uint_as_float(v & 0xffff0000u); }
DEVI unsigned pk2(float lo, float hi) { bf16v2 p; p.x = (__bf16)lo; p.y = (__bf16)hi; return __builtin_bit_cast(unsigned, p); }

DEVI const void* uni_ptr(const void* p) {
  const u64 v = (u64)p;
  return (const void*)(((u64)(unsigned)__builtin_amdgcn_readfirstlane((unsigned)(v >> 32)) << 32) | (unsigned)__builtin_amdgcn_readfirstlane((unsigned)v));
}
DEVI void dma16(const void* sbase, unsigned voff, unsigned ldsaddr) {
  ldsaddr = __builtin_amdgcn_readfirstlane(ldsaddr);
  asm volatile("s_mov_b32 m0, %2\n\ts_nop 0\n\tglobal_load_lds_dwordx4 %0, %1 sc1" :: "v"(voff), "s"(sbase), "s"(ldsaddr) : "memory");
}
template <int N, int IMM> DEVI void dma_grp(const void* sbase, const unsigned* v, unsigned lb) {
  static_assert(N == 1 || N == 2 || N == 4, "");
  if constexpr (N == 4)
    asm volatile("s_add_u32 m0, %1, %2\n\ts_nop 0\n\tglobal_load_lds_dwordx4 %3, %0 sc1\n\tglobal_load_lds_dwordx4 %4, %0 offset:1024 sc1\n\t"
                 "global_load_lds_dwordx4 %5, %0 offset:2048 sc1\n\tglobal_load_lds_dwordx4 %6, %0 offset:3072 sc1"
                 :: "s"(sbase), "s"(lb), "i"(IMM), "v"(v[0]), "v"(v[1]), "v"(v[2]), "v"(v[3]) : "memory", "scc");
  else if constexpr (N == 2)
    asm volatile("s_add_u32 m0, %1, %2\n\ts_nop 0\n\tglobal_load_lds_dwordx4 %3, %0 sc1\n\tglobal_load_lds_dwordx4 %4, %0 offset:1024 sc1"
                 :: "s"(sbase), "s"(lb), "i"(IMM), "v"(v[0]), "v"(v[1]) : "memory", "scc");
  else
    asm volatile("s_add_u32 m0, %1, %2\n\ts_nop 0\n\tglobal_load_lds_dwordx4 %3, %0 sc1" :: "s"(sbase), "s"(lb), "i"(IMM), "v"(v[0]) : "memory", "scc");
}
template <int N> DEVI void wait_vm() { asm volatile("s_waitcnt vmcnt(%0)" :: "i"(N) : "memory"); }
DEVI void wait_lgkm0() { asm volatile("" ::: "memory"); __builtin_amdgcn_s_waitcnt(0xC07F); asm volatile("" ::: "memory"); }
DEVI void mfma_ip(f32x4& c, const bf16x8& a, const bf16x8& b) { asm volatile("v_mfma_f32_16x16x32_bf16 %0, %1, %2, %0" : "+v"(c) : "v"(a), "v"(b)); }
DEVI void mfma_gap() { __builtin_amdgcn_sched_barrier(0); asm volatile("s_nop 7"); __builtin_amdgcn_sched_barrier(0); }
DEVI void bar() { asm volatile("" ::: "memory"); __builtin_amdgcn_s_barrier(); asm volatile("" ::: "memory"); }
DEVI void sched0() { __builtin_amdgcn_sched_barrier(0); }

DEVI bf16x8 lds_b128(unsigned addr) { return *(const LAS bf16x8*)(uintptr_t)addr; }
DEVI bf16x4 lds_tr4(unsigned addr) { return __builtin_amdgcn_ds_read_tr16_b64_v4i16((LAS bf16x4*)(uintptr_t)addr); }

typedef __amdgpu_buffer_rsrc_t rsrc_t;
DEVI rsrc_t mkrsrc(const void* p, unsigned bytes) { return __builtin_amdgcn_make_buffer_rsrc((void*)p, 0, bytes, 0x00020000); }
DEVI void st16(rsrc_t r, unsigned off, u32x4 v) { __builtin_amdgcn_raw_buffer_store_b128(v, r, off, 0, 16); }
DEVI void ld_raw(u32x4& v, rsrc_t r, unsigned off) { asm volatile("buffer_load_dwordx4 %0, %1, %2, 0 offen sc1 ; raw load" : "=&v"(v) : "v"(off), "s"(r) : "memory"); }
DEVI void ld_again(u32x4& v, rsrc_t r, unsigned off) { asm volatile("buffer_load_dwordx4 %0, %1, %2, 0 offen sc1\n\ts_waitcnt vmcnt(0)" : "=&v"(v) : "v"(off), "s"(r) : "memory"); }
DEVI void ld_ok(u32x4& v) { asm volatile("; raw ok %0" : "+v"(v)); }
#define RLX_AGENT __ATOMIC_RELAXED, __HIP_MEMORY_SCOPE_AGENT

template <int N> DEVI float ror16(float v) { return __int_as_float(__builtin_amdgcn_update_dpp(0, __float_as_int(v), 0x120 + N, 0xf, 0xf, false)); }
DEVI float red16(float v) { v += ror16<8>(v); v += ror16<4>(v); v += ror16<2>(v); v += ror16<1>(v); return v; }
DEVI void xch32(float v, float& a, float& b) { unsigned u = __float_as_uint(v); auto r = __builtin_amdgcn_permlane32_swap(u, u, false, false); unsigned x = r[0], y = r[1]; a = __uint_as_float(x); b = __uint_as_float(y); }
DEVI void xch16(float v, float& a, float& b) { unsigned u = __float_as_uint(v); auto r = __builtin_amdgcn_permlane16_swap(u, u, false, false); unsigned x = r[0], y = r[1]; a = __uint_as_float(x); b = __uint_as_float(y); }
DEVI void swp32(float& a, float& b) { auto r = __builtin_amdgcn_permlane32_swap(__float_as_uint(a), __float_as_uint(b), false, false); unsigned x = r[0], y = r[1]; a = __uint_as_float(x); b = __uint_as_float(y); }
DEVI void swp16(float& a, float& b) { auto r = __builtin_amdgcn_permlane16_swap(__float_as_uint(a), __float_as_uint(b), false, false); unsigned x = r[0], y = r[1]; a = __uint_as_float(x); b = __uint_as_float(y); }
DEVI float colsum4(float v) { float a, b; xch32(v, a, b); v = a + b; xch16(v, a, b); return a + b; }
DEVI float vmax(float a, float b) { float r; asm("v_max_f32 %0, %1, %2" : "=v"(r) : "v"(a), "v"(b)); return r; }
DEVI float vmin3(float a, float b, float c) { float r; asm("v_min3_f32 %0, %1, %2, %3" : "=v"(r) : "v"(a), "v"(b), "v"(c)); return r; }
DEVI float vmax3(float a, float b, float c) { float r; asm("v_max3_f32 %0, %1, %2, %3" : "=v"(r) : "v"(a), "v"(b), "v"(c)); return r; }

DEVI unsigned src_kc(int c, int ld) { int row = c >> 3, ch = (c & 7) ^ ((row >> 1) & 7); return (unsigned)(row * ld * 2 + ch * 16); }
DEVI int km_f(int k) { return ((k >> 1) & 1) | (((k >> 3) & 1) << 1); }
DEVI unsigned ph(unsigned x) { return ((x >> 7) << 8) | (x & 127u); }
DEVI unsigned rd_kc(int lane, int ks) { int r = lane & 15, g = lane >> 4; return (unsigned)(r * 128 + (((ks * 4 + g) ^ (r >> 1)) << 4)); }
DEVI unsigned rd_km(int lane, int ct) {
  int g = lane >> 4, q = (lane & 15) >> 2, p = lane & 3;
  int f = ((q >> 1) & 1) | ((g & 1) << 1);
  return (unsigned)((ct >> 2) * 8192 + (8 * g + q) * 128 + (((ct & 3) ^ f) << 5) + 8 * p);
}


constexpr unsigned CODE_BYTES = 47104;
DEVI void code_warm(int part, int lane, unsigned scratch) {
  unsigned long long pc;
  asm volatile("s_getpc_b64 s[40:41]\n\ts_add_u32 s40, s40, mega_kernel@rel32@lo+4\n\ts_addc_u32 s41, s41, mega_kernel@rel32@hi+12\n\ts_mov_b64 %0, s[40:41]" : "=s"(pc) :: "s40", "s41", "scc");
  const unsigned off = (unsigned)(part * 64 + lane) * 128u;
  scratch = __builtin_amdgcn_readfirstlane(scratch);
  if (off < CODE_BYTES) asm volatile("s_nop 3\n\ts_mov_b32 m0, %2\n\ts_nop 0\n\tglobal_load_lds_dword %0, %1" :: "v"(off), "s"(pc), "s"(scratch) : "memory");
}

DEVI unsigned src_km(int lt, int ld) { const int k = lt >> 3; return (unsigned)(k * ld * 2 + (((lt & 7) ^ (km_f(k) << 1)) << 4)); }
DEVI void touch4(const void* sbase, unsigned voff, unsigned ldsaddr) {
  ldsaddr = __builtin_amdgcn_readfirstlane(ldsaddr); sbase = uni_ptr(sbase);
  asm volatile("s_nop 3\n\ts_mov_b32 m0, %2\n\ts_nop 0\n\tglobal_load_lds_dword %0, %1" :: "v"(voff), "s"(sbase), "s"(ldsaddr) : "memory");
}

template <int IMM> DEVI void spf1(const void* base, unsigned soff) { asm volatile("s_load_dword s101, %0, %1 offset:%2" :: "s"(base), "s"(soff), "i"(IMM) : "s101"); }
template <int IMM = 0> DEVI void spf2(const void* base, unsigned soff) { asm volatile("s_load_dwordx2 s[100:101], %0, %1 offset:%2" :: "s"(base), "s"(soff), "i"(IMM + 60) : "s100", "s101"); }
template <int ST> DEVI void spf2x6(const void* base, unsigned soff) {
  asm volatile("s_load_dwordx2 s[100:101], %0, %1 offset:%2\n\ts_load_dwordx2 s[100:101], %0, %1 offset:%3\n\ts_load_dwordx2 s[100:101], %0, %1 offset:%4\n\t"
               "s_load_dwordx2 s[100:101], %0, %1 offset:%5\n\ts_load_dwordx2 s[100:101], %0, %1 offset:%6\n\ts_load_dwordx2 s[100:101], %0, %1 offset:%7"
               :: "s"(base), "s"(soff), "i"(60), "i"(60 + ST), "i"(60 + 2 * ST), "i"(60 + 3 * ST), "i"(60 + 4 * ST), "i"(60 + 5 * ST) : "s100", "s101");
}
template <int ST> DEVI void spf1x12(const void* base, unsigned soff) {
  spf1<0>(base, soff); spf1<ST>(base, soff); spf1<2 * ST>(base, soff); spf1<3 * ST>(base, soff); spf1<4 * ST>(base, soff); spf1<5 * ST>(base, soff);
  spf1<6 * ST>(base, soff); spf1<7 * ST>(base, soff); spf1<8 * ST>(base, soff); spf1<9 * ST>(base, soff); spf1<10 * ST>(base, soff); spf1<11 * ST>(base, soff);
}
DEVI int lgkm_out() { int n; asm volatile("s_getreg_b32 %0, hwreg(HW_REG_IB_STS, 8, 4)" : "=s"(n)); return n; }
DEVI int slow_res(const void* w) { return (3 - ((unsigned)(uintptr_t)w >> 7)) & 7; }

constexpr int WNE = 14, ANE = 24, BNE = 13, QNE = 46;
struct Plan { int n; signed char q[40], idx[40], k[40], h[40], g[40]; };
template <class O, bool PKM, bool QKM> constexpr Plan make_plan() {
  Plan p{};
  int nr = 0;
  for (int c = 0; c < O::NRO; ++c) nr += ((O::ro[c] >> 4) ? QKM : PKM) ? 4 : 2;
  int prev = 0;
  for (int c = 0; c < O::NRO; ++c) {
    const int q = O::ro[c] >> 4, i = O::ro[c] & 15;
    int av[2] = {0, 0}, k0 = -1;
    for (int m = 0; m < 2 * O::NP; ++m) {
      const int mm = (m + 2 * O::NPEND) % (2 * O::NP);
      if ((q ? O::pj[m >> 1] : O::pi[m >> 1]) == i && m < 2 * O::NPEND) av[((m >> 1) ^ m) & 1] = m;
      if ((q ? O::pj[mm >> 1] : O::pi[mm >> 1]) == i && k0 < 0) k0 = ((mm >> 1) ^ mm) & 1;
    }
    for (int kk = 0; kk < 2; ++kk)
      for (int h = 0; h < ((q ? QKM : PKM) ? 2 : 1); ++h) {
        const int k = k0 ^ kk;
        int g = p.n * O::NE / nr;
        if (g < av[k]) g = av[k];
        if (g < prev + (p.n && O::NE >= nr ? 1 : 0)) g = prev + (p.n && O::NE >= nr ? 1 : 0);
        p.q[p.n] = q; p.idx[p.n] = i; p.k[p.n] = k; p.h[p.n] = h; p.g[p.n] = g; prev = g; ++p.n;
      }
  }
  return p;
}
struct OBase { static constexpr int pidx(int i) { return i; } static constexpr int poff(int) { return 0; } static constexpr int qoff(int j) { return j * 2048; } static constexpr bool swp(int) { return false; } };
template <bool PF, int NE_> struct O44 : OBase {
  static constexpr int TP = 4, TQ = 4, NPA = 4, NP = 16, NPEND = 8, NRO = 8, NE = NE_;
  static constexpr signed char pi[16] = {2, 3, 3, 2, 2, 3, 3, 2, 0, 1, 1, 0, 0, 1, 1, 0}, pj[16] = {0, 0, 1, 1, 2, 2, 3, 3, 0, 0, 1, 1, 2, 2, 3, 3};
  static constexpr signed char ro[8] = {0, PF ? 1 : 16, PF ? 16 : 1, 17, 18, 19, 2, 3};
};
template <bool PF, int NE_> struct O42 : OBase {
  static constexpr int TP = 4, TQ = 2, NPA = 4, NP = 8, NPEND = 4, NRO = 6, NE = NE_;
  static constexpr signed char pi[8] = {2, 3, 3, 2, 0, 1, 1, 0}, pj[8] = {0, 0, 1, 1, 0, 0, 1, 1};
  static constexpr signed char ro[6] = {0, PF ? 1 : 16, PF ? 16 : 1, 17, 3, 2};
};
template <int TH> struct OQ {
  static constexpr int TP = 6, TQ = 5, NPA = 2, NP = 26, NPEND = 13, NRO = 11, NE = QNE;
  static constexpr signed char pi[26] = {3, 4, 5, 5, 4, 3, 3, 4, 5, 5, TH ? 5 : 3, 4, TH ? 3 : 4, 0, 1, 2, 2, 1, 0, 0, 1, 2, 2, TH ? 2 : 0, 1, TH ? 0 : 1};
  static constexpr signed char pj[26] = {0, 0, 0, 1, 1, 1, 2, 2, 2, TH ? 4 : 3, 3, 3, TH ? 3 : 4, 0, 0, 0, 1, 1, 1, 2, 2, 2, TH ? 4 : 3, 3, 3, TH ? 3 : 4};
  static constexpr signed char ro[11] = {0, 1, 16, 2, 17, 18, TH ? 20 : 19, TH ? 19 : 20, 3, 4, 5};
  static constexpr int pidx(int i) { return i / 3; }
  static constexpr int poff(int i) { return i % 3 * 8192; }
  static constexpr int qoff(int j) { return j == 4 ? 0 : (1 + 4 * TH + j) * 2048; }
  static constexpr bool swp(int i) { return i % 3 == 2; }
};
template <bool PKM, bool QKM, int PB, int QB, int XB, int NS, int NT, bool WISP, int BASE, int ROT, int WLD, int WSUB, int ASTR, int ASTEP>
struct Gemm {
  static constexpr int SB = PB + QB + XB, CNT = (PB + QB) / 4096 + (XB ? 1 : 0);
  static constexpr int WB = WISP ? PB : QB, AB = WISP ? QB : PB, WOFF = WISP ? 0 : PB, AOFF = WISP ? PB + XB : 0;
  static constexpr int XL = XB / 64, H = NS - 1, CA = AB / 4096 + (XB ? 1 : 0), WSTEP = 64 * WLD * 2;
  static_assert(H * CNT <= 63 && BASE + NS * SB <= 163840, "");
  struct Loader {
    static constexpr int NW = WB / 4096, NA = AB / 4096;
    const char* w; const char* ab; unsigned wv[NW], av[NA], xv, lwb, lab, lx; int rot, islot, t; bool xl;
    DEVI Loader(const void* w_, const void* ab_, int lw, int lane, int rot_ = 0)
        : w((const char*)w_ - 3072), ab((const char*)ab_ - 3072), lwb(BASE + WOFF + lw * (NW == 6 ? 2048 : WB / 4)), lab(BASE + AOFF + lw * (AB / 4)), lx(BASE + PB + lw * (XL * 16)),
          rot(rot_), islot((H + ROT) % NS), t(0), xl(lane < XL) {
      const unsigned w0 = (lane >> 3) * (WLD * 2), f0 = (lane & 7) ^ ((lane >> 3) & 2);
#pragma unroll
      for (int i = 0; i < NW; ++i) {
        const unsigned p = NW == 6 ? (i >> 1) * 8 + lw * 2 + (i & 1) : lw * NW + i;
        wv[i] = w0 + ((f0 ^ ((i & 1) << 2)) << 4) + (NW == 6 ? (i >> 1) * WSUB + (lw * 2 + (i & 1)) * (8 * WLD * 2) - (i & 1) * 1024 : (p >> 3) * WSUB + (p & 7) * (8 * WLD * 2) - i * 1024) + 3072;
        asm volatile("" : "+v"(wv[i]));
      }
      asm volatile("" : "+s"(w), "+s"(lwb));
      if (XB) asm volatile("" : "+s"(rot));
      else seta(0, 0, lw, lane);
    }
    DEVI void seta(unsigned a0, unsigned xv_, int lw, int lane) {
      xv = xv_ + 3072;
#pragma unroll
      for (int i = 0; i < NA; ++i) { const unsigned p = lw * NA + i; av[i] = (XB ? a0 + src_kc(p * 64 + lane, D) : p * 2048 + ph(lane * 16)) + 3072 - i * 1024; asm volatile("" : "+v"(av[i])); }
      asm volatile("" : "+s"(ab), "+s"(lab));
      if (XB) { asm volatile("" : "+v"(xv)); asm volatile("" : "+s"(lx)); }
    }
    DEVI int kord(int n) const { if constexpr (XB != 0) return (n + rot) & (NT - 1); else return n; }
    template <int SO> DEVI void issue_w(const char* p, unsigned so = 0) const {
      if constexpr (NW == 6) { dma_grp<2, SO>(p, wv, lwb + so); dma_grp<2, SO + 8192>(p, wv + 2, lwb + so); dma_grp<2, SO + 16384>(p, wv + 4, lwb + so); }
      else dma_grp<NW, SO>(p, wv, lwb + so);
    }
    template <int SO> DEVI void issue_a(const char* p, unsigned so = 0) const {
      if (XB) { if (xl) dma_grp<1, SO>(p, &xv, lx + so); }
      dma_grp<NA, SO>(p, av, lab + so);
    }
    template <int S0, bool W, bool A> DEVI void pre() const {
      if constexpr (S0 < H) {
        if (W) issue_w<((S0 + ROT) % NS) * SB>(w + (kord(0) + S0) * WSTEP);
        if (A) issue_a<((S0 + ROT) % NS) * SB>(ab + (kord(0) + S0) * ASTEP);
        pre<S0 + 1, W, A>();
      }
    }
    DEVI void pre_w() const { pre<0, true, false>(); sched0(); }
    template <int T> DEVI void peel() const {
      if constexpr (T < H - 1) {
        wait_vm<(H - 1 - T) * CA + T * CNT>();
        bar();
        issue_w<((T + H + ROT) % NS) * SB>(w + (T + H) * WSTEP); issue_a<((T + H + ROT) % NS) * SB>(ab + (T + H) * ASTEP);
        peel<T + 1>();
      }
    }
    DEVI void start() {
      sched0();
      bar();
      pre<0, false, true>();
      sched0();
      if constexpr (H == 2) wait_vm<CA>();
      else { peel<0>(); islot = (2 * H - 1 + ROT) % NS; t = H - 1; }
    }
    template <class HK> DEVI void run(int tend, HK side) {
#pragma unroll 1
      for (; t < tend; ++t) {
        const char* wk = w + (t + H) * WSTEP; const char* ak = ab + (t + H) * ASTEP;
        unsigned lw_ = lwb + islot * SB, la_ = lab + islot * SB;
        islot = islot + 1 == NS ? 0 : islot + 1;
        asm volatile("" : "+s"(wk), "+s"(ak), "+s"(lw_), "+s"(la_));
        wait_vm<(H - 1) * CNT>();
        bar();
        dma_grp<NW, 0>(wk, wv, lw_);
        side(t);
        dma_grp<NA, 0>(ak, av, la_);
        sched0();
      }
    }
    template <int T, int X, class A, class B, class C> DEVI void step(A pre_, B side, C post) const {
      const int k = kord(T + H);
      const char* wk = w + k * WSTEP; const char* ak = ab + k * ASTEP;
      asm volatile("" : "+s"(wk), "+s"(ak));
      wait_vm<(H - 1) * CNT + X>();
      bar();
      pre_(BASE + ((T + ROT) % NS) * SB);
      issue_w<((T + H + ROT) % NS) * SB>(wk);
      sched0();
      side();
      sched0();
      issue_a<((T + H + ROT) % NS) * SB>(ak);
      post();
      sched0();
    }
    template <int I> DEVI void tail() const { if constexpr (I >= 0) { sched0(); wait_vm<I * CNT>(); bar(); tail<I - 1>(); } }
  };
  static constexpr int NWIN = (NS * SB + 65535) / 65536;
  template <class O> struct Comp {
    static constexpr int TP = O::TP, TQ = O::TQ, NM = 2 * O::NP, NPA = PKM ? O::NPA : 2, NQA = QKM ? TQ : 2;
    static constexpr Plan PL = make_plan<O, PKM, QKM>();
    unsigned prd[NWIN][NPA], qrd[NWIN][NQA];
    bf16x8 fp[TP][2], fq[TQ][2]; bf16x4 lo;
    DEVI void setwin() {
#pragma unroll
      for (int w = 1; w < NWIN; ++w) {
#pragma unroll
        for (int i = 0; i < NPA; ++i) { prd[w][i] = prd[0][i] + w * 65536; asm volatile("" : "+v"(prd[w][i])); }
#pragma unroll
        for (int i = 0; i < NQA; ++i) { qrd[w][i] = qrd[0][i] + w * 65536; asm volatile("" : "+v"(qrd[w][i])); }
      }
#pragma unroll
      for (int i = 0; i < NPA; ++i) asm volatile("" : "+v"(prd[0][i]));
#pragma unroll
      for (int i = 0; i < NQA; ++i) asm volatile("" : "+v"(qrd[0][i]));
    }
    DEVI void setup(int wp, int wq, int lane) {
      const unsigned b = BASE;
      if (PKM) {
#pragma unroll
        for (int i = 0; i < TP; ++i) prd[0][i] = b + rd_km(lane, wp * TP + i);
      } else { prd[0][0] = b + rd_kc(lane, 0) + wp * TP * 2048; prd[0][1] = b + rd_kc(lane, 1) + wp * TP * 2048; }
      if (QKM) {
#pragma unroll
        for (int j = 0; j < TQ; ++j) qrd[0][j] = b + rd_km(lane, wq * TQ + j);
      } else { qrd[0][0] = b + rd_kc(lane, 0) + wq * TQ * 2048; qrd[0][1] = b + rd_kc(lane, 1) + wq * TQ * 2048; }
    }
    template <int C, int N> DEVI static unsigned wa(const unsigned (&b)[NWIN][N], int i) { return b[C >> 16][i] + (C & 0xffff); }
    template <int SLOT, int R> DEVI void rd() {
      constexpr int q = PL.q[R], i = PL.idx[R], k = PL.k[R], h = PL.h[R];
      bf16x8& f = q ? fq[q ? i : 0][k] : fp[q ? 0 : i][k];
      if constexpr (q ? QKM : PKM) {
        unsigned a;
        if constexpr (q) a = wa<SLOT * SB + PB + k * 4096 + h * 512, NQA>(qrd, i); else a = wa<SLOT * SB + O::poff(i) + k * 4096 + h * 512, NPA>(prd, O::pidx(i));
        const bf16x4 x = lds_tr4(a);
        if constexpr (h) f = __builtin_shufflevector(lo, x, 0, 1, 2, 3, 4, 5, 6, 7); else lo = x;
      } else if constexpr (q) f = lds_b128(wa<SLOT * SB + PB + O::qoff(i), 2>(qrd, k));
      else f = lds_b128(wa<SLOT * SB + i * 2048, 2>(prd, k));
    }
    template <int SLOT, int M, int R> DEVI void rds() { if constexpr (R < PL.n) { if constexpr (PL.g[R] == M) rd<SLOT, R>(); if constexpr (PL.g[R] <= M) rds<SLOT, M, R + 1>(); } }
    template <int M, bool IP = false> DEVI void mm(f32x4 (&acc)[TP][TQ]) {
      constexpr int i = O::pi[M >> 1], j = O::pj[M >> 1], k = ((M >> 1) ^ M) & 1;
      if constexpr (IP) { if constexpr (O::swp(i)) mfma_ip(acc[i][j], fq[j][k], fp[i][k]); else mfma_ip(acc[i][j], fp[i][k], fq[j][k]); }
      else if constexpr (O::swp(i)) acc[i][j] = __builtin_amdgcn_mfma_f32_16x16x32_bf16(fq[j][k], fp[i][k], acc[i][j], 0, 0, 0);
      else acc[i][j] = __builtin_amdgcn_mfma_f32_16x16x32_bf16(fp[i][k], fq[j][k], acc[i][j], 0, 0, 0);
    }
    template <int A, int B> DEVI void pend(f32x4 (&acc)[TP][TQ]) { if constexpr (A < B) { mm<2 * A>(acc); mm<2 * A + 1>(acc); pend<A + 1, B>(acc); } }
    template <int SLOT, bool FIRST, int M> DEVI void body(f32x4 (&acc)[TP][TQ]) {
      if constexpr (M < NM) {
        if constexpr (!FIRST || M >= 2 * O::NPEND) { mm<M, true>(acc); sched0(); }
        rds<SLOT, M, 0>();
        sched0();
        body<SLOT, FIRST, M + 1>(acc);
      }
    }
    template <int SLOT, bool FIRST> DEVI void step(f32x4 (&acc)[TP][TQ]) { wait_lgkm0(); bar(); sched0(); body<SLOT, FIRST, 0>(acc); }
    template <int I, int N, int T0, class MID> DEVI void steps(f32x4 (&acc)[TP][TQ], MID mid) {
      if constexpr (I < N) { mid(I); step<(I + T0 + ROT) % NS, false>(acc); steps<I + 1, N, T0>(acc, mid); }
    }
    template <bool CONT> DEVI void prep(f32x4 (&acc)[TP][TQ]) {
      setwin();
#pragma unroll
      for (int p = 0; p < O::NP; ++p) asm volatile("" : "+v"(acc[O::pi[p]][O::pj[p]]));
      if constexpr (!CONT && NT % NS == 0) {
#pragma unroll
        for (int p = 0; p < 2 * O::NPEND; ++p) {
          fp[O::pi[p >> 1]][p & 1] = (bf16x8){0, 0, 0, 0, 0, 0, 0, 0}; fq[O::pj[p >> 1]][p & 1] = (bf16x8){0, 0, 0, 0, 0, 0, 0, 0};
        }
#pragma unroll
        for (int p = 0; p < 2 * O::NPEND; ++p) asm volatile("" : "+v"(fp[O::pi[p >> 1]][p & 1]), "+v"(fq[O::pj[p >> 1]][p & 1]));
      }
    }
    template <bool CONT, class MID> DEVI void run_steps(f32x4 (&acc)[TP][TQ], MID mid) {
      constexpr int T0 = CONT || NT % NS == 0 ? 0 : 1;
      if constexpr (T0) step<ROT % NS, true>(acc);
#pragma unroll 1
      for (int it = 0; it < (NT - T0) / NS; ++it) {
        steps<0, NS, T0>(acc, [&](int i) { mid(it - (NT - T0) / NS, i); });
      }
      steps<0, (NT - T0) % NS, T0>(acc, [](int) {});
      wait_lgkm0();
      mfma_gap();
    }
  };
};

DEVI f32x4 gelu_tanh(f32x4 u) {
  f32x4 a = u * (-2.302208198f + -0.1029432397f * (u * u));
#pragma unroll
  for (int e = 0; e < 4; ++e) a[e] = __builtin_amdgcn_exp2f(a[e]);
  a += 1.f;
#pragma unroll
  for (int e = 0; e < 4; ++e) a[e] = __builtin_amdgcn_rcpf(a[e]);
  return u * a;
}

template <int tilesP, int tilesQ, int gp, int gq> DEVI void tile_map(unsigned bid, int& tp, int& tq) {
  const unsigned xcd = bid & 7, idx = bid >> 3;
  constexpr unsigned gP = tilesP / gp, gQ = tilesQ / gq;
  static_assert(gP * gQ == 8, "");
  tp = (xcd % gP) * gp + idx % gp;
  tq = (xcd / gP) * gq + idx / gp;
}

template <int V> struct IC { static constexpr int value = V; };
template <int A, int B, class FN> DEVI void for_c(FN f) { if constexpr (A < B) { f(IC<A>{}); for_c<A + 1, B>(f); } }
DEVI float sel(bool c, float a, float b) { return c ? a : b; }
DEVI u32x2 pk4(const f32x4& v) { return (u32x2){pk2(v[0], v[1]), pk2(v[2], v[3])}; }
DEVI u32x4 cat4(u32x2 a, u32x2 b) { return (u32x4){a.x, a.y, b.x, b.y}; }
DEVI bf16x8 cat(u32x2 a, u32x2 b) { return __builtin_bit_cast(bf16x8, cat4(a, b)); }
DEVI f32x4 mfma(const bf16x8& a, const bf16x8& b, const f32x4& c) { return __builtin_amdgcn_mfma_f32_16x16x32_bf16(a, b, c, 0, 0, 0); }
DEVI u32x4 pack_pair(const f32x4& a, const f32x4& b) {
  const unsigned ax = pk2(a[0], a[1]), ay = pk2(a[2], a[3]), bx = pk2(b[0], b[1]), by = pk2(b[2], b[3]);
  auto rx = __builtin_amdgcn_permlane16_swap(ax, bx, false, false);
  auto ry = __builtin_amdgcn_permlane16_swap(ay, by, false, false);
  const unsigned x0 = rx[0], x1 = rx[1], y0 = ry[0], y1 = ry[1];
  return (u32x4){x0, y0, x1, y1};
}
DEVI int pair_chunk(int fq) { return ((fq & 1) << 1) | (fq >> 1); }
DEVI void sel_frags(bool paired, int fr, int fq, bf16x8 (&s)[2]) {
  const unsigned one = fr & 1 ? 0x3F800000u : 0x3F80u;
#pragma unroll
  for (int i = 0; i < 2; ++i) {
    const bool on = paired ? (fq & 1) == i && (fr >> 3) == (fq >> 1) : (fr >> 2) == fq;
    const int d = paired ? (fr & 7) >> 1 : i * 2 + ((fr >> 1) & 1);
    u32x4 v;
#pragma unroll
    for (int e = 0; e < 4; ++e) v[e] = on && d == e ? one : 0u;
    s[i] = __builtin_bit_cast(bf16x8, v);
  }
}
template <int TQ> DEVI void tile_add(f32x4 (&acc)[4][TQ], const bf16x8 (&s)[2], unsigned a) {
  bf16x8 t[4];
#pragma unroll
  for (int p = 0; p < 4; ++p) t[p] = lds_b128(a + p * 1024);
#pragma unroll
  for (int p = 0; p < 4; ++p)
#pragma unroll
    for (int i = 0; i < 2; ++i) acc[(p & 1) * 2 + i][p >> 1] = mfma(s[i], t[p], acc[(p & 1) * 2 + i][p >> 1]);
}
template <int C> DEVI float dppf(float v) { return __int_as_float(__builtin_amdgcn_update_dpp(0, __float_as_int(v), C, 0xf, 0xf, false)); }
DEVI float red8(float v) { v += dppf<0xB1>(v); v += dppf<0x4E>(v); v += dppf<0x141>(v); return v; }
DEVI float red4(float v) { v += dppf<0xB1>(v); v += dppf<0x4E>(v); return v; }

constexpr unsigned SENT = 0xBF800000u;
constexpr int SPIN_MAX = 1 << 21;

constexpr unsigned MB = 1u << 20;
constexpr unsigned WS_O = 0, WS_H = 8 * MB, WS_U = 16 * MB, WS_X2 = 32 * MB, WS_XP = 40 * MB  ;
constexpr unsigned WS_ST = 120 * MB, ST_CS = 2 * S * 64, WS_XCH = 122 * MB  , WS_EPOCH = 123 * MB, WS_ABORT = 123 * MB + 256  ;
DEVI unsigned tag2(unsigned t) { return t * 0x9E3779B1u ^ 0x5BD1E995u; }
constexpr int MEDGE = 256 * 64;
constexpr unsigned WS_MAIL = 124 * MB;

constexpr int NL = 2, QBR = 1, FLS = 3, NPL = 6, SW0 = 2, SW1 = 6, XD = 16, XS = 4, JA = 22, PS = 3, TBA = 3, TC1 = 12, NW1 = 9, PWL = 1, ET = 20, SPW = 1, PA_1 = 3, PA_2 = 3, ATT4 = 1;
constexpr int L_X = 0, L_TAB = 16384, L_CS = L_TAB + 1152, L_TAG = L_CS + 768, L_BIAS = L_TAG + 128, L_SCR = L_BIAS + 512  , L_QKV = 20480;
static_assert(L_SCR + 1536 <= L_QKV, "");
typedef Gemm<true, false, 24576, 16384, 2048, 3, D / 64, true, 32768, 1, 3 * D, 2 * D, 32 * D * 2, 128> GQ;
typedef Gemm<true, false, 16384, 8192, 0, 4, D / 64, true, 65536, 1, D, 128, 8192, 16384> GW;
typedef Gemm<true, true, 16384, 16384, 0, 3, S / 64, true, 65536, 0, F, 128, 8192, 32768> G1;
typedef Gemm<false, true, 16384, 16384, 0, 3, JA, false, 65536, (3 - JA % 3) % 3, S, 1024, 8192, 32768> G2A;
typedef Gemm<false, true, 16384, 8192, 0, 4, F / 64 - 2 * JA, false, 65536, 0, S, 128, 8192, 32768> G2;

DEVI unsigned get_tag() { return __builtin_amdgcn_readfirstlane(*(volatile LAS unsigned*)(uintptr_t)L_TAG); }
DEVI bool spin_fail(int spin, char* ws) {
  if ((spin & 255) != 255) return false;
  gu32* ab = (gu32*)(ws + WS_ABORT);
  const unsigned tag = get_tag();
  if (spin >= SPIN_MAX) { __hip_atomic_store(ab, tag, RLX_AGENT); return true; }
  return __builtin_amdgcn_readfirstlane(__hip_atomic_load(ab, RLX_AGENT)) == tag;
}
DEVI void sync_post(char* ws, int edge, int post, int wid) {
  unsigned o = post >= 0 && wid == 0 ? post * 4u : MB, one = 1u;
  const rsrc_t r = mkrsrc(ws + WS_MAIL + edge * (MEDGE * 4), MEDGE * 4);
  asm volatile("s_waitcnt vmcnt(0) lgkmcnt(0)" : "+v"(o), "+v"(one) : "s"(r) : "memory");
  sched0();
  bar();
  if (wid == 0) __builtin_amdgcn_raw_buffer_store_b32(one, r, o, 0, 16);
  sched0();
}
DEVI void sync_wait(char* ws, int edge, int line, int lane, unsigned want, int wid) {
  sched0();
  if (wid == 3) {
    gu32* pw = (gu32*)(ws + WS_MAIL) + edge * MEDGE + line * 64 + (lane & 31);
    __builtin_amdgcn_s_sleep(SW0);
    for (int spin = 0;; ++spin) {
      const unsigned v = __hip_atomic_load(pw, RLX_AGENT);
      if (__all(v >= want) || spin_fail(spin, ws)) break;
      __builtin_amdgcn_s_sleep(SW1);
    }
  }
  bar();
}
DEVI unsigned halo_off(int s0, int lw, int lane) {
  const int c = lw * 32 + (lane & 31), row = c >> 3, ch = (c & 7) ^ ((row >> 1) & 7), tok = s0 - 16 + row;
  return (unsigned)((tok < 0 ? 0 : tok) * D * 2 + ch * 16);
}
DEVI int qkv_rot(int bid) { return (bid & 3) * 4 + (bid & 4) * 2; }
DEVI unsigned x_off(int tp, int tq, int w, int fr, int fq) { return (unsigned)(((tq * 64 + (w >> 1) * 32 + fr) * D + tp * 128 + (w & 1) * 64 + pair_chunk(fq) * 8) * 2); }

DEVI void pfb_q(const u16* w, int bid, int j, int n) {
  const int r = slow_res(w);
  if ((r >> 2) == (bid & 1)) {
    const unsigned o = (unsigned)(((bid & 2) * 4 + r) * 128 + (j >> 1) * (3 * D * 2) + (j & 1) * 64 + ((n + qkv_rot(bid)) & 15) * GQ::WSTEP);
    spf1<0>(w, o); spf1<2048>(w, o); spf1<4096>(w, o);
  }
}
constexpr int PFR = NPL * 128 * 1024, PF_END = TBA * 256 + NW1 * PFR / (F * 2) / 8 * 8;
static_assert(TBA * 256 * F * 2 + NW1 * PFR <= S * F * 2 && 3 * PFR >= D * D * 2, "");
struct Pfc {
  const char* p; const char* q; int pc;
  DEVI Pfc(const u16* wo, const u16* w1, unsigned g) : p((const char*)wo + g * (NPL * 128)), q((const char*)w1 + TBA * 256 * F * 2 + g * (NPL * 128)), pc(0) {}
  DEVI void go() {
    unsigned t, u;
    asm volatile("s_getreg_b32 %2, hwreg(HW_REG_IB_STS, 8, 4)\n\ts_cmp_lt_u32 %1, %5\n\ts_cselect_b32 %3, 0, 1\n\ts_or_b32 %2, %2, %3\n\ts_cbranch_scc1 1f\n\t"
                 "s_load_dwordx2 s[100:101], %0, 60\n\ts_load_dwordx2 s[100:101], %0, 188\n\ts_load_dwordx2 s[100:101], %0, 316\n\t"
                 "s_load_dwordx2 s[100:101], %0, 444\n\ts_load_dwordx2 s[100:101], %0, 572\n\ts_load_dwordx2 s[100:101], %0, 700\n\t"
                 ".if %8 == 7\n\ts_load_dwordx2 s[100:101], %0, 828\n\t.endif\n\t"
                 "s_mov_b32 %2, %7\n\ts_cmp_lt_u32 %1, 2\n\ts_cselect_b32 %2, %6, %2\n\ts_mov_b64 s[40:41], %0\n\ts_add_u32 s40, s40, %2\n\ts_addc_u32 s41, s41, 0\n\t"
                 "s_cmp_eq_u32 %1, 2\n\ts_cselect_b64 %0, %4, s[40:41]\n\ts_add_u32 %1, %1, 1\n1:"
                 : "+s"(p), "+s"(pc), "=&s"(t), "=&s"(u) : "s"(q), "i"(3 + NW1), "i"((D * D * 2 - PFR) / 2), "i"(PFR), "i"(NPL) : "s40", "s41", "s100", "s101", "scc");
  }
};
DEVI void spare_touch(const u16* w1, const u16* w2, int bid, int wid, int lane) {
  const int j = ((bid >> 3) - 28) * 8 + wid, fr = lane & 15, fq = lane >> 4;
  const unsigned scr = L_QKV + wid * 256;
#pragma unroll 1
  for (int r = PF_END + (j * 2 + ((bid >> 2) & 1)) * 4; r < S; r += 256) { if (fr < 14) touch4(w1 + (size_t)r * F + (bid & 3) * 896, fq * (F * 2) + fr * 128, scr); wait_vm<SPW>(); }
#pragma unroll 1
  for (int r = (j * 2 + (bid & 1)) * 4; r < JA * 64; r += 256) { touch4(w2 + (size_t)(r + (bid & 2) * JA * 32) * S + (bid & 4) * 256, fq * (S * 2) + fr * 128, scr); wait_vm<SPW>(); }
#pragma unroll 1
  for (int r = JA * 128 + ((31 - j) * 2 + (bid & 1)) * 8; r < F; r += 512) { touch4(w2 + (size_t)r * S + ((bid & 7) >> 1) * 512, (lane >> 3) * (S * 2) + (lane & 7) * 128, scr); wait_vm<SPW>(); }
}

template <int TH> DEVI void qkv_attn(char* ws, int l, int h, int lane, int hp, int sb, int cw) {
  asm volatile("; half %1" : "+v"(lane) : "i"(TH));
  const int fr = lane & 15, fq = lane >> 4;
  GQ::Comp<OQ<TH>> g;
#pragma unroll
  for (int c = 0; c < 2; ++c) { g.prd[0][c] = 32768 + rd_km(lane, h * 2 + c); g.qrd[0][c] = 32768 + rd_kc(lane, c); }
  f32x4 acc[6][5];
#pragma unroll
  for (int i = 0; i < 6; ++i)
#pragma unroll
    for (int j = 0; j < 5; ++j) acc[i][j] = (f32x4){0.f, 0.f, 0.f, 0.f};
  const int par = fr & 1, da = fr - 4 * fq - par;
  const bool ca = da >= 0, cb = da >= 2;
  const unsigned tm = L_TAB + (16 + 64 * TH) * 4, tc = L_CS + h * 128;
  unsigned am = tm + fr * 4 - 320, a4 = tm + fq * 16 - 320, aa = tm + 576 + (4 * fq + par - (ca ? 0 : 16)) * 4, ab = tm + 576 + (4 * fq + par + 2 - (cb ? 0 : 16)) * 4;
  unsigned c4 = tc + fq * 16, c1 = tc + 512 + fr * 4, xb = L_QKV + h * 3072 + lane * 16, hm = par ? 0xffff0000u : 0xffffu, m0 = ca ? 0u : hm, m1 = cb ? 0u : hm;
  unsigned oo = ph((unsigned)((((sb * 2 + TH) * 16 + hp) << 13) + fr * 128 + (((h * 4 + pair_chunk(fq)) ^ (fr >> 1)) << 4)));
  asm volatile("" : "+v"(am), "+v"(a4), "+v"(aa), "+v"(ab), "+v"(c4), "+v"(c1), "+v"(xb), "+v"(hm), "+v"(m0), "+v"(m1), "+v"(oo));
  g.template prep<false>(acc);
  if (l) sync_wait(ws, 3, sb * 16 + hp, lane, lane < 24 && (lane >= 8 || sb > 0) ? 1u : 0u, TH * 2 + h);
  g.template run_steps<false>(acc, [&](int it, int i) { if (i == 0 && cw >= 0 && it == -2) code_warm(cw, lane, L_SCR + (TH * 2 + h) * 256); });
  const rsrc_t ro = mkrsrc(ws + WS_O, S * D * 4);
  if (!PWL || l == 0) bar();
  float mt[5], cv[2], rq[4], ra[4], rb[4]; f32x4 m4[5], cq[2], ck[2];
#pragma unroll
  for (int c = 0; c < 2; ++c) {
    ck[c] = *(const LAS f32x4*)(uintptr_t)(c4 + 256 + c * 64); cv[c] = *(const LAS float*)(uintptr_t)(c1 + c * 64);
    cq[c] = *(const LAS f32x4*)(uintptr_t)(c4 + c * 64);
  }
#pragma unroll
  for (int j = 4; j >= 0; --j) {
    const int o = 320 + (j == 4 ? -64 - 256 * TH : j * 64);
    if (j < 4 || !TH) mt[j] = *(const LAS float*)(uintptr_t)(am + o);
    if (j < 4 || TH) m4[j] = *(const LAS f32x4*)(uintptr_t)(a4 + o);
  }
#pragma unroll
  for (int j = 0; j < 4; ++j) { rq[j] = *(const LAS float*)(uintptr_t)(am + 896 + j * 64); ra[j] = *(const LAS float*)(uintptr_t)(aa + j * 64); rb[j] = *(const LAS float*)(uintptr_t)(ab + j * 64); }
  u32x2 kp[2][5], qp[2][4], vp[2][5];
  auto fk = [&](int j) { kp[0][(j + 1) % 5] = pk4(acc[1][j] + ck[0] * mt[j]); kp[1][(j + 1) % 5] = pk4(acc[4][j] + ck[1] * mt[j]); };
  auto fv = [&](int j) { vp[0][(j + 1) % 5] = pk4(acc[2][j] + m4[j] * cv[0]); vp[1][(j + 1) % 5] = pk4(acc[5][j] + m4[j] * cv[1]); };
  auto fqq = [&](int j) { qp[0][j] = pk4(acc[0][j] + cq[0] * mt[j]); qp[1][j] = pk4(acc[3][j] + cq[1] * mt[j]); };
  g.template pend<9, 13>(acc);
#pragma unroll
  for (int i = 0; i < 8; ++i) { __builtin_amdgcn_sched_group_barrier(0x100, 3, 0); __builtin_amdgcn_sched_group_barrier(0x8, 1, 0); }
  sched0();
  volatile LAS unsigned* xf = (volatile LAS unsigned*)(uintptr_t)(L_TAG + 16 + (h * 2 + TH) * 4);
  g.template pend<6, 9>(acc);
  if (TH) { fv(4); *(volatile LAS bf16x8*)(uintptr_t)(xb + 2048) = cat(vp[0][0], vp[1][0]); *xf = l + 1; fk(3); }
  else { fk(3); fv(3); *(volatile LAS bf16x8*)(uintptr_t)xb = cat(kp[0][4], kp[1][4]); *(volatile LAS bf16x8*)(uintptr_t)(xb + 1024) = cat(vp[0][4], vp[1][4]); *xf = l + 1; }
#pragma unroll
  for (int i = 0; i < 6; ++i) { __builtin_amdgcn_sched_group_barrier(0x8, 1, 0); __builtin_amdgcn_sched_group_barrier(0x2, 3, 0); }
  sched0();
  g.template pend<3, 6>(acc); g.template pend<0, 3>(acc);
  fqq(3); fk(2); fqq(2); fk(1);
#pragma unroll
  for (int i = 0; i < 6; ++i) { __builtin_amdgcn_sched_group_barrier(0x8, 1, 0); __builtin_amdgcn_sched_group_barrier(0x2, 2, 0); __builtin_amdgcn_sched_group_barrier(0x8, 1, 0); __builtin_amdgcn_sched_group_barrier(0x2, 3, 0); }
  sched0();
  const f32x4 z = {0.f, 0.f, 0.f, 0.f};
  f32x4 sc[4][2]; float a[4], b[4], m[4];
  auto scores = [&](int j) {
#pragma unroll
    for (int u = 1; u >= 0; --u) sc[j][u] = mfma(cat(kp[0][j + u], kp[1][j + u]), cat(qp[0][j], qp[1][j]), z);
  };
  auto pick = [&](int j) {
    const float r = rq[j] * (0.17677669529663687f * 1.4426950408889634f);
    const f32x4 s0 = sc[j][0], s1 = sc[j][1];
    a[j] = sel(par, sel(ca, s1[1], s0[1]), sel(ca, s1[0], s0[0])) * (r * ra[j]);
    b[j] = sel(par, sel(cb, s1[3], s0[3]), sel(cb, s1[2], s0[2])) * (r * rb[j]);
    if (!TH && j == 0) { a[j] = sel(ca || sb, a[j], -1e9f * 1.4426950408889634f); b[j] = sel(cb || sb, b[j], -1e9f * 1.4426950408889634f); }
    m[j] = __builtin_fmaxf(a[j], b[j]);
  };
  auto expo = [&](int j) { a[j] = __builtin_amdgcn_exp2f(a[j] - m[j]); b[j] = __builtin_amdgcn_exp2f(b[j] - m[j]); m[j] = a[j] + b[j]; };
  auto pv = [&](int j) {
    const unsigned pa = pk2(a[j] * ra[j], a[j] * ra[j]), pb = pk2(b[j] * rb[j], b[j] * rb[j]);
    const bf16x8 pf = __builtin_bit_cast(bf16x8, (u32x4){pa & m0, pb & m1, pa & (m0 ^ hm), pb & (m1 ^ hm)});
#pragma unroll
    for (int c = 0; c < 2; ++c) sc[j][c] = mfma(cat(vp[c][j], vp[c][j + 1]), pf, z);
  };
  auto outp = [&](int j) { st16(ro, oo + j * 4096, pack_pair(sc[j][0] * m[j], sc[j][1] * m[j])); };
  auto soft2 = [&](int j) {
    float u, w;
    pick(j); pick(j + 1);
    swp32(m[j], m[j + 1]); u = vmax(m[j], m[j + 1]); xch16(u, u, w); u = vmax(u, w); xch32(u, m[j], m[j + 1]);
    expo(j); expo(j + 1);
    pv(j + 1); pv(j);
    swp32(m[j], m[j + 1]); u = m[j] + m[j + 1]; xch16(u, u, w); u = __builtin_amdgcn_rcpf(u + w); xch32(u, m[j], m[j + 1]);
    outp(j + 1); outp(j);
  };
  unsigned f; u32x4 xv, xk;
  auto xrd = [&]() {
    f = xf[1 - 2 * TH]; xv = *(volatile LAS u32x4*)(uintptr_t)(xb + (TH ? 1024 : 2048));
    if (TH) xk = *(volatile LAS u32x4*)(uintptr_t)xb;
  };
  auto xget = [&]() {
    sched0();
    while (__builtin_expect(__builtin_amdgcn_readfirstlane(f) != l + 1, 0)) xrd();
    vp[0][0] = (u32x2){xv[0], xv[1]}; vp[1][0] = (u32x2){xv[2], xv[3]};
    if (TH) { kp[0][0] = (u32x2){xk[0], xk[1]}; kp[1][0] = (u32x2){xk[2], xk[3]}; }
  };
  if (ATT4) {
    scores(3); scores(2);
    fqq(1);
    xrd();
    sched0();
    fk(0); fqq(0);
    if (TH) fv(3); else fk(4);
    scores(1);
    fv(2); fv(1); fv(0);
    pick(3); pick(2);
#pragma unroll
    for (int c = 0; c < 2; ++c) asm volatile("" : "+v"(qp[c][0]), "+v"(kp[c][1]), "+v"(vp[c][1]), "+v"(vp[c][2]), "+v"(vp[c][3]), "+v"(vp[c][4]), "+v"(sc[1][c]), "+v"(a[2 + c]), "+v"(b[2 + c]), "+v"(m[2 + c]));
    if (!TH) asm volatile("" : "+v"(kp[0][0]), "+v"(kp[1][0]));
    xget();
    scores(0);
    pick(1); pick(0);
    float u, w;
    swp32(m[0], m[2]); swp32(m[1], m[3]); u = m[0]; w = m[1];
    asm volatile("v_max_f32 %0, %0, %2\n\tv_max_f32 %1, %1, %3\n\ts_nop 1" : "+v"(u), "+v"(w) : "v"(m[2]), "v"(m[3]));
    swp16(u, w); u = vmax(u, w);
    xch16(u, u, w); xch32(u, m[0], m[2]); xch32(w, m[1], m[3]);
    expo(3); expo(2); expo(1); expo(0);
    pv(3); pv(2); pv(1); pv(0);
    swp32(m[0], m[2]); swp32(m[1], m[3]); u = m[0] + m[2]; w = m[1] + m[3];
    swp16(u, w); u = __builtin_amdgcn_rcpf(u + w);
    xch16(u, u, w); xch32(u, m[0], m[2]); xch32(w, m[1], m[3]);
    outp(3); outp(2); outp(1); outp(0);
  } else {
    scores(3); scores(2);
    sched0();
    if (TH) fv(3); else fk(4);
    fv(2);
    xrd();
    sched0();
    fv(1); fqq(1); fk(0); fqq(0);
    soft2(2);
    fv(0);
#pragma unroll
    for (int c = 0; c < 2; ++c) asm volatile("" : "+v"(qp[c][0]), "+v"(qp[c][1]), "+v"(kp[c][1]), "+v"(vp[c][1]));
    xget();
    scores(1); scores(0);
    soft2(0);
  }
}

extern "C" __global__ __launch_bounds__(512, 2) void mega_kernel(const u16* x0, const u16* wqkv, const u16* wo, const u16* w1, const u16* w2, const u16* b1, const u16* b2, u16* out, char* ws) {
  const int wid0 = __builtin_amdgcn_readfirstlane(threadIdx.x >> 6);
  asm volatile("s_getpc_b64 s[40:41]\n\ts_andn2_b32 s40, s40, 63\n\ts_load_dwordx2 s[100:101], s[40:41], %0 offset:124\n\ts_load_dwordx2 s[100:101], s[40:41], %0 offset:252"
               :: "s"(__builtin_amdgcn_readfirstlane(threadIdx.x) << 2) : "s40", "s41", "s100", "s101", "scc");
  sched0();
#define PHASE_VARS \
      int tid = threadIdx.x, bid = blockIdx.x, wid = wid0; \
      asm volatile("" : "+v"(tid)); asm volatile("" : "+s"(bid), "+s"(wid), "+s"(ws)); \
      const int lw = wid - 4, lt = tid - 256, lane = tid & 63, fr = lane & 15, fq = lane >> 4, wp = wid & 1, wq = wid >> 1; \
      const unsigned j = (bid >> 3) * 4 + lw; \
      int hp, sb; tile_map<16, 16, 4, 8>(bid, hp, sb);            \
      int tp, tq; tile_map<8, 32, 4, 8>(bid, tp, tq);             \
      int fp, fq1; tile_map<28, 8, 7, 4>(bid, fp, fq1);           \
      const int cw = bid < 16 ? wid + 4 * (bid >> 3) : -1; \
      (void)cw; (void)j; (void)lt; (void)wp; (void)wq; (void)lw; (void)fr; (void)fq; (void)hp; (void)sb; (void)tp; (void)tq; (void)fp; (void)fq1;
#define LOADER_VARS PHASE_VARS asm volatile("" : "+s"(wqkv), "+s"(wo), "+s"(w1), "+s"(w2));

  if (__builtin_expect(wid0 >= 4, 1)) {
    const u16* xin = x0;
    Pfc pf(wo, w1, blockIdx.x * 4 + wid0 - 4);
#pragma unroll 1
    for (int l = 0; l < NL; ++l) {
      {
        LOADER_VARS
        const int s0 = sb * 128;
        GQ::Loader L(wqkv + hp * 64, xin, lw, lane, qkv_rot(bid));
        L.issue_w<GQ::SB>(L.w + L.kord(0) * GQ::WSTEP);
        sched0();
        L.seta((unsigned)(s0 * D * 2), halo_off(s0, lw, lane), lw, lane);
        if (l == 0) { L.issue_a<GQ::SB>(L.ab + L.kord(0) * 128); L.pre<1, true, true>(); }
        else {
          L.pre<1, true, false>();
          if (QBR == 0) bar();
#pragma unroll
          for (int n = 2; n < 7; ++n) pfb_q(wqkv, bid, j, n);
          if (QBR == 1) bar();
        }
        sched0();
        const rsrc_t rs = mkrsrc(ws + WS_ST, MB);
        f32x4 c = {0.f, 0.f, 0.f, 0.f}; f32x2 s1 = {0.f, 0.f}, s2 = {0.f, 0.f}, o = {0.f, 0.f}; float pv = 0.f;
        bf16x8 ones = {0x3F80, 0x3F80, 0x3F80, 0x3F80, 0x3F80, 0x3F80, 0x3F80, 0x3F80};
        const int ct = lw & 1 ? 8 + (sb & 3) : sb & 7; const bool cs = l == 0 && lw >= 2;
        int tpf = (slow_res(wqkv) >> 2) != (bid & 1) ? 0 : lw < 2 ? 10 : 9;
        unsigned ra = GQ::AOFF + ((hp & 3) * 32 + lw * 8) * 128 + lane * 16, rb = rd_km(lane, ct);
        unsigned so = lane & 7 ? MB : ph((unsigned)(((l * S + s0 + (hp & 3) * 32 + lw * 8 + (lane >> 3)) * 4 + (hp >> 2)) * 16));
        unsigned co = fr >= 2 || !cs ? MB : ph((unsigned)(ST_CS + ((hp * 96 + ct * 8 + fq * 2 + fr) * 2 + (sb >> 3)) * 16));
        unsigned to[3], xo = x_off(tp, tq, lw, fr, fq), q2 = ((bid & 2) * 4 + slow_res(wqkv)) * 128 + ((bid >> 3) * 2 + (lw & 1)) * (3 * D * 2);
#pragma unroll
        for (int i = 0; i < 3; ++i) {
          const int r = s0 - 16 + (i == 2 && lw ? 64 : i * 64) + (lt >> 2);
          to[i] = ph((unsigned)(((l * S + (r < 0 ? 0 : r)) * 4 + (lt & 3)) * 16));
        }
        unsigned ta = (lt & 3) < (lw ? 2 : 3) ? L_TAB + ((lt & 3) * 64 + (lt >> 2)) * 4 : L_SCR, ts = ph((unsigned)(ST_CS + (hp * 192 + (lw ? lt - 64 : lt)) * 16));
        asm volatile("" : "+v"(ra), "+v"(rb), "+v"(so), "+v"(co), "+v"(to[0]), "+v"(to[1]), "+v"(to[2]), "+v"(xo), "+v"(ta), "+v"(ts), "+v"(c), "+v"(s1), "+v"(s2), "+v"(ones), "+v"(o));
        asm volatile("" : "+s"(q2), "+s"(tpf));
        auto pfq = [&](int n) { const unsigned o = q2 + L.kord(n) * GQ::WSTEP; spf2<0>(wqkv, o); spf2<2048>(wqkv, o); spf2<4096>(wqkv, o); };
        if (l) L.start();
        unsigned tag, tg2; u32x4 gs, a[3];
        auto rowst = [&](int i) {
          const float m0 = __uint_as_float(a[i][0]), m = red4(m0) * 0.25f, q = red4(__uint_as_float(a[i][2]) + 256.f * (m0 - m) * (m0 - m));
          o = (f32x2){sel((lt & 3) == i, m, o.x), sel((lt & 3) == i, q, o.y)};
          asm volatile("" : "+v"(o));
        };
        for_c<0, 16 - GQ::H>([&](auto tc) {
          constexpr int t = tc.value;
          u32x4 v; unsigned tv;
          L.step<t, t == 5 || t == 6 ? 1 : t == 9 || t == 10 ? 4 : 0>([&](unsigned ls) {
            if (t < 4) v = *(const LAS u32x4*)(uintptr_t)(ls + ra);
            if (t == 3) tv = *(volatile LAS unsigned*)(uintptr_t)L_TAG;
          }, [&]() {
            if (t < 8 && cs) {
              constexpr int ls = 32768 + (t + 1) % 3 * GQ::SB;
              bf16x4 x[4];
#pragma unroll
              for (int e = 0; e < 4; ++e) x[e] = lds_tr4(rb + (ls & ~0x7fff) + (ls & 0x7fff) + (e >> 1) * 4096 + (e & 1) * 512);
#pragma unroll
              for (int ks = 0; ks < 2; ++ks) c = mfma(__builtin_shufflevector(x[2 * ks], x[2 * ks + 1], 0, 1, 2, 3, 4, 5, 6, 7), ones, c);
            } else if (t >= 4) {
              if (t >= 10 || t >= tpf) { if (t < TC1) pf.go(); }
              else if (lw < 2) pfq(t + 4);
              else if (t == 8) { pfq(14); pfq(15); }
            }
            if (t < 4) {
              if (t == 0) pv = bflo(v[0]);
#pragma unroll
              for (int e = 0; e < 4; ++e) { const f32x2 d = (f32x2){bflo(v[e]), bfhi(v[e])} - pv; s1 += d; s2 += d * d; }
              asm volatile("" : "+v"(s1), "+v"(s2));
            }
            if (t == 3) { tag = __builtin_amdgcn_readfirstlane(tv); tg2 = tag2(tag); if (tpf == 10) pfq(7); }
            if (t == 4) {
              const float d1 = (s1.x + s1.y) * (1.f / 32.f), ml = pv + d1, m = red8(ml) * 0.125f;
              const float q = red8(vmax((s2.x + s2.y) - 32.f * d1 * d1, 0.f) + 32.f * (ml - m) * (ml - m));
              gs = (u32x4){__float_as_uint(m), tag, __float_as_uint(q), tg2};
              asm volatile("" : "+v"(gs));
            }
            if (t == 8) { gs = (u32x4){__float_as_uint(sel(fr, c[2], c[0])), tag, __float_as_uint(sel(fr, c[3], c[1])), tg2}; asm volatile("" : "+v"(gs)); }
            if (t == 11) {
#pragma unroll
              for (int i = 0; i < 3; ++i) ld_ok(a[i]);
              for (int spin = 0;; ++spin) {
                unsigned bad = 0;
#pragma unroll
                for (int i = 0; i < 3; ++i) bad |= (a[i][1] ^ tag) | (a[i][3] ^ tg2);
                if (__builtin_expect(__all(bad == 0), 1) || spin_fail(spin, ws)) break;
                __builtin_amdgcn_s_sleep(8);
#pragma unroll
                for (int i = 0; i < 3; ++i) ld_again(a[i], rs, to[i]);
              }
              rowst(0);
            }
            if (t == 12) { rowst(1); rowst(2); }
            if (t == 13) { LAS float* p = (LAS float*)(uintptr_t)ta; p[0] = -o.x; p[144] = __builtin_amdgcn_rsqf(o.y * (1.f / D) + EPS); }
          }, [&]() {
            if (t == 4) st16(rs, so, gs);
            if (t == 8) {
              st16(rs, co, gs);
              sched0();
#pragma unroll
              for (int i = 0; i < 3; ++i) ld_raw(a[i], rs, to[i]);
            }
            if (t == 13) ld_raw(gs, rs, ts);
          });
        });
        sched0(); wait_vm<GQ::CNT + 1>(); bar();
        wait_lgkm0(); wait_vm<1>(); bar();
        wait_vm<0>();
        ld_ok(gs);
        if (l == 0) {
          for (int spin = 0;; ++spin) {
            if (__builtin_expect(__all(((gs[1] ^ tag) | (gs[3] ^ tg2)) == 0), 1) || spin_fail(spin, ws)) break;
            __builtin_amdgcn_s_sleep(8);
            ld_again(gs, rs, ts);
          }
          float c0 = __uint_as_float(gs[0]), c1 = __uint_as_float(gs[2]);
          c0 += dppf<0xB1>(c0); c1 += dppf<0xB1>(c1);
          *(LAS float*)(uintptr_t)(L_CS + (lw ? lt - 64 : lt) * 4) = sel(lt & 1, c1, c0);
        }
        if (l == 0) {
#pragma unroll
          for (int p = 0; p < 4; ++p) dma16((const char*)xin + (p >> 1) * 32768 + (p & 1) * 64, xo, L_X + lw * 4096 + p * 1024);
        }
        if (l == 0 && lw < TBA) { if (fr < 14) touch4(w1 + (size_t)(((bid >> 3) * TBA + lw) * 8 + (bid & 4)) * F + (bid & 3) * 896, fq * (F * 2) + fr * 128, L_SCR + lw * 256); }
        if (l == 0 && PWL) { wait_lgkm0(); bar(); }
      }
      {
        LOADER_VARS
        GW::Loader L(wo + tp * 128, ws + WS_O + (size_t)tq * 32 * 8192, lw, lane);
        unsigned po = ((bid & 1) * 8 + (slow_res(wo) ^ (j & 1))) * 128 + (j >> 1) * (D * 2);
        if (!PWL) { wait_lgkm0(); bar(); L.pre_w(); }
#pragma unroll
        for (int k = GW::H; k < 10; ++k) spf2(wo, po + k * GW::WSTEP);
        asm volatile("" : "+s"(po));
        if (lw < TBA) wait_vm<(PWL ? 0 : GW::H * 4) + 1>(); else wait_vm<PWL ? 0 : GW::H * 4>();
        sched0();
        bar();
        if (PWL) L.pre_w();
#pragma unroll
        for (int k = 10; k < 16; ++k) spf2(wo, po + k * GW::WSTEP);
        L.start();
        L.run(16 - GW::H, [&](int) { pf.go(); });
        pf.go();
        L.tail<GW::H - 1>();
        bar();
      }
      {
        LOADER_VARS
        if (bid < 224) {
          G1::Loader L(w1 + fp * 128, ws + WS_H + (size_t)fq1 * 64 * 16384, lw, lane);
          L.pre_w();
          const int g0 = (bid & 3) * 14, ga = (g0 + ((slow_res(w1) - g0) & 7)) ^ (j & 1), gb = ga + 8 < g0 + 14 ? ga + 8 : ga;
          const unsigned j2 = j < 32 ? 112 + (j & 15) : j;
          unsigned pa = (j >> 1) * (F * 2) + ga * 128, pb = (j >> 1) * (F * 2) + gb * 128, pd = (j2 >> 1) * (F * 2) + (j & 16 ? gb : ga) * 128;
          asm volatile("" : "+s"(pa), "+s"(pb), "+s"(pd));
          auto pfb = [&](int k) { spf2(w1, pa + k * G1::WSTEP); spf2(w1, pb + k * G1::WSTEP); spf2(w1, pd + k * G1::WSTEP); };
#pragma unroll
          for (int k = G1::H; k < G1::H + PA_1; ++k) pfb(k);
          wait_lgkm0(); pf.go();
          sched0();
          bar();
          pf.go();
          L.start();
          L.run(32 - G1::H, [&](int t) {
            if (pf.pc < 3 + NW1) pf.go();
            else if (t < 32 - G1::H - PA_1) pfb(t + G1::H + PA_1);
          });
          L.tail<G1::H - 1>();
          bar();
        } else {
          wait_lgkm0(); pf.go();
          bar();
          if (l == 0) {
            __builtin_amdgcn_s_sleep(24);
            while (pf.pc < 3 + NW1) { wait_lgkm0(); pf.go(); }
            spare_touch(w1, w2, bid, wid, lane);
          }
        }
        pf.pc = 99;
      }
      {
        LOADER_VARS
        constexpr int NB = F / 64 - 2 * JA;
        const char* ub = ws + WS_U + (size_t)tp * 112 * 16384;
        G2A::Loader A((const char*)(w2 + (tq & 23) * 64) + (tq & 8 ? JA * G2::WSTEP : 0), ub + (tq & 8 ? JA * 32768 : 0), lw, lane);
        A.pre_w();
        unsigned pb = ((bid & 4) * 4 + (j & 1) * 8 + slow_res(w2)) * 128 + (j >> 1) * (S * 2) + JA * G2::WSTEP, pa = pb - (bid & 2 ? 0 : JA * G2::WSTEP);
        asm volatile("" : "+s"(pa), "+s"(pb));
        auto side = [&](int n) { n += G2A::H + PA_2; if (n < JA + NB) spf2(w2, (n < JA ? pa : pb) + n * G2::WSTEP); };
#pragma unroll
        for (int k = 0; k < PA_2; ++k) side(k - PA_2);
        G2::Loader L((const char*)(w2 + tq * 64) + 2 * JA * G2::WSTEP, ub + 2 * JA * 32768, lw, lane);
        gu32* xf = (gu32*)(ws + WS_MAIL) + (l * 4 + 2) * MEDGE + (tq * 8 + tp) * 64 + 28 + lw;
        const char* xp = ws + WS_XP + (unsigned)(((bid ^ 2) * 4 + lw) << 14);
        unsigned fl = L_QKV + lw * 256, xv = ph(lane * 16);
        asm volatile("" : "+s"(xf), "+s"(xp), "+s"(fl)); asm volatile("" : "+v"(xv));
        sched0();
        bar();
        A.start();
        A.run(JA - G2A::H, [&](int t) { side(t); });
        auto stage = [&](auto s) { L.issue_w<s.value * G2::SB>(L.w + s.value * G2::WSTEP); L.issue_a<s.value * G2::SB>(L.ab + s.value * 32768); sched0(); };
        wait_vm<G2A::CNT>(); bar(); stage(IC<0>{}); side(JA - 2);
        wait_vm<G2::CNT>(); bar(); stage(IC<1>{}); side(JA - 1);
        wait_vm<G2::CNT>(); bar(); stage(IC<2>{}); stage(IC<3>{}); side(JA);
        L.t = 1; L.islot = 0;
        L.run(NB - G2::H, [&](int t) {
          if (t == NB - G2::H - FLS) asm volatile("s_mov_b32 m0, %2\n\ts_nop 0\n\tglobal_load_lds_dword %0, %1 sc1" :: "v"(0u), "s"(xf), "s"(fl) : "memory");
          side(JA + t);
        });
        auto xdma = [&]() {
#pragma unroll
          for (int i = 0; i < 4; ++i) dma16(xp + i * 2048, xv, L_X + lw * 4096 + i * 1024);
          sched0();
        };
        sched0(); wait_vm<2 * G2::CNT>(); bar();
        if (__builtin_amdgcn_readfirstlane(*(volatile LAS unsigned*)(uintptr_t)fl)) { xdma(); wait_vm<G2::CNT + 4>(); bar(); wait_vm<4>(); bar(); }
        else {
          wait_vm<G2::CNT>(); bar(); wait_vm<0>(); bar();
          for (int spin = 0;; ++spin) {
            if (__builtin_amdgcn_readfirstlane(__hip_atomic_load(xf, RLX_AGENT)) || spin_fail(spin, ws)) break;
            __builtin_amdgcn_s_sleep(2);
          }
          xdma();
        }
        wait_vm<0>(); bar();
        xin = (const u16*)(ws + WS_X2);
        if (NL == 1) bar();
      }
    }
    return;
  }

  {
    PHASE_VARS
    if (cw >= 0) code_warm(cw, lane, L_SCR + wid * 256);
    pfb_q(wqkv, bid, (bid >> 3) * 4 + wid, 0); pfb_q(wqkv, bid, (bid >> 3) * 4 + wid, 1);
    if (wid == 3) {
      if ((slow_res(wqkv) >> 2) == (bid & 1)) {
        __builtin_amdgcn_s_sleep(ET);
        if ((lane & 3) < 3 && lane < 40) touch4(wqkv, (unsigned)(((qkv_rot(bid) * 64 + 128 + (bid >> 3) * 10 + (lane >> 2)) & 1023) * (3 * D * 2) + (lane & 3) * (D * 2) + ((bid & 2) * 4 + slow_res(wqkv)) * 128), L_SCR + 768);
      }
    } else {
      const unsigned ep = __hip_atomic_load((gu32*)(ws + WS_EPOCH), RLX_AGENT);
      const rsrc_t rz = mkrsrc(ws + WS_XCH, 3 * MB);
      if (tid < 128) st16(rz, ph((((tid >> 6) * 256 + tq * 8 + tp) * 64 + lane) * 16), (u32x4){0u, SENT, 0u, SENT});
      else {
        const int e = (tid >> 3) & 7, line = e == 0 || e == 7 ? (2 * sb + (e & 1)) * 8 + (hp & 7) : e * 256 + bid;
        st16(rz, WS_MAIL - WS_XCH + line * 256 + (tid & 7) * 16, (u32x4){0u, 0u, 0u, 0u});
      }
      if (wid == 1 && bid < 224) touch4(b1 + fp * 128, lane * 4, L_BIAS);
      if (wid == 2) touch4(b2 + tq * 64, (lane & 31) * 4, L_BIAS + 256);
      if (wid == 0) { *(LAS unsigned*)(uintptr_t)L_TAG = ep + 1u; *(LAS u32x4*)(uintptr_t)(L_TAG + 16) = (u32x4){0u, 0u, 0u, 0u}; }
      asm volatile("s_waitcnt vmcnt(0)" :: "v"(ep) : "memory");
    }
  }
#pragma unroll 1
  for (int l = 0; l < NL; ++l) {
    {
      PHASE_VARS
      if (wid & 2) qkv_attn<1>(ws, l, wp, lane, hp, sb, cw);
      else qkv_attn<0>(ws, l, wp, lane, hp, sb, cw);
      sync_post(ws, l * 4, lane < 16 ? (sb * 16 + lane) * 64 + hp : -1, wid);
    }
    {
      PHASE_VARS
      GW::Comp<O42<false, WNE>> g; g.setup(wp, wq, lane);
      f32x4 acc[4][2];
#pragma unroll
      for (int i = 0; i < 4; ++i)
#pragma unroll
        for (int jj = 0; jj < 2; ++jj) acc[i][jj] = (f32x4){0.f, 0.f, 0.f, 0.f};
      unsigned xa = L_X + wid * 4096 + lane * 16;
      { bf16x8 s[2]; sel_frags(true, fr, fq, s); tile_add(acc, s, xa); }
      const rsrc_t rx = mkrsrc(ws + WS_XCH + l * (MB / 2), MB / 2), rh = mkrsrc(ws + WS_H, S * D * 4);
      unsigned xs = fq & 1 ? MB : ph((unsigned)(((tq * 8 + tp) * 64 + wp * 32 + wq * 16 + fr) * 16 + fq * 4)), xl = ph((unsigned)((tq * 512 + fq * 128 + wq * 16 + fr) * 16));
      const unsigned h0 = ph((unsigned)(((tp * 32 + tq) << 14) + wp * 8192 + (wq * 32 + fr) * 128 + ((pair_chunk(fq) ^ (km_f(fr) << 1)) << 4)));
      u32x4 ho = {h0, h0 ^ 64, h0 + 4096, (h0 ^ 64) + 4096};
      bf16x8 ones = {0x3F80, 0x3F80, 0x3F80, 0x3F80, 0x3F80, 0x3F80, 0x3F80, 0x3F80};
      asm volatile("" : "+v"(xa), "+v"(xs), "+v"(xl), "+v"(ho), "+v"(ones));
      g.prep<false>(acc);
      mfma_gap();
      sync_wait(ws, l * 4, tq * 8 + tp, lane, (lane & 31) < 16 ? 1u : 0u, wid);
      g.run_steps<false>(acc, [&](int it, int i) { if (i == 0 && cw >= 0 && it == -2) code_warm(cw, lane, L_SCR + wid * 256); });
      bar();
      sched0();
      g.pend<0, 4>(acc);
      u32x4 pk[2][2]; float mp[2], q[2];
#pragma unroll
      for (int jj = 0; jj < 2; ++jj) {
        f32x4 sm = {0.f, 0.f, 0.f, 0.f};
#pragma unroll
        for (int i = 0; i < 2; ++i) { pk[jj][i] = cat4(pk4(acc[2 * i][jj]), pk4(acc[2 * i + 1][jj])); asm volatile("" : "+v"(pk[jj][i])); sm = mfma(ones, __builtin_bit_cast(bf16x8, pk[jj][i]), sm); }
        mp[jj] = sm[0] * (1.f / 64.f); asm volatile("" : "+v"(mp[jj]));
      }
#pragma unroll
      for (int jj = 0; jj < 2; ++jj) {
        f32x4 qv = {0.f, 0.f, 0.f, 0.f};
#pragma unroll
        for (int i = 0; i < 4; ++i) {
          const unsigned x = pk[jj][i >> 1][(i & 1) * 2], y = pk[jj][i >> 1][(i & 1) * 2 + 1];
          acc[i][jj] = (f32x4){bflo(x), bfhi(x), bflo(y), bfhi(y)} - mp[jj]; qv += acc[i][jj] * acc[i][jj];
        }
        q[jj] = (qv[0] + qv[2]) + (qv[1] + qv[3]);
      }
      {
        float u, w;
        swp32(q[0], q[1]); xch16(q[0] + q[1], u, w);
        __builtin_amdgcn_raw_buffer_store_b64((u32x2){__float_as_uint(sel(fq < 2, mp[0], mp[1])), __float_as_uint(u + w)}, rx, xs, 0, 16);
      }
      sched0();
#pragma unroll
      for (int p = 0; p < 4; ++p) *(LAS u32x4*)(uintptr_t)(xa + p * 1024) = pk[p >> 1][p & 1];
      if (XD) __builtin_amdgcn_s_sleep(XD);
      f32x4 gr[4];
      for (int spin = 0;; ++spin) {
        asm volatile("" ::: "memory");
#pragma unroll
        for (int p = 0; p < 4; ++p) gr[p] = __builtin_bit_cast(f32x4, __builtin_amdgcn_raw_buffer_load_b128(rx, xl + p * 1024, 0, 16));
        const float lo = vmin3(vmin3(gr[0][1], gr[0][3], gr[1][1]), vmin3(gr[1][3], gr[2][1], gr[2][3]), vmin3(gr[3][1], gr[3][3], gr[3][3]));
        if (!__any(lo < 0.f) || spin_fail(spin, ws)) break;
        __builtin_amdgcn_s_sleep(XS);
      }
      {
        const f32x4 t = (gr[0] + gr[1]) + (gr[2] + gr[3]);
        float m[2], u, w, v, y;
#pragma unroll
        for (int jj = 0; jj < 2; ++jj) {
          m[jj] = t[2 * jj] * 0.25f;
          float e = 0.f;
#pragma unroll
          for (int p = 0; p < 4; ++p) { const float d = gr[p][2 * jj] - m[jj]; e += d * d; }
          q[jj] = t[2 * jj + 1] + 64.f * e;
        }
        swp32(m[0], m[1]); swp32(q[0], q[1]);
        xch16((m[0] + m[1]) * 0.5f, u, w); xch16((q[0] + q[1]) + 128.f * (m[0] - m[1]) * (m[0] - m[1]), v, y);
        const float var = ((v + y) + 256.f * (u - w) * (u - w)) * (1.f / D), r1 = __builtin_amdgcn_rsqf(var + EPS);
        xch32((u + w) * 0.5f, m[0], m[1]);
        xch32(r1 * __builtin_amdgcn_rsqf(var * r1 * r1 + EPS), q[0], q[1]);
#pragma unroll
        for (int jj = 0; jj < 2; ++jj) {
          const float c = (mp[jj] - m[jj]) * q[jj];
#pragma unroll
          for (int i = 0; i < 2; ++i) st16(rh, ho[jj * 2 + i], pack_pair(acc[2 * i][jj] * q[jj] + c, acc[2 * i + 1][jj] * q[jj] + c));
        }
      }
      sync_post(ws, l * 4 + 1, lane < 28 ? (tp * 28 + lane) * 64 + tq : -1, wid);
      if (l == 0 && bid == 0 && wid == 0) { const unsigned tag = get_tag(); if (lane == 0) __hip_atomic_store((gu32*)(ws + WS_EPOCH), tag, RLX_AGENT); }
    }
    {
      PHASE_VARS
      if (bid >= 224) {
        if (l == 0) { __builtin_amdgcn_s_sleep(24); spare_touch(w1, w2, bid, wid, lane); }
      } else {
        G1::Comp<O44<false, 32>> g; g.setup(wp, wq, lane);
        f32x4 acc[4][4];
#pragma unroll
        for (int i = 0; i < 4; ++i) { const u32x2 b = *(const LAS u32x2*)(uintptr_t)(L_BIAS + (wp * 4 + i) * 32 + fq * 8);
#pragma unroll
          for (int jj = 0; jj < 4; ++jj) acc[i][jj] = (f32x4){bflo(b.x), bfhi(b.x), bflo(b.y), bfhi(b.y)}; }
        const rsrc_t ro = mkrsrc(ws + WS_U, D * F * 4);
        const unsigned u0 = ph((unsigned)(((fq1 * 56 + fp * 2 + wp) << 14) + (wq * 64 + fr) * 128 + ((pair_chunk(fq) ^ (fr >> 1)) << 4)));
        u32x4 uo = {u0, u0 + 4096, u0 + 8192, u0 + 12288}, uo2 = uo ^ 64;
        asm volatile("" : "+v"(uo), "+v"(uo2));
        g.prep<false>(acc);
        sync_wait(ws, l * 4 + 1, fq1 * 28 + fp, lane, 1u, wid);
        g.run_steps<false>(acc, [&](int it, int i) { if (i == 0 && cw >= 0 && it == -2) code_warm(cw, lane, L_SCR + wid * 256); });
        bar();
        sched0();
        g.pend<0, 8>(acc);
#pragma unroll
        for (int i = 0; i < 2; ++i)
#pragma unroll
          for (int jj = 0; jj < 4; ++jj) st16(ro, (i ? uo2 : uo)[jj], pack_pair(gelu_tanh(acc[2 * i][jj]), gelu_tanh(acc[2 * i + 1][jj])));
      }
      sync_post(ws, l * 4 + 2, bid < 224 && lane < 32 ? (lane * 8 + fq1) * 64 + fp : -1, wid);
    }
    {
      PHASE_VARS
      asm volatile("" : "+s"(out));
      G2A::Comp<O44<true, ANE>> ga; ga.setup(wp, wq, lane);
#pragma unroll
      for (int jj = 0; jj < 4; ++jj) ga.qrd[0][jj] = 65536 + rd_km(lane, wq * 2 + (jj & 1)) + (((jj >> 1) ^ (tq >> 3)) & 1) * 8192;
      G2::Comp<O42<true, BNE>> g; g.setup(wp, wq, lane);
      g.setwin();
      f32x4 ac4[4][4], acc[4][2];
#pragma unroll
      for (int jj = 0; jj < 2; ++jj) { const float b = bf2f(*(const LAS u16*)(uintptr_t)(L_BIAS + 256 + (wq * 2 + jj) * 32 + fr * 2));
#pragma unroll
        for (int i = 0; i < 4; ++i) { ac4[i][jj] = (f32x4){b, b, b, b}; ac4[i][jj + 2] = (f32x4){0.f, 0.f, 0.f, 0.f}; } }
      unsigned xa = L_X + wid * 4096 + lane * 16, xo = (unsigned)((bid * 4 + wid) << 14) + ph(lane * 16), oo = x_off(tp, tq, wid, fr, fq), xo2 = xo + 4096, oo2 = oo + 32768;
      unsigned fo = lane ? MB : (((tq ^ 8) * 8 + tp) * 64 + 28 + wid) * 4u, one = 1u;
      bf16x8 s[2]; sel_frags(false, fr, fq, s); tile_add(ac4, s, xa);
      const rsrc_t rx = mkrsrc(ws + WS_XP, 16 * MB), rf = mkrsrc(ws + WS_MAIL + (l * 4 + 2) * (MEDGE * 4), MEDGE * 4), ro = mkrsrc(l ? (const void*)out : (const void*)(ws + WS_X2), S * D * 2);
      asm volatile("" : "+v"(xa), "+v"(xo), "+v"(oo), "+v"(xo2), "+v"(oo2), "+v"(fo), "+v"(one), "+v"(s[0]), "+v"(s[1]));
      ga.prep<false>(ac4);
      mfma_gap();
      sync_wait(ws, l * 4 + 2, tq * 8 + tp, lane, (lane & 31) < 28 ? 1u : 0u, wid);
      ga.run_steps<false>(ac4, [&](int it, int i) { if (i == 0 && cw >= 0 && it == -2) code_warm(cw, lane, L_SCR + wid * 256); });
      ga.pend<4, 8>(ac4);
#pragma unroll
      for (int i = 0; i < 2; ++i)
#pragma unroll
        for (int jj = 0; jj < 2; ++jj) st16(rx, (jj ? xo2 : xo) + i * 2048, cat4(pk4(ac4[2 * i][jj + 2]), pk4(ac4[2 * i + 1][jj + 2])));
#pragma unroll
      for (int i = 0; i < 4; ++i) { acc[i][0] = ac4[i][0]; acc[i][1] = ac4[i][1]; }
#pragma unroll
      for (int k = 0; k < 2; ++k) { g.fp[2][k] = ga.fp[2][k]; g.fp[3][k] = ga.fp[3][k]; g.fq[0][k] = ga.fq[0][k]; g.fq[1][k] = ga.fq[1][k]; }
      sched0();
      g.run_steps<true>(acc, [&](int it, int i) {
        if (i == PS % 4 && it == PS / 4 - (F / 64 - 2 * JA) / 4) {
          wait_vm<0>();
          __builtin_amdgcn_raw_buffer_store_b32(one, rf, fo, 0, 16);
        }
      });
      bar();
      sched0();
      g.pend<0, 4>(acc);
      tile_add(acc, s, xa);
      u32x4 xr[4];
#pragma unroll
      for (int i = 0; i < 2; ++i)
#pragma unroll
        for (int jj = 0; jj < 2; ++jj) { xr[jj * 2 + i] = pack_pair(acc[2 * i][jj], acc[2 * i + 1][jj]); st16(ro, (jj ? oo2 : oo) + i * 64, xr[jj * 2 + i]); }
      if (l == 0) {
#pragma unroll
        for (int p = 0; p < 4; ++p) *(LAS u32x4*)(uintptr_t)(xa + p * 1024) = xr[p];
        const int sbc = (tq >> 1) + (lane >> 4);
        const bool pv = lane < 16 || (lane < 32 && (tq & 1) && sbc < 16);
        sync_post(ws, 3, pv ? (sbc * 16 + (lane & 15)) * 64 + (tq - 2 * sbc + 1) * 8 + tp : -1, wid);
      }
    }
  }
}

extern "C" void kernel_launch(void* const* d_in, const int* in_sizes, int n_in,
                              void* d_out, int out_size, void* d_ws, size_t ws_size,
                              hipStream_t stream) {
  (void)hipFuncSetAttribute((const void*)mega_kernel, hipFuncAttributeMaxDynamicSharedMemorySize, 163840);
  mega_kernel<<<256, 512, 163840, stream>>>((const u16*)d_in[0], (const u16*)d_in[1], (const u16*)d_in[2], (const u16*)d_in[4], (const u16*)d_in[6], (const u16*)d_in[5], (const u16*)d_in[7], (u16*)d_out, (char*)d_ws);
}
```

```cpp
#define LEAN
#define STAMP
#define CSTAMP 0
#define PROBE 0
#define PROBE0 0
#include <hip/hip_runtime.h>
#include <hip/hip_bf16.h>
#include <stdint.h>

typedef unsigned short u16;
typedef unsigned long long u64;
typedef short bf16x8 __attribute__((ext_vector_type(8)));
typedef short bf16x4 __attribute__((ext_vector_type(4)));
typedef float f32x4 __attribute__((ext_vector_type(4)));
typedef float f32x2 __attribute__((ext_vector_type(2)));
typedef unsigned u32x4 __attribute__((ext_vector_type(4)));
typedef unsigned u32x2 __attribute__((ext_vector_type(2)));
typedef __bf16 bf16v2 __attribute__((ext_vector_type(2)));
#define LAS __attribute__((address_space(3)))
#define GAS __attribute__((address_space(1)))
#define DEVI __device__ __forceinline__
typedef __attribute__((address_space(1))) unsigned gu32;
typedef __attribute__((address_space(1))) unsigned long long gu64;

constexpr int S = 2048, D = 1024, F = 3584;
constexpr float EPS = 1e-5f;

DEVI float bf2f(u16 v) { return __uint_as_float(((unsigned)v) << 16); }
DEVI float bflo(unsigned v) { return __uint_as_float(v << 16); }
DEVI float bfhi(unsigned v) { return __uint_as_float(v & 0xffff0000u); }
DEVI unsigned pk2(float lo, float hi) { bf16v2 p; p.x = (__bf16)lo; p.y = (__bf16)hi; return __builtin_bit_cast(unsigned, p); }

DEVI const void* uni_ptr(const void* p) {
  const u64 v = (u64)p;
  return (const void*)(((u64)(unsigned)__builtin_amdgcn_readfirstlane((unsigned)(v >> 32)) << 32) | (unsigned)__builtin_amdgcn_readfirstlane((unsigned)v));
}
DEVI void dma16(const void* sbase, unsigned voff, unsigned ldsaddr) {
  ldsaddr = __builtin_amdgcn_readfirstlane(ldsaddr);
  asm volatile("s_mov_b32 m0, %2\n\ts_nop 0\n\tglobal_load_lds_dwordx4 %0, %1 sc1" :: "v"(voff), "s"(sbase), "s"(ldsaddr) : "memory");
}
template <int N, int IMM> DEVI void dma_grp(const void* sbase, const unsigned* v, unsigned lb) {
  static_assert(N == 1 || N == 2 || N == 4, "");
  if constexpr (N == 4)
    asm volatile("s_add_u32 m0, %1, %2\n\ts_nop 0\n\tglobal_load_lds_dwordx4 %3, %0 sc1\n\tglobal_load_lds_dwordx4 %4, %0 offset:1024 sc1\n\t"
                 "global_load_lds_dwordx4 %5, %0 offset:2048 sc1\n\tglobal_load_lds_dwordx4 %6, %0 offset:3072 sc1"
                 :: "s"(sbase), "s"(lb), "i"(IMM), "v"(v[0]), "v"(v[1]), "v"(v[2]), "v"(v[3]) : "memory", "scc");
  else if constexpr (N == 2)
    asm volatile("s_add_u32 m0, %1, %2\n\ts_nop 0\n\tglobal_load_lds_dwordx4 %3, %0 sc1\n\tglobal_load_lds_dwordx4 %4, %0 offset:1024 sc1"
                 :: "s"(sbase), "s"(lb), "i"(IMM), "v"(v[0]), "v"(v[1]) : "memory", "scc");
  else
    asm volatile("s_add_u32 m0, %1, %2\n\ts_nop 0\n\tglobal_load_lds_dwordx4 %3, %0 sc1" :: "s"(sbase), "s"(lb), "i"(IMM), "v"(v[0]) : "memory", "scc");
}
template <int N> DEVI void wait_vm() { asm volatile("s_waitcnt vmcnt(%0)" :: "i"(N) : "memory"); }
DEVI void wait_lgkm0() { asm volatile("" ::: "memory"); __builtin_amdgcn_s_waitcnt(0xC07F); asm volatile("" ::: "memory"); }
DEVI void mfma_ip(f32x4& c, const bf16x8& a, const bf16x8& b) { asm volatile("v_mfma_f32_16x16x32_bf16 %0, %1, %2, %0" : "+v"(c) : "v"(a), "v"(b)); }
DEVI void mfma_gap() { __builtin_amdgcn_sched_barrier(0); asm volatile("s_nop 7"); __builtin_amdgcn_sched_barrier(0); }
DEVI void bar() { asm volatile("" ::: "memory"); __builtin_amdgcn_s_barrier(); asm volatile("" ::: "memory"); }
DEVI void sched0() { __builtin_amdgcn_sched_barrier(0); }

DEVI bf16x8 lds_b128(unsigned addr) { return *(const LAS bf16x8*)(uintptr_t)addr; }
DEVI bf16x4 lds_tr4(unsigned addr) { return __builtin_amdgcn_ds_read_tr16_b64_v4i16((LAS bf16x4*)(uintptr_t)addr); }

typedef __amdgpu_buffer_rsrc_t rsrc_t;
DEVI rsrc_t mkrsrc(const void* p, unsigned bytes) { return __builtin_amdgcn_make_buffer_rsrc((void*)p, 0, bytes, 0x00020000); }
DEVI void st16(rsrc_t r, unsigned off, u32x4 v) { __builtin_amdgcn_raw_buffer_store_b128(v, r, off, 0, 16); }
DEVI void ld_raw(u32x4& v, rsrc_t r, unsigned off) { asm volatile("buffer_load_dwordx4 %0, %1, %2, 0 offen sc1 ; raw load" : "=&v"(v) : "v"(off), "s"(r) : "memory"); }
DEVI void ld_again(u32x4& v, rsrc_t r, unsigned off) { asm volatile("buffer_load_dwordx4 %0, %1, %2, 0 offen sc1\n\ts_waitcnt vmcnt(0)" : "=&v"(v) : "v"(off), "s"(r) : "memory"); }
DEVI void ld_ok(u32x4& v) { asm volatile("; raw ok %0" : "+v"(v)); }
#define RLX_AGENT __ATOMIC_RELAXED, __HIP_MEMORY_SCOPE_AGENT

template <int N> DEVI float ror16(float v) { return __int_as_float(__builtin_amdgcn_update_dpp(0, __float_as_int(v), 0x120 + N, 0xf, 0xf, false)); }
DEVI float red16(float v) { v += ror16<8>(v); v += ror16<4>(v); v += ror16<2>(v); v += ror16<1>(v); return v; }
DEVI void xch32(float v, float& a, float& b) { unsigned u = __float_as_uint(v); auto r = __builtin_amdgcn_permlane32_swap(u, u, false, false); unsigned x = r[0], y = r[1]; a = __uint_as_float(x); b = __uint_as_float(y); }
DEVI void xch16(float v, float& a, float& b) { unsigned u = __float_as_uint(v); auto r = __builtin_amdgcn_permlane16_swap(u, u, false, false); unsigned x = r[0], y = r[1]; a = __uint_as_float(x); b = __uint_as_float(y); }
DEVI void swp32(float& a, float& b) { auto r = __builtin_amdgcn_permlane32_swap(__float_as_uint(a), __float_as_uint(b), false, false); unsigned x = r[0], y = r[1]; a = __uint_as_float(x); b = __uint_as_float(y); }
DEVI void swp16(float& a, float& b) { auto r = __builtin_amdgcn_permlane16_swap(__float_as_uint(a), __float_as_uint(b), false, false); unsigned x = r[0], y = r[1]; a = __uint_as_float(x); b = __uint_as_float(y); }
DEVI float colsum4(float v) { float a, b; xch32(v, a, b); v = a + b; xch16(v, a, b); return a + b; }
DEVI float vmax(float a, float b) { float r; asm("v_max_f32 %0, %1, %2" : "=v"(r) : "v"(a), "v"(b)); return r; }
DEVI float vmin3(float a, float b, float c) { float r; asm("v_min3_f32 %0, %1, %2, %3" : "=v"(r) : "v"(a), "v"(b), "v"(c)); return r; }
DEVI float vmax3(float a, float b, float c) { float r; asm("v_max3_f32 %0, %1, %2, %3" : "=v"(r) : "v"(a), "v"(b), "v"(c)); return r; }

DEVI unsigned src_kc(int c, int ld) { int row = c >> 3, ch = (c & 7) ^ ((row >> 1) & 7); return (unsigned)(row * ld * 2 + ch * 16); }
DEVI int km_f(int k) { return ((k >> 1) & 1) | (((k >> 3) & 1) << 1); }
DEVI unsigned ph(unsigned x) { return ((x >> 7) << 8) | (x & 127u); }
DEVI unsigned rd_kc(int lane, int ks) { int r = lane & 15, g = lane >> 4; return (unsigned)(r * 128 + (((ks * 4 + g) ^ (r >> 1)) << 4)); }
DEVI unsigned rd_km(int lane, int ct) {
  int g = lane >> 4, q = (lane & 15) >> 2, p = lane & 3;
  int f = ((q >> 1) & 1) | ((g & 1) << 1);
  return (unsigned)((ct >> 2) * 8192 + (8 * g + q) * 128 + (((ct & 3) ^ f) << 5) + 8 * p);
}

DEVI void stamp(unsigned a) { unsigned x, y;
  asm volatile("s_memrealtime s[98:99]\n\tv_mov_b32 %1, %2\n\ts_waitcnt lgkmcnt(0)\n\tv_mov_b32 %0, s98\n\tds_write_b32 %1, %0" : "=&v"(x), "=&v"(y) : "s"(a) : "s98", "s99"); }
#define ST(k, i) do { if (__builtin_amdgcn_readfirstlane(threadIdx.x) == 0) stamp(19968 + ((k) * 8 + (i)) * 4); } while (0)
#define STL(k, i) do { if (__builtin_amdgcn_readfirstlane(threadIdx.x) == 256) stamp(19968 + ((k) * 8 + (i)) * 4); } while (0)

constexpr unsigned CODE_BYTES = 47104;
DEVI void code_warm(int part, int lane, unsigned scratch) {
  unsigned long long pc;
  asm volatile("s_getpc_b64 s[40:41]\n\ts_add_u32 s40, s40, mega_kernel@rel32@lo+4\n\ts_addc_u32 s41, s41, mega_kernel@rel32@hi+12\n\ts_mov_b64 %0, s[40:41]" : "=s"(pc) :: "s40", "s41", "scc");
  const unsigned off = (unsigned)(part * 64 + lane) * 128u;
  scratch = __builtin_amdgcn_readfirstlane(scratch);
  if (off < CODE_BYTES) asm volatile("s_nop 3\n\ts_mov_b32 m0, %2\n\ts_nop 0\n\tglobal_load_lds_dword %0, %1" :: "v"(off), "s"(pc), "s"(scratch) : "memory");
}

DEVI unsigned src_km(int lt, int ld) { const int k = lt >> 3; return (unsigned)(k * ld * 2 + (((lt & 7) ^ (km_f(k) << 1)) << 4)); }
DEVI void touch4(const void* sbase, unsigned voff, unsigned ldsaddr) {
  ldsaddr = __builtin_amdgcn_readfirstlane(ldsaddr); sbase = uni_ptr(sbase);
  asm volatile("s_nop 3\n\ts_mov_b32 m0, %2\n\ts_nop 0\n\tglobal_load_lds_dword %0, %1" :: "v"(voff), "s"(sbase), "s"(ldsaddr) : "memory");
}

template <int IMM> DEVI void spf1(const void* base, unsigned soff) { asm volatile("s_load_dword s101, %0, %1 offset:%2" :: "s"(base), "s"(soff), "i"(IMM) : "s101"); }
template <int IMM = 0> DEVI void spf2(const void* base, unsigned soff) { asm volatile("s_load_dwordx2 s[100:101], %0, %1 offset:%2" :: "s"(base), "s"(soff), "i"(IMM + 60) : "s100", "s101"); }
template <int ST> DEVI void spf2x6(const void* base, unsigned soff) {
  asm volatile("s_load_dwordx2 s[100:101], %0, %1 offset:%2\n\ts_load_dwordx2 s[100:101], %0, %1 offset:%3\n\ts_load_dwordx2 s[100:101], %0, %1 offset:%4\n\t"
               "s_load_dwordx2 s[100:101], %0, %1 offset:%5\n\ts_load_dwordx2 s[100:101], %0, %1 offset:%6\n\ts_load_dwordx2 s[100:101], %0, %1 offset:%7"
               :: "s"(base), "s"(soff), "i"(60), "i"(60 + ST), "i"(60 + 2 * ST), "i"(60 + 3 * ST), "i"(60 + 4 * ST), "i"(60 + 5 * ST) : "s100", "s101");
}
template <int ST> DEVI void spf1x12(const void* base, unsigned soff) {
  spf1<0>(base, soff); spf1<ST>(base, soff); spf1<2 * ST>(base, soff); spf1<3 * ST>(base, soff); spf1<4 * ST>(base, soff); spf1<5 * ST>(base, soff);
  spf1<6 * ST>(base, soff); spf1<7 * ST>(base, soff); spf1<8 * ST>(base, soff); spf1<9 * ST>(base, soff); spf1<10 * ST>(base, soff); spf1<11 * ST>(base, soff);
}
DEVI int lgkm_out() { int n; asm volatile("s_getreg_b32 %0, hwreg(HW_REG_IB_STS, 8, 4)" : "=s"(n)); return n; }
DEVI int slow_res(const void* w) { return (3 - ((unsigned)(uintptr_t)w >> 7)) & 7; }

#define MTD
#define MT(i)
#define MTOUT(k, w)
#define CSP , char* ws, int l
#define CSA , ws, l
#define WNE 14
#define ANE 24
#define BNE 13
#define QNE 46
struct Plan { int n; signed char q[40], idx[40], k[40], h[40], g[40]; };
template <class O, bool PKM, bool QKM> constexpr Plan make_plan() {
  Plan p{};
  int nr = 0;
  for (int c = 0; c < O::NRO; ++c) nr += ((O::ro[c] >> 4) ? QKM : PKM) ? 4 : 2;
  int prev = 0;
  for (int c = 0; c < O::NRO; ++c) {
    const int q = O::ro[c] >> 4, i = O::ro[c] & 15;
    int av[2] = {0, 0}, k0 = -1;
    for (int m = 0; m < 2 * O::NP; ++m) {
      const int mm = (m + 2 * O::NPEND) % (2 * O::NP);
      if ((q ? O::pj[m >> 1] : O::pi[m >> 1]) == i && m < 2 * O::NPEND) av[((m >> 1) ^ m) & 1] = m;
      if ((q ? O::pj[mm >> 1] : O::pi[mm >> 1]) == i && k0 < 0) k0 = ((mm >> 1) ^ mm) & 1;
    }
    for (int kk = 0; kk < 2; ++kk)
      for (int h = 0; h < ((q ? QKM : PKM) ? 2 : 1); ++h) {
        const int k = k0 ^ kk;
        int g = p.n * O::NE / nr;
        if (g < av[k]) g = av[k];
        if (g < prev + (p.n && O::NE >= nr ? 1 : 0)) g = prev + (p.n && O::NE >= nr ? 1 : 0);
        p.q[p.n] = q; p.idx[p.n] = i; p.k[p.n] = k; p.h[p.n] = h; p.g[p.n] = g; prev = g; ++p.n;
      }
  }
  return p;
}
struct OBase { static constexpr int pidx(int i) { return i; } static constexpr int poff(int) { return 0; } static constexpr int qoff(int j) { return j * 2048; } static constexpr bool swp(int) { return false; } };
template <bool PF, int NE_> struct O44 : OBase {
  static constexpr int TP = 4, TQ = 4, NPA = 4, NP = 16, NPEND = 8, NRO = 8, NE = NE_;
  static constexpr signed char pi[16] = {2, 3, 3, 2, 2, 3, 3, 2, 0, 1, 1, 0, 0, 1, 1, 0}, pj[16] = {0, 0, 1, 1, 2, 2, 3, 3, 0, 0, 1, 1, 2, 2, 3, 3};
  static constexpr signed char ro[8] = {0, PF ? 1 : 16, PF ? 16 : 1, 17, 18, 19, 2, 3};
};
template <bool PF, int NE_> struct O42 : OBase {
  static constexpr int TP = 4, TQ = 2, NPA = 4, NP = 8, NPEND = 4, NRO = 6, NE = NE_;
  static constexpr signed char pi[8] = {2, 3, 3, 2, 0, 1, 1, 0}, pj[8] = {0, 0, 1, 1, 0, 0, 1, 1};
  static constexpr signed char ro[6] = {0, PF ? 1 : 16, PF ? 16 : 1, 17, 3, 2};
};
template <int TH> struct OQ {
  static constexpr int TP = 6, TQ = 5, NPA = 2, NP = 26, NPEND = 13, NRO = 11, NE = QNE;
  static constexpr signed char pi[26] = {3, 4, 5, 5, 4, 3, 3, 4, 5, 5, TH ? 5 : 3, 4, TH ? 3 : 4, 0, 1, 2, 2, 1, 0, 0, 1, 2, 2, TH ? 2 : 0, 1, TH ? 0 : 1};
  static constexpr signed char pj[26] = {0, 0, 0, 1, 1, 1, 2, 2, 2, TH ? 4 : 3, 3, 3, TH ? 3 : 4, 0, 0, 0, 1, 1, 1, 2, 2, 2, TH ? 4 : 3, 3, 3, TH ? 3 : 4};
  static constexpr signed char ro[11] = {0, 1, 16, 2, 17, 18, TH ? 20 : 19, TH ? 19 : 20, 3, 4, 5};
  static constexpr int pidx(int i) { return i / 3; }
  static constexpr int poff(int i) { return i % 3 * 8192; }
  static constexpr int qoff(int j) { return j == 4 ? 0 : (1 + 4 * TH + j) * 2048; }
  static constexpr bool swp(int i) { return i % 3 == 2; }
};
template <bool PKM, bool QKM, int PB, int QB, int XB, int NS, int NT, bool WISP, int BASE, int ROT, int WLD, int WSUB, int ASTR, int ASTEP>
struct Gemm {
  static constexpr int SB = PB + QB + XB, CNT = (PB + QB) / 4096 + (XB ? 1 : 0);
  static constexpr int WB = WISP ? PB : QB, AB = WISP ? QB : PB, WOFF = WISP ? 0 : PB, AOFF = WISP ? PB + XB : 0;
  static constexpr int XL = XB / 64, H = NS - 1, CA = AB / 4096 + (XB ? 1 : 0), WSTEP = 64 * WLD * 2;
  static_assert(H * CNT <= 63 && BASE + NS * SB <= 163840, "");
  struct Loader {
    static constexpr int NW = WB / 4096, NA = AB / 4096;
    const char* w; const char* ab; unsigned wv[NW], av[NA], xv, lwb, lab, lx; int rot, islot, t; bool xl;
    DEVI Loader(const void* w_, const void* ab_, int lw, int lane, int rot_ = 0)
        : w((const char*)w_ - 3072), ab((const char*)ab_ - 3072), lwb(BASE + WOFF + lw * (NW == 6 ? 2048 : WB / 4)), lab(BASE + AOFF + lw * (AB / 4)), lx(BASE + PB + lw * (XL * 16)),
          rot(rot_), islot((H + ROT) % NS), t(0), xl(lane < XL) {
      const unsigned w0 = (lane >> 3) * (WLD * 2), f0 = (lane & 7) ^ ((lane >> 3) & 2);
#pragma unroll
      for (int i = 0; i < NW; ++i) {
        const unsigned p = NW == 6 ? (i >> 1) * 8 + lw * 2 + (i & 1) : lw * NW + i;
        wv[i] = w0 + ((f0 ^ ((i & 1) << 2)) << 4) + (NW == 6 ? (i >> 1) * WSUB + (lw * 2 + (i & 1)) * (8 * WLD * 2) - (i & 1) * 1024 : (p >> 3) * WSUB + (p & 7) * (8 * WLD * 2) - i * 1024) + 3072;
        asm volatile("" : "+v"(wv[i]));
      }
      asm volatile("" : "+s"(w), "+s"(lwb));
      if (XB) asm volatile("" : "+s"(rot));
      else seta(0, 0, lw, lane);
    }
    DEVI void seta(unsigned a0, unsigned xv_, int lw, int lane) {
      xv = xv_ + 3072;
#pragma unroll
      for (int i = 0; i < NA; ++i) { const unsigned p = lw * NA + i; av[i] = (XB ? a0 + src_kc(p * 64 + lane, D) : p * 2048 + ph(lane * 16)) + 3072 - i * 1024; asm volatile("" : "+v"(av[i])); }
      asm volatile("" : "+s"(ab), "+s"(lab));
      if (XB) { asm volatile("" : "+v"(xv)); asm volatile("" : "+s"(lx)); }
    }
    DEVI int kord(int n) const { if constexpr (XB != 0) return (n + rot) & (NT - 1); else return n; }
    template <int SO> DEVI void issue_w(const char* p, unsigned so = 0) const {
      if constexpr (NW == 6) { dma_grp<2, SO>(p, wv, lwb + so); dma_grp<2, SO + 8192>(p, wv + 2, lwb + so); dma_grp<2, SO + 16384>(p, wv + 4, lwb + so); }
      else dma_grp<NW, SO>(p, wv, lwb + so);
    }
    template <int SO> DEVI void issue_a(const char* p, unsigned so = 0) const {
      if (XB) { if (xl) dma_grp<1, SO>(p, &xv, lx + so); }
      dma_grp<NA, SO>(p, av, lab + so);
    }
    template <int S0, bool W, bool A> DEVI void pre() const {
      if constexpr (S0 < H) {
        if (W) issue_w<((S0 + ROT) % NS) * SB>(w + (kord(0) + S0) * WSTEP);
        if (A) issue_a<((S0 + ROT) % NS) * SB>(ab + (kord(0) + S0) * ASTEP);
        pre<S0 + 1, W, A>();
      }
    }
    DEVI void pre_w() const { pre<0, true, false>(); sched0(); }
    template <int T> DEVI void peel() const {
      if constexpr (T < H - 1) {
        wait_vm<(H - 1 - T) * CA + T * CNT>();
        bar();
        issue_w<((T + H + ROT) % NS) * SB>(w + (T + H) * WSTEP); issue_a<((T + H + ROT) % NS) * SB>(ab + (T + H) * ASTEP);
        peel<T + 1>();
      }
    }
    DEVI void start() {
      sched0();
      bar();
      pre<0, false, true>();
      sched0();
      if constexpr (H == 2) wait_vm<CA>();
      else { peel<0>(); islot = (2 * H - 1 + ROT) % NS; t = H - 1; }
    }
    template <class HK> DEVI void run(int tend, HK side) {
#pragma unroll 1
      for (; t < tend; ++t) {
        const char* wk = w + (t + H) * WSTEP; const char* ak = ab + (t + H) * ASTEP;
        unsigned lw_ = lwb + islot * SB, la_ = lab + islot * SB;
        islot = islot + 1 == NS ? 0 : islot + 1;
        asm volatile("" : "+s"(wk), "+s"(ak), "+s"(lw_), "+s"(la_));
        wait_vm<(H - 1) * CNT>();
        bar();
        dma_grp<NW, 0>(wk, wv, lw_);
        side(t);
        dma_grp<NA, 0>(ak, av, la_);
        sched0();
      }
    }
    template <int T, int X, class A, class B, class C> DEVI void step(A pre_, B side, C post) const {
      const int k = kord(T + H);
      const char* wk = w + k * WSTEP; const char* ak = ab + k * ASTEP;
      asm volatile("" : "+s"(wk), "+s"(ak));
      wait_vm<(H - 1) * CNT + X>();
      bar();
      pre_(BASE + ((T + ROT) % NS) * SB);
      issue_w<((T + H + ROT) % NS) * SB>(wk);
      sched0();
      side();
      sched0();
      issue_a<((T + H + ROT) % NS) * SB>(ak);
      post();
      sched0();
    }
    template <int I> DEVI void tail() const { if constexpr (I >= 0) { sched0(); wait_vm<I * CNT>(); bar(); tail<I - 1>(); } }
  };
  static constexpr int NWIN = (NS * SB + 65535) / 65536;
  template <class O> struct Comp {
    static constexpr int TP = O::TP, TQ = O::TQ, NM = 2 * O::NP, NPA = PKM ? O::NPA : 2, NQA = QKM ? TQ : 2;
    static constexpr Plan PL = make_plan<O, PKM, QKM>();
    unsigned prd[NWIN][NPA], qrd[NWIN][NQA];
    bf16x8 fp[TP][2], fq[TQ][2]; bf16x4 lo;
    DEVI void setwin() {
#pragma unroll
      for (int w = 1; w < NWIN; ++w) {
#pragma unroll
        for (int i = 0; i < NPA; ++i) { prd[w][i] = prd[0][i] + w * 65536; asm volatile("" : "+v"(prd[w][i])); }
#pragma unroll
        for (int i = 0; i < NQA; ++i) { qrd[w][i] = qrd[0][i] + w * 65536; asm volatile("" : "+v"(qrd[w][i])); }
      }
#pragma unroll
      for (int i = 0; i < NPA; ++i) asm volatile("" : "+v"(prd[0][i]));
#pragma unroll
      for (int i = 0; i < NQA; ++i) asm volatile("" : "+v"(qrd[0][i]));
    }
    DEVI void setup(int wp, int wq, int lane) {
      const unsigned b = BASE;
      if (PKM) {
#pragma unroll
        for (int i = 0; i < TP; ++i) prd[0][i] = b + rd_km(lane, wp * TP + i);
      } else { prd[0][0] = b + rd_kc(lane, 0) + wp * TP * 2048; prd[0][1] = b + rd_kc(lane, 1) + wp * TP * 2048; }
      if (QKM) {
#pragma unroll
        for (int j = 0; j < TQ; ++j) qrd[0][j] = b + rd_km(lane, wq * TQ + j);
      } else { qrd[0][0] = b + rd_kc(lane, 0) + wq * TQ * 2048; qrd[0][1] = b + rd_kc(lane, 1) + wq * TQ * 2048; }
    }
    template <int C, int N> DEVI static unsigned wa(const unsigned (&b)[NWIN][N], int i) { return b[C >> 16][i] + (C & 0xffff); }
    template <int SLOT, int R> DEVI void rd() {
      constexpr int q = PL.q[R], i = PL.idx[R], k = PL.k[R], h = PL.h[R];
      bf16x8& f = q ? fq[q ? i : 0][k] : fp[q ? 0 : i][k];
      if constexpr (q ? QKM : PKM) {
        unsigned a;
        if constexpr (q) a = wa<SLOT * SB + PB + k * 4096 + h * 512, NQA>(qrd, i); else a = wa<SLOT * SB + O::poff(i) + k * 4096 + h * 512, NPA>(prd, O::pidx(i));
        const bf16x4 x = lds_tr4(a);
        if constexpr (h) f = __builtin_shufflevector(lo, x, 0, 1, 2, 3, 4, 5, 6, 7); else lo = x;
      } else if constexpr (q) f = lds_b128(wa<SLOT * SB + PB + O::qoff(i), 2>(qrd, k));
      else f = lds_b128(wa<SLOT * SB + i * 2048, 2>(prd, k));
    }
    template <int SLOT, int M, int R> DEVI void rds() { if constexpr (R < PL.n) { if constexpr (PL.g[R] == M) rd<SLOT, R>(); if constexpr (PL.g[R] <= M) rds<SLOT, M, R + 1>(); } }
    template <int M, bool IP = false> DEVI void mm(f32x4 (&acc)[TP][TQ]) {
      constexpr int i = O::pi[M >> 1], j = O::pj[M >> 1], k = ((M >> 1) ^ M) & 1;
      if constexpr (IP) { if constexpr (O::swp(i)) mfma_ip(acc[i][j], fq[j][k], fp[i][k]); else mfma_ip(acc[i][j], fp[i][k], fq[j][k]); }
      else if constexpr (O::swp(i)) acc[i][j] = __builtin_amdgcn_mfma_f32_16x16x32_bf16(fq[j][k], fp[i][k], acc[i][j], 0, 0, 0);
      else acc[i][j] = __builtin_amdgcn_mfma_f32_16x16x32_bf16(fp[i][k], fq[j][k], acc[i][j], 0, 0, 0);
    }
    template <int A, int B> DEVI void pend(f32x4 (&acc)[TP][TQ]) { if constexpr (A < B) { mm<2 * A>(acc); mm<2 * A + 1>(acc); pend<A + 1, B>(acc); } }
    template <int SLOT, bool FIRST, int M> DEVI void body(f32x4 (&acc)[TP][TQ]) {
      if constexpr (M < NM) {
        if constexpr (!FIRST || M >= 2 * O::NPEND) { mm<M, true>(acc); sched0(); }
        rds<SLOT, M, 0>();
        sched0();
        body<SLOT, FIRST, M + 1>(acc);
      }
    }
    template <int SLOT, bool FIRST> DEVI void step(f32x4 (&acc)[TP][TQ]) { wait_lgkm0(); bar(); sched0(); body<SLOT, FIRST, 0>(acc); }
    template <int I, int N, int T0, class MID> DEVI void steps(f32x4 (&acc)[TP][TQ], MID mid) {
      if constexpr (I < N) { mid(I); step<(I + T0 + ROT) % NS, false>(acc); steps<I + 1, N, T0>(acc, mid); }
    }
    template <bool CONT> DEVI void prep(f32x4 (&acc)[TP][TQ]) {
      setwin();
#pragma unroll
      for (int p = 0; p < O::NP; ++p) asm volatile("" : "+v"(acc[O::pi[p]][O::pj[p]]));
      if constexpr (!CONT && NT % NS == 0) {
#pragma unroll
        for (int p = 0; p < 2 * O::NPEND; ++p) {
          fp[O::pi[p >> 1]][p & 1] = (bf16x8){0, 0, 0, 0, 0, 0, 0, 0}; fq[O::pj[p >> 1]][p & 1] = (bf16x8){0, 0, 0, 0, 0, 0, 0, 0};
        }
#pragma unroll
        for (int p = 0; p < 2 * O::NPEND; ++p) asm volatile("" : "+v"(fp[O::pi[p >> 1]][p & 1]), "+v"(fq[O::pj[p >> 1]][p & 1]));
      }
    }
    template <bool CONT, class MID> DEVI void run_steps(f32x4 (&acc)[TP][TQ], MID mid CSP) {
      constexpr int T0 = CONT || NT % NS == 0 ? 0 : 1;
      if constexpr (T0) step<ROT % NS, true>(acc);
#pragma unroll 1
      for (int it = 0; it < (NT - T0) / NS; ++it) {
        { constexpr int NI = (NT - T0) / NS, kk = 10 + (XB ? 0 : NT == 16 ? 1 : NT == 32 ? 2 : CONT ? 4 : 3);
          if ((NI <= 8 || !(it & 1)) && l == CSTAMP) ST(kk, NI > 8 ? it >> 1 : it); }
        steps<0, NS, T0>(acc, [&](int i) { mid(it - (NT - T0) / NS, i); });
      }
      steps<0, (NT - T0) % NS, T0>(acc, [](int) {});
      wait_lgkm0();
      mfma_gap();
    }
  };
};

DEVI f32x4 gelu_tanh(f32x4 u) {
  f32x4 a = u * (-2.302208198f + -0.1029432397f * (u * u));
#pragma unroll
  for (int e = 0; e < 4; ++e) a[e] = __builtin_amdgcn_exp2f(a[e]);
  a += 1.f;
#pragma unroll
  for (int e = 0; e < 4; ++e) a[e] = __builtin_amdgcn_rcpf(a[e]);
  return u * a;
}

template <int tilesP, int tilesQ, int gp, int gq> DEVI void tile_map(unsigned bid, int& tp, int& tq) {
  const unsigned xcd = bid & 7, idx = bid >> 3;
  constexpr unsigned gP = tilesP / gp, gQ = tilesQ / gq;
  static_assert(gP * gQ == 8, "");
  tp = (xcd % gP) * gp + idx % gp;
  tq = (xcd / gP) * gq + idx / gp;
}

template <int V> struct IC { static constexpr int value = V; };
template <int A, int B, class FN> DEVI void for_c(FN f) { if constexpr (A < B) { f(IC<A>{}); for_c<A + 1, B>(f); } }
DEVI float sel(bool c, float a, float b) { return c ? a : b; }
DEVI u32x2 pk4(const f32x4& v) { return (u32x2){pk2(v[0], v[1]), pk2(v[2], v[3])}; }
DEVI u32x4 cat4(u32x2 a, u32x2 b) { return (u32x4){a.x, a.y, b.x, b.y}; }
DEVI bf16x8 cat(u32x2 a, u32x2 b) { return __builtin_bit_cast(bf16x8, cat4(a, b)); }
DEVI f32x4 mfma(const bf16x8& a, const bf16x8& b, const f32x4& c) { return __builtin_amdgcn_mfma_f32_16x16x32_bf16(a, b, c, 0, 0, 0); }
DEVI u32x4 pack_pair(const f32x4& a, const f32x4& b) {
  const unsigned ax = pk2(a[0], a[1]), ay = pk2(a[2], a[3]), bx = pk2(b[0], b[1]), by = pk2(b[2], b[3]);
  auto rx = __builtin_amdgcn_permlane16_swap(ax, bx, false, false);
  auto ry = __builtin_amdgcn_permlane16_swap(ay, by, false, false);
  const unsigned x0 = rx[0], x1 = rx[1], y0 = ry[0], y1 = ry[1];
  return (u32x4){x0, y0, x1, y1};
}
DEVI int pair_chunk(int fq) { return ((fq & 1) << 1) | (fq >> 1); }
DEVI void sel_frags(bool paired, int fr, int fq, bf16x8 (&s)[2]) {
  const unsigned one = fr & 1 ? 0x3F800000u : 0x3F80u;
#pragma unroll
  for (int i = 0; i < 2; ++i) {
    const bool on = paired ? (fq & 1) == i && (fr >> 3) == (fq >> 1) : (fr >> 2) == fq;
    const int d = paired ? (fr & 7) >> 1 : i * 2 + ((fr >> 1) & 1);
    u32x4 v;
#pragma unroll
    for (int e = 0; e < 4; ++e) v[e] = on && d == e ? one : 0u;
    s[i] = __builtin_bit_cast(bf16x8, v);
  }
}
template <int TQ> DEVI void tile_add(f32x4 (&acc)[4][TQ], const bf16x8 (&s)[2], unsigned a) {
  bf16x8 t[4];
#pragma unroll
  for (int p = 0; p < 4; ++p) t[p] = lds_b128(a + p * 1024);
#pragma unroll
  for (int p = 0; p < 4; ++p)
#pragma unroll
    for (int i = 0; i < 2; ++i) acc[(p & 1) * 2 + i][p >> 1] = mfma(s[i], t[p], acc[(p & 1) * 2 + i][p >> 1]);
}
template <int C> DEVI float dppf(float v) { return __int_as_float(__builtin_amdgcn_update_dpp(0, __float_as_int(v), C, 0xf, 0xf, false)); }
DEVI float red8(float v) { v += dppf<0xB1>(v); v += dppf<0x4E>(v); v += dppf<0x141>(v); return v; }
DEVI float red4(float v) { v += dppf<0xB1>(v); v += dppf<0x4E>(v); return v; }

constexpr unsigned SENT = 0xBF800000u;
constexpr int SPIN_MAX = 1 << 21;

constexpr unsigned MB = 1u << 20;
constexpr unsigned WS_O = 0, WS_H = 8 * MB, WS_U = 16 * MB, WS_X2 = 32 * MB, WS_XP = 40 * MB  ;
constexpr unsigned WS_ST = 120 * MB, ST_CS = 2 * S * 64, WS_XCH = 122 * MB  , WS_EPOCH = 123 * MB, WS_ABORT = 123 * MB + 256  ;
DEVI unsigned tag2(unsigned t) { return t * 0x9E3779B1u ^ 0x5BD1E995u; }
constexpr int MEDGE = 256 * 64;
constexpr unsigned WS_MAIL = 124 * MB;

#define NL 2
#define QBR 1
#define FLS 3
#define NPL 6
#define SW0 2
#define SW1 6
#define XD 16
#define XS 4
#define JA 22
#define PS 3
#define TBA 3
#define TC1 12
#define NW1 9
#define PWL 1
#define ET 20
#define SPW 1
#define PA_1 3
#define PA_2 3
#define ATT4 1
constexpr int L_X = 0, L_TAB = 16384, L_CS = L_TAB + 1152, L_TAG = L_CS + 768, L_BIAS = L_TAG + 128, L_SCR = L_BIAS + 512  , L_QKV = 20480;
static_assert(L_SCR + 1536 <= L_QKV, "");
typedef Gemm<true, false, 24576, 16384, 2048, 3, D / 64, true, 32768, 1, 3 * D, 2 * D, 32 * D * 2, 128> GQ;
typedef Gemm<true, false, 16384, 8192, 0, 4, D / 64, true, 65536, 1, D, 128, 8192, 16384> GW;
typedef Gemm<true, true, 16384, 16384, 0, 3, S / 64, true, 65536, 0, F, 128, 8192, 32768> G1;
typedef Gemm<false, true, 16384, 16384, 0, 3, JA, false, 65536, (3 - JA % 3) % 3, S, 1024, 8192, 32768> G2A;
typedef Gemm<false, true, 16384, 8192, 0, 4, F / 64 - 2 * JA, false, 65536, 0, S, 128, 8192, 32768> G2;

DEVI unsigned get_tag() { return __builtin_amdgcn_readfirstlane(*(volatile LAS unsigned*)(uintptr_t)L_TAG); }
DEVI bool spin_fail(int spin, char* ws) {
  if ((spin & 255) != 255) return false;
  gu32* ab = (gu32*)(ws + WS_ABORT);
  const unsigned tag = get_tag();
  if (spin >= SPIN_MAX) { __hip_atomic_store(ab, tag, RLX_AGENT); return true; }
  return __builtin_amdgcn_readfirstlane(__hip_atomic_load(ab, RLX_AGENT)) == tag;
}
DEVI void sync_post(char* ws, int edge, int post, int wid) {
  unsigned o = post >= 0 && wid == 0 ? post * 4u : MB, one = 1u;
  const rsrc_t r = mkrsrc(ws + WS_MAIL + edge * (MEDGE * 4), MEDGE * 4);
  asm volatile("s_waitcnt vmcnt(0) lgkmcnt(0)" : "+v"(o), "+v"(one) : "s"(r) : "memory");
  sched0();
  bar();
  ST(edge + 1, 6);
  if (wid == 0) __builtin_amdgcn_raw_buffer_store_b32(one, r, o, 0, 16);
  sched0();
}
DEVI void sync_wait(char* ws, int edge, int line, int lane, unsigned want, int wid) {
  sched0();
  if (wid == 3) {
    gu32* pw = (gu32*)(ws + WS_MAIL) + edge * MEDGE + line * 64 + (lane & 31);
    __builtin_amdgcn_s_sleep(SW0);
    for (int spin = 0;; ++spin) {
      const unsigned v = __hip_atomic_load(pw, RLX_AGENT);
      if (__all(v >= want) || spin_fail(spin, ws)) break;
      __builtin_amdgcn_s_sleep(SW1);
    }
  }
  bar();
  ST(edge + 1, 4);
  ST(edge + 2, 0);
}
DEVI unsigned halo_off(int s0, int lw, int lane) {
  const int c = lw * 32 + (lane & 31), row = c >> 3, ch = (c & 7) ^ ((row >> 1) & 7), tok = s0 - 16 + row;
  return (unsigned)((tok < 0 ? 0 : tok) * D * 2 + ch * 16);
}
DEVI int qkv_rot(int bid) { return (bid & 3) * 4 + (bid & 4) * 2; }
DEVI unsigned x_off(int tp, int tq, int w, int fr, int fq) { return (unsigned)(((tq * 64 + (w >> 1) * 32 + fr) * D + tp * 128 + (w & 1) * 64 + pair_chunk(fq) * 8) * 2); }

DEVI void pfb_q(const u16* w, int bid, int j, int n) {
  const int r = slow_res(w);
  if ((r >> 2) == (bid & 1)) {
    const unsigned o = (unsigned)(((bid & 2) * 4 + r) * 128 + (j >> 1) * (3 * D * 2) + (j & 1) * 64 + ((n + qkv_rot(bid)) & 15) * GQ::WSTEP);
    spf1<0>(w, o); spf1<2048>(w, o); spf1<4096>(w, o);
  }
}
constexpr int PFR = NPL * 128 * 1024, PF_END = TBA * 256 + NW1 * PFR / (F * 2) / 8 * 8;
static_assert(TBA * 256 * F * 2 + NW1 * PFR <= S * F * 2 && 3 * PFR >= D * D * 2, "");
struct Pfc {
  const char* p; const char* q; int pc;
  DEVI Pfc(const u16* wo, const u16* w1, unsigned g) : p((const char*)wo + g * (NPL * 128)), q((const char*)w1 + TBA * 256 * F * 2 + g * (NPL * 128)), pc(0) {}
  DEVI void go() {
    unsigned t, u;
    asm volatile("s_getreg_b32 %2, hwreg(HW_REG_IB_STS, 8, 4)\n\ts_cmp_lt_u32 %1, %5\n\ts_cselect_b32 %3, 0, 1\n\ts_or_b32 %2, %2, %3\n\ts_cbranch_scc1 1f\n\t"
                 "s_load_dwordx2 s[100:101], %0, 60\n\ts_load_dwordx2 s[100:101], %0, 188\n\ts_load_dwordx2 s[100:101], %0, 316\n\t"
                 "s_load_dwordx2 s[100:101], %0, 444\n\ts_load_dwordx2 s[100:101], %0, 572\n\ts_load_dwordx2 s[100:101], %0, 700\n\t"
                 ".if %8 == 7\n\ts_load_dwordx2 s[100:101], %0, 828\n\t.endif\n\t"
                 "s_mov_b32 %2, %7\n\ts_cmp_lt_u32 %1, 2\n\ts_cselect_b32 %2, %6, %2\n\ts_mov_b64 s[40:41], %0\n\ts_add_u32 s40, s40, %2\n\ts_addc_u32 s41, s41, 0\n\t"
                 "s_cmp_eq_u32 %1, 2\n\ts_cselect_b64 %0, %4, s[40:41]\n\ts_add_u32 %1, %1, 1\n1:"
                 : "+s"(p), "+s"(pc), "=&s"(t), "=&s"(u) : "s"(q), "i"(3 + NW1), "i"((D * D * 2 - PFR) / 2), "i"(PFR), "i"(NPL) : "s40", "s41", "s100", "s101", "scc");
  }
};
DEVI void spare_touch(const u16* w1, const u16* w2, int bid, int wid, int lane) {
  const int j = ((bid >> 3) - 28) * 8 + wid, fr = lane & 15, fq = lane >> 4;
  const unsigned scr = L_QKV + wid * 256;
#pragma unroll 1
  for (int r = PF_END + (j * 2 + ((bid >> 2) & 1)) * 4; r < S; r += 256) { if (fr < 14) touch4(w1 + (size_t)r * F + (bid & 3) * 896, fq * (F * 2) + fr * 128, scr); wait_vm<SPW>(); }
#pragma unroll 1
  for (int r = (j * 2 + (bid & 1)) * 4; r < JA * 64; r += 256) { touch4(w2 + (size_t)(r + (bid & 2) * JA * 32) * S + (bid & 4) * 256, fq * (S * 2) + fr * 128, scr); wait_vm<SPW>(); }
#pragma unroll 1
  for (int r = JA * 128 + ((31 - j) * 2 + (bid & 1)) * 8; r < F; r += 512) { touch4(w2 + (size_t)r * S + ((bid & 7) >> 1) * 512, (lane >> 3) * (S * 2) + (lane & 7) * 128, scr); wait_vm<SPW>(); }
}

template <int TH> DEVI void qkv_attn(char* ws, int l, int h, int lane, int hp, int sb, int cw) {
  asm volatile("; half %1" : "+v"(lane) : "i"(TH));
  const int fr = lane & 15, fq = lane >> 4;
  GQ::Comp<OQ<TH>> g;
#pragma unroll
  for (int c = 0; c < 2; ++c) { g.prd[0][c] = 32768 + rd_km(lane, h * 2 + c); g.qrd[0][c] = 32768 + rd_kc(lane, c); }
  f32x4 acc[6][5];
#pragma unroll
  for (int i = 0; i < 6; ++i)
#pragma unroll
    for (int j = 0; j < 5; ++j) acc[i][j] = (f32x4){0.f, 0.f, 0.f, 0.f};
  const int par = fr & 1, da = fr - 4 * fq - par;
  const bool ca = da >= 0, cb = da >= 2;
  const unsigned tm = L_TAB + (16 + 64 * TH) * 4, tc = L_CS + h * 128;
  unsigned am = tm + fr * 4 - 320, a4 = tm + fq * 16 - 320, aa = tm + 576 + (4 * fq + par - (ca ? 0 : 16)) * 4, ab = tm + 576 + (4 * fq + par + 2 - (cb ? 0 : 16)) * 4;
  unsigned c4 = tc + fq * 16, c1 = tc + 512 + fr * 4, xb = L_QKV + h * 3072 + lane * 16, hm = par ? 0xffff0000u : 0xffffu, m0 = ca ? 0u : hm, m1 = cb ? 0u : hm;
  unsigned oo = ph((unsigned)((((sb * 2 + TH) * 16 + hp) << 13) + fr * 128 + (((h * 4 + pair_chunk(fq)) ^ (fr >> 1)) << 4)));
  asm volatile("" : "+v"(am), "+v"(a4), "+v"(aa), "+v"(ab), "+v"(c4), "+v"(c1), "+v"(xb), "+v"(hm), "+v"(m0), "+v"(m1), "+v"(oo));
  g.template prep<false>(acc);
  if (l) sync_wait(ws, 3, sb * 16 + hp, lane, lane < 24 && (lane >= 8 || sb > 0) ? 1u : 0u, TH * 2 + h);
  g.template run_steps<false>(acc, [&](int it, int i) { if (i == 0 && cw >= 0 && it == -2) code_warm(cw, lane, L_SCR + (TH * 2 + h) * 256); } CSA);
  const rsrc_t ro = mkrsrc(ws + WS_O, S * D * 4);
  MTD;
  MT(0);
  if (!PWL || l == 0) bar();
  ST(1 + l * 4, 2);
  float mt[5], cv[2], rq[4], ra[4], rb[4]; f32x4 m4[5], cq[2], ck[2];
#pragma unroll
  for (int c = 0; c < 2; ++c) {
    ck[c] = *(const LAS f32x4*)(uintptr_t)(c4 + 256 + c * 64); cv[c] = *(const LAS float*)(uintptr_t)(c1 + c * 64);
    cq[c] = *(const LAS f32x4*)(uintptr_t)(c4 + c * 64);
  }
#pragma unroll
  for (int j = 4; j >= 0; --j) {
    const int o = 320 + (j == 4 ? -64 - 256 * TH : j * 64);
    if (j < 4 || !TH) mt[j] = *(const LAS float*)(uintptr_t)(am + o);
    if (j < 4 || TH) m4[j] = *(const LAS f32x4*)(uintptr_t)(a4 + o);
  }
#pragma unroll
  for (int j = 0; j < 4; ++j) { rq[j] = *(const LAS float*)(uintptr_t)(am + 896 + j * 64); ra[j] = *(const LAS float*)(uintptr_t)(aa + j * 64); rb[j] = *(const LAS float*)(uintptr_t)(ab + j * 64); }
  u32x2 kp[2][5], qp[2][4], vp[2][5];
  auto fk = [&](int j) { kp[0][(j + 1) % 5] = pk4(acc[1][j] + ck[0] * mt[j]); kp[1][(j + 1) % 5] = pk4(acc[4][j] + ck[1] * mt[j]); };
  auto fv = [&](int j) { vp[0][(j + 1) % 5] = pk4(acc[2][j] + m4[j] * cv[0]); vp[1][(j + 1) % 5] = pk4(acc[5][j] + m4[j] * cv[1]); };
  auto fqq = [&](int j) { qp[0][j] = pk4(acc[0][j] + cq[0] * mt[j]); qp[1][j] = pk4(acc[3][j] + cq[1] * mt[j]); };
  g.template pend<9, 13>(acc);
#pragma unroll
  for (int i = 0; i < 8; ++i) { __builtin_amdgcn_sched_group_barrier(0x100, 3, 0); __builtin_amdgcn_sched_group_barrier(0x8, 1, 0); }
  sched0();
  MT(2);
  volatile LAS unsigned* xf = (volatile LAS unsigned*)(uintptr_t)(L_TAG + 16 + (h * 2 + TH) * 4);
  g.template pend<6, 9>(acc);
  if (TH) { fv(4); *(volatile LAS bf16x8*)(uintptr_t)(xb + 2048) = cat(vp[0][0], vp[1][0]); *xf = l + 1; fk(3); }
  else { fk(3); fv(3); *(volatile LAS bf16x8*)(uintptr_t)xb = cat(kp[0][4], kp[1][4]); *(volatile LAS bf16x8*)(uintptr_t)(xb + 1024) = cat(vp[0][4], vp[1][4]); *xf = l + 1; }
#pragma unroll
  for (int i = 0; i < 6; ++i) { __builtin_amdgcn_sched_group_barrier(0x8, 1, 0); __builtin_amdgcn_sched_group_barrier(0x2, 3, 0); }
  sched0();
  g.template pend<3, 6>(acc); g.template pend<0, 3>(acc);
  fqq(3); fk(2); fqq(2); fk(1);
#pragma unroll
  for (int i = 0; i < 6; ++i) { __builtin_amdgcn_sched_group_barrier(0x8, 1, 0); __builtin_amdgcn_sched_group_barrier(0x2, 2, 0); __builtin_amdgcn_sched_group_barrier(0x8, 1, 0); __builtin_amdgcn_sched_group_barrier(0x2, 3, 0); }
  sched0();
  const f32x4 z = {0.f, 0.f, 0.f, 0.f};
  f32x4 sc[4][2]; float a[4], b[4], m[4];
  auto scores = [&](int j) {
#pragma unroll
    for (int u = 1; u >= 0; --u) sc[j][u] = mfma(cat(kp[0][j + u], kp[1][j + u]), cat(qp[0][j], qp[1][j]), z);
  };
  auto pick = [&](int j) {
    const float r = rq[j] * (0.17677669529663687f * 1.4426950408889634f);
    const f32x4 s0 = sc[j][0], s1 = sc[j][1];
    a[j] = sel(par, sel(ca, s1[1], s0[1]), sel(ca, s1[0], s0[0])) * (r * ra[j]);
    b[j] = sel(par, sel(cb, s1[3], s0[3]), sel(cb, s1[2], s0[2])) * (r * rb[j]);
    if (!TH && j == 0) { a[j] = sel(ca || sb, a[j], -1e9f * 1.4426950408889634f); b[j] = sel(cb || sb, b[j], -1e9f * 1.4426950408889634f); }
    m[j] = __builtin_fmaxf(a[j], b[j]);
  };
  auto expo = [&](int j) { a[j] = __builtin_amdgcn_exp2f(a[j] - m[j]); b[j] = __builtin_amdgcn_exp2f(b[j] - m[j]); m[j] = a[j] + b[j]; };
  auto pv = [&](int j) {
    const unsigned pa = pk2(a[j] * ra[j], a[j] * ra[j]), pb = pk2(b[j] * rb[j], b[j] * rb[j]);
    const bf16x8 pf = __builtin_bit_cast(bf16x8, (u32x4){pa & m0, pb & m1, pa & (m0 ^ hm), pb & (m1 ^ hm)});
#pragma unroll
    for (int c = 0; c < 2; ++c) sc[j][c] = mfma(cat(vp[c][j], vp[c][j + 1]), pf, z);
  };
  auto outp = [&](int j) { st16(ro, oo + j * 4096, pack_pair(sc[j][0] * m[j], sc[j][1] * m[j])); };
  auto soft2 = [&](int j) {
    float u, w;
    pick(j); pick(j + 1);
    swp32(m[j], m[j + 1]); u = vmax(m[j], m[j + 1]); xch16(u, u, w); u = vmax(u, w); xch32(u, m[j], m[j + 1]);
    expo(j); expo(j + 1);
    pv(j + 1); pv(j);
    swp32(m[j], m[j + 1]); u = m[j] + m[j + 1]; xch16(u, u, w); u = __builtin_amdgcn_rcpf(u + w); xch32(u, m[j], m[j + 1]);
    outp(j + 1); outp(j);
  };
  unsigned f; u32x4 xv, xk;
  auto xrd = [&]() {
    f = xf[1 - 2 * TH]; xv = *(volatile LAS u32x4*)(uintptr_t)(xb + (TH ? 1024 : 2048));
    if (TH) xk = *(volatile LAS u32x4*)(uintptr_t)xb;
  };
  auto xget = [&]() {
    sched0();
    while (__builtin_expect(__builtin_amdgcn_readfirstlane(f) != l + 1, 0)) xrd();
    vp[0][0] = (u32x2){xv[0], xv[1]}; vp[1][0] = (u32x2){xv[2], xv[3]};
    if (TH) { kp[0][0] = (u32x2){xk[0], xk[1]}; kp[1][0] = (u32x2){xk[2], xk[3]}; }
  };
  MT(3);
  if (ATT4) {
    scores(3); scores(2);
    fqq(1);
    xrd();
    sched0();
    fk(0); fqq(0);
    if (TH) fv(3); else fk(4);
    scores(1);
    fv(2); fv(1); fv(0);
    pick(3); pick(2);
#pragma unroll
    for (int c = 0; c < 2; ++c) asm volatile("" : "+v"(qp[c][0]), "+v"(kp[c][1]), "+v"(vp[c][1]), "+v"(vp[c][2]), "+v"(vp[c][3]), "+v"(vp[c][4]), "+v"(sc[1][c]), "+v"(a[2 + c]), "+v"(b[2 + c]), "+v"(m[2 + c]));
    if (!TH) asm volatile("" : "+v"(kp[0][0]), "+v"(kp[1][0]));
    MT(4);
    xget();
    scores(0);
    pick(1); pick(0);
    float u, w;
    swp32(m[0], m[2]); swp32(m[1], m[3]); u = m[0]; w = m[1];
    asm volatile("v_max_f32 %0, %0, %2\n\tv_max_f32 %1, %1, %3\n\ts_nop 1" : "+v"(u), "+v"(w) : "v"(m[2]), "v"(m[3]));
    swp16(u, w); u = vmax(u, w);
    xch16(u, u, w); xch32(u, m[0], m[2]); xch32(w, m[1], m[3]);
    MT(5);
    expo(3); expo(2); expo(1); expo(0);
    pv(3); pv(2); pv(1); pv(0);
    swp32(m[0], m[2]); swp32(m[1], m[3]); u = m[0] + m[2]; w = m[1] + m[3];
    swp16(u, w); u = __builtin_amdgcn_rcpf(u + w);
    xch16(u, u, w); xch32(u, m[0], m[2]); xch32(w, m[1], m[3]);
    MT(6);
    outp(3); outp(2); outp(1); outp(0);
  } else {
    scores(3); scores(2);
    sched0();
    if (TH) fv(3); else fk(4);
    fv(2);
    xrd();
    sched0();
    MT(4);
    fv(1); fqq(1); fk(0); fqq(0);
    soft2(2);
    fv(0);
#pragma unroll
    for (int c = 0; c < 2; ++c) asm volatile("" : "+v"(qp[c][0]), "+v"(qp[c][1]), "+v"(kp[c][1]), "+v"(vp[c][1]));
    MT(5);
    xget();
    MT(6);
    scores(1); scores(0);
    soft2(0);
  }
  MT(7);
  MTOUT(10 + TH, TH * 2);
}

extern "C" __global__ __launch_bounds__(512, 2) void mega_kernel(const u16* x0, const u16* wqkv, const u16* wo, const u16* w1, const u16* w2, const u16* b1, const u16* b2, u16* out, char* ws) {
  const int wid0 = __builtin_amdgcn_readfirstlane(threadIdx.x >> 6);
  asm volatile("s_getpc_b64 s[40:41]\n\ts_andn2_b32 s40, s40, 63\n\ts_load_dwordx2 s[100:101], s[40:41], %0 offset:124\n\ts_load_dwordx2 s[100:101], s[40:41], %0 offset:252"
               :: "s"(__builtin_amdgcn_readfirstlane(threadIdx.x) << 2) : "s40", "s41", "s100", "s101", "scc");
  sched0();
#define PHASE_VARS \
      int tid = threadIdx.x, bid = blockIdx.x, wid = wid0; \
      asm volatile("" : "+v"(tid)); asm volatile("" : "+s"(bid), "+s"(wid), "+s"(ws)); \
      const int lw = wid - 4, lt = tid - 256, lane = tid & 63, fr = lane & 15, fq = lane >> 4, wp = wid & 1, wq = wid >> 1; \
      const unsigned j = (bid >> 3) * 4 + lw; \
      int hp, sb; tile_map<16, 16, 4, 8>(bid, hp, sb);            \
      int tp, tq; tile_map<8, 32, 4, 8>(bid, tp, tq);             \
      int fp, fq1; tile_map<28, 8, 7, 4>(bid, fp, fq1);           \
      const int cw = bid < 16 ? wid + 4 * (bid >> 3) : -1; \
      (void)cw; (void)j; (void)lt; (void)wp; (void)wq; (void)lw; (void)fr; (void)fq; (void)hp; (void)sb; (void)tp; (void)tq; (void)fp; (void)fq1;
#define LOADER_VARS PHASE_VARS asm volatile("" : "+s"(wqkv), "+s"(wo), "+s"(w1), "+s"(w2));

  if (__builtin_expect(wid0 >= 4, 1)) {
    const u16* xin = x0;
    Pfc pf(wo, w1, blockIdx.x * 4 + wid0 - 4);
#pragma unroll 1
    for (int l = 0; l < NL; ++l) {
      {
        LOADER_VARS
        const int s0 = sb * 128;
        GQ::Loader L(wqkv + hp * 64, xin, lw, lane, qkv_rot(bid));
        L.issue_w<GQ::SB>(L.w + L.kord(0) * GQ::WSTEP);
        sched0();
        L.seta((unsigned)(s0 * D * 2), halo_off(s0, lw, lane), lw, lane);
        if (l == 0) { L.issue_a<GQ::SB>(L.ab + L.kord(0) * 128); L.pre<1, true, true>(); STL(0, 1); }
        else {
          L.pre<1, true, false>();
          if (QBR == 0) bar();
#pragma unroll
          for (int n = 2; n < 7; ++n) pfb_q(wqkv, bid, j, n);
          if (QBR == 1) bar();
        }
        sched0();
        const rsrc_t rs = mkrsrc(ws + WS_ST, MB);
        f32x4 c = {0.f, 0.f, 0.f, 0.f}; f32x2 s1 = {0.f, 0.f}, s2 = {0.f, 0.f}, o = {0.f, 0.f}; float pv = 0.f;
        bf16x8 ones = {0x3F80, 0x3F80, 0x3F80, 0x3F80, 0x3F80, 0x3F80, 0x3F80, 0x3F80};
        const int ct = lw & 1 ? 8 + (sb & 3) : sb & 7; const bool cs = l == 0 && lw >= 2;
        int tpf = (slow_res(wqkv) >> 2) != (bid & 1) ? 0 : lw < 2 ? 10 : 9;
        unsigned ra = GQ::AOFF + ((hp & 3) * 32 + lw * 8) * 128 + lane * 16, rb = rd_km(lane, ct);
        unsigned so = lane & 7 ? MB : ph((unsigned)(((l * S + s0 + (hp & 3) * 32 + lw * 8 + (lane >> 3)) * 4 + (hp >> 2)) * 16));
        unsigned co = fr >= 2 || !cs ? MB : ph((unsigned)(ST_CS + ((hp * 96 + ct * 8 + fq * 2 + fr) * 2 + (sb >> 3)) * 16));
        unsigned to[3], xo = x_off(tp, tq, lw, fr, fq), q2 = ((bid & 2) * 4 + slow_res(wqkv)) * 128 + ((bid >> 3) * 2 + (lw & 1)) * (3 * D * 2);
#pragma unroll
        for (int i = 0; i < 3; ++i) {
          const int r = s0 - 16 + (i == 2 && lw ? 64 : i * 64) + (lt >> 2);
          to[i] = ph((unsigned)(((l * S + (r < 0 ? 0 : r)) * 4 + (lt & 3)) * 16));
        }
        unsigned ta = (lt & 3) < (lw ? 2 : 3) ? L_TAB + ((lt & 3) * 64 + (lt >> 2)) * 4 : L_SCR, ts = ph((unsigned)(ST_CS + (hp * 192 + (lw ? lt - 64 : lt)) * 16));
        asm volatile("" : "+v"(ra), "+v"(rb), "+v"(so), "+v"(co), "+v"(to[0]), "+v"(to[1]), "+v"(to[2]), "+v"(xo), "+v"(ta), "+v"(ts), "+v"(c), "+v"(s1), "+v"(s2), "+v"(ones), "+v"(o));
        asm volatile("" : "+s"(q2), "+s"(tpf));
        auto pfq = [&](int n) { const unsigned o = q2 + L.kord(n) * GQ::WSTEP; spf2<0>(wqkv, o); spf2<2048>(wqkv, o); spf2<4096>(wqkv, o); };
        if (l) L.start();
        else STL(0, 2);
        unsigned tag, tg2; u32x4 gs, a[3];
        auto rowst = [&](int i) {
          const float m0 = __uint_as_float(a[i][0]), m = red4(m0) * 0.25f, q = red4(__uint_as_float(a[i][2]) + 256.f * (m0 - m) * (m0 - m));
          o = (f32x2){sel((lt & 3) == i, m, o.x), sel((lt & 3) == i, q, o.y)};
          asm volatile("" : "+v"(o));
        };
        for_c<0, 16 - GQ::H>([&](auto tc) {
          constexpr int t = tc.value;
          u32x4 v; unsigned tv;
          L.step<t, t == 5 || t == 6 ? 1 : t == 9 || t == 10 ? 4 : 0>([&](unsigned ls) {
            if (t < 4) v = *(const LAS u32x4*)(uintptr_t)(ls + ra);
            if (t == 3) tv = *(volatile LAS unsigned*)(uintptr_t)L_TAG;
          }, [&]() {
            if (t < 8 && cs) {
              constexpr int ls = 32768 + (t + 1) % 3 * GQ::SB;
              bf16x4 x[4];
#pragma unroll
              for (int e = 0; e < 4; ++e) x[e] = lds_tr4(rb + (ls & ~0x7fff) + (ls & 0x7fff) + (e >> 1) * 4096 + (e & 1) * 512);
#pragma unroll
              for (int ks = 0; ks < 2; ++ks) c = mfma(__builtin_shufflevector(x[2 * ks], x[2 * ks + 1], 0, 1, 2, 3, 4, 5, 6, 7), ones, c);
            } else if (t >= 4) {
              if (t >= 10 || t >= tpf) { if (t < TC1) pf.go(); }
              else if (lw < 2) pfq(t + 4);
              else if (t == 8) { pfq(14); pfq(15); }
            }
            if (t < 4) {
              if (t == 0) pv = bflo(v[0]);
#pragma unroll
              for (int e = 0; e < 4; ++e) { const f32x2 d = (f32x2){bflo(v[e]), bfhi(v[e])} - pv; s1 += d; s2 += d * d; }
              asm volatile("" : "+v"(s1), "+v"(s2));
            }
            if (t == 3) { tag = __builtin_amdgcn_readfirstlane(tv); tg2 = tag2(tag); if (tpf == 10) pfq(7); }
            if (t == 4) {
              const float d1 = (s1.x + s1.y) * (1.f / 32.f), ml = pv + d1, m = red8(ml) * 0.125f;
              const float q = red8(vmax((s2.x + s2.y) - 32.f * d1 * d1, 0.f) + 32.f * (ml - m) * (ml - m));
              gs = (u32x4){__float_as_uint(m), tag, __float_as_uint(q), tg2};
              asm volatile("" : "+v"(gs));
            }
            if (t == 8) { gs = (u32x4){__float_as_uint(sel(fr, c[2], c[0])), tag, __float_as_uint(sel(fr, c[3], c[1])), tg2}; asm volatile("" : "+v"(gs)); }
            if (t == 11) {
#pragma unroll
              for (int i = 0; i < 3; ++i) ld_ok(a[i]);
              for (int spin = 0;; ++spin) {
                unsigned bad = 0;
#pragma unroll
                for (int i = 0; i < 3; ++i) bad |= (a[i][1] ^ tag) | (a[i][3] ^ tg2);
                if (__builtin_expect(__all(bad == 0), 1) || spin_fail(spin, ws)) break;
                __builtin_amdgcn_s_sleep(8);
#pragma unroll
                for (int i = 0; i < 3; ++i) ld_again(a[i], rs, to[i]);
              }
              rowst(0);
            }
            if (t == 12) { rowst(1); rowst(2); }
            if (t == 13) { LAS float* p = (LAS float*)(uintptr_t)ta; p[0] = -o.x; p[144] = __builtin_amdgcn_rsqf(o.y * (1.f / D) + EPS); }
          }, [&]() {
            if (t == 4) st16(rs, so, gs);
            if (t == 8) {
              st16(rs, co, gs);
              sched0();
#pragma unroll
              for (int i = 0; i < 3; ++i) ld_raw(a[i], rs, to[i]);
            }
            if (t == 13) ld_raw(gs, rs, ts);
          });
        });
        sched0(); wait_vm<GQ::CNT + 1>(); bar();
        wait_lgkm0(); wait_vm<1>(); bar();
        wait_vm<0>();
        ld_ok(gs);
        if (l == 0) {
          for (int spin = 0;; ++spin) {
            if (__builtin_expect(__all(((gs[1] ^ tag) | (gs[3] ^ tg2)) == 0), 1) || spin_fail(spin, ws)) break;
            __builtin_amdgcn_s_sleep(8);
            ld_again(gs, rs, ts);
          }
          float c0 = __uint_as_float(gs[0]), c1 = __uint_as_float(gs[2]);
          c0 += dppf<0xB1>(c0); c1 += dppf<0xB1>(c1);
          *(LAS float*)(uintptr_t)(L_CS + (lw ? lt - 64 : lt) * 4) = sel(lt & 1, c1, c0);
        }
        if (l == 0) {
#pragma unroll
          for (int p = 0; p < 4; ++p) dma16((const char*)xin + (p >> 1) * 32768 + (p & 1) * 64, xo, L_X + lw * 4096 + p * 1024);
        }
        if (l == 0 && lw < TBA) { if (fr < 14) touch4(w1 + (size_t)(((bid >> 3) * TBA + lw) * 8 + (bid & 4)) * F + (bid & 3) * 896, fq * (F * 2) + fr * 128, L_SCR + lw * 256); }
        if (l == 0 && PWL) { wait_lgkm0(); bar(); }
      }
      {
        LOADER_VARS
        GW::Loader L(wo + tp * 128, ws + WS_O + (size_t)tq * 32 * 8192, lw, lane);
        unsigned po = ((bid & 1) * 8 + (slow_res(wo) ^ (j & 1))) * 128 + (j >> 1) * (D * 2);
        if (!PWL) { wait_lgkm0(); bar(); L.pre_w(); }
#pragma unroll
        for (int k = GW::H; k < 10; ++k) spf2(wo, po + k * GW::WSTEP);
        asm volatile("" : "+s"(po));
        if (lw < TBA) wait_vm<(PWL ? 0 : GW::H * 4) + 1>(); else wait_vm<PWL ? 0 : GW::H * 4>();
        sched0();
        bar();
        if (PWL) L.pre_w();
#pragma unroll
        for (int k = 10; k < 16; ++k) spf2(wo, po + k * GW::WSTEP);
        L.start();
        L.run(16 - GW::H, [&](int) { pf.go(); });
        pf.go();
        L.tail<GW::H - 1>();
        bar();
      }
      {
        LOADER_VARS
        if (bid < 224) {
          G1::Loader L(w1 + fp * 128, ws + WS_H + (size_t)fq1 * 64 * 16384, lw, lane);
          L.pre_w();
          const int g0 = (bid & 3) * 14, ga = (g0 + ((slow_res(w1) - g0) & 7)) ^ (j & 1), gb = ga + 8 < g0 + 14 ? ga + 8 : ga;
          const unsigned j2 = j < 32 ? 112 + (j & 15) : j;
          unsigned pa = (j >> 1) * (F * 2) + ga * 128, pb = (j >> 1) * (F * 2) + gb * 128, pd = (j2 >> 1) * (F * 2) + (j & 16 ? gb : ga) * 128;
          asm volatile("" : "+s"(pa), "+s"(pb), "+s"(pd));
          auto pfb = [&](int k) { spf2(w1, pa + k * G1::WSTEP); spf2(w1, pb + k * G1::WSTEP); spf2(w1, pd + k * G1::WSTEP); };
#pragma unroll
          for (int k = G1::H; k < G1::H + PA_1; ++k) pfb(k);
          wait_lgkm0(); pf.go();
          sched0();
          bar();
          pf.go();
          L.start();
          L.run(32 - G1::H, [&](int t) {
            if (pf.pc < 3 + NW1) pf.go();
            else if (t < 32 - G1::H - PA_1) pfb(t + G1::H + PA_1);
          });
          L.tail<G1::H - 1>();
          bar();
        } else {
          wait_lgkm0(); pf.go();
          bar();
          if (l == 0) {
            __builtin_amdgcn_s_sleep(24);
            while (pf.pc < 3 + NW1) { wait_lgkm0(); pf.go(); }
            spare_touch(w1, w2, bid, wid, lane);
          }
        }
        pf.pc = 99;
      }
      {
        LOADER_VARS
        constexpr int NB = F / 64 - 2 * JA;
        const char* ub = ws + WS_U + (size_t)tp * 112 * 16384;
        G2A::Loader A((const char*)(w2 + (tq & 23) * 64) + (tq & 8 ? JA * G2::WSTEP : 0), ub + (tq & 8 ? JA * 32768 : 0), lw, lane);
        A.pre_w();
        unsigned pb = ((bid & 4) * 4 + (j & 1) * 8 + slow_res(w2)) * 128 + (j >> 1) * (S * 2) + JA * G2::WSTEP, pa = pb - (bid & 2 ? 0 : JA * G2::WSTEP);
        asm volatile("" : "+s"(pa), "+s"(pb));
        auto side = [&](int n) { n += G2A::H + PA_2; if (n < JA + NB) spf2(w2, (n < JA ? pa : pb) + n * G2::WSTEP); };
#pragma unroll
        for (int k = 0; k < PA_2; ++k) side(k - PA_2);
        G2::Loader L((const char*)(w2 + tq * 64) + 2 * JA * G2::WSTEP, ub + 2 * JA * 32768, lw, lane);
        gu32* xf = (gu32*)(ws + WS_MAIL) + (l * 4 + 2) * MEDGE + (tq * 8 + tp) * 64 + 28 + lw;
        const char* xp = ws + WS_XP + (unsigned)(((bid ^ 2) * 4 + lw) << 14);
        unsigned fl = L_QKV + lw * 256, xv = ph(lane * 16);
        asm volatile("" : "+s"(xf), "+s"(xp), "+s"(fl)); asm volatile("" : "+v"(xv));
        sched0();
        bar();
        A.start();
        A.run(JA - G2A::H, [&](int t) { side(t); });
        auto stage = [&](auto s) { L.issue_w<s.value * G2::SB>(L.w + s.value * G2::WSTEP); L.issue_a<s.value * G2::SB>(L.ab + s.value * 32768); sched0(); };
        wait_vm<G2A::CNT>(); bar(); stage(IC<0>{}); side(JA - 2);
        wait_vm<G2::CNT>(); bar(); stage(IC<1>{}); side(JA - 1);
        wait_vm<G2::CNT>(); bar(); stage(IC<2>{}); stage(IC<3>{}); side(JA);
        L.t = 1; L.islot = 0;
        L.run(NB - G2::H, [&](int t) {
          if (t == NB - G2::H - FLS) asm volatile("s_mov_b32 m0, %2\n\ts_nop 0\n\tglobal_load_lds_dword %0, %1 sc1" :: "v"(0u), "s"(xf), "s"(fl) : "memory");
          side(JA + t);
        });
        auto xdma = [&]() {
#pragma unroll
          for (int i = 0; i < 4; ++i) dma16(xp + i * 2048, xv, L_X + lw * 4096 + i * 1024);
          sched0();
        };
        sched0(); wait_vm<2 * G2::CNT>(); bar();
        if (__builtin_amdgcn_readfirstlane(*(volatile LAS unsigned*)(uintptr_t)fl)) { xdma(); wait_vm<G2::CNT + 4>(); bar(); wait_vm<4>(); bar(); }
        else {
          wait_vm<G2::CNT>(); bar(); wait_vm<0>(); bar();
          for (int spin = 0;; ++spin) {
            if (__builtin_amdgcn_readfirstlane(__hip_atomic_load(xf, RLX_AGENT)) || spin_fail(spin, ws)) break;
            __builtin_amdgcn_s_sleep(2);
          }
          STL(4 + l * 4, 7);
          xdma();
        }
        wait_vm<0>(); bar();
        xin = (const u16*)(ws + WS_X2);
        if (NL == 1) bar();
      }
    }
    return;
  }

  {
    ST(0, 0);
    PHASE_VARS
    if (cw >= 0) code_warm(cw, lane, L_SCR + wid * 256);
    pfb_q(wqkv, bid, (bid >> 3) * 4 + wid, 0); pfb_q(wqkv, bid, (bid >> 3) * 4 + wid, 1);
    if (wid == 3) {
      if ((slow_res(wqkv) >> 2) == (bid & 1)) {
        __builtin_amdgcn_s_sleep(ET);
        if ((lane & 3) < 3 && lane < 40) touch4(wqkv, (unsigned)(((qkv_rot(bid) * 64 + 128 + (bid >> 3) * 10 + (lane >> 2)) & 1023) * (3 * D * 2) + (lane & 3) * (D * 2) + ((bid & 2) * 4 + slow_res(wqkv)) * 128), L_SCR + 768);
      }
    } else {
      const unsigned ep = __hip_atomic_load((gu32*)(ws + WS_EPOCH), RLX_AGENT);
      const rsrc_t rz = mkrsrc(ws + WS_XCH, 3 * MB);
      if (tid < 128) st16(rz, ph((((tid >> 6) * 256 + tq * 8 + tp) * 64 + lane) * 16), (u32x4){0u, SENT, 0u, SENT});
      else {
        const int e = (tid >> 3) & 7, line = e == 0 || e == 7 ? (2 * sb + (e & 1)) * 8 + (hp & 7) : e * 256 + bid;
        st16(rz, WS_MAIL - WS_XCH + line * 256 + (tid & 7) * 16, (u32x4){0u, 0u, 0u, 0u});
      }
      if (wid == 1 && bid < 224) touch4(b1 + fp * 128, lane * 4, L_BIAS);
      if (wid == 2) touch4(b2 + tq * 64, (lane & 31) * 4, L_BIAS + 256);
      if (wid == 0) { *(LAS unsigned*)(uintptr_t)L_TAG = ep + 1u; *(LAS u32x4*)(uintptr_t)(L_TAG + 16) = (u32x4){0u, 0u, 0u, 0u}; }
      asm volatile("s_waitcnt vmcnt(0)" :: "v"(ep) : "memory");
    }
    ST(0, 4);
    ST(1, 0);
  }
#pragma unroll 1
  for (int l = 0; l < NL; ++l) {
    {
      PHASE_VARS
      if (wid & 2) qkv_attn<1>(ws, l, wp, lane, hp, sb, cw);
      else qkv_attn<0>(ws, l, wp, lane, hp, sb, cw);
      ST(1 + l * 4, 5);
      sync_post(ws, l * 4, lane < 16 ? (sb * 16 + lane) * 64 + hp : -1, wid);
    }
    {
      PHASE_VARS
      GW::Comp<O42<false, WNE>> g; g.setup(wp, wq, lane);
      f32x4 acc[4][2];
#pragma unroll
      for (int i = 0; i < 4; ++i)
#pragma unroll
        for (int jj = 0; jj < 2; ++jj) acc[i][jj] = (f32x4){0.f, 0.f, 0.f, 0.f};
      unsigned xa = L_X + wid * 4096 + lane * 16;
      { bf16x8 s[2]; sel_frags(true, fr, fq, s); tile_add(acc, s, xa); }
      const rsrc_t rx = mkrsrc(ws + WS_XCH + l * (MB / 2), MB / 2), rh = mkrsrc(ws + WS_H, S * D * 4);
      unsigned xs = fq & 1 ? MB : ph((unsigned)(((tq * 8 + tp) * 64 + wp * 32 + wq * 16 + fr) * 16 + fq * 4)), xl = ph((unsigned)((tq * 512 + fq * 128 + wq * 16 + fr) * 16));
      const unsigned h0 = ph((unsigned)(((tp * 32 + tq) << 14) + wp * 8192 + (wq * 32 + fr) * 128 + ((pair_chunk(fq) ^ (km_f(fr) << 1)) << 4)));
      u32x4 ho = {h0, h0 ^ 64, h0 + 4096, (h0 ^ 64) + 4096};
      bf16x8 ones = {0x3F80, 0x3F80, 0x3F80, 0x3F80, 0x3F80, 0x3F80, 0x3F80, 0x3F80};
      asm volatile("" : "+v"(xa), "+v"(xs), "+v"(xl), "+v"(ho), "+v"(ones));
      g.prep<false>(acc);
      mfma_gap();
      sync_wait(ws, l * 4, tq * 8 + tp, lane, (lane & 31) < 16 ? 1u : 0u, wid);
      g.run_steps<false>(acc, [&](int it, int i) { if (i == 0 && cw >= 0 && it == -2) code_warm(cw, lane, L_SCR + wid * 256); } CSA);
      bar();
      sched0();
      g.pend<0, 4>(acc);
      ST(2 + l * 4, 2);
      u32x4 pk[2][2]; float mp[2], q[2];
#pragma unroll
      for (int jj = 0; jj < 2; ++jj) {
        f32x4 sm = {0.f, 0.f, 0.f, 0.f};
#pragma unroll
        for (int i = 0; i < 2; ++i) { pk[jj][i] = cat4(pk4(acc[2 * i][jj]), pk4(acc[2 * i + 1][jj])); asm volatile("" : "+v"(pk[jj][i])); sm = mfma(ones, __builtin_bit_cast(bf16x8, pk[jj][i]), sm); }
        mp[jj] = sm[0] * (1.f / 64.f); asm volatile("" : "+v"(mp[jj]));
      }
#pragma unroll
      for (int jj = 0; jj < 2; ++jj) {
        f32x4 qv = {0.f, 0.f, 0.f, 0.f};
#pragma unroll
        for (int i = 0; i < 4; ++i) {
          const unsigned x = pk[jj][i >> 1][(i & 1) * 2], y = pk[jj][i >> 1][(i & 1) * 2 + 1];
          acc[i][jj] = (f32x4){bflo(x), bfhi(x), bflo(y), bfhi(y)} - mp[jj]; qv += acc[i][jj] * acc[i][jj];
        }
        q[jj] = (qv[0] + qv[2]) + (qv[1] + qv[3]);
      }
      {
        float u, w;
        swp32(q[0], q[1]); xch16(q[0] + q[1], u, w);
        __builtin_amdgcn_raw_buffer_store_b64((u32x2){__float_as_uint(sel(fq < 2, mp[0], mp[1])), __float_as_uint(u + w)}, rx, xs, 0, 16);
      }
      sched0();
#pragma unroll
      for (int p = 0; p < 4; ++p) *(LAS u32x4*)(uintptr_t)(xa + p * 1024) = pk[p >> 1][p & 1];
      if (XD) __builtin_amdgcn_s_sleep(XD);
      f32x4 gr[4];
      for (int spin = 0;; ++spin) {
        asm volatile("" ::: "memory");
#pragma unroll
        for (int p = 0; p < 4; ++p) gr[p] = __builtin_bit_cast(f32x4, __builtin_amdgcn_raw_buffer_load_b128(rx, xl + p * 1024, 0, 16));
        const float lo = vmin3(vmin3(gr[0][1], gr[0][3], gr[1][1]), vmin3(gr[1][3], gr[2][1], gr[2][3]), vmin3(gr[3][1], gr[3][3], gr[3][3]));
        if (!__any(lo < 0.f) || spin_fail(spin, ws)) break;
        __builtin_amdgcn_s_sleep(XS);
      }
      ST(2 + l * 4, 5);
      {
        const f32x4 t = (gr[0] + gr[1]) + (gr[2] + gr[3]);
        float m[2], u, w, v, y;
#pragma unroll
        for (int jj = 0; jj < 2; ++jj) {
          m[jj] = t[2 * jj] * 0.25f;
          float e = 0.f;
#pragma unroll
          for (int p = 0; p < 4; ++p) { const float d = gr[p][2 * jj] - m[jj]; e += d * d; }
          q[jj] = t[2 * jj + 1] + 64.f * e;
        }
        swp32(m[0], m[1]); swp32(q[0], q[1]);
        xch16((m[0] + m[1]) * 0.5f, u, w); xch16((q[0] + q[1]) + 128.f * (m[0] - m[1]) * (m[0] - m[1]), v, y);
        const float var = ((v + y) + 256.f * (u - w) * (u - w)) * (1.f / D), r1 = __builtin_amdgcn_rsqf(var + EPS);
        xch32((u + w) * 0.5f, m[0], m[1]);
        xch32(r1 * __builtin_amdgcn_rsqf(var * r1 * r1 + EPS), q[0], q[1]);
        ST(2 + l * 4, 3);
#pragma unroll
        for (int jj = 0; jj < 2; ++jj) {
          const float c = (mp[jj] - m[jj]) * q[jj];
#pragma unroll
          for (int i = 0; i < 2; ++i) st16(rh, ho[jj * 2 + i], pack_pair(acc[2 * i][jj] * q[jj] + c, acc[2 * i + 1][jj] * q[jj] + c));
        }
      }
      sync_post(ws, l * 4 + 1, lane < 28 ? (tp * 28 + lane) * 64 + tq : -1, wid);
      if (l == 0 && bid == 0 && wid == 0) { const unsigned tag = get_tag(); if (lane == 0) __hip_atomic_store((gu32*)(ws + WS_EPOCH), tag, RLX_AGENT); }
    }
    {
      PHASE_VARS
      if (bid >= 224) {
        ST(2 + l * 4, 4);
        ST(3 + l * 4, 0);
        if (l == 0) { __builtin_amdgcn_s_sleep(24); spare_touch(w1, w2, bid, wid, lane); }
      } else {
        G1::Comp<O44<false, 32>> g; g.setup(wp, wq, lane);
        f32x4 acc[4][4];
#pragma unroll
        for (int i = 0; i < 4; ++i) { const u32x2 b = *(const LAS u32x2*)(uintptr_t)(L_BIAS + (wp * 4 + i) * 32 + fq * 8);
#pragma unroll
          for (int jj = 0; jj < 4; ++jj) acc[i][jj] = (f32x4){bflo(b.x), bfhi(b.x), bflo(b.y), bfhi(b.y)}; }
        const rsrc_t ro = mkrsrc(ws + WS_U, D * F * 4);
        const unsigned u0 = ph((unsigned)(((fq1 * 56 + fp * 2 + wp) << 14) + (wq * 64 + fr) * 128 + ((pair_chunk(fq) ^ (fr >> 1)) << 4)));
        u32x4 uo = {u0, u0 + 4096, u0 + 8192, u0 + 12288}, uo2 = uo ^ 64;
        asm volatile("" : "+v"(uo), "+v"(uo2));
        g.prep<false>(acc);
        sync_wait(ws, l * 4 + 1, fq1 * 28 + fp, lane, 1u, wid);
        g.run_steps<false>(acc, [&](int it, int i) { if (i == 0 && cw >= 0 && it == -2) code_warm(cw, lane, L_SCR + wid * 256); } CSA);
        bar();
        sched0();
        g.pend<0, 8>(acc);
        ST(3 + l * 4, 2);
#pragma unroll
        for (int i = 0; i < 2; ++i)
#pragma unroll
          for (int jj = 0; jj < 4; ++jj) st16(ro, (i ? uo2 : uo)[jj], pack_pair(gelu_tanh(acc[2 * i][jj]), gelu_tanh(acc[2 * i + 1][jj])));
        ST(3 + l * 4, 3);
      }
      sync_post(ws, l * 4 + 2, bid < 224 && lane < 32 ? (lane * 8 + fq1) * 64 + fp : -1, wid);
    }
    {
      PHASE_VARS
      asm volatile("" : "+s"(out));
      G2A::Comp<O44<true, ANE>> ga; ga.setup(wp, wq, lane);
#pragma unroll
      for (int jj = 0; jj < 4; ++jj) ga.qrd[0][jj] = 65536 + rd_km(lane, wq * 2 + (jj & 1)) + (((jj >> 1) ^ (tq >> 3)) & 1) * 8192;
      G2::Comp<O42<true, BNE>> g; g.setup(wp, wq, lane);
      g.setwin();
      f32x4 ac4[4][4], acc[4][2];
#pragma unroll
      for (int jj = 0; jj < 2; ++jj) { const float b = bf2f(*(const LAS u16*)(uintptr_t)(L_BIAS + 256 + (wq * 2 + jj) * 32 + fr * 2));
#pragma unroll
        for (int i = 0; i < 4; ++i) { ac4[i][jj] = (f32x4){b, b, b, b}; ac4[i][jj + 2] = (f32x4){0.f, 0.f, 0.f, 0.f}; } }
      unsigned xa = L_X + wid * 4096 + lane * 16, xo = (unsigned)((bid * 4 + wid) << 14) + ph(lane * 16), oo = x_off(tp, tq, wid, fr, fq), xo2 = xo + 4096, oo2 = oo + 32768;
      unsigned fo = lane ? MB : (((tq ^ 8) * 8 + tp) * 64 + 28 + wid) * 4u, one = 1u;
      bf16x8 s[2]; sel_frags(false, fr, fq, s); tile_add(ac4, s, xa);
      const rsrc_t rx = mkrsrc(ws + WS_XP, 16 * MB), rf = mkrsrc(ws + WS_MAIL + (l * 4 + 2) * (MEDGE * 4), MEDGE * 4), ro = mkrsrc(l ? (const void*)out : (const void*)(ws + WS_X2), S * D * 2);
      asm volatile("" : "+v"(xa), "+v"(xo), "+v"(oo), "+v"(xo2), "+v"(oo2), "+v"(fo), "+v"(one), "+v"(s[0]), "+v"(s[1]));
      ga.prep<false>(ac4);
      mfma_gap();
      sync_wait(ws, l * 4 + 2, tq * 8 + tp, lane, (lane & 31) < 28 ? 1u : 0u, wid);
      ga.run_steps<false>(ac4, [&](int it, int i) { if (i == 0 && cw >= 0 && it == -2) code_warm(cw, lane, L_SCR + wid * 256); } CSA);
      ga.pend<4, 8>(ac4);
      ST(4 + l * 4, 1);
#pragma unroll
      for (int i = 0; i < 2; ++i)
#pragma unroll
        for (int jj = 0; jj < 2; ++jj) st16(rx, (jj ? xo2 : xo) + i * 2048, cat4(pk4(ac4[2 * i][jj + 2]), pk4(ac4[2 * i + 1][jj + 2])));
#pragma unroll
      for (int i = 0; i < 4; ++i) { acc[i][0] = ac4[i][0]; acc[i][1] = ac4[i][1]; }
#pragma unroll
      for (int k = 0; k < 2; ++k) { g.fp[2][k] = ga.fp[2][k]; g.fp[3][k] = ga.fp[3][k]; g.fq[0][k] = ga.fq[0][k]; g.fq[1][k] = ga.fq[1][k]; }
      sched0();
      g.run_steps<true>(acc, [&](int it, int i) {
        if (i == PS % 4 && it == PS / 4 - (F / 64 - 2 * JA) / 4) {
          wait_vm<0>();
          ST(4 + l * 4, 5);
          __builtin_amdgcn_raw_buffer_store_b32(one, rf, fo, 0, 16);
        }
      } CSA);
      bar();
      sched0();
      g.pend<0, 4>(acc);
      ST(4 + l * 4, 2);
      tile_add(acc, s, xa);
      u32x4 xr[4];
#pragma unroll
      for (int i = 0; i < 2; ++i)
#pragma unroll
        for (int jj = 0; jj < 2; ++jj) { xr[jj * 2 + i] = pack_pair(acc[2 * i][jj], acc[2 * i + 1][jj]); st16(ro, (jj ? oo2 : oo) + i * 64, xr[jj * 2 + i]); }
      ST(4 + l * 4, 3);
      if (l == 0) {
#pragma unroll
        for (int p = 0; p < 4; ++p) *(LAS u32x4*)(uintptr_t)(xa + p * 1024) = xr[p];
        const int sbc = (tq >> 1) + (lane >> 4);
        const bool pv = lane < 16 || (lane < 32 && (tq & 1) && sbc < 16);
        sync_post(ws, 3, pv ? (sbc * 16 + (lane & 15)) * 64 + (tq - 2 * sbc + 1) * 8 + tp : -1, wid);
      } else ST(8, 4);
    }
  }
  if (wid0 == 0 && blockIdx.x == 45) {
    wait_lgkm0();
    const unsigned d = *(volatile LAS unsigned*)(uintptr_t)(19968 + PROBE * 4) - *(volatile LAS unsigned*)(uintptr_t)(19968 + PROBE0 * 4);
    const u64 e = __builtin_amdgcn_s_memrealtime() + 4 * (d < 20000 ? d : 0);
    while (__builtin_amdgcn_s_memrealtime() < e) __builtin_amdgcn_s_sleep(1);
  }
  return;
  if (wid0 == 0) {
    const u64 hi = __builtin_amdgcn_s_memrealtime() & ~0xffffffffull;
    wait_lgkm0();
    for (int e = threadIdx.x; e < 128; e += 64) ((gu64*)(ws + (200u << 20)))[((e >> 3) * 1024 + blockIdx.x) * 8 + (e & 7)] = hi | *(volatile LAS unsigned*)(uintptr_t)(19968 + e * 4);
  }
}

extern "C" void kernel_launch(void* const* d_in, const int* in_sizes, int n_in,
                              void* d_out, int out_size, void* d_ws, size_t ws_size,
                              hipStream_t stream) {
  (void)hipFuncSetAttribute((const void*)mega_kernel, hipFuncAttributeMaxDynamicSharedMemorySize, 163840);
  mega_kernel<<<256, 512, 163840, stream>>>((const u16*)d_in[0], (const u16*)d_in[1], (const u16*)d_in[2], (const u16*)d_in[4], (const u16*)d_in[6], (const u16*)d_in[5], (const u16*)d_in[7], (u16*)d_out, (char*)d_ws);
}
```

```cpp
#define LEAN
#define STAMP
#define CSTAMP 0
#define PROBE 0
#define PROBE0 0
#define PROBEF 4
#include <hip/hip_runtime.h>
#include <hip/hip_bf16.h>
#include <stdint.h>

typedef unsigned short u16;
typedef unsigned long long u64;
typedef short bf16x8 __attribute__((ext_vector_type(8)));
typedef short bf16x4 __attribute__((ext_vector_type(4)));
typedef float f32x4 __attribute__((ext_vector_type(4)));
typedef float f32x2 __attribute__((ext_vector_type(2)));
typedef unsigned u32x4 __attribute__((ext_vector_type(4)));
typedef unsigned u32x2 __attribute__((ext_vector_type(2)));
typedef __bf16 bf16v2 __attribute__((ext_vector_type(2)));
#define LAS __attribute__((address_space(3)))
#define GAS __attribute__((address_space(1)))
#define DEVI __device__ __forceinline__
typedef __attribute__((address_space(1))) unsigned gu32;
typedef __attribute__((address_space(1))) unsigned long long gu64;

constexpr int S = 2048, D = 1024, F = 3584;
constexpr float EPS = 1e-5f;

DEVI float bf2f(u16 v) { return __uint_as_float(((unsigned)v) << 16); }
DEVI float bflo(unsigned v) { return __uint_as_float(v << 16); }
DEVI float bfhi(unsigned v) { return __uint_as_float(v & 0xffff0000u); }
DEVI unsigned pk2(float lo, float hi) { bf16v2 p; p.x = (__bf16)lo; p.y = (__bf16)hi; return __builtin_bit_cast(unsigned, p); }

DEVI const void* uni_ptr(const void* p) {
  const u64 v = (u64)p;
  return (const void*)(((u64)(unsigned)__builtin_amdgcn_readfirstlane((unsigned)(v >> 32)) << 32) | (unsigned)__builtin_amdgcn_readfirstlane((unsigned)v));
}
DEVI void dma16(const void* sbase, unsigned voff, unsigned ldsaddr) {
  ldsaddr = __builtin_amdgcn_readfirstlane(ldsaddr);
  asm volatile("s_mov_b32 m0, %2\n\ts_nop 0\n\tglobal_load_lds_dwordx4 %0, %1 sc1" :: "v"(voff), "s"(sbase), "s"(ldsaddr) : "memory");
}
template <int N, int IMM> DEVI void dma_grp(const void* sbase, const unsigned* v, unsigned lb) {
  static_assert(N == 1 || N == 2 || N == 4, "");
  if constexpr (N == 4)
    asm volatile("s_add_u32 m0, %1, %2\n\ts_nop 0\n\tglobal_load_lds_dwordx4 %3, %0 sc1\n\tglobal_load_lds_dwordx4 %4, %0 offset:1024 sc1\n\t"
                 "global_load_lds_dwordx4 %5, %0 offset:2048 sc1\n\tglobal_load_lds_dwordx4 %6, %0 offset:3072 sc1"
                 :: "s"(sbase), "s"(lb), "i"(IMM), "v"(v[0]), "v"(v[1]), "v"(v[2]), "v"(v[3]) : "memory", "scc");
  else if constexpr (N == 2)
    asm volatile("s_add_u32 m0, %1, %2\n\ts_nop 0\n\tglobal_load_lds_dwordx4 %3, %0 sc1\n\tglobal_load_lds_dwordx4 %4, %0 offset:1024 sc1"
                 :: "s"(sbase), "s"(lb), "i"(IMM), "v"(v[0]), "v"(v[1]) : "memory", "scc");
  else
    asm volatile("s_add_u32 m0, %1, %2\n\ts_nop 0\n\tglobal_load_lds_dwordx4 %3, %0 sc1" :: "s"(sbase), "s"(lb), "i"(IMM), "v"(v[0]) : "memory", "scc");
}
template <int N> DEVI void wait_vm() { asm volatile("s_waitcnt vmcnt(%0)" :: "i"(N) : "memory"); }
DEVI void wait_lgkm0() { asm volatile("" ::: "memory"); __builtin_amdgcn_s_waitcnt(0xC07F); asm volatile("" ::: "memory"); }
DEVI void mfma_ip(f32x4& c, const bf16x8& a, const bf16x8& b) { asm volatile("v_mfma_f32_16x16x32_bf16 %0, %1, %2, %0" : "+v"(c) : "v"(a), "v"(b)); }
DEVI void mfma_gap() { __builtin_amdgcn_sched_barrier(0); asm volatile("s_nop 7"); __builtin_amdgcn_sched_barrier(0); }
DEVI void bar() { asm volatile("" ::: "memory"); __builtin_amdgcn_s_barrier(); asm volatile("" ::: "memory"); }
DEVI void sched0() { __builtin_amdgcn_sched_barrier(0); }

DEVI bf16x8 lds_b128(unsigned addr) { return *(const LAS bf16x8*)(uintptr_t)addr; }
DEVI bf16x4 lds_tr4(unsigned addr) { return __builtin_amdgcn_ds_read_tr16_b64_v4i16((LAS bf16x4*)(uintptr_t)addr); }

typedef __amdgpu_buffer_rsrc_t rsrc_t;
DEVI rsrc_t mkrsrc(const void* p, unsigned bytes) { return __builtin_amdgcn_make_buffer_rsrc((void*)p, 0, bytes, 0x00020000); }
DEVI void st16(rsrc_t r, unsigned off, u32x4 v) { __builtin_amdgcn_raw_buffer_store_b128(v, r, off, 0, 16); }
DEVI void ld_raw(u32x4& v, rsrc_t r, unsigned off) { asm volatile("buffer_load_dwordx4 %0, %1, %2, 0 offen sc1 ; raw load" : "=&v"(v) : "v"(off), "s"(r) : "memory"); }
DEVI void ld_again(u32x4& v, rsrc_t r, unsigned off) { asm volatile("buffer_load_dwordx4 %0, %1, %2, 0 offen sc1\n\ts_waitcnt vmcnt(0)" : "=&v"(v) : "v"(off), "s"(r) : "memory"); }
DEVI void ld_ok(u32x4& v) { asm volatile("; raw ok %0" : "+v"(v)); }
#define RLX_AGENT __ATOMIC_RELAXED, __HIP_MEMORY_SCOPE_AGENT

template <int N> DEVI float ror16(float v) { return __int_as_float(__builtin_amdgcn_update_dpp(0, __float_as_int(v), 0x120 + N, 0xf, 0xf, false)); }
DEVI float red16(float v) { v += ror16<8>(v); v += ror16<4>(v); v += ror16<2>(v); v += ror16<1>(v); return v; }
DEVI void xch32(float v, float& a, float& b) { unsigned u = __float_as_uint(v); auto r = __builtin_amdgcn_permlane32_swap(u, u, false, false); unsigned x = r[0], y = r[1]; a = __uint_as_float(x); b = __uint_as_float(y); }
DEVI void xch16(float v, float& a, float& b) { unsigned u = __float_as_uint(v); auto r = __builtin_amdgcn_permlane16_swap(u, u, false, false); unsigned x = r[0], y = r[1]; a = __uint_as_float(x); b = __uint_as_float(y); }
DEVI void swp32(float& a, float& b) { auto r = __builtin_amdgcn_permlane32_swap(__float_as_uint(a), __float_as_uint(b), false, false); unsigned x = r[0], y = r[1]; a = __uint_as_float(x); b = __uint_as_float(y); }
DEVI void swp16(float& a, float& b) { auto r = __builtin_amdgcn_permlane16_swap(__float_as_uint(a), __float_as_uint(b), false, false); unsigned x = r[0], y = r[1]; a = __uint_as_float(x); b = __uint_as_float(y); }
DEVI float colsum4(float v) { float a, b; xch32(v, a, b); v = a + b; xch16(v, a, b); return a + b; }
DEVI float vmax(float a, float b) { float r; asm("v_max_f32 %0, %1, %2" : "=v"(r) : "v"(a), "v"(b)); return r; }
DEVI float vmin3(float a, float b, float c) { float r; asm("v_min3_f32 %0, %1, %2, %3" : "=v"(r) : "v"(a), "v"(b), "v"(c)); return r; }
DEVI float vmax3(float a, float b, float c) { float r; asm("v_max3_f32 %0, %1, %2, %3" : "=v"(r) : "v"(a), "v"(b), "v"(c)); return r; }

DEVI unsigned src_kc(int c, int ld) { int row = c >> 3, ch = (c & 7) ^ ((row >> 1) & 7); return (unsigned)(row * ld * 2 + ch * 16); }
DEVI int km_f(int k) { return ((k >> 1) & 1) | (((k >> 3) & 1) << 1); }
DEVI unsigned ph(unsigned x) { return ((x >> 7) << 8) | (x & 127u); }
DEVI unsigned rd_kc(int lane, int ks) { int r = lane & 15, g = lane >> 4; return (unsigned)(r * 128 + (((ks * 4 + g) ^ (r >> 1)) << 4)); }
DEVI unsigned rd_km(int lane, int ct) {
  int g = lane >> 4, q = (lane & 15) >> 2, p = lane & 3;
  int f = ((q >> 1) & 1) | ((g & 1) << 1);
  return (unsigned)((ct >> 2) * 8192 + (8 * g + q) * 128 + (((ct & 3) ^ f) << 5) + 8 * p);
}

DEVI void stamp(unsigned a) { unsigned x, y;
  asm volatile("s_memrealtime s[98:99]\n\tv_mov_b32 %1, %2\n\ts_waitcnt lgkmcnt(0)\n\tv_mov_b32 %0, s98\n\tds_write_b32 %1, %0" : "=&v"(x), "=&v"(y) : "s"(a) : "s98", "s99"); }
#define ST(k, i) do { if (__builtin_amdgcn_readfirstlane(threadIdx.x) == 0) stamp(19968 + ((k) * 8 + (i)) * 4); } while (0)
#define STL(k, i) do { if (__builtin_amdgcn_readfirstlane(threadIdx.x) == 256) stamp(19968 + ((k) * 8 + (i)) * 4); } while (0)

constexpr unsigned CODE_BYTES = 47104;
DEVI void code_warm(int part, int lane, unsigned scratch) {
  unsigned long long pc;
  asm volatile("s_getpc_b64 s[40:41]\n\ts_add_u32 s40, s40, mega_kernel@rel32@lo+4\n\ts_addc_u32 s41, s41, mega_kernel@rel32@hi+12\n\ts_mov_b64 %0, s[40:41]" : "=s"(pc) :: "s40", "s41", "scc");
  const unsigned off = (unsigned)(part * 64 + lane) * 128u;
  scratch = __builtin_amdgcn_readfirstlane(scratch);
  if (off < CODE_BYTES) asm volatile("s_nop 3\n\ts_mov_b32 m0, %2\n\ts_nop 0\n\tglobal_load_lds_dword %0, %1" :: "v"(off), "s"(pc), "s"(scratch) : "memory");
}

DEVI unsigned src_km(int lt, int ld) { const int k = lt >> 3; return (unsigned)(k * ld * 2 + (((lt & 7) ^ (km_f(k) << 1)) << 4)); }
DEVI void touch4(const void* sbase, unsigned voff, unsigned ldsaddr) {
  ldsaddr = __builtin_amdgcn_readfirstlane(ldsaddr); sbase = uni_ptr(sbase);
  asm volatile("s_nop 3\n\ts_mov_b32 m0, %2\n\ts_nop 0\n\tglobal_load_lds_dword %0, %1" :: "v"(voff), "s"(sbase), "s"(ldsaddr) : "memory");
}

template <int IMM> DEVI void spf1(const void* base, unsigned soff) { asm volatile("s_load_dword s101, %0, %1 offset:%2" :: "s"(base), "s"(soff), "i"(IMM) : "s101"); }
template <int IMM = 0> DEVI void spf2(const void* base, unsigned soff) { asm volatile("s_load_dwordx2 s[100:101], %0, %1 offset:%2" :: "s"(base), "s"(soff), "i"(IMM + 60) : "s100", "s101"); }
template <int ST> DEVI void spf2x6(const void* base, unsigned soff) {
  asm volatile("s_load_dwordx2 s[100:101], %0, %1 offset:%2\n\ts_load_dwordx2 s[100:101], %0, %1 offset:%3\n\ts_load_dwordx2 s[100:101], %0, %1 offset:%4\n\t"
               "s_load_dwordx2 s[100:101], %0, %1 offset:%5\n\ts_load_dwordx2 s[100:101], %0, %1 offset:%6\n\ts_load_dwordx2 s[100:101], %0, %1 offset:%7"
               :: "s"(base), "s"(soff), "i"(60), "i"(60 + ST), "i"(60 + 2 * ST), "i"(60 + 3 * ST), "i"(60 + 4 * ST), "i"(60 + 5 * ST) : "s100", "s101");
}
template <int ST> DEVI void spf1x12(const void* base, unsigned soff) {
  spf1<0>(base, soff); spf1<ST>(base, soff); spf1<2 * ST>(base, soff); spf1<3 * ST>(base, soff); spf1<4 * ST>(base, soff); spf1<5 * ST>(base, soff);
  spf1<6 * ST>(base, soff); spf1<7 * ST>(base, soff); spf1<8 * ST>(base, soff); spf1<9 * ST>(base, soff); spf1<10 * ST>(base, soff); spf1<11 * ST>(base, soff);
}
DEVI int lgkm_out() { int n; asm volatile("s_getreg_b32 %0, hwreg(HW_REG_IB_STS, 8, 4)" : "=s"(n)); return n; }
DEVI int slow_res(const void* w) { return (3 - ((unsigned)(uintptr_t)w >> 7)) & 7; }

#define MTD
#define MT(i)
#define MTOUT(k, w)
#define CSP , char* ws, int l
#define CSA , ws, l
#define WNE 14
#define ANE 24
#define BNE 13
#define QNE 46
struct Plan { int n; signed char q[40], idx[40], k[40], h[40], g[40]; };
template <class O, bool PKM, bool QKM> constexpr Plan make_plan() {
  Plan p{};
  int nr = 0;
  for (int c = 0; c < O::NRO; ++c) nr += ((O::ro[c] >> 4) ? QKM : PKM) ? 4 : 2;
  int prev = 0;
  for (int c = 0; c < O::NRO; ++c) {
    const int q = O::ro[c] >> 4, i = O::ro[c] & 15;
    int av[2] = {0, 0}, k0 = -1;
    for (int m = 0; m < 2 * O::NP; ++m) {
      const int mm = (m + 2 * O::NPEND) % (2 * O::NP);
      if ((q ? O::pj[m >> 1] : O::pi[m >> 1]) == i && m < 2 * O::NPEND) av[((m >> 1) ^ m) & 1] = m;
      if ((q ? O::pj[mm >> 1] : O::pi[mm >> 1]) == i && k0 < 0) k0 = ((mm >> 1) ^ mm) & 1;
    }
    for (int kk = 0; kk < 2; ++kk)
      for (int h = 0; h < ((q ? QKM : PKM) ? 2 : 1); ++h) {
        const int k = k0 ^ kk;
        int g = p.n * O::NE / nr;
        if (g < av[k]) g = av[k];
        if (g < prev + (p.n && O::NE >= nr ? 1 : 0)) g = prev + (p.n && O::NE >= nr ? 1 : 0);
        p.q[p.n] = q; p.idx[p.n] = i; p.k[p.n] = k; p.h[p.n] = h; p.g[p.n] = g; prev = g; ++p.n;
      }
  }
  return p;
}
struct OBase { static constexpr int pidx(int i) { return i; } static constexpr int poff(int) { return 0; } static constexpr int qoff(int j) { return j * 2048; } static constexpr bool swp(int) { return false; } };
template <bool PF, int NE_> struct O44 : OBase {
  static constexpr int TP = 4, TQ = 4, NPA = 4, NP = 16, NPEND = 8, NRO = 8, NE = NE_;
  static constexpr signed char pi[16] = {2, 3, 3, 2, 2, 3, 3, 2, 0, 1, 1, 0, 0, 1, 1, 0}, pj[16] = {0, 0, 1, 1, 2, 2, 3, 3, 0, 0, 1, 1, 2, 2, 3, 3};
  static constexpr signed char ro[8] = {0, PF ? 1 : 16, PF ? 16 : 1, 17, 18, 19, 2, 3};
};
template <bool PF, int NE_> struct O42 : OBase {
  static constexpr int TP = 4, TQ = 2, NPA = 4, NP = 8, NPEND = 4, NRO = 6, NE = NE_;
  static constexpr signed char pi[8] = {2, 3, 3, 2, 0, 1, 1, 0}, pj[8] = {0, 0, 1, 1, 0, 0, 1, 1};
  static constexpr signed char ro[6] = {0, PF ? 1 : 16, PF ? 16 : 1, 17, 3, 2};
};
template <int TH> struct OQ {
  static constexpr int TP = 6, TQ = 5, NPA = 2, NP = 26, NPEND = 13, NRO = 11, NE = QNE;
  static constexpr signed char pi[26] = {3, 4, 5, 5, 4, 3, 3, 4, 5, 5, TH ? 5 : 3, 4, TH ? 3 : 4, 0, 1, 2, 2, 1, 0, 0, 1, 2, 2, TH ? 2 : 0, 1, TH ? 0 : 1};
  static constexpr signed char pj[26] = {0, 0, 0, 1, 1, 1, 2, 2, 2, TH ? 4 : 3, 3, 3, TH ? 3 : 4, 0, 0, 0, 1, 1, 1, 2, 2, 2, TH ? 4 : 3, 3, 3, TH ? 3 : 4};
  static constexpr signed char ro[11] = {0, 1, 16, 2, 17, 18, TH ? 20 : 19, TH ? 19 : 20, 3, 4, 5};
  static constexpr int pidx(int i) { return i / 3; }
  static constexpr int poff(int i) { return i % 3 * 8192; }
  static constexpr int qoff(int j) { return j == 4 ? 0 : (1 + 4 * TH + j) * 2048; }
  static constexpr bool swp(int i) { return i % 3 == 2; }
};
template <bool PKM, bool QKM, int PB, int QB, int XB, int NS, int NT, bool WISP, int BASE, int ROT, int WLD, int WSUB, int ASTR, int ASTEP>
struct Gemm {
  static constexpr int SB = PB + QB + XB, CNT = (PB + QB) / 4096 + (XB ? 1 : 0);
  static constexpr int WB = WISP ? PB : QB, AB = WISP ? QB : PB, WOFF = WISP ? 0 : PB, AOFF = WISP ? PB + XB : 0;
  static constexpr int XL = XB / 64, H = NS - 1, CA = AB / 4096 + (XB ? 1 : 0), WSTEP = 64 * WLD * 2;
  static_assert(H * CNT <= 63 && BASE + NS * SB <= 163840, "");
  struct Loader {
    static constexpr int NW = WB / 4096, NA = AB / 4096;
    const char* w; const char* ab; unsigned wv[NW], av[NA], xv, lwb, lab, lx; int rot, islot, t; bool xl;
    DEVI Loader(const void* w_, const void* ab_, int lw, int lane, int rot_ = 0)
        : w((const char*)w_ - 3072), ab((const char*)ab_ - 3072), lwb(BASE + WOFF + lw * (NW == 6 ? 2048 : WB / 4)), lab(BASE + AOFF + lw * (AB / 4)), lx(BASE + PB + lw * (XL * 16)),
          rot(rot_), islot((H + ROT) % NS), t(0), xl(lane < XL) {
      const unsigned w0 = (lane >> 3) * (WLD * 2), f0 = (lane & 7) ^ ((lane >> 3) & 2);
#pragma unroll
      for (int i = 0; i < NW; ++i) {
        const unsigned p = NW == 6 ? (i >> 1) * 8 + lw * 2 + (i & 1) : lw * NW + i;
        wv[i] = w0 + ((f0 ^ ((i & 1) << 2)) << 4) + (NW == 6 ? (i >> 1) * WSUB + (lw * 2 + (i & 1)) * (8 * WLD * 2) - (i & 1) * 1024 : (p >> 3) * WSUB + (p & 7) * (8 * WLD * 2) - i * 1024) + 3072;
        asm volatile("" : "+v"(wv[i]));
      }
      asm volatile("" : "+s"(w), "+s"(lwb));
      if (XB) asm volatile("" : "+s"(rot));
      else seta(0, 0, lw, lane);
    }
    DEVI void seta(unsigned a0, unsigned xv_, int lw, int lane) {
      xv = xv_ + 3072;
#pragma unroll
      for (int i = 0; i < NA; ++i) { const unsigned p = lw * NA + i; av[i] = (XB ? a0 + src_kc(p * 64 + lane, D) : p * 2048 + ph(lane * 16)) + 3072 - i * 1024; asm volatile("" : "+v"(av[i])); }
      asm volatile("" : "+s"(ab), "+s"(lab));
      if (XB) { asm volatile("" : "+v"(xv)); asm volatile("" : "+s"(lx)); }
    }
    DEVI int kord(int n) const { if constexpr (XB != 0) return (n + rot) & (NT - 1); else return n; }
    template <int SO> DEVI void issue_w(const char* p, unsigned so = 0) const {
      if constexpr (NW == 6) { dma_grp<2, SO>(p, wv, lwb + so); dma_grp<2, SO + 8192>(p, wv + 2, lwb + so); dma_grp<2, SO + 16384>(p, wv + 4, lwb + so); }
      else dma_grp<NW, SO>(p, wv, lwb + so);
    }
    template <int SO> DEVI void issue_a(const char* p, unsigned so = 0) const {
      if (XB) { if (xl) dma_grp<1, SO>(p, &xv, lx + so); }
      dma_grp<NA, SO>(p, av, lab + so);
    }
    template <int S0, bool W, bool A> DEVI void pre() const {
      if constexpr (S0 < H) {
        if (W) issue_w<((S0 + ROT) % NS) * SB>(w + (kord(0) + S0) * WSTEP);
        if (A) issue_a<((S0 + ROT) % NS) * SB>(ab + (kord(0) + S0) * ASTEP);
        pre<S0 + 1, W, A>();
      }
    }
    DEVI void pre_w() const { pre<0, true, false>(); sched0(); }
    template <int T> DEVI void peel() const {
      if constexpr (T < H - 1) {
        wait_vm<(H - 1 - T) * CA + T * CNT>();
        bar();
        issue_w<((T + H + ROT) % NS) * SB>(w + (T + H) * WSTEP); issue_a<((T + H + ROT) % NS) * SB>(ab + (T + H) * ASTEP);
        peel<T + 1>();
      }
    }
    DEVI void start() {
      sched0();
      bar();
      pre<0, false, true>();
      sched0();
      if constexpr (H == 2) wait_vm<CA>();
      else { peel<0>(); islot = (2 * H - 1 + ROT) % NS; t = H - 1; }
    }
    template <class HK> DEVI void run(int tend, HK side) {
#pragma unroll 1
      for (; t < tend; ++t) {
        const char* wk = w + (t + H) * WSTEP; const char* ak = ab + (t + H) * ASTEP;
        unsigned lw_ = lwb + islot * SB, la_ = lab + islot * SB;
        islot = islot + 1 == NS ? 0 : islot + 1;
        asm volatile("" : "+s"(wk), "+s"(ak), "+s"(lw_), "+s"(la_));
        wait_vm<(H - 1) * CNT>();
        bar();
        dma_grp<NW, 0>(wk, wv, lw_);
        side(t);
        dma_grp<NA, 0>(ak, av, la_);
        sched0();
      }
    }
    template <int T, int X, class A, class B, class C> DEVI void step(A pre_, B side, C post) const {
      const int k = kord(T + H);
      const char* wk = w + k * WSTEP; const char* ak = ab + k * ASTEP;
      asm volatile("" : "+s"(wk), "+s"(ak));
      wait_vm<(H - 1) * CNT + X>();
      bar();
      pre_(BASE + ((T + ROT) % NS) * SB);
      issue_w<((T + H + ROT) % NS) * SB>(wk);
      sched0();
      side();
      sched0();
      issue_a<((T + H + ROT) % NS) * SB>(ak);
      post();
      sched0();
    }
    template <int I> DEVI void tail() const { if constexpr (I >= 0) { sched0(); wait_vm<I * CNT>(); bar(); tail<I - 1>(); } }
  };
  static constexpr int NWIN = (NS * SB + 65535) / 65536;
  template <class O> struct Comp {
    static constexpr int TP = O::TP, TQ = O::TQ, NM = 2 * O::NP, NPA = PKM ? O::NPA : 2, NQA = QKM ? TQ : 2;
    static constexpr Plan PL = make_plan<O, PKM, QKM>();
    unsigned prd[NWIN][NPA], qrd[NWIN][NQA];
    bf16x8 fp[TP][2], fq[TQ][2]; bf16x4 lo;
    DEVI void setwin() {
#pragma unroll
      for (int w = 1; w < NWIN; ++w) {
#pragma unroll
        for (int i = 0; i < NPA; ++i) { prd[w][i] = prd[0][i] + w * 65536; asm volatile("" : "+v"(prd[w][i])); }
#pragma unroll
        for (int i = 0; i < NQA; ++i) { qrd[w][i] = qrd[0][i] + w * 65536; asm volatile("" : "+v"(qrd[w][i])); }
      }
#pragma unroll
      for (int i = 0; i < NPA; ++i) asm volatile("" : "+v"(prd[0][i]));
#pragma unroll
      for (int i = 0; i < NQA; ++i) asm volatile("" : "+v"(qrd[0][i]));
    }
    DEVI void setup(int wp, int wq, int lane) {
      const unsigned b = BASE;
      if (PKM) {
#pragma unroll
        for (int i = 0; i < TP; ++i) prd[0][i] = b + rd_km(lane, wp * TP + i);
      } else { prd[0][0] = b + rd_kc(lane, 0) + wp * TP * 2048; prd[0][1] = b + rd_kc(lane, 1) + wp * TP * 2048; }
      if (QKM) {
#pragma unroll
        for (int j = 0; j < TQ; ++j) qrd[0][j] = b + rd_km(lane, wq * TQ + j);
      } else { qrd[0][0] = b + rd_kc(lane, 0) + wq * TQ * 2048; qrd[0][1] = b + rd_kc(lane, 1) + wq * TQ * 2048; }
    }
    template <int C, int N> DEVI static unsigned wa(const unsigned (&b)[NWIN][N], int i) { return b[C >> 16][i] + (C & 0xffff); }
    template <int SLOT, int R> DEVI void rd() {
      constexpr int q = PL.q[R], i = PL.idx[R], k = PL.k[R], h = PL.h[R];
      bf16x8& f = q ? fq[q ? i : 0][k] : fp[q ? 0 : i][k];
      if constexpr (q ? QKM : PKM) {
        unsigned a;
        if constexpr (q) a = wa<SLOT * SB + PB + k * 4096 + h * 512, NQA>(qrd, i); else a = wa<SLOT * SB + O::poff(i) + k * 4096 + h * 512, NPA>(prd, O::pidx(i));
        const bf16x4 x = lds_tr4(a);
        if constexpr (h) f = __builtin_shufflevector(lo, x, 0, 1, 2, 3, 4, 5, 6, 7); else lo = x;
      } else if constexpr (q) f = lds_b128(wa<SLOT * SB + PB + O::qoff(i), 2>(qrd, k));
      else f = lds_b128(wa<SLOT * SB + i * 2048, 2>(prd, k));
    }
    template <int SLOT, int M, int R> DEVI void rds() { if constexpr (R < PL.n) { if constexpr (PL.g[R] == M) rd<SLOT, R>(); if constexpr (PL.g[R] <= M) rds<SLOT, M, R + 1>(); } }
    template <int M, bool IP = false> DEVI void mm(f32x4 (&acc)[TP][TQ]) {
      constexpr int i = O::pi[M >> 1], j = O::pj[M >> 1], k = ((M >> 1) ^ M) & 1;
      if constexpr (IP) { if constexpr (O::swp(i)) mfma_ip(acc[i][j], fq[j][k], fp[i][k]); else mfma_ip(acc[i][j], fp[i][k], fq[j][k]); }
      else if constexpr (O::swp(i)) acc[i][j] = __builtin_amdgcn_mfma_f32_16x16x32_bf16(fq[j][k], fp[i][k], acc[i][j], 0, 0, 0);
      else acc[i][j] = __builtin_amdgcn_mfma_f32_16x16x32_bf16(fp[i][k], fq[j][k], acc[i][j], 0, 0, 0);
    }
    template <int A, int B> DEVI void pend(f32x4 (&acc)[TP][TQ]) { if constexpr (A < B) { mm<2 * A>(acc); mm<2 * A + 1>(acc); pend<A + 1, B>(acc); } }
    template <int SLOT, bool FIRST, int M> DEVI void body(f32x4 (&acc)[TP][TQ]) {
      if constexpr (M < NM) {
        if constexpr (!FIRST || M >= 2 * O::NPEND) { mm<M, true>(acc); sched0(); }
        rds<SLOT, M, 0>();
        sched0();
        body<SLOT, FIRST, M + 1>(acc);
      }
    }
    template <int SLOT, bool FIRST> DEVI void step(f32x4 (&acc)[TP][TQ]) { wait_lgkm0(); bar(); sched0(); body<SLOT, FIRST, 0>(acc); }
    template <int I, int N, int T0, class MID> DEVI void steps(f32x4 (&acc)[TP][TQ], MID mid) {
      if constexpr (I < N) { mid(I); step<(I + T0 + ROT) % NS, false>(acc); steps<I + 1, N, T0>(acc, mid); }
    }
    template <bool CONT> DEVI void prep(f32x4 (&acc)[TP][TQ]) {
      setwin();
#pragma unroll
      for (int p = 0; p < O::NP; ++p) asm volatile("" : "+v"(acc[O::pi[p]][O::pj[p]]));
      if constexpr (!CONT && NT % NS == 0) {
#pragma unroll
        for (int p = 0; p < 2 * O::NPEND; ++p) {
          fp[O::pi[p >> 1]][p & 1] = (bf16x8){0, 0, 0, 0, 0, 0, 0, 0}; fq[O::pj[p >> 1]][p & 1] = (bf16x8){0, 0, 0, 0, 0, 0, 0, 0};
        }
#pragma unroll
        for (int p = 0; p < 2 * O::NPEND; ++p) asm volatile("" : "+v"(fp[O::pi[p >> 1]][p & 1]), "+v"(fq[O::pj[p >> 1]][p & 1]));
      }
    }
    template <bool CONT, class MID> DEVI void run_steps(f32x4 (&acc)[TP][TQ], MID mid CSP) {
      constexpr int T0 = CONT || NT % NS == 0 ? 0 : 1;
      if constexpr (T0) step<ROT % NS, true>(acc);
#pragma unroll 1
      for (int it = 0; it < (NT - T0) / NS; ++it) {
        { constexpr int NI = (NT - T0) / NS, kk = 10 + (XB ? 0 : NT == 16 ? 1 : NT == 32 ? 2 : CONT ? 4 : 3);
          if ((NI <= 8 || !(it & 1)) && l == CSTAMP) ST(kk, NI > 8 ? it >> 1 : it); }
        steps<0, NS, T0>(acc, [&](int i) { mid(it - (NT - T0) / NS, i); });
      }
      steps<0, (NT - T0) % NS, T0>(acc, [](int) {});
      wait_lgkm0();
      mfma_gap();
    }
  };
};

DEVI f32x4 gelu_tanh(f32x4 u) {
  f32x4 a = u * (-2.302208198f + -0.1029432397f * (u * u));
#pragma unroll
  for (int e = 0; e < 4; ++e) a[e] = __builtin_amdgcn_exp2f(a[e]);
  a += 1.f;
#pragma unroll
  for (int e = 0; e < 4; ++e) a[e] = __builtin_amdgcn_rcpf(a[e]);
  return u * a;
}

template <int tilesP, int tilesQ, int gp, int gq> DEVI void tile_map(unsigned bid, int& tp, int& tq) {
  const unsigned xcd = bid & 7, idx = bid >> 3;
  constexpr unsigned gP = tilesP / gp, gQ = tilesQ / gq;
  static_assert(gP * gQ == 8, "");
  tp = (xcd % gP) * gp + idx % gp;
  tq = (xcd / gP) * gq + idx / gp;
}

template <int V> struct IC { static constexpr int value = V; };
template <int A, int B, class FN> DEVI void for_c(FN f) { if constexpr (A < B) { f(IC<A>{}); for_c<A + 1, B>(f); } }
DEVI float sel(bool c, float a, float b) { return c ? a : b; }
DEVI u32x2 pk4(const f32x4& v) { return (u32x2){pk2(v[0], v[1]), pk2(v[2], v[3])}; }
DEVI u32x4 cat4(u32x2 a, u32x2 b) { return (u32x4){a.x, a.y, b.x, b.y}; }
DEVI bf16x8 cat(u32x2 a, u32x2 b) { return __builtin_bit_cast(bf16x8, cat4(a, b)); }
DEVI f32x4 mfma(const bf16x8& a, const bf16x8& b, const f32x4& c) { return __builtin_amdgcn_mfma_f32_16x16x32_bf16(a, b, c, 0, 0, 0); }
DEVI u32x4 pack_pair(const f32x4& a, const f32x4& b) {
  const unsigned ax = pk2(a[0], a[1]), ay = pk2(a[2], a[3]), bx = pk2(b[0], b[1]), by = pk2(b[2], b[3]);
  auto rx = __builtin_amdgcn_permlane16_swap(ax, bx, false, false);
  auto ry = __builtin_amdgcn_permlane16_swap(ay, by, false, false);
  const unsigned x0 = rx[0], x1 = rx[1], y0 = ry[0], y1 = ry[1];
  return (u32x4){x0, y0, x1, y1};
}
DEVI int pair_chunk(int fq) { return ((fq & 1) << 1) | (fq >> 1); }
DEVI void sel_frags(bool paired, int fr, int fq, bf16x8 (&s)[2]) {
  const unsigned one = fr & 1 ? 0x3F800000u : 0x3F80u;
#pragma unroll
  for (int i = 0; i < 2; ++i) {
    const bool on = paired ? (fq & 1) == i && (fr >> 3) == (fq >> 1) : (fr >> 2) == fq;
    const int d = paired ? (fr & 7) >> 1 : i * 2 + ((fr >> 1) & 1);
    u32x4 v;
#pragma unroll
    for (int e = 0; e < 4; ++e) v[e] = on && d == e ? one : 0u;
    s[i] = __builtin_bit_cast(bf16x8, v);
  }
}
template <int TQ> DEVI void tile_add(f32x4 (&acc)[4][TQ], const bf16x8 (&s)[2], unsigned a) {
  bf16x8 t[4];
#pragma unroll
  for (int p = 0; p < 4; ++p) t[p] = lds_b128(a + p * 1024);
#pragma unroll
  for (int p = 0; p < 4; ++p)
#pragma unroll
    for (int i = 0; i < 2; ++i) acc[(p & 1) * 2 + i][p >> 1] = mfma(s[i], t[p], acc[(p & 1) * 2 + i][p >> 1]);
}
template <int C> DEVI float dppf(float v) { return __int_as_float(__builtin_amdgcn_update_dpp(0, __float_as_int(v), C, 0xf, 0xf, false)); }
DEVI float red8(float v) { v += dppf<0xB1>(v); v += dppf<0x4E>(v); v += dppf<0x141>(v); return v; }
DEVI float red4(float v) { v += dppf<0xB1>(v); v += dppf<0x4E>(v); return v; }

constexpr unsigned SENT = 0xBF800000u;
constexpr int SPIN_MAX = 1 << 21;

constexpr unsigned MB = 1u << 20;
constexpr unsigned WS_O = 0, WS_H = 8 * MB, WS_U = 16 * MB, WS_X2 = 32 * MB, WS_XP = 40 * MB  ;
constexpr unsigned WS_ST = 120 * MB, ST_CS = 2 * S * 64, WS_XCH = 122 * MB  , WS_EPOCH = 123 * MB, WS_ABORT = 123 * MB + 256  ;
DEVI unsigned tag2(unsigned t) { return t * 0x9E3779B1u ^ 0x5BD1E995u; }
constexpr int MEDGE = 256 * 64;
constexpr unsigned WS_MAIL = 124 * MB;

#define NL 2
#define QBR 1
#define FLS 3
#define NPL 6
#define SW0 2
#define SW1 6
#define XD 16
#define XS 4
#define JA 22
#define PS 3
#define TBA 3
#define TC1 12
#define NW1 9
#define PWL 1
#define ET 20
#define SPW 1
#define PA_1 3
#define PA_2 3
#define ATT4 1
constexpr int L_X = 0, L_TAB = 16384, L_CS = L_TAB + 1152, L_TAG = L_CS + 768, L_BIAS = L_TAG + 128, L_SCR = L_BIAS + 512  , L_QKV = 20480;
static_assert(L_SCR + 1536 <= L_QKV, "");
typedef Gemm<true, false, 24576, 16384, 2048, 3, D / 64, true, 32768, 1, 3 * D, 2 * D, 32 * D * 2, 128> GQ;
typedef Gemm<true, false, 16384, 8192, 0, 4, D / 64, true, 65536, 1, D, 128, 8192, 16384> GW;
typedef Gemm<true, true, 16384, 16384, 0, 3, S / 64, true, 65536, 0, F, 128, 8192, 32768> G1;
typedef Gemm<false, true, 16384, 16384, 0, 3, JA, false, 65536, (3 - JA % 3) % 3, S, 1024, 8192, 32768> G2A;
typedef Gemm<false, true, 16384, 8192, 0, 4, F / 64 - 2 * JA, false, 65536, 0, S, 128, 8192, 32768> G2;

DEVI unsigned get_tag() { return __builtin_amdgcn_readfirstlane(*(volatile LAS unsigned*)(uintptr_t)L_TAG); }
DEVI bool spin_fail(int spin, char* ws) {
  if ((spin & 255) != 255) return false;
  gu32* ab = (gu32*)(ws + WS_ABORT);
  const unsigned tag = get_tag();
  if (spin >= SPIN_MAX) { __hip_atomic_store(ab, tag, RLX_AGENT); return true; }
  return __builtin_amdgcn_readfirstlane(__hip_atomic_load(ab, RLX_AGENT)) == tag;
}
DEVI void sync_post(char* ws, int edge, int post, int wid) {
  unsigned o = post >= 0 && wid == 0 ? post * 4u : MB, one = 1u;
  const rsrc_t r = mkrsrc(ws + WS_MAIL + edge * (MEDGE * 4), MEDGE * 4);
  asm volatile("s_waitcnt vmcnt(0) lgkmcnt(0)" : "+v"(o), "+v"(one) : "s"(r) : "memory");
  sched0();
  bar();
  ST(edge + 1, 6);
  if (wid == 0) __builtin_amdgcn_raw_buffer_store_b32(one, r, o, 0, 16);
  sched0();
}
DEVI void sync_wait(char* ws, int edge, int line, int lane, unsigned want, int wid) {
  sched0();
  if (wid == 3) {
    gu32* pw = (gu32*)(ws + WS_MAIL) + edge * MEDGE + line * 64 + (lane & 31);
    __builtin_amdgcn_s_sleep(SW0);
    for (int spin = 0;; ++spin) {
      const unsigned v = __hip_atomic_load(pw, RLX_AGENT);
      if (__all(v >= want) || spin_fail(spin, ws)) break;
      __builtin_amdgcn_s_sleep(SW1);
    }
  }
  bar();
  ST(edge + 1, 4);
  ST(edge + 2, 0);
}
DEVI unsigned halo_off(int s0, int lw, int lane) {
  const int c = lw * 32 + (lane & 31), row = c >> 3, ch = (c & 7) ^ ((row >> 1) & 7), tok = s0 - 16 + row;
  return (unsigned)((tok < 0 ? 0 : tok) * D * 2 + ch * 16);
}
DEVI int qkv_rot(int bid) { return (bid & 3) * 4 + (bid & 4) * 2; }
DEVI unsigned x_off(int tp, int tq, int w, int fr, int fq) { return (unsigned)(((tq * 64 + (w >> 1) * 32 + fr) * D + tp * 128 + (w & 1) * 64 + pair_chunk(fq) * 8) * 2); }

DEVI void pfb_q(const u16* w, int bid, int j, int n) {
  const int r = slow_res(w);
  if ((r >> 2) == (bid & 1)) {
    const unsigned o = (unsigned)(((bid & 2) * 4 + r) * 128 + (j >> 1) * (3 * D * 2) + (j & 1) * 64 + ((n + qkv_rot(bid)) & 15) * GQ::WSTEP);
    spf1<0>(w, o); spf1<2048>(w, o); spf1<4096>(w, o);
  }
}
constexpr int PFR = NPL * 128 * 1024, PF_END = TBA * 256 + NW1 * PFR / (F * 2) / 8 * 8;
static_assert(TBA * 256 * F * 2 + NW1 * PFR <= S * F * 2 && 3 * PFR >= D * D * 2, "");
struct Pfc {
  const char* p; const char* q; int pc;
  DEVI Pfc(const u16* wo, const u16* w1, unsigned g) : p((const char*)wo + g * (NPL * 128)), q((const char*)w1 + TBA * 256 * F * 2 + g * (NPL * 128)), pc(0) {}
  DEVI void go() {
    unsigned t, u;
    asm volatile("s_getreg_b32 %2, hwreg(HW_REG_IB_STS, 8, 4)\n\ts_cmp_lt_u32 %1, %5\n\ts_cselect_b32 %3, 0, 1\n\ts_or_b32 %2, %2, %3\n\ts_cbranch_scc1 1f\n\t"
                 "s_load_dwordx2 s[100:101], %0, 60\n\ts_load_dwordx2 s[100:101], %0, 188\n\ts_load_dwordx2 s[100:101], %0, 316\n\t"
                 "s_load_dwordx2 s[100:101], %0, 444\n\ts_load_dwordx2 s[100:101], %0, 572\n\ts_load_dwordx2 s[100:101], %0, 700\n\t"
                 ".if %8 == 7\n\ts_load_dwordx2 s[100:101], %0, 828\n\t.endif\n\t"
                 "s_mov_b32 %2, %7\n\ts_cmp_lt_u32 %1, 2\n\ts_cselect_b32 %2, %6, %2\n\ts_mov_b64 s[40:41], %0\n\ts_add_u32 s40, s40, %2\n\ts_addc_u32 s41, s41, 0\n\t"
                 "s_cmp_eq_u32 %1, 2\n\ts_cselect_b64 %0, %4, s[40:41]\n\ts_add_u32 %1, %1, 1\n1:"
                 : "+s"(p), "+s"(pc), "=&s"(t), "=&s"(u) : "s"(q), "i"(3 + NW1), "i"((D * D * 2 - PFR) / 2), "i"(PFR), "i"(NPL) : "s40", "s41", "s100", "s101", "scc");
  }
};
DEVI void spare_touch(const u16* w1, const u16* w2, int bid, int wid, int lane) {
  const int j = ((bid >> 3) - 28) * 8 + wid, fr = lane & 15, fq = lane >> 4;
  const unsigned scr = L_QKV + wid * 256;
#pragma unroll 1
  for (int r = PF_END + (j * 2 + ((bid >> 2) & 1)) * 4; r < S; r += 256) { if (fr < 14) touch4(w1 + (size_t)r * F + (bid & 3) * 896, fq * (F * 2) + fr * 128, scr); wait_vm<SPW>(); }
#pragma unroll 1
  for (int r = (j * 2 + (bid & 1)) * 4; r < JA * 64; r += 256) { touch4(w2 + (size_t)(r + (bid & 2) * JA * 32) * S + (bid & 4) * 256, fq * (S * 2) + fr * 128, scr); wait_vm<SPW>(); }
#pragma unroll 1
  for (int r = JA * 128 + ((31 - j) * 2 + (bid & 1)) * 8; r < F; r += 512) { touch4(w2 + (size_t)r * S + ((bid & 7) >> 1) * 512, (lane >> 3) * (S * 2) + (lane & 7) * 128, scr); wait_vm<SPW>(); }
}

template <int TH> DEVI void qkv_attn(char* ws, int l, int h, int lane, int hp, int sb, int cw) {
  asm volatile("; half %1" : "+v"(lane) : "i"(TH));
  const int fr = lane & 15, fq = lane >> 4;
  GQ::Comp<OQ<TH>> g;
#pragma unroll
  for (int c = 0; c < 2; ++c) { g.prd[0][c] = 32768 + rd_km(lane, h * 2 + c); g.qrd[0][c] = 32768 + rd_kc(lane, c); }
  f32x4 acc[6][5];
#pragma unroll
  for (int i = 0; i < 6; ++i)
#pragma unroll
    for (int j = 0; j < 5; ++j) acc[i][j] = (f32x4){0.f, 0.f, 0.f, 0.f};
  const int par = fr & 1, da = fr - 4 * fq - par;
  const bool ca = da >= 0, cb = da >= 2;
  const unsigned tm = L_TAB + (16 + 64 * TH) * 4, tc = L_CS + h * 128;
  unsigned am = tm + fr * 4 - 320, a4 = tm + fq * 16 - 320, aa = tm + 576 + (4 * fq + par - (ca ? 0 : 16)) * 4, ab = tm + 576 + (4 * fq + par + 2 - (cb ? 0 : 16)) * 4;
  unsigned c4 = tc + fq * 16, c1 = tc + 512 + fr * 4, xb = L_QKV + h * 3072 + lane * 16, hm = par ? 0xffff0000u : 0xffffu, m0 = ca ? 0u : hm, m1 = cb ? 0u : hm;
  unsigned oo = ph((unsigned)((((sb * 2 + TH) * 16 + hp) << 13) + fr * 128 + (((h * 4 + pair_chunk(fq)) ^ (fr >> 1)) << 4)));
  asm volatile("" : "+v"(am), "+v"(a4), "+v"(aa), "+v"(ab), "+v"(c4), "+v"(c1), "+v"(xb), "+v"(hm), "+v"(m0), "+v"(m1), "+v"(oo));
  g.template prep<false>(acc);
  if (l) sync_wait(ws, 3, sb * 16 + hp, lane, lane < 24 && (lane >= 8 || sb > 0) ? 1u : 0u, TH * 2 + h);
  g.template run_steps<false>(acc, [&](int it, int i) { if (i == 0 && cw >= 0 && it == -2) code_warm(cw, lane, L_SCR + (TH * 2 + h) * 256); } CSA);
  const rsrc_t ro = mkrsrc(ws + WS_O, S * D * 4);
  MTD;
  MT(0);
  if (!PWL || l == 0) bar();
  ST(1 + l * 4, 2);
  float mt[5], cv[2], rq[4], ra[4], rb[4]; f32x4 m4[5], cq[2], ck[2];
#pragma unroll
  for (int c = 0; c < 2; ++c) {
    ck[c] = *(const LAS f32x4*)(uintptr_t)(c4 + 256 + c * 64); cv[c] = *(const LAS float*)(uintptr_t)(c1 + c * 64);
    cq[c] = *(const LAS f32x4*)(uintptr_t)(c4 + c * 64);
  }
#pragma unroll
  for (int j = 4; j >= 0; --j) {
    const int o = 320 + (j == 4 ? -64 - 256 * TH : j * 64);
    if (j < 4 || !TH) mt[j] = *(const LAS float*)(uintptr_t)(am + o);
    if (j < 4 || TH) m4[j] = *(const LAS f32x4*)(uintptr_t)(a4 + o);
  }
#pragma unroll
  for (int j = 0; j < 4; ++j) { rq[j] = *(const LAS float*)(uintptr_t)(am + 896 + j * 64); ra[j] = *(const LAS float*)(uintptr_t)(aa + j * 64); rb[j] = *(const LAS float*)(uintptr_t)(ab + j * 64); }
  u32x2 kp[2][5], qp[2][4], vp[2][5];
  auto fk = [&](int j) { kp[0][(j + 1) % 5] = pk4(acc[1][j] + ck[0] * mt[j]); kp[1][(j + 1) % 5] = pk4(acc[4][j] + ck[1] * mt[j]); };
  auto fv = [&](int j) { vp[0][(j + 1) % 5] = pk4(acc[2][j] + m4[j] * cv[0]); vp[1][(j + 1) % 5] = pk4(acc[5][j] + m4[j] * cv[1]); };
  auto fqq = [&](int j) { qp[0][j] = pk4(acc[0][j] + cq[0] * mt[j]); qp[1][j] = pk4(acc[3][j] + cq[1] * mt[j]); };
  g.template pend<9, 13>(acc);
#pragma unroll
  for (int i = 0; i < 8; ++i) { __builtin_amdgcn_sched_group_barrier(0x100, 3, 0); __builtin_amdgcn_sched_group_barrier(0x8, 1, 0); }
  sched0();
  MT(2);
  volatile LAS unsigned* xf = (volatile LAS unsigned*)(uintptr_t)(L_TAG + 16 + (h * 2 + TH) * 4);
  g.template pend<6, 9>(acc);
  if (TH) { fv(4); *(volatile LAS bf16x8*)(uintptr_t)(xb + 2048) = cat(vp[0][0], vp[1][0]); *xf = l + 1; fk(3); }
  else { fk(3); fv(3); *(volatile LAS bf16x8*)(uintptr_t)xb = cat(kp[0][4], kp[1][4]); *(volatile LAS bf16x8*)(uintptr_t)(xb + 1024) = cat(vp[0][4], vp[1][4]); *xf = l + 1; }
#pragma unroll
  for (int i = 0; i < 6; ++i) { __builtin_amdgcn_sched_group_barrier(0x8, 1, 0); __builtin_amdgcn_sched_group_barrier(0x2, 3, 0); }
  sched0();
  g.template pend<3, 6>(acc); g.template pend<0, 3>(acc);
  fqq(3); fk(2); fqq(2); fk(1);
#pragma unroll
  for (int i = 0; i < 6; ++i) { __builtin_amdgcn_sched_group_barrier(0x8, 1, 0); __builtin_amdgcn_sched_group_barrier(0x2, 2, 0); __builtin_amdgcn_sched_group_barrier(0x8, 1, 0); __builtin_amdgcn_sched_group_barrier(0x2, 3, 0); }
  sched0();
  const f32x4 z = {0.f, 0.f, 0.f, 0.f};
  f32x4 sc[4][2]; float a[4], b[4], m[4];
  auto scores = [&](int j) {
#pragma unroll
    for (int u = 1; u >= 0; --u) sc[j][u] = mfma(cat(kp[0][j + u], kp[1][j + u]), cat(qp[0][j], qp[1][j]), z);
  };
  auto pick = [&](int j) {
    const float r = rq[j] * (0.17677669529663687f * 1.4426950408889634f);
    const f32x4 s0 = sc[j][0], s1 = sc[j][1];
    a[j] = sel(par, sel(ca, s1[1], s0[1]), sel(ca, s1[0], s0[0])) * (r * ra[j]);
    b[j] = sel(par, sel(cb, s1[3], s0[3]), sel(cb, s1[2], s0[2])) * (r * rb[j]);
    if (!TH && j == 0) { a[j] = sel(ca || sb, a[j], -1e9f * 1.4426950408889634f); b[j] = sel(cb || sb, b[j], -1e9f * 1.4426950408889634f); }
    m[j] = __builtin_fmaxf(a[j], b[j]);
  };
  auto expo = [&](int j) { a[j] = __builtin_amdgcn_exp2f(a[j] - m[j]); b[j] = __builtin_amdgcn_exp2f(b[j] - m[j]); m[j] = a[j] + b[j]; };
  auto pv = [&](int j) {
    const unsigned pa = pk2(a[j] * ra[j], a[j] * ra[j]), pb = pk2(b[j] * rb[j], b[j] * rb[j]);
    const bf16x8 pf = __builtin_bit_cast(bf16x8, (u32x4){pa & m0, pb & m1, pa & (m0 ^ hm), pb & (m1 ^ hm)});
#pragma unroll
    for (int c = 0; c < 2; ++c) sc[j][c] = mfma(cat(vp[c][j], vp[c][j + 1]), pf, z);
  };
  auto outp = [&](int j) { st16(ro, oo + j * 4096, pack_pair(sc[j][0] * m[j], sc[j][1] * m[j])); };
  auto soft2 = [&](int j) {
    float u, w;
    pick(j); pick(j + 1);
    swp32(m[j], m[j + 1]); u = vmax(m[j], m[j + 1]); xch16(u, u, w); u = vmax(u, w); xch32(u, m[j], m[j + 1]);
    expo(j); expo(j + 1);
    pv(j + 1); pv(j);
    swp32(m[j], m[j + 1]); u = m[j] + m[j + 1]; xch16(u, u, w); u = __builtin_amdgcn_rcpf(u + w); xch32(u, m[j], m[j + 1]);
    outp(j + 1); outp(j);
  };
  unsigned f; u32x4 xv, xk;
  auto xrd = [&]() {
    f = xf[1 - 2 * TH]; xv = *(volatile LAS u32x4*)(uintptr_t)(xb + (TH ? 1024 : 2048));
    if (TH) xk = *(volatile LAS u32x4*)(uintptr_t)xb;
  };
  auto xget = [&]() {
    sched0();
    while (__builtin_expect(__builtin_amdgcn_readfirstlane(f) != l + 1, 0)) xrd();
    vp[0][0] = (u32x2){xv[0], xv[1]}; vp[1][0] = (u32x2){xv[2], xv[3]};
    if (TH) { kp[0][0] = (u32x2){xk[0], xk[1]}; kp[1][0] = (u32x2){xk[2], xk[3]}; }
  };
  MT(3);
  if (ATT4) {
    scores(3); scores(2);
    fqq(1);
    xrd();
    sched0();
    fk(0); fqq(0);
    if (TH) fv(3); else fk(4);
    scores(1);
    fv(2); fv(1); fv(0);
    pick(3); pick(2);
#pragma unroll
    for (int c = 0; c < 2; ++c) asm volatile("" : "+v"(qp[c][0]), "+v"(kp[c][1]), "+v"(vp[c][1]), "+v"(vp[c][2]), "+v"(vp[c][3]), "+v"(vp[c][4]), "+v"(sc[1][c]), "+v"(a[2 + c]), "+v"(b[2 + c]), "+v"(m[2 + c]));
    if (!TH) asm volatile("" : "+v"(kp[0][0]), "+v"(kp[1][0]));
    MT(4);
    xget();
    scores(0);
    pick(1); pick(0);
    float u, w;
    swp32(m[0], m[2]); swp32(m[1], m[3]); u = m[0]; w = m[1];
    asm volatile("v_max_f32 %0, %0, %2\n\tv_max_f32 %1, %1, %3\n\ts_nop 1" : "+v"(u), "+v"(w) : "v"(m[2]), "v"(m[3]));
    swp16(u, w); u = vmax(u, w);
    xch16(u, u, w); xch32(u, m[0], m[2]); xch32(w, m[1], m[3]);
    MT(5);
    expo(3); expo(2); expo(1); expo(0);
    pv(3); pv(2); pv(1); pv(0);
    swp32(m[0], m[2]); swp32(m[1], m[3]); u = m[0] + m[2]; w = m[1] + m[3];
    swp16(u, w); u = __builtin_amdgcn_rcpf(u + w);
    xch16(u, u, w); xch32(u, m[0], m[2]); xch32(w, m[1], m[3]);
    MT(6);
    outp(3); outp(2); outp(1); outp(0);
  } else {
    scores(3); scores(2);
    sched0();
    if (TH) fv(3); else fk(4);
    fv(2);
    xrd();
    sched0();
    MT(4);
    fv(1); fqq(1); fk(0); fqq(0);
    soft2(2);
    fv(0);
#pragma unroll
    for (int c = 0; c < 2; ++c) asm volatile("" : "+v"(qp[c][0]), "+v"(qp[c][1]), "+v"(kp[c][1]), "+v"(vp[c][1]));
    MT(5);
    xget();
    MT(6);
    scores(1); scores(0);
    soft2(0);
  }
  MT(7);
  MTOUT(10 + TH, TH * 2);
}

extern "C" __global__ __launch_bounds__(512, 2) void mega_kernel(const u16* x0, const u16* wqkv, const u16* wo, const u16* w1, const u16* w2, const u16* b1, const u16* b2, u16* out, char* ws) {
  const int wid0 = __builtin_amdgcn_readfirstlane(threadIdx.x >> 6);
  ST(0, 7);
  STL(0, 6);
  asm volatile("s_getpc_b64 s[40:41]\n\ts_andn2_b32 s40, s40, 63\n\ts_load_dwordx2 s[100:101], s[40:41], %0 offset:124\n\ts_load_dwordx2 s[100:101], s[40:41], %0 offset:252"
               :: "s"(__builtin_amdgcn_readfirstlane(threadIdx.x) << 2) : "s40", "s41", "s100", "s101", "scc");
  sched0();
#define PHASE_VARS \
      int tid = threadIdx.x, bid = blockIdx.x, wid = wid0; \
      asm volatile("" : "+v"(tid)); asm volatile("" : "+s"(bid), "+s"(wid), "+s"(ws)); \
      const int lw = wid - 4, lt = tid - 256, lane = tid & 63, fr = lane & 15, fq = lane >> 4, wp = wid & 1, wq = wid >> 1; \
      const unsigned j = (bid >> 3) * 4 + lw; \
      int hp, sb; tile_map<16, 16, 4, 8>(bid, hp, sb);            \
      int tp, tq; tile_map<8, 32, 4, 8>(bid, tp, tq);             \
      int fp, fq1; tile_map<28, 8, 7, 4>(bid, fp, fq1);           \
      const int cw = bid < 16 ? wid + 4 * (bid >> 3) : -1; \
      (void)cw; (void)j; (void)lt; (void)wp; (void)wq; (void)lw; (void)fr; (void)fq; (void)hp; (void)sb; (void)tp; (void)tq; (void)fp; (void)fq1;
#define LOADER_VARS PHASE_VARS asm volatile("" : "+s"(wqkv), "+s"(wo), "+s"(w1), "+s"(w2));

  if (__builtin_expect(wid0 >= 4, 1)) {
    const u16* xin = x0;
    Pfc pf(wo, w1, blockIdx.x * 4 + wid0 - 4);
#pragma unroll 1
    for (int l = 0; l < NL; ++l) {
      {
        LOADER_VARS
        const int s0 = sb * 128;
        GQ::Loader L(wqkv + hp * 64, xin, lw, lane, qkv_rot(bid));
        L.issue_w<GQ::SB>(L.w + L.kord(0) * GQ::WSTEP);
        sched0();
        L.seta((unsigned)(s0 * D * 2), halo_off(s0, lw, lane), lw, lane);
        if (l == 0) { L.issue_a<GQ::SB>(L.ab + L.kord(0) * 128); L.pre<1, true, true>(); STL(0, 1); }
        else {
          L.pre<1, true, false>();
          if (QBR == 0) bar();
#pragma unroll
          for (int n = 2; n < 7; ++n) pfb_q(wqkv, bid, j, n);
          if (QBR == 1) bar();
        }
        sched0();
        const rsrc_t rs = mkrsrc(ws + WS_ST, MB);
        f32x4 c = {0.f, 0.f, 0.f, 0.f}; f32x2 s1 = {0.f, 0.f}, s2 = {0.f, 0.f}, o = {0.f, 0.f}; float pv = 0.f;
        bf16x8 ones = {0x3F80, 0x3F80, 0x3F80, 0x3F80, 0x3F80, 0x3F80, 0x3F80, 0x3F80};
        const int ct = lw & 1 ? 8 + (sb & 3) : sb & 7; const bool cs = l == 0 && lw >= 2;
        int tpf = (slow_res(wqkv) >> 2) != (bid & 1) ? 0 : lw < 2 ? 10 : 9;
        unsigned ra = GQ::AOFF + ((hp & 3) * 32 + lw * 8) * 128 + lane * 16, rb = rd_km(lane, ct);
        unsigned so = lane & 7 ? MB : ph((unsigned)(((l * S + s0 + (hp & 3) * 32 + lw * 8 + (lane >> 3)) * 4 + (hp >> 2)) * 16));
        unsigned co = fr >= 2 || !cs ? MB : ph((unsigned)(ST_CS + ((hp * 96 + ct * 8 + fq * 2 + fr) * 2 + (sb >> 3)) * 16));
        unsigned to[3], xo = x_off(tp, tq, lw, fr, fq), q2 = ((bid & 2) * 4 + slow_res(wqkv)) * 128 + ((bid >> 3) * 2 + (lw & 1)) * (3 * D * 2);
#pragma unroll
        for (int i = 0; i < 3; ++i) {
          const int r = s0 - 16 + (i == 2 && lw ? 64 : i * 64) + (lt >> 2);
          to[i] = ph((unsigned)(((l * S + (r < 0 ? 0 : r)) * 4 + (lt & 3)) * 16));
        }
        unsigned ta = (lt & 3) < (lw ? 2 : 3) ? L_TAB + ((lt & 3) * 64 + (lt >> 2)) * 4 : L_SCR, ts = ph((unsigned)(ST_CS + (hp * 192 + (lw ? lt - 64 : lt)) * 16));
        asm volatile("" : "+v"(ra), "+v"(rb), "+v"(so), "+v"(co), "+v"(to[0]), "+v"(to[1]), "+v"(to[2]), "+v"(xo), "+v"(ta), "+v"(ts), "+v"(c), "+v"(s1), "+v"(s2), "+v"(ones), "+v"(o));
        asm volatile("" : "+s"(q2), "+s"(tpf));
        auto pfq = [&](int n) { const unsigned o = q2 + L.kord(n) * GQ::WSTEP; spf2<0>(wqkv, o); spf2<2048>(wqkv, o); spf2<4096>(wqkv, o); };
        if (l) L.start();
        else STL(0, 2);
        unsigned tag, tg2; u32x4 gs, a[3];
        auto rowst = [&](int i) {
          const float m0 = __uint_as_float(a[i][0]), m = red4(m0) * 0.25f, q = red4(__uint_as_float(a[i][2]) + 256.f * (m0 - m) * (m0 - m));
          o = (f32x2){sel((lt & 3) == i, m, o.x), sel((lt & 3) == i, q, o.y)};
          asm volatile("" : "+v"(o));
        };
        for_c<0, 16 - GQ::H>([&](auto tc) {
          constexpr int t = tc.value;
          u32x4 v; unsigned tv;
          L.step<t, t == 5 || t == 6 ? 1 : t == 9 || t == 10 ? 4 : 0>([&](unsigned ls) {
            if (t < 4) v = *(const LAS u32x4*)(uintptr_t)(ls + ra);
            if (t == 3) tv = *(volatile LAS unsigned*)(uintptr_t)L_TAG;
          }, [&]() {
            if (t < 8 && cs) {
              constexpr int ls = 32768 + (t + 1) % 3 * GQ::SB;
              bf16x4 x[4];
#pragma unroll
              for (int e = 0; e < 4; ++e) x[e] = lds_tr4(rb + (ls & ~0x7fff) + (ls & 0x7fff) + (e >> 1) * 4096 + (e & 1) * 512);
#pragma unroll
              for (int ks = 0; ks < 2; ++ks) c = mfma(__builtin_shufflevector(x[2 * ks], x[2 * ks + 1], 0, 1, 2, 3, 4, 5, 6, 7), ones, c);
            } else if (t >= 4) {
              if (t >= 10 || t >= tpf) { if (t < TC1) pf.go(); }
              else if (lw < 2) pfq(t + 4);
              else if (t == 8) { pfq(14); pfq(15); }
            }
            if (t < 4) {
              if (t == 0) pv = bflo(v[0]);
#pragma unroll
              for (int e = 0; e < 4; ++e) { const f32x2 d = (f32x2){bflo(v[e]), bfhi(v[e])} - pv; s1 += d; s2 += d * d; }
              asm volatile("" : "+v"(s1), "+v"(s2));
            }
            if (t == 3) { tag = __builtin_amdgcn_readfirstlane(tv); tg2 = tag2(tag); if (tpf == 10) pfq(7); }
            if (t == 4) {
              const float d1 = (s1.x + s1.y) * (1.f / 32.f), ml = pv + d1, m = red8(ml) * 0.125f;
              const float q = red8(vmax((s2.x + s2.y) - 32.f * d1 * d1, 0.f) + 32.f * (ml - m) * (ml - m));
              gs = (u32x4){__float_as_uint(m), tag, __float_as_uint(q), tg2};
              asm volatile("" : "+v"(gs));
            }
            if (t == 8) { gs = (u32x4){__float_as_uint(sel(fr, c[2], c[0])), tag, __float_as_uint(sel(fr, c[3], c[1])), tg2}; asm volatile("" : "+v"(gs)); }
            if (t == 11) {
#pragma unroll
              for (int i = 0; i < 3; ++i) ld_ok(a[i]);
              for (int spin = 0;; ++spin) {
                unsigned bad = 0;
#pragma unroll
                for (int i = 0; i < 3; ++i) bad |= (a[i][1] ^ tag) | (a[i][3] ^ tg2);
                if (__builtin_expect(__all(bad == 0), 1) || spin_fail(spin, ws)) break;
                __builtin_amdgcn_s_sleep(8);
#pragma unroll
                for (int i = 0; i < 3; ++i) ld_again(a[i], rs, to[i]);
              }
              rowst(0);
            }
            if (t == 12) { rowst(1); rowst(2); }
            if (t == 13) { LAS float* p = (LAS float*)(uintptr_t)ta; p[0] = -o.x; p[144] = __builtin_amdgcn_rsqf(o.y * (1.f / D) + EPS); }
          }, [&]() {
            if (t == 4) st16(rs, so, gs);
            if (t == 8) {
              st16(rs, co, gs);
              sched0();
#pragma unroll
              for (int i = 0; i < 3; ++i) ld_raw(a[i], rs, to[i]);
            }
            if (t == 13) ld_raw(gs, rs, ts);
          });
        });
        sched0(); wait_vm<GQ::CNT + 1>(); bar();
        wait_lgkm0(); wait_vm<1>(); bar();
        wait_vm<0>();
        ld_ok(gs);
        if (l == 0) {
          for (int spin = 0;; ++spin) {
            if (__builtin_expect(__all(((gs[1] ^ tag) | (gs[3] ^ tg2)) == 0), 1) || spin_fail(spin, ws)) break;
            __builtin_amdgcn_s_sleep(8);
            ld_again(gs, rs, ts);
          }
          float c0 = __uint_as_float(gs[0]), c1 = __uint_as_float(gs[2]);
          c0 += dppf<0xB1>(c0); c1 += dppf<0xB1>(c1);
          *(LAS float*)(uintptr_t)(L_CS + (lw ? lt - 64 : lt) * 4) = sel(lt & 1, c1, c0);
        }
        if (l == 0) {
#pragma unroll
          for (int p = 0; p < 4; ++p) dma16((const char*)xin + (p >> 1) * 32768 + (p & 1) * 64, xo, L_X + lw * 4096 + p * 1024);
        }
        if (l == 0 && lw < TBA) { if (fr < 14) touch4(w1 + (size_t)(((bid >> 3) * TBA + lw) * 8 + (bid & 4)) * F + (bid & 3) * 896, fq * (F * 2) + fr * 128, L_SCR + lw * 256); }
        if (l == 0 && PWL) { wait_lgkm0(); bar(); }
      }
      {
        LOADER_VARS
        GW::Loader L(wo + tp * 128, ws + WS_O + (size_t)tq * 32 * 8192, lw, lane);
        unsigned po = ((bid & 1) * 8 + (slow_res(wo) ^ (j & 1))) * 128 + (j >> 1) * (D * 2);
        if (!PWL) { wait_lgkm0(); bar(); L.pre_w(); }
#pragma unroll
        for (int k = GW::H; k < 10; ++k) spf2(wo, po + k * GW::WSTEP);
        asm volatile("" : "+s"(po));
        if (lw < TBA) wait_vm<(PWL ? 0 : GW::H * 4) + 1>(); else wait_vm<PWL ? 0 : GW::H * 4>();
        sched0();
        bar();
        if (PWL) L.pre_w();
#pragma unroll
        for (int k = 10; k < 16; ++k) spf2(wo, po + k * GW::WSTEP);
        L.start();
        L.run(16 - GW::H, [&](int) { pf.go(); });
        pf.go();
        L.tail<GW::H - 1>();
        bar();
      }
      {
        LOADER_VARS
        if (bid < 224) {
          G1::Loader L(w1 + fp * 128, ws + WS_H + (size_t)fq1 * 64 * 16384, lw, lane);
          L.pre_w();
          const int g0 = (bid & 3) * 14, ga = (g0 + ((slow_res(w1) - g0) & 7)) ^ (j & 1), gb = ga + 8 < g0 + 14 ? ga + 8 : ga;
          const unsigned j2 = j < 32 ? 112 + (j & 15) : j;
          unsigned pa = (j >> 1) * (F * 2) + ga * 128, pb = (j >> 1) * (F * 2) + gb * 128, pd = (j2 >> 1) * (F * 2) + (j & 16 ? gb : ga) * 128;
          asm volatile("" : "+s"(pa), "+s"(pb), "+s"(pd));
          auto pfb = [&](int k) { spf2(w1, pa + k * G1::WSTEP); spf2(w1, pb + k * G1::WSTEP); spf2(w1, pd + k * G1::WSTEP); };
#pragma unroll
          for (int k = G1::H; k < G1::H + PA_1; ++k) pfb(k);
          wait_lgkm0(); pf.go();
          sched0();
          bar();
          pf.go();
          L.start();
          L.run(32 - G1::H, [&](int t) {
            if (pf.pc < 3 + NW1) pf.go();
            else if (t < 32 - G1::H - PA_1) pfb(t + G1::H + PA_1);
          });
          L.tail<G1::H - 1>();
          bar();
        } else {
          wait_lgkm0(); pf.go();
          bar();
          if (l == 0) {
            __builtin_amdgcn_s_sleep(24);
            while (pf.pc < 3 + NW1) { wait_lgkm0(); pf.go(); }
            spare_touch(w1, w2, bid, wid, lane);
          }
        }
        pf.pc = 99;
      }
      {
        LOADER_VARS
        constexpr int NB = F / 64 - 2 * JA;
        const char* ub = ws + WS_U + (size_t)tp * 112 * 16384;
        G2A::Loader A((const char*)(w2 + (tq & 23) * 64) + (tq & 8 ? JA * G2::WSTEP : 0), ub + (tq & 8 ? JA * 32768 : 0), lw, lane);
        A.pre_w();
        unsigned pb = ((bid & 4) * 4 + (j & 1) * 8 + slow_res(w2)) * 128 + (j >> 1) * (S * 2) + JA * G2::WSTEP, pa = pb - (bid & 2 ? 0 : JA * G2::WSTEP);
        asm volatile("" : "+s"(pa), "+s"(pb));
        auto side = [&](int n) { n += G2A::H + PA_2; if (n < JA + NB) spf2(w2, (n < JA ? pa : pb) + n * G2::WSTEP); };
#pragma unroll
        for (int k = 0; k < PA_2; ++k) side(k - PA_2);
        G2::Loader L((const char*)(w2 + tq * 64) + 2 * JA * G2::WSTEP, ub + 2 * JA * 32768, lw, lane);
        gu32* xf = (gu32*)(ws + WS_MAIL) + (l * 4 + 2) * MEDGE + (tq * 8 + tp) * 64 + 28 + lw;
        const char* xp = ws + WS_XP + (unsigned)(((bid ^ 2) * 4 + lw) << 14);
        unsigned fl = L_QKV + lw * 256, xv = ph(lane * 16);
        asm volatile("" : "+s"(xf), "+s"(xp), "+s"(fl)); asm volatile("" : "+v"(xv));
        sched0();
        bar();
        A.start();
        A.run(JA - G2A::H, [&](int t) { side(t); });
        auto stage = [&](auto s) { L.issue_w<s.value * G2::SB>(L.w + s.value * G2::WSTEP); L.issue_a<s.value * G2::SB>(L.ab + s.value * 32768); sched0(); };
        wait_vm<G2A::CNT>(); bar(); stage(IC<0>{}); side(JA - 2);
        wait_vm<G2::CNT>(); bar(); stage(IC<1>{}); side(JA - 1);
        wait_vm<G2::CNT>(); bar(); stage(IC<2>{}); stage(IC<3>{}); side(JA);
        L.t = 1; L.islot = 0;
        L.run(NB - G2::H, [&](int t) {
          if (t == NB - G2::H - FLS) asm volatile("s_mov_b32 m0, %2\n\ts_nop 0\n\tglobal_load_lds_dword %0, %1 sc1" :: "v"(0u), "s"(xf), "s"(fl) : "memory");
          side(JA + t);
        });
        auto xdma = [&]() {
#pragma unroll
          for (int i = 0; i < 4; ++i) dma16(xp + i * 2048, xv, L_X + lw * 4096 + i * 1024);
          sched0();
        };
        sched0(); wait_vm<2 * G2::CNT>(); bar();
        if (__builtin_amdgcn_readfirstlane(*(volatile LAS unsigned*)(uintptr_t)fl)) { xdma(); wait_vm<G2::CNT + 4>(); bar(); wait_vm<4>(); bar(); }
        else {
          wait_vm<G2::CNT>(); bar(); wait_vm<0>(); bar();
          for (int spin = 0;; ++spin) {
            if (__builtin_amdgcn_readfirstlane(__hip_atomic_load(xf, RLX_AGENT)) || spin_fail(spin, ws)) break;
            __builtin_amdgcn_s_sleep(2);
          }
          STL(4 + l * 4, 7);
          xdma();
        }
        wait_vm<0>(); bar();
        xin = (const u16*)(ws + WS_X2);
        if (NL == 1) bar();
      }
    }
    return;
  }

  {
    ST(0, 0);
    PHASE_VARS
    if (cw >= 0) code_warm(cw, lane, L_SCR + wid * 256);
    pfb_q(wqkv, bid, (bid >> 3) * 4 + wid, 0); pfb_q(wqkv, bid, (bid >> 3) * 4 + wid, 1);
    if (wid == 3) {
      if ((slow_res(wqkv) >> 2) == (bid & 1)) {
        __builtin_amdgcn_s_sleep(ET);
        if ((lane & 3) < 3 && lane < 40) touch4(wqkv, (unsigned)(((qkv_rot(bid) * 64 + 128 + (bid >> 3) * 10 + (lane >> 2)) & 1023) * (3 * D * 2) + (lane & 3) * (D * 2) + ((bid & 2) * 4 + slow_res(wqkv)) * 128), L_SCR + 768);
      }
    } else {
      const unsigned ep = __hip_atomic_load((gu32*)(ws + WS_EPOCH), RLX_AGENT);
      const rsrc_t rz = mkrsrc(ws + WS_XCH, 3 * MB);
      if (tid < 128) st16(rz, ph((((tid >> 6) * 256 + tq * 8 + tp) * 64 + lane) * 16), (u32x4){0u, SENT, 0u, SENT});
      else {
        const int e = (tid >> 3) & 7, line = e == 0 || e == 7 ? (2 * sb + (e & 1)) * 8 + (hp & 7) : e * 256 + bid;
        st16(rz, WS_MAIL - WS_XCH + line * 256 + (tid & 7) * 16, (u32x4){0u, 0u, 0u, 0u});
      }
      if (wid == 1 && bid < 224) touch4(b1 + fp * 128, lane * 4, L_BIAS);
      if (wid == 2) touch4(b2 + tq * 64, (lane & 31) * 4, L_BIAS + 256);
      if (wid == 0) { *(LAS unsigned*)(uintptr_t)L_TAG = ep + 1u; *(LAS u32x4*)(uintptr_t)(L_TAG + 16) = (u32x4){0u, 0u, 0u, 0u}; }
      asm volatile("s_waitcnt vmcnt(0)" :: "v"(ep) : "memory");
    }
    ST(0, 4);
    ST(1, 0);
  }
#pragma unroll 1
  for (int l = 0; l < NL; ++l) {
    {
      PHASE_VARS
      if (wid & 2) qkv_attn<1>(ws, l, wp, lane, hp, sb, cw);
      else qkv_attn<0>(ws, l, wp, lane, hp, sb, cw);
      ST(1 + l * 4, 5);
      sync_post(ws, l * 4, lane < 16 ? (sb * 16 + lane) * 64 + hp : -1, wid);
    }
    {
      PHASE_VARS
      GW::Comp<O42<false, WNE>> g; g.setup(wp, wq, lane);
      f32x4 acc[4][2];
#pragma unroll
      for (int i = 0; i < 4; ++i)
#pragma unroll
        for (int jj = 0; jj < 2; ++jj) acc[i][jj] = (f32x4){0.f, 0.f, 0.f, 0.f};
      unsigned xa = L_X + wid * 4096 + lane * 16;
      { bf16x8 s[2]; sel_frags(true, fr, fq, s); tile_add(acc, s, xa); }
      const rsrc_t rx = mkrsrc(ws + WS_XCH + l * (MB / 2), MB / 2), rh = mkrsrc(ws + WS_H, S * D * 4);
      unsigned xs = fq & 1 ? MB : ph((unsigned)(((tq * 8 + tp) * 64 + wp * 32 + wq * 16 + fr) * 16 + fq * 4)), xl = ph((unsigned)((tq * 512 + fq * 128 + wq * 16 + fr) * 16));
      const unsigned h0 = ph((unsigned)(((tp * 32 + tq) << 14) + wp * 8192 + (wq * 32 + fr) * 128 + ((pair_chunk(fq) ^ (km_f(fr) << 1)) << 4)));
      u32x4 ho = {h0, h0 ^ 64, h0 + 4096, (h0 ^ 64) + 4096};
      bf16x8 ones = {0x3F80, 0x3F80, 0x3F80, 0x3F80, 0x3F80, 0x3F80, 0x3F80, 0x3F80};
      asm volatile("" : "+v"(xa), "+v"(xs), "+v"(xl), "+v"(ho), "+v"(ones));
      g.prep<false>(acc);
      mfma_gap();
      sync_wait(ws, l * 4, tq * 8 + tp, lane, (lane & 31) < 16 ? 1u : 0u, wid);
      g.run_steps<false>(acc, [&](int it, int i) { if (i == 0 && cw >= 0 && it == -2) code_warm(cw, lane, L_SCR + wid * 256); } CSA);
      bar();
      sched0();
      g.pend<0, 4>(acc);
      ST(2 + l * 4, 2);
      u32x4 pk[2][2]; float mp[2], q[2];
#pragma unroll
      for (int jj = 0; jj < 2; ++jj) {
        f32x4 sm = {0.f, 0.f, 0.f, 0.f};
#pragma unroll
        for (int i = 0; i < 2; ++i) { pk[jj][i] = cat4(pk4(acc[2 * i][jj]), pk4(acc[2 * i + 1][jj])); asm volatile("" : "+v"(pk[jj][i])); sm = mfma(ones, __builtin_bit_cast(bf16x8, pk[jj][i]), sm); }
        mp[jj] = sm[0] * (1.f / 64.f); asm volatile("" : "+v"(mp[jj]));
      }
#pragma unroll
      for (int jj = 0; jj < 2; ++jj) {
        f32x4 qv = {0.f, 0.f, 0.f, 0.f};
#pragma unroll
        for (int i = 0; i < 4; ++i) {
          const unsigned x = pk[jj][i >> 1][(i & 1) * 2], y = pk[jj][i >> 1][(i & 1) * 2 + 1];
          acc[i][jj] = (f32x4){bflo(x), bfhi(x), bflo(y), bfhi(y)} - mp[jj]; qv += acc[i][jj] * acc[i][jj];
        }
        q[jj] = (qv[0] + qv[2]) + (qv[1] + qv[3]);
      }
      {
        float u, w;
        swp32(q[0], q[1]); xch16(q[0] + q[1], u, w);
        __builtin_amdgcn_raw_buffer_store_b64((u32x2){__float_as_uint(sel(fq < 2, mp[0], mp[1])), __float_as_uint(u + w)}, rx, xs, 0, 16);
      }
      sched0();
#pragma unroll
      for (int p = 0; p < 4; ++p) *(LAS u32x4*)(uintptr_t)(xa + p * 1024) = pk[p >> 1][p & 1];
      if (XD) __builtin_amdgcn_s_sleep(XD);
      f32x4 gr[4];
      for (int spin = 0;; ++spin) {
        asm volatile("" ::: "memory");
#pragma unroll
        for (int p = 0; p < 4; ++p) gr[p] = __builtin_bit_cast(f32x4, __builtin_amdgcn_raw_buffer_load_b128(rx, xl + p * 1024, 0, 16));
        const float lo = vmin3(vmin3(gr[0][1], gr[0][3], gr[1][1]), vmin3(gr[1][3], gr[2][1], gr[2][3]), vmin3(gr[3][1], gr[3][3], gr[3][3]));
        if (!__any(lo < 0.f) || spin_fail(spin, ws)) break;
        __builtin_amdgcn_s_sleep(XS);
      }
      ST(2 + l * 4, 5);
      {
        const f32x4 t = (gr[0] + gr[1]) + (gr[2] + gr[3]);
        float m[2], u, w, v, y;
#pragma unroll
        for (int jj = 0; jj < 2; ++jj) {
          m[jj] = t[2 * jj] * 0.25f;
          float e = 0.f;
#pragma unroll
          for (int p = 0; p < 4; ++p) { const float d = gr[p][2 * jj] - m[jj]; e += d * d; }
          q[jj] = t[2 * jj + 1] + 64.f * e;
        }
        swp32(m[0], m[1]); swp32(q[0], q[1]);
        xch16((m[0] + m[1]) * 0.5f, u, w); xch16((q[0] + q[1]) + 128.f * (m[0] - m[1]) * (m[0] - m[1]), v, y);
        const float var = ((v + y) + 256.f * (u - w) * (u - w)) * (1.f / D), r1 = __builtin_amdgcn_rsqf(var + EPS);
        xch32((u + w) * 0.5f, m[0], m[1]);
        xch32(r1 * __builtin_amdgcn_rsqf(var * r1 * r1 + EPS), q[0], q[1]);
        ST(2 + l * 4, 3);
#pragma unroll
        for (int jj = 0; jj < 2; ++jj) {
          const float c = (mp[jj] - m[jj]) * q[jj];
#pragma unroll
          for (int i = 0; i < 2; ++i) st16(rh, ho[jj * 2 + i], pack_pair(acc[2 * i][jj] * q[jj] + c, acc[2 * i + 1][jj] * q[jj] + c));
        }
      }
      sync_post(ws, l * 4 + 1, lane < 28 ? (tp * 28 + lane) * 64 + tq : -1, wid);
      if (l == 0 && bid == 0 && wid == 0) { const unsigned tag = get_tag(); if (lane == 0) __hip_atomic_store((gu32*)(ws + WS_EPOCH), tag, RLX_AGENT); }
    }
    {
      PHASE_VARS
      if (bid >= 224) {
        ST(2 + l * 4, 4);
        ST(3 + l * 4, 0);
        if (l == 0) { __builtin_amdgcn_s_sleep(24); spare_touch(w1, w2, bid, wid, lane); }
      } else {
        G1::Comp<O44<false, 32>> g; g.setup(wp, wq, lane);
        f32x4 acc[4][4];
#pragma unroll
        for (int i = 0; i < 4; ++i) { const u32x2 b = *(const LAS u32x2*)(uintptr_t)(L_BIAS + (wp * 4 + i) * 32 + fq * 8);
#pragma unroll
          for (int jj = 0; jj < 4; ++jj) acc[i][jj] = (f32x4){bflo(b.x), bfhi(b.x), bflo(b.y), bfhi(b.y)}; }
        const rsrc_t ro = mkrsrc(ws + WS_U, D * F * 4);
        const unsigned u0 = ph((unsigned)(((fq1 * 56 + fp * 2 + wp) << 14) + (wq * 64 + fr) * 128 + ((pair_chunk(fq) ^ (fr >> 1)) << 4)));
        u32x4 uo = {u0, u0 + 4096, u0 + 8192, u0 + 12288}, uo2 = uo ^ 64;
        asm volatile("" : "+v"(uo), "+v"(uo2));
        g.prep<false>(acc);
        sync_wait(ws, l * 4 + 1, fq1 * 28 + fp, lane, 1u, wid);
        g.run_steps<false>(acc, [&](int it, int i) { if (i == 0 && cw >= 0 && it == -2) code_warm(cw, lane, L_SCR + wid * 256); } CSA);
        bar();
        sched0();
        g.pend<0, 8>(acc);
        ST(3 + l * 4, 2);
#pragma unroll
        for (int i = 0; i < 2; ++i)
#pragma unroll
          for (int jj = 0; jj < 4; ++jj) st16(ro, (i ? uo2 : uo)[jj], pack_pair(gelu_tanh(acc[2 * i][jj]), gelu_tanh(acc[2 * i + 1][jj])));
        ST(3 + l * 4, 3);
      }
      sync_post(ws, l * 4 + 2, bid < 224 && lane < 32 ? (lane * 8 + fq1) * 64 + fp : -1, wid);
    }
    {
      PHASE_VARS
      asm volatile("" : "+s"(out));
      G2A::Comp<O44<true, ANE>> ga; ga.setup(wp, wq, lane);
#pragma unroll
      for (int jj = 0; jj < 4; ++jj) ga.qrd[0][jj] = 65536 + rd_km(lane, wq * 2 + (jj & 1)) + (((jj >> 1) ^ (tq >> 3)) & 1) * 8192;
      G2::Comp<O42<true, BNE>> g; g.setup(wp, wq, lane);
      g.setwin();
      f32x4 ac4[4][4], acc[4][2];
#pragma unroll
      for (int jj = 0; jj < 2; ++jj) { const float b = bf2f(*(const LAS u16*)(uintptr_t)(L_BIAS + 256 + (wq * 2 + jj) * 32 + fr * 2));
#pragma unroll
        for (int i = 0; i < 4; ++i) { ac4[i][jj] = (f32x4){b, b, b, b}; ac4[i][jj + 2] = (f32x4){0.f, 0.f, 0.f, 0.f}; } }
      unsigned xa = L_X + wid * 4096 + lane * 16, xo = (unsigned)((bid * 4 + wid) << 14) + ph(lane * 16), oo = x_off(tp, tq, wid, fr, fq), xo2 = xo + 4096, oo2 = oo + 32768;
      unsigned fo = lane ? MB : (((tq ^ 8) * 8 + tp) * 64 + 28 + wid) * 4u, one = 1u;
      bf16x8 s[2]; sel_frags(false, fr, fq, s); tile_add(ac4, s, xa);
      const rsrc_t rx = mkrsrc(ws + WS_XP, 16 * MB), rf = mkrsrc(ws + WS_MAIL + (l * 4 + 2) * (MEDGE * 4), MEDGE * 4), ro = mkrsrc(l ? (const void*)out : (const void*)(ws + WS_X2), S * D * 2);
      asm volatile("" : "+v"(xa), "+v"(xo), "+v"(oo), "+v"(xo2), "+v"(oo2), "+v"(fo), "+v"(one), "+v"(s[0]), "+v"(s[1]));
      ga.prep<false>(ac4);
      mfma_gap();
      sync_wait(ws, l * 4 + 2, tq * 8 + tp, lane, (lane & 31) < 28 ? 1u : 0u, wid);
      ga.run_steps<false>(ac4, [&](int it, int i) { if (i == 0 && cw >= 0 && it == -2) code_warm(cw, lane, L_SCR + wid * 256); } CSA);
      ga.pend<4, 8>(ac4);
      ST(4 + l * 4, 1);
#pragma unroll
      for (int i = 0; i < 2; ++i)
#pragma unroll
        for (int jj = 0; jj < 2; ++jj) st16(rx, (jj ? xo2 : xo) + i * 2048, cat4(pk4(ac4[2 * i][jj + 2]), pk4(ac4[2 * i + 1][jj + 2])));
#pragma unroll
      for (int i = 0; i < 4; ++i) { acc[i][0] = ac4[i][0]; acc[i][1] = ac4[i][1]; }
#pragma unroll
      for (int k = 0; k < 2; ++k) { g.fp[2][k] = ga.fp[2][k]; g.fp[3][k] = ga.fp[3][k]; g.fq[0][k] = ga.fq[0][k]; g.fq[1][k] = ga.fq[1][k]; }
      sched0();
      g.run_steps<true>(acc, [&](int it, int i) {
        if (i == PS % 4 && it == PS / 4 - (F / 64 - 2 * JA) / 4) {
          wait_vm<0>();
          ST(4 + l * 4, 5);
          __builtin_amdgcn_raw_buffer_store_b32(one, rf, fo, 0, 16);
        }
      } CSA);
      bar();
      sched0();
      g.pend<0, 4>(acc);
      ST(4 + l * 4, 2);
      tile_add(acc, s, xa);
      u32x4 xr[4];
#pragma unroll
      for (int i = 0; i < 2; ++i)
#pragma unroll
        for (int jj = 0; jj < 2; ++jj) { xr[jj * 2 + i] = pack_pair(acc[2 * i][jj], acc[2 * i + 1][jj]); st16(ro, (jj ? oo2 : oo) + i * 64, xr[jj * 2 + i]); }
      ST(4 + l * 4, 3);
      if (l == 0) {
#pragma unroll
        for (int p = 0; p < 4; ++p) *(LAS u32x4*)(uintptr_t)(xa + p * 1024) = xr[p];
        const int sbc = (tq >> 1) + (lane >> 4);
        const bool pv = lane < 16 || (lane < 32 && (tq & 1) && sbc < 16);
        sync_post(ws, 3, pv ? (sbc * 16 + (lane & 15)) * 64 + (tq - 2 * sbc + 1) * 8 + tp : -1, wid);
      } else ST(8, 4);
    }
  }
  if (wid0 == 0 && blockIdx.x == 45) {
    wait_lgkm0();
    const unsigned d = *(volatile LAS unsigned*)(uintptr_t)(19968 + PROBE * 4) - *(volatile LAS unsigned*)(uintptr_t)(19968 + PROBE0 * 4);
    const u64 e = __builtin_amdgcn_s_memrealtime() + PROBEF * (d < 20000 ? d : 0);
    while (__builtin_amdgcn_s_memrealtime() < e) __builtin_amdgcn_s_sleep(1);
  }
  return;
  if (wid0 == 0) {
    const u64 hi = __builtin_amdgcn_s_memrealtime() & ~0xffffffffull;
    wait_lgkm0();
    for (int e = threadIdx.x; e < 128; e += 64) ((gu64*)(ws + (200u << 20)))[((e >> 3) * 1024 + blockIdx.x) * 8 + (e & 7)] = hi | *(volatile LAS unsigned*)(uintptr_t)(19968 + e * 4);
  }
}

extern "C" void kernel_launch(void* const* d_in, const int* in_sizes, int n_in,
                              void* d_out, int out_size, void* d_ws, size_t ws_size,
                              hipStream_t stream) {
  (void)hipFuncSetAttribute((const void*)mega_kernel, hipFuncAttributeMaxDynamicSharedMemorySize, 163840);
  mega_kernel<<<256, 512, 163840, stream>>>((const u16*)d_in[0], (const u16*)d_in[1], (const u16*)d_in[2], (const u16*)d_in[4], (const u16*)d_in[6], (const u16*)d_in[5], (const u16*)d_in[7], (u16*)d_out, (char*)d_ws);
}
```

```cpp
#include <hip/hip_runtime.h>
#include <hip/hip_bf16.h>
#include <stdint.h>

typedef unsigned short u16;
typedef unsigned long long u64;
typedef short bf16x8 __attribute__((ext_vector_type(8)));
typedef short bf16x4 __attribute__((ext_vector_type(4)));
typedef float f32x4 __attribute__((ext_vector_type(4)));
typedef float f32x2 __attribute__((ext_vector_type(2)));
typedef unsigned u32x4 __attribute__((ext_vector_type(4)));
typedef unsigned u32x2 __attribute__((ext_vector_type(2)));
typedef __bf16 bf16v2 __attribute__((ext_vector_type(2)));
#define LAS __attribute__((address_space(3)))
#define GAS __attribute__((address_space(1)))
#define DEVI __device__ __forceinline__
typedef __attribute__((address_space(1))) unsigned gu32;
typedef __attribute__((address_space(1))) unsigned long long gu64;

constexpr int S = 2048, D = 1024, F = 3584;
constexpr float EPS = 1e-5f;

DEVI float bf2f(u16 v) { return __uint_as_float(((unsigned)v) << 16); }
DEVI float bflo(unsigned v) { return __uint_as_float(v << 16); }
DEVI float bfhi(unsigned v) { return __uint_as_float(v & 0xffff0000u); }
DEVI unsigned pk2(float lo, float hi) { bf16v2 p; p.x = (__bf16)lo; p.y = (__bf16)hi; return __builtin_bit_cast(unsigned, p); }

DEVI const void* uni_ptr(const void* p) {
  const u64 v = (u64)p;
  return (const void*)(((u64)(unsigned)__builtin_amdgcn_readfirstlane((unsigned)(v >> 32)) << 32) | (unsigned)__builtin_amdgcn_readfirstlane((unsigned)v));
}
DEVI void dma16(const void* sbase, unsigned voff, unsigned ldsaddr) {
  ldsaddr = __builtin_amdgcn_readfirstlane(ldsaddr);
  asm volatile("s_mov_b32 m0, %2\n\ts_nop 0\n\tglobal_load_lds_dwordx4 %0, %1 sc1" :: "v"(voff), "s"(sbase), "s"(ldsaddr) : "memory");
}
template <int N, int IMM> DEVI void dma_grp(const void* sbase, const unsigned* v, unsigned lb) {
  static_assert(N == 1 || N == 2 || N == 4, "");
  if constexpr (N == 4)
    asm volatile("s_add_u32 m0, %1, %2\n\ts_nop 0\n\tglobal_load_lds_dwordx4 %3, %0 sc1\n\tglobal_load_lds_dwordx4 %4, %0 offset:1024 sc1\n\t"
                 "global_load_lds_dwordx4 %5, %0 offset:2048 sc1\n\tglobal_load_lds_dwordx4 %6, %0 offset:3072 sc1"
                 :: "s"(sbase), "s"(lb), "i"(IMM), "v"(v[0]), "v"(v[1]), "v"(v[2]), "v"(v[3]) : "memory", "scc");
  else if constexpr (N == 2)
    asm volatile("s_add_u32 m0, %1, %2\n\ts_nop 0\n\tglobal_load_lds_dwordx4 %3, %0 sc1\n\tglobal_load_lds_dwordx4 %4, %0 offset:1024 sc1"
                 :: "s"(sbase), "s"(lb), "i"(IMM), "v"(v[0]), "v"(v[1]) : "memory", "scc");
  else
    asm volatile("s_add_u32 m0, %1, %2\n\ts_nop 0\n\tglobal_load_lds_dwordx4 %3, %0 sc1" :: "s"(sbase), "s"(lb), "i"(IMM), "v"(v[0]) : "memory", "scc");
}
template <int N> DEVI void wait_vm() { asm volatile("s_waitcnt vmcnt(%0)" :: "i"(N) : "memory"); }
DEVI void wait_lgkm0() { asm volatile("" ::: "memory"); __builtin_amdgcn_s_waitcnt(0xC07F); asm volatile("" ::: "memory"); }
DEVI void mfma_ip(f32x4& c, const bf16x8& a, const bf16x8& b) { asm volatile("v_mfma_f32_16x16x32_bf16 %0, %1, %2, %0" : "+v"(c) : "v"(a), "v"(b)); }
DEVI void mfma_gap() { __builtin_amdgcn_sched_barrier(0); asm volatile("s_nop 7"); __builtin_amdgcn_sched_barrier(0); }
DEVI void bar() { asm volatile("" ::: "memory"); __builtin_amdgcn_s_barrier(); asm volatile("" ::: "memory"); }
DEVI void sched0() { __builtin_amdgcn_sched_barrier(0); }

DEVI bf16x8 lds_b128(unsigned addr) { return *(const LAS bf16x8*)(uintptr_t)addr; }
DEVI bf16x4 lds_tr4(unsigned addr) { return __builtin_amdgcn_ds_read_tr16_b64_v4i16((LAS bf16x4*)(uintptr_t)addr); }

typedef __amdgpu_buffer_rsrc_t rsrc_t;
DEVI rsrc_t mkrsrc(const void* p, unsigned bytes) { return __builtin_amdgcn_make_buffer_rsrc((void*)p, 0, bytes, 0x00020000); }
DEVI void st16(rsrc_t r, unsigned off, u32x4 v) { __builtin_amdgcn_raw_buffer_store_b128(v, r, off, 0, 16); }
DEVI void ld_raw(u32x4& v, rsrc_t r, unsigned off) { asm volatile("buffer_load_dwordx4 %0, %1, %2, 0 offen sc1 ; raw load" : "=&v"(v) : "v"(off), "s"(r) : "memory"); }
DEVI void ld_again(u32x4& v, rsrc_t r, unsigned off) { asm volatile("buffer_load_dwordx4 %0, %1, %2, 0 offen sc1\n\ts_waitcnt vmcnt(0)" : "=&v"(v) : "v"(off), "s"(r) : "memory"); }
DEVI void ld_ok(u32x4& v) { asm volatile("; raw ok %0" : "+v"(v)); }
#define RLX_AGENT __ATOMIC_RELAXED, __HIP_MEMORY_SCOPE_AGENT

template <int N> DEVI float ror16(float v) { return __int_as_float(__builtin_amdgcn_update_dpp(0, __float_as_int(v), 0x120 + N, 0xf, 0xf, false)); }
DEVI float red16(float v) { v += ror16<8>(v); v += ror16<4>(v); v += ror16<2>(v); v += ror16<1>(v); return v; }
DEVI void xch32(float v, float& a, float& b) { unsigned u = __float_as_uint(v); auto r = __builtin_amdgcn_permlane32_swap(u, u, false, false); unsigned x = r[0], y = r[1]; a = __uint_as_float(x); b = __uint_as_float(y); }
DEVI void xch16(float v, float& a, float& b) { unsigned u = __float_as_uint(v); auto r = __builtin_amdgcn_permlane16_swap(u, u, false, false); unsigned x = r[0], y = r[1]; a = __uint_as_float(x); b = __uint_as_float(y); }
DEVI void swp32(float& a, float& b) { auto r = __builtin_amdgcn_permlane32_swap(__float_as_uint(a), __float_as_uint(b), false, false); unsigned x = r[0], y = r[1]; a = __uint_as_float(x); b = __uint_as_float(y); }
DEVI void swp16(float& a, float& b) { auto r = __builtin_amdgcn_permlane16_swap(__float_as_uint(a), __float_as_uint(b), false, false); unsigned x = r[0], y = r[1]; a = __uint_as_float(x); b = __uint_as_float(y); }
DEVI float colsum4(float v) { float a, b; xch32(v, a, b); v = a + b; xch16(v, a, b); return a + b; }
DEVI float vmax(float a, float b) { float r; asm("v_max_f32 %0, %1, %2" : "=v"(r) : "v"(a), "v"(b)); return r; }
DEVI float vmin3(float a, float b, float c) { float r; asm("v_min3_f32 %0, %1, %2, %3" : "=v"(r) : "v"(a), "v"(b), "v"(c)); return r; }
DEVI float vmax3(float a, float b, float c) { float r; asm("v_max3_f32 %0, %1, %2, %3" : "=v"(r) : "v"(a), "v"(b), "v"(c)); return r; }

DEVI unsigned src_kc(int c, int ld) { int row = c >> 3, ch = (c & 7) ^ ((row >> 1) & 7); return (unsigned)(row * ld * 2 + ch * 16); }
DEVI int km_f(int k) { return ((k >> 1) & 1) | (((k >> 3) & 1) << 1); }
DEVI unsigned ph(unsigned x) { return ((x >> 7) << 8) | (x & 127u); }
DEVI unsigned rd_kc(int lane, int ks) { int r = lane & 15, g = lane >> 4; return (unsigned)(r * 128 + (((ks * 4 + g) ^ (r >> 1)) << 4)); }
DEVI unsigned rd_km(int lane, int ct) {
  int g = lane >> 4, q = (lane & 15) >> 2, p = lane & 3;
  int f = ((q >> 1) & 1) | ((g & 1) << 1);
  return (unsigned)((ct >> 2) * 8192 + (8 * g + q) * 128 + (((ct & 3) ^ f) << 5) + 8 * p);
}


constexpr unsigned CODE_BYTES = 50176;
DEVI void code_warm(int part, int lane, unsigned scratch) {
  unsigned long long pc;
  asm volatile("s_getpc_b64 s[40:41]\n\ts_add_u32 s40, s40, mega_kernel@rel32@lo+4\n\ts_addc_u32 s41, s41, mega_kernel@rel32@hi+12\n\ts_mov_b64 %0, s[40:41]" : "=s"(pc) :: "s40", "s41", "scc");
  const unsigned off = (unsigned)(part * 64 + lane) * 128u;
  scratch = __builtin_amdgcn_readfirstlane(scratch);
  if (off < CODE_BYTES) asm volatile("s_nop 3\n\ts_mov_b32 m0, %2\n\ts_nop 0\n\tglobal_load_lds_dword %0, %1" :: "v"(off), "s"(pc), "s"(scratch) : "memory");
}

DEVI unsigned src_km(int lt, int ld) { const int k = lt >> 3; return (unsigned)(k * ld * 2 + (((lt & 7) ^ (km_f(k) << 1)) << 4)); }
DEVI void touch4(const void* sbase, unsigned voff, unsigned ldsaddr) {
  ldsaddr = __builtin_amdgcn_readfirstlane(ldsaddr); sbase = uni_ptr(sbase);
  asm volatile("s_nop 3\n\ts_mov_b32 m0, %2\n\ts_nop 0\n\tglobal_load_lds_dword %0, %1" :: "v"(voff), "s"(sbase), "s"(ldsaddr) : "memory");
}

template <int IMM> DEVI void spf1(const void* base, unsigned soff) { asm volatile("s_load_dword s101, %0, %1 offset:%2" :: "s"(base), "s"(soff), "i"(IMM) : "s101"); }
template <int IMM = 0> DEVI void spf2(const void* base, unsigned soff) { asm volatile("s_load_dwordx2 s[100:101], %0, %1 offset:%2" :: "s"(base), "s"(soff), "i"(IMM + 60) : "s100", "s101"); }
template <int ST> DEVI void spf2x6(const void* base, unsigned soff) {
  asm volatile("s_load_dwordx2 s[100:101], %0, %1 offset:%2\n\ts_load_dwordx2 s[100:101], %0, %1 offset:%3\n\ts_load_dwordx2 s[100:101], %0, %1 offset:%4\n\t"
               "s_load_dwordx2 s[100:101], %0, %1 offset:%5\n\ts_load_dwordx2 s[100:101], %0, %1 offset:%6\n\ts_load_dwordx2 s[100:101], %0, %1 offset:%7"
               :: "s"(base), "s"(soff), "i"(60), "i"(60 + ST), "i"(60 + 2 * ST), "i"(60 + 3 * ST), "i"(60 + 4 * ST), "i"(60 + 5 * ST) : "s100", "s101");
}
template <int ST> DEVI void spf1x12(const void* base, unsigned soff) {
  spf1<0>(base, soff); spf1<ST>(base, soff); spf1<2 * ST>(base, soff); spf1<3 * ST>(base, soff); spf1<4 * ST>(base, soff); spf1<5 * ST>(base, soff);
  spf1<6 * ST>(base, soff); spf1<7 * ST>(base, soff); spf1<8 * ST>(base, soff); spf1<9 * ST>(base, soff); spf1<10 * ST>(base, soff); spf1<11 * ST>(base, soff);
}
DEVI int lgkm_out() { int n; asm volatile("s_getreg_b32 %0, hwreg(HW_REG_IB_STS, 8, 4)" : "=s"(n)); return n; }
DEVI int slow_res(const void* w) { return (3 - ((unsigned)(uintptr_t)w >> 7)) & 7; }

constexpr int WNE = 14, ANE = 24, BNE = 13, QNE = 46;
struct Plan { int n; signed char q[40], idx[40], k[40], h[40], g[40]; };
template <class O, bool PKM, bool QKM> constexpr Plan make_plan() {
  Plan p{};
  int nr = 0;
  for (int c = 0; c < O::NRO; ++c) nr += ((O::ro[c] >> 4) ? QKM : PKM) ? 4 : 2;
  int prev = 0;
  for (int c = 0; c < O::NRO; ++c) {
    const int q = O::ro[c] >> 4, i = O::ro[c] & 15;
    int av[2] = {0, 0}, k0 = -1;
    for (int m = 0; m < 2 * O::NP; ++m) {
      const int mm = (m + 2 * O::NPEND) % (2 * O::NP);
      if ((q ? O::pj[m >> 1] : O::pi[m >> 1]) == i && m < 2 * O::NPEND) av[((m >> 1) ^ m) & 1] = m;
      if ((q ? O::pj[mm >> 1] : O::pi[mm >> 1]) == i && k0 < 0) k0 = ((mm >> 1) ^ mm) & 1;
    }
    for (int kk = 0; kk < 2; ++kk)
      for (int h = 0; h < ((q ? QKM : PKM) ? 2 : 1); ++h) {
        const int k = k0 ^ kk;
        int g = p.n * O::NE / nr;
        if (g < av[k]) g = av[k];
        if (g < prev + (p.n && O::NE >= nr ? 1 : 0)) g = prev + (p.n && O::NE >= nr ? 1 : 0);
        p.q[p.n] = q; p.idx[p.n] = i; p.k[p.n] = k; p.h[p.n] = h; p.g[p.n] = g; prev = g; ++p.n;
      }
  }
  return p;
}
struct OBase { static constexpr int pidx(int i) { return i; } static constexpr int poff(int) { return 0; } static constexpr int qoff(int j) { return j * 2048; } static constexpr bool swp(int) { return false; } };
template <bool PF, int NE_> struct O44 : OBase {
  static constexpr int TP = 4, TQ = 4, NPA = 4, NP = 16, NPEND = 8, NRO = 8, NE = NE_;
  static constexpr signed char pi[16] = {2, 3, 3, 2, 2, 3, 3, 2, 0, 1, 1, 0, 0, 1, 1, 0}, pj[16] = {0, 0, 1, 1, 2, 2, 3, 3, 0, 0, 1, 1, 2, 2, 3, 3};
  static constexpr signed char ro[8] = {0, PF ? 1 : 16, PF ? 16 : 1, 17, 18, 19, 2, 3};
};
template <bool PF, int NE_> struct O42 : OBase {
  static constexpr int TP = 4, TQ = 2, NPA = 4, NP = 8, NPEND = 4, NRO = 6, NE = NE_;
  static constexpr signed char pi[8] = {2, 3, 3, 2, 0, 1, 1, 0}, pj[8] = {0, 0, 1, 1, 0, 0, 1, 1};
  static constexpr signed char ro[6] = {0, PF ? 1 : 16, PF ? 16 : 1, 17, 3, 2};
};
template <int TH> struct OQ {
  static constexpr int TP = 6, TQ = 5, NPA = 2, NP = 26, NPEND = 13, NRO = 11, NE = QNE;
  static constexpr signed char pi[26] = {3, 4, 5, 5, 4, 3, 3, 4, 5, 5, TH ? 5 : 3, 4, TH ? 3 : 4, 0, 1, 2, 2, 1, 0, 0, 1, 2, 2, TH ? 2 : 0, 1, TH ? 0 : 1};
  static constexpr signed char pj[26] = {0, 0, 0, 1, 1, 1, 2, 2, 2, TH ? 4 : 3, 3, 3, TH ? 3 : 4, 0, 0, 0, 1, 1, 1, 2, 2, 2, TH ? 4 : 3, 3, 3, TH ? 3 : 4};
  static constexpr signed char ro[11] = {0, 1, 16, 2, 17, 18, TH ? 20 : 19, TH ? 19 : 20, 3, 4, 5};
  static constexpr int pidx(int i) { return i / 3; }
  static constexpr int poff(int i) { return i % 3 * 8192; }
  static constexpr int qoff(int j) { return j == 4 ? 0 : (1 + 4 * TH + j) * 2048; }
  static constexpr bool swp(int i) { return i % 3 == 2; }
};
template <bool PKM, bool QKM, int PB, int QB, int XB, int NS, int NT, bool WISP, int BASE, int ROT, int WLD, int WSUB, int ASTEP, int RLX = 0, bool QF = false>
struct Gemm {
  static constexpr int SB = PB + QB + XB, CNT = (PB + QB) / 4096 + (XB ? 1 : 0);
  static constexpr int PO = QF ? QB : 0, QO = QF ? 0 : PB, WB = WISP ? PB : QB, AB = WISP ? QB : PB, WOFF = WISP ? PO : QO, AOFF = WISP ? QO + XB : PO;
  static constexpr int XL = XB / 64, H = NS - 1 - RLX, CA = AB / 4096 + (XB ? 1 : 0), WSTEP = 64 * WLD * 2;
  static_assert(H * CNT <= 63 && BASE + NS * SB <= 163840, "");
  struct Loader {
    static constexpr int NW = WB / 4096, NA = AB / 4096;
    const char* w; const char* ab; unsigned wv[NW], av[NA], xv, lwb, lab, lx; int rot, islot, t; bool xl;
    DEVI Loader(const void* w_, const void* ab_, int lw, int lane, int rot_ = 0)
        : w((const char*)w_ - 3072), ab((const char*)ab_ - 3072), lwb(BASE + WOFF + lw * (NW == 6 ? 2048 : WB / 4)), lab(BASE + AOFF + lw * (AB / 4)), lx(BASE + PB + lw * (XL * 16)),
          rot(rot_), islot((H + ROT) % NS), t(0), xl(lane < XL) {
      const unsigned w0 = (lane >> 3) * (WLD * 2), f0 = (lane & 7) ^ ((lane >> 3) & 2);
#pragma unroll
      for (int i = 0; i < NW; ++i) {
        const unsigned p = NW == 6 ? (i >> 1) * 8 + lw * 2 + (i & 1) : lw * NW + i;
        wv[i] = w0 + ((f0 ^ ((i & 1) << 2)) << 4) + (NW == 6 ? (i >> 1) * WSUB + (lw * 2 + (i & 1)) * (8 * WLD * 2) - (i & 1) * 1024 : (p >> 3) * WSUB + (p & 7) * (8 * WLD * 2) - i * 1024) + 3072;
        asm volatile("" : "+v"(wv[i]));
      }
      asm volatile("" : "+s"(w), "+s"(lwb));
      if (XB) asm volatile("" : "+s"(rot));
      else seta(0, 0, lw, lane);
    }
    DEVI void seta(unsigned a0, unsigned xv_, int lw, int lane) {
      xv = xv_ + 3072;
#pragma unroll
      for (int i = 0; i < NA; ++i) { const unsigned p = lw * NA + i; av[i] = (XB ? a0 + src_kc(p * 64 + lane, D) : p * 2048 + ph(lane * 16)) + 3072 - i * 1024; asm volatile("" : "+v"(av[i])); }
      asm volatile("" : "+s"(ab), "+s"(lab));
      if (XB) { asm volatile("" : "+v"(xv)); asm volatile("" : "+s"(lx)); }
    }
    DEVI int kord(int n) const { if constexpr (XB != 0) return (n + rot) & (NT - 1); else return n; }
    template <int SO> DEVI void issue_w(const char* p, unsigned so = 0) const {
      if constexpr (NW == 6) { dma_grp<2, SO>(p, wv, lwb + so); dma_grp<2, SO + 8192>(p, wv + 2, lwb + so); dma_grp<2, SO + 16384>(p, wv + 4, lwb + so); }
      else dma_grp<NW, SO>(p, wv, lwb + so);
    }
    template <int SO> DEVI void issue_a(const char* p, unsigned so = 0) const {
      if (XB) { if (xl) dma_grp<1, SO>(p, &xv, lx + so); }
      dma_grp<NA, SO>(p, av, lab + so);
    }
    template <int S0, bool W, bool A> DEVI void pre() const {
      if constexpr (S0 < H) {
        if (W) issue_w<((S0 + ROT) % NS) * SB>(w + (kord(0) + S0) * WSTEP);
        if (A) issue_a<((S0 + ROT) % NS) * SB>(ab + (kord(0) + S0) * ASTEP);
        pre<S0 + 1, W, A>();
      }
    }
    template <int T> DEVI void pre_w1() const { issue_w<((T + ROT) % NS) * SB>(w + (kord(0) + T) * WSTEP); sched0(); }
    DEVI void pre_w() const { pre<0, true, false>(); sched0(); }
    template <int T> DEVI void peel() const {
      if constexpr (T < H - 1) {
        wait_vm<(H - 1 - T) * CA + T * CNT>();
        bar();
        issue_w<((T + H + ROT) % NS) * SB>(w + (T + H) * WSTEP); issue_a<((T + H + ROT) % NS) * SB>(ab + (T + H) * ASTEP);
        peel<T + 1>();
      }
    }
    DEVI void start() {
      sched0();
      bar();
      pre<0, false, true>();
      sched0();
      if constexpr (H == 2) wait_vm<CA>();
      else { peel<0>(); islot = (2 * H - 1 + ROT) % NS; t = H - 1; }
    }
    template <class HK> DEVI void run(int tend, HK side) {
#pragma unroll 1
      for (; t < tend; ++t) {
        const char* wk = w + (t + H) * WSTEP; const char* ak = ab + (t + H) * ASTEP;
        unsigned lw_ = lwb + islot * SB, la_ = lab + islot * SB;
        islot = islot + 1 == NS ? 0 : islot + 1;
        asm volatile("" : "+s"(wk), "+s"(ak), "+s"(lw_), "+s"(la_));
        wait_vm<(H - 1) * CNT>();
        bar();
        dma_grp<NW, 0>(wk, wv, lw_);
        side(t);
        dma_grp<NA, 0>(ak, av, la_);
        sched0();
      }
    }
    template <int T, int X, class A, class B, class C> DEVI void step(A pre_, B side, C post) const {
      const int k = kord(T + H);
      const char* wk = w + k * WSTEP; const char* ak = ab + k * ASTEP;
      asm volatile("" : "+s"(wk), "+s"(ak));
      wait_vm<(H - 1) * CNT + X>();
      bar();
      pre_(BASE + ((T + ROT) % NS) * SB);
      issue_w<((T + H + ROT) % NS) * SB>(wk);
      sched0();
      side();
      sched0();
      issue_a<((T + H + ROT) % NS) * SB>(ak);
      post();
      sched0();
    }
    template <int I> DEVI void tail() const { if constexpr (I >= 0) { sched0(); wait_vm<I * CNT>(); bar(); tail<I - 1>(); } }
  };
  static constexpr int NWIN = (NS * SB + 65535) / 65536;
  template <class O> struct Comp {
    static constexpr int TP = O::TP, TQ = O::TQ, NM = 2 * O::NP, NPA = PKM ? O::NPA : 2, NQA = QKM ? TQ : 2;
    static constexpr Plan PL = make_plan<O, PKM, QKM>();
    unsigned prd[NWIN][NPA], qrd[NWIN][NQA];
    bf16x8 fp[TP][2], fq[TQ][2]; bf16x4 lo;
    DEVI void setwin() {
#pragma unroll
      for (int w = 1; w < NWIN; ++w) {
#pragma unroll
        for (int i = 0; i < NPA; ++i) { prd[w][i] = prd[0][i] + w * 65536; asm volatile("" : "+v"(prd[w][i])); }
#pragma unroll
        for (int i = 0; i < NQA; ++i) { qrd[w][i] = qrd[0][i] + w * 65536; asm volatile("" : "+v"(qrd[w][i])); }
      }
#pragma unroll
      for (int i = 0; i < NPA; ++i) asm volatile("" : "+v"(prd[0][i]));
#pragma unroll
      for (int i = 0; i < NQA; ++i) asm volatile("" : "+v"(qrd[0][i]));
    }
    DEVI void setup(int wp, int wq, int lane) {
      const unsigned b = BASE;
      if (PKM) {
#pragma unroll
        for (int i = 0; i < TP; ++i) prd[0][i] = b + rd_km(lane, wp * TP + i);
      } else { prd[0][0] = b + rd_kc(lane, 0) + wp * TP * 2048; prd[0][1] = b + rd_kc(lane, 1) + wp * TP * 2048; }
      if (QKM) {
#pragma unroll
        for (int j = 0; j < TQ; ++j) qrd[0][j] = b + rd_km(lane, wq * TQ + j);
      } else { qrd[0][0] = b + rd_kc(lane, 0) + wq * TQ * 2048; qrd[0][1] = b + rd_kc(lane, 1) + wq * TQ * 2048; }
    }
    template <int C, int N> DEVI static unsigned wa(const unsigned (&b)[NWIN][N], int i) { return b[C >> 16][i] + (C & 0xffff); }
    template <int SLOT, int R> DEVI void rd() {
      constexpr int q = PL.q[R], i = PL.idx[R], k = PL.k[R], h = PL.h[R];
      bf16x8& f = q ? fq[q ? i : 0][k] : fp[q ? 0 : i][k];
      if constexpr (q ? QKM : PKM) {
        unsigned a;
        if constexpr (q) a = wa<SLOT * SB + QO + k * 4096 + h * 512, NQA>(qrd, i); else a = wa<SLOT * SB + PO + O::poff(i) + k * 4096 + h * 512, NPA>(prd, O::pidx(i));
        const bf16x4 x = lds_tr4(a);
        if constexpr (h) f = __builtin_shufflevector(lo, x, 0, 1, 2, 3, 4, 5, 6, 7); else lo = x;
      } else if constexpr (q) f = lds_b128(wa<SLOT * SB + QO + O::qoff(i), 2>(qrd, k));
      else f = lds_b128(wa<SLOT * SB + PO + i * 2048, 2>(prd, k));
    }
    template <int SLOT, int M, int R> DEVI void rds() { if constexpr (R < PL.n) { if constexpr (PL.g[R] == M) rd<SLOT, R>(); if constexpr (PL.g[R] <= M) rds<SLOT, M, R + 1>(); } }
    template <int M, bool IP = false> DEVI void mm(f32x4 (&acc)[TP][TQ]) {
      constexpr int i = O::pi[M >> 1], j = O::pj[M >> 1], k = ((M >> 1) ^ M) & 1;
      if constexpr (IP) { if constexpr (O::swp(i)) mfma_ip(acc[i][j], fq[j][k], fp[i][k]); else mfma_ip(acc[i][j], fp[i][k], fq[j][k]); }
      else if constexpr (O::swp(i)) acc[i][j] = __builtin_amdgcn_mfma_f32_16x16x32_bf16(fq[j][k], fp[i][k], acc[i][j], 0, 0, 0);
      else acc[i][j] = __builtin_amdgcn_mfma_f32_16x16x32_bf16(fp[i][k], fq[j][k], acc[i][j], 0, 0, 0);
    }
    template <int A, int B> DEVI void pend(f32x4 (&acc)[TP][TQ]) { if constexpr (A < B) { mm<2 * A>(acc); mm<2 * A + 1>(acc); pend<A + 1, B>(acc); } }
    template <int SLOT, bool FIRST, int M> DEVI void body(f32x4 (&acc)[TP][TQ]) {
      if constexpr (M < NM) {
        if constexpr (!FIRST || M >= 2 * O::NPEND) { mm<M, true>(acc); sched0(); }
        rds<SLOT, M, 0>();
        sched0();
        body<SLOT, FIRST, M + 1>(acc);
      }
    }
    template <int SLOT, bool FIRST> DEVI void step(f32x4 (&acc)[TP][TQ]) { if (!RLX) wait_lgkm0(); bar(); sched0(); body<SLOT, FIRST, 0>(acc); }
    template <int I, int N, int T0, class MID> DEVI void steps(f32x4 (&acc)[TP][TQ], MID mid) {
      if constexpr (I < N) { mid(I); step<(I + T0 + ROT) % NS, false>(acc); steps<I + 1, N, T0>(acc, mid); }
    }
    DEVI void prep(f32x4 (&acc)[TP][TQ]) {
      setwin();
#pragma unroll
      for (int p = 0; p < O::NP; ++p) asm volatile("" : "+v"(acc[O::pi[p]][O::pj[p]]));
    }
    template <bool CONT, class MID> DEVI void run_steps(f32x4 (&acc)[TP][TQ], MID mid) {
      constexpr int T0 = CONT ? 0 : 1;
      if constexpr (T0) step<ROT % NS, true>(acc);
#pragma unroll 1
      for (int it = 0; it < (NT - T0) / NS; ++it) {
        steps<0, NS, T0>(acc, [&](int i) { mid(it - (NT - T0) / NS, i); });
      }
      steps<0, (NT - T0) % NS, T0>(acc, [](int) {});
      wait_lgkm0();
      mfma_gap();
    }
  };
};

DEVI f32x4 gelu_tanh(f32x4 u) {
  f32x4 a = u * (-2.302208198f + -0.1029432397f * (u * u));
#pragma unroll
  for (int e = 0; e < 4; ++e) a[e] = __builtin_amdgcn_exp2f(a[e]);
  a += 1.f;
#pragma unroll
  for (int e = 0; e < 4; ++e) a[e] = __builtin_amdgcn_rcpf(a[e]);
  return u * a;
}

template <int tilesP, int tilesQ, int gp, int gq> DEVI void tile_map(unsigned bid, int& tp, int& tq) {
  const unsigned xcd = bid & 7, idx = bid >> 3;
  constexpr unsigned gP = tilesP / gp, gQ = tilesQ / gq;
  static_assert(gP * gQ == 8, "");
  tp = (xcd % gP) * gp + idx % gp;
  tq = (xcd / gP) * gq + idx / gp;
}

template <int V> struct IC { static constexpr int value = V; };
template <int A, int B, class FN> DEVI void for_c(FN f) { if constexpr (A < B) { f(IC<A>{}); for_c<A + 1, B>(f); } }
DEVI float sel(bool c, float a, float b) { return c ? a : b; }
DEVI u32x2 pk4(const f32x4& v) { return (u32x2){pk2(v[0], v[1]), pk2(v[2], v[3])}; }
DEVI u32x4 cat4(u32x2 a, u32x2 b) { return (u32x4){a.x, a.y, b.x, b.y}; }
DEVI bf16x8 cat(u32x2 a, u32x2 b) { return __builtin_bit_cast(bf16x8, cat4(a, b)); }
DEVI f32x4 mfma(const bf16x8& a, const bf16x8& b, const f32x4& c) { return __builtin_amdgcn_mfma_f32_16x16x32_bf16(a, b, c, 0, 0, 0); }
DEVI u32x4 pack_pair(const f32x4& a, const f32x4& b) {
  const unsigned ax = pk2(a[0], a[1]), ay = pk2(a[2], a[3]), bx = pk2(b[0], b[1]), by = pk2(b[2], b[3]);
  auto rx = __builtin_amdgcn_permlane16_swap(ax, bx, false, false);
  auto ry = __builtin_amdgcn_permlane16_swap(ay, by, false, false);
  const unsigned x0 = rx[0], x1 = rx[1], y0 = ry[0], y1 = ry[1];
  return (u32x4){x0, y0, x1, y1};
}
DEVI int pair_chunk(int fq) { return ((fq & 1) << 1) | (fq >> 1); }
DEVI void sel_frags(bool paired, int fr, int fq, bf16x8 (&s)[2]) {
  const unsigned one = fr & 1 ? 0x3F800000u : 0x3F80u;
#pragma unroll
  for (int i = 0; i < 2; ++i) {
    const bool on = paired ? (fq & 1) == i && (fr >> 3) == (fq >> 1) : (fr >> 2) == fq;
    const int d = paired ? (fr & 7) >> 1 : i * 2 + ((fr >> 1) & 1);
    u32x4 v;
#pragma unroll
    for (int e = 0; e < 4; ++e) v[e] = on && d == e ? one : 0u;
    s[i] = __builtin_bit_cast(bf16x8, v);
  }
}
template <int TQ> DEVI void tile_add(f32x4 (&acc)[4][TQ], const bf16x8 (&s)[2], unsigned a) {
  bf16x8 t[4];
#pragma unroll
  for (int p = 0; p < 4; ++p) t[p] = lds_b128(a + p * 1024);
#pragma unroll
  for (int p = 0; p < 4; ++p)
#pragma unroll
    for (int i = 0; i < 2; ++i) acc[(p & 1) * 2 + i][p >> 1] = mfma(s[i], t[p], acc[(p & 1) * 2 + i][p >> 1]);
}
template <int C> DEVI float dppf(float v) { return __int_as_float(__builtin_amdgcn_update_dpp(0, __float_as_int(v), C, 0xf, 0xf, false)); }
DEVI float red8(float v) { v += dppf<0xB1>(v); v += dppf<0x4E>(v); v += dppf<0x141>(v); return v; }
DEVI float red4(float v) { v += dppf<0xB1>(v); v += dppf<0x4E>(v); return v; }

constexpr unsigned SENT = 0xBF800000u;
constexpr int SPIN_MAX = 1 << 21;

constexpr unsigned MB = 1u << 20;
constexpr unsigned WS_O = 0, WS_H = 8 * MB, WS_U = 16 * MB, WS_X2 = 32 * MB, WS_XP = 40 * MB  ;
constexpr unsigned WS_ST = 120 * MB, ST_CS = 2 * S * 64, WS_XCH = 122 * MB  , WS_EPOCH = 123 * MB, WS_ABORT = 123 * MB + 256  ;
DEVI unsigned tag2(unsigned t) { return t * 0x9E3779B1u ^ 0x5BD1E995u; }
constexpr int MEDGE = 256 * 64;
constexpr unsigned WS_MAIL = 124 * MB;

constexpr int NL = 2, QBR = 3, FLS = 3, NPL = 6, SPON = 1, PC0 = 0, STG = 1, WSP = 1, SWW = 2, SW0 = 8, SW1 = 6, XD = 16, XS = 4, JA = 22, PS = 3, TBA = 3, TC1 = 12, NW1 = 9, EW = 4, ET = 20, SPW = 1, PA_1 = 3, PA_2 = 3, ATT4 = 1;
constexpr int L_X = 0, L_TAB = 16384, L_CS = L_TAB + 1152, L_TAG = L_CS + 768, L_BIAS = L_TAG + 128, L_SCR = L_BIAS + 512  , L_QKV = 20480;
static_assert(L_SCR + 1536 <= L_QKV, "");
typedef Gemm<true, false, 24576, 16384, 2048, 3, D / 64, true, 32768, 0, 3 * D, 2 * D, 128> GQ;
typedef Gemm<true, false, 16384, 8192, 0, 5, D / 64, true, 40960, 2, D, 128, 16384, 1, true> GW;
typedef Gemm<true, true, 16384, 16384, 0, 4, S / 64, true, 32768, 1, F, 128, 32768, 0, true> G1;
typedef Gemm<false, true, 16384, 16384, 0, 3, JA, false, 65536, (3 - JA % 3) % 3, S, 1024, 32768> G2A;
typedef Gemm<false, true, 16384, 8192, 0, 4, F / 64 - 2 * JA, false, 65536, 0, S, 128, 32768> G2;

DEVI unsigned get_tag() { return __builtin_amdgcn_readfirstlane(*(volatile LAS unsigned*)(uintptr_t)L_TAG); }
DEVI bool spin_fail(int spin, char* ws) {
  if ((spin & 255) != 255) return false;
  gu32* ab = (gu32*)(ws + WS_ABORT);
  const unsigned tag = get_tag();
  if (spin >= SPIN_MAX) { __hip_atomic_store(ab, tag, RLX_AGENT); return true; }
  return __builtin_amdgcn_readfirstlane(__hip_atomic_load(ab, RLX_AGENT)) == tag;
}
DEVI void sync_post(char* ws, int edge, int post, int wid) {
  unsigned o = post >= 0 && wid == 0 ? post * 4u : MB, one = 1u;
  const rsrc_t r = mkrsrc(ws + WS_MAIL + edge * (MEDGE * 4), MEDGE * 4);
  asm volatile("s_waitcnt vmcnt(0) lgkmcnt(0)" : "+v"(o), "+v"(one) : "s"(r) : "memory");
  sched0();
  bar();
  if (wid == 0) __builtin_amdgcn_raw_buffer_store_b32(one, r, o, 0, 16);
  sched0();
}
template <int DELAY = SW0> DEVI void sync_wait(char* ws, int edge, int line, int lane, unsigned all, unsigned first, int wid) {
  sched0();
  if (wid == 3) {
    gu32* pw = (gu32*)(ws + WS_MAIL) + edge * MEDGE + line * 64 + (lane & 31);
    if (DELAY) __builtin_amdgcn_s_sleep(DELAY);
    if (!STG) first = all;
    for (int spin = 0;; ++spin) {
      unsigned miss = ~(unsigned)__builtin_amdgcn_ballot_w64(__hip_atomic_load(pw, RLX_AGENT) != 0);
      if (spin_fail(spin, ws)) miss = 0;
      if (first && !(miss & first)) { bar(); first = 0; }
      if (!(miss & all)) break;
      __builtin_amdgcn_s_sleep(SW1);
    }
  } else bar();
}
DEVI unsigned halo_off(int s0, int lw, int lane) {
  const int c = lw * 32 + (lane & 31), row = c >> 3, ch = (c & 7) ^ ((row >> 1) & 7), tok = s0 - 16 + row;
  return (unsigned)((tok < 0 ? 0 : tok) * D * 2 + ch * 16);
}
DEVI int qkv_rot(int bid) { return (bid & 3) * 4 + (bid & 4) * 2; }
DEVI unsigned x_off(int tp, int tq, int w, int fr, int fq) { return (unsigned)(((tq * 64 + (w >> 1) * 32 + fr) * D + tp * 128 + (w & 1) * 64 + pair_chunk(fq) * 8) * 2); }

DEVI void pfb_q(const u16* w, int bid, int j, int n) {
  const int r = slow_res(w);
  if ((r >> 2) == (bid & 1)) {
    const unsigned o = (unsigned)(((bid & 2) * 4 + r) * 128 + (j >> 1) * (3 * D * 2) + (j & 1) * 64 + ((n + qkv_rot(bid)) & 15) * GQ::WSTEP);
    spf1<0>(w, o); spf1<2048>(w, o); spf1<4096>(w, o);
  }
}
constexpr int PFR = NPL * 128 * 1024, PF_END = TBA * 256 + NW1 * PFR / (F * 2) / 8 * 8;
static_assert(TBA * 256 * F * 2 + NW1 * PFR <= S * F * 2 && 3 * PFR >= D * D * 2, "");
struct Pfc {
  const char* p; const char* q; int pc;
  DEVI Pfc(const u16* wo, const u16* w1, unsigned g) : p((const char*)wo + g * (NPL * 128)), q((const char*)w1 + TBA * 256 * F * 2 + g * (NPL * 128)), pc(PC0) {}
  DEVI void go() {
    unsigned t, u;
    asm volatile("s_getreg_b32 %2, hwreg(HW_REG_IB_STS, 8, 4)\n\ts_cmp_lt_u32 %1, %5\n\ts_cselect_b32 %3, 0, 1\n\ts_or_b32 %2, %2, %3\n\ts_cbranch_scc1 1f\n\t"
                 "s_load_dwordx2 s[100:101], %0, 60\n\ts_load_dwordx2 s[100:101], %0, 188\n\ts_load_dwordx2 s[100:101], %0, 316\n\t"
                 "s_load_dwordx2 s[100:101], %0, 444\n\ts_load_dwordx2 s[100:101], %0, 572\n\ts_load_dwordx2 s[100:101], %0, 700\n\t"
                 ".if %8 == 7\n\ts_load_dwordx2 s[100:101], %0, 828\n\t.endif\n\t"
                 "s_mov_b32 %2, %7\n\ts_cmp_lt_u32 %1, 2\n\ts_cselect_b32 %2, %6, %2\n\ts_mov_b64 s[40:41], %0\n\ts_add_u32 s40, s40, %2\n\ts_addc_u32 s41, s41, 0\n\t"
                 "s_cmp_eq_u32 %1, 2\n\ts_cselect_b64 %0, %4, s[40:41]\n\ts_add_u32 %1, %1, 1\n1:"
                 : "+s"(p), "+s"(pc), "=&s"(t), "=&s"(u) : "s"(q), "i"(3 + NW1), "i"((D * D * 2 - PFR) / 2), "i"(PFR), "i"(NPL) : "s40", "s41", "s100", "s101", "scc");
  }
};
DEVI void spare_touch(const u16* w1, const u16* w2, int bid, int wid, int lane) {
  if (!SPON) return;
  const int j = ((bid >> 3) - 28) * 8 + wid, fr = lane & 15, fq = lane >> 4;
  const unsigned scr = L_QKV + wid * 256;
#pragma unroll 1
  for (int r = PF_END + (j * 2 + ((bid >> 2) & 1)) * 4; r < S; r += 256) { if (fr < 14) touch4(w1 + (size_t)r * F + (bid & 3) * 896, fq * (F * 2) + fr * 128, scr); wait_vm<SPW>(); }
#pragma unroll 1
  for (int r = (j * 2 + (bid & 1)) * 4; r < JA * 64; r += 256) { touch4(w2 + (size_t)(r + (bid & 2) * JA * 32) * S + (bid & 4) * 256, fq * (S * 2) + fr * 128, scr); wait_vm<SPW>(); }
#pragma unroll 1
  for (int r = JA * 128 + ((31 - j) * 2 + (bid & 1)) * 8; r < F; r += 512) { touch4(w2 + (size_t)r * S + ((bid & 7) >> 1) * 512, (lane >> 3) * (S * 2) + (lane & 7) * 128, scr); wait_vm<SPW>(); }
}

template <int TH> DEVI void qkv_attn(char* ws, int l, int h, int lane, int hp, int sb, int cw, int r0, int tp, int tq) {
  asm volatile("; half %1" : "+v"(lane) : "i"(TH));
  const int fr = lane & 15, fq = lane >> 4;
  GQ::Comp<OQ<TH>> g;
#pragma unroll
  for (int c = 0; c < 2; ++c) { g.prd[0][c] = 32768 + rd_km(lane, h * 2 + c); g.qrd[0][c] = 32768 + rd_kc(lane, c); }
  f32x4 acc[6][5];
#pragma unroll
  for (int i = 0; i < 6; ++i)
#pragma unroll
    for (int j = 0; j < 5; ++j) acc[i][j] = (f32x4){0.f, 0.f, 0.f, 0.f};
  const int par = fr & 1, da = fr - 4 * fq - par;
  const bool ca = da >= 0, cb = da >= 2;
  const unsigned tm = L_TAB + (16 + 64 * TH) * 4, tc = L_CS + h * 128;
  unsigned am = tm + fr * 4 - 320, a4 = tm + fq * 16 - 320, aa = tm + 576 + (4 * fq + par - (ca ? 0 : 16)) * 4, ab = tm + 576 + (4 * fq + par + 2 - (cb ? 0 : 16)) * 4;
  unsigned c4 = tc + fq * 16, c1 = tc + 512 + fr * 4, xb = L_QKV + h * 3072 + lane * 16, hm = par ? 0xffff0000u : 0xffffu, m0 = ca ? 0u : hm, m1 = cb ? 0u : hm;
  unsigned oo = ph((unsigned)((((sb * 2 + TH) * 16 + hp) << 13) + fr * 128 + (((h * 4 + pair_chunk(fq)) ^ (fr >> 1)) << 4)));
  asm volatile("" : "+v"(am), "+v"(a4), "+v"(aa), "+v"(ab), "+v"(c4), "+v"(c1), "+v"(xb), "+v"(hm), "+v"(m0), "+v"(m1), "+v"(oo));
  g.prep(acc);
  if (l) {
    const int sbc = (tq >> 1) + fq;
    sync_post(ws, 3, !TH && (lane < 16 || (lane < 32 && (tq & 1) && sbc < 16)) ? (sbc * 16 + fr) * 64 + (tq - 2 * sbc + 1) * 8 + tp : -1, TH * 2 + h);
  }
  if (l) sync_wait(ws, 3, sb * 16 + hp, lane, sb ? 0xffffffu : 0xffff00u, (sb ? 0x10101u : 0x10100u) << r0, TH * 2 + h);
  g.template run_steps<false>(acc, [&](int it, int i) { if (i == 0 && cw >= 0 && it == -2) code_warm(cw, lane, L_SCR + (TH * 2 + h) * 256); });
  const rsrc_t ro = mkrsrc(ws + WS_O, S * D * 4);
  if (l == 0) bar();
  float mt[5], cv[2], rq[4], ra[4], rb[4]; f32x4 m4[5], cq[2], ck[2];
#pragma unroll
  for (int c = 0; c < 2; ++c) {
    ck[c] = *(const LAS f32x4*)(uintptr_t)(c4 + 256 + c * 64); cv[c] = *(const LAS float*)(uintptr_t)(c1 + c * 64);
    cq[c] = *(const LAS f32x4*)(uintptr_t)(c4 + c * 64);
  }
#pragma unroll
  for (int j = 4; j >= 0; --j) {
    const int o = 320 + (j == 4 ? -64 - 256 * TH : j * 64);
    if (j < 4 || !TH) mt[j] = *(const LAS float*)(uintptr_t)(am + o);
    if (j < 4 || TH) m4[j] = *(const LAS f32x4*)(uintptr_t)(a4 + o);
  }
#pragma unroll
  for (int j = 0; j < 4; ++j) { rq[j] = *(const LAS float*)(uintptr_t)(am + 896 + j * 64); ra[j] = *(const LAS float*)(uintptr_t)(aa + j * 64); rb[j] = *(const LAS float*)(uintptr_t)(ab + j * 64); }
  u32x2 kp[2][5], qp[2][4], vp[2][5];
  auto fk = [&](int j) { kp[0][(j + 1) % 5] = pk4(acc[1][j] + ck[0] * mt[j]); kp[1][(j + 1) % 5] = pk4(acc[4][j] + ck[1] * mt[j]); };
  auto fv = [&](int j) { vp[0][(j + 1) % 5] = pk4(acc[2][j] + m4[j] * cv[0]); vp[1][(j + 1) % 5] = pk4(acc[5][j] + m4[j] * cv[1]); };
  auto fqq = [&](int j) { qp[0][j] = pk4(acc[0][j] + cq[0] * mt[j]); qp[1][j] = pk4(acc[3][j] + cq[1] * mt[j]); };
  g.template pend<9, 13>(acc);
#pragma unroll
  for (int i = 0; i < 8; ++i) { __builtin_amdgcn_sched_group_barrier(0x100, 3, 0); __builtin_amdgcn_sched_group_barrier(0x8, 1, 0); }
  sched0();
  volatile LAS unsigned* xf = (volatile LAS unsigned*)(uintptr_t)(L_TAG + 16 + (h * 2 + TH) * 4);
  g.template pend<6, 9>(acc);
  if (TH) { fv(4); *(volatile LAS bf16x8*)(uintptr_t)(xb + 2048) = cat(vp[0][0], vp[1][0]); *xf = l + 1; fk(3); }
  else { fk(3); fv(3); *(volatile LAS bf16x8*)(uintptr_t)xb = cat(kp[0][4], kp[1][4]); *(volatile LAS bf16x8*)(uintptr_t)(xb + 1024) = cat(vp[0][4], vp[1][4]); *xf = l + 1; }
#pragma unroll
  for (int i = 0; i < 6; ++i) { __builtin_amdgcn_sched_group_barrier(0x8, 1, 0); __builtin_amdgcn_sched_group_barrier(0x2, 3, 0); }
  sched0();
  g.template pend<3, 6>(acc); g.template pend<0, 3>(acc);
  fqq(3); fk(2); fqq(2); fk(1);
#pragma unroll
  for (int i = 0; i < 6; ++i) { __builtin_amdgcn_sched_group_barrier(0x8, 1, 0); __builtin_amdgcn_sched_group_barrier(0x2, 2, 0); __builtin_amdgcn_sched_group_barrier(0x8, 1, 0); __builtin_amdgcn_sched_group_barrier(0x2, 3, 0); }
  sched0();
  const f32x4 z = {0.f, 0.f, 0.f, 0.f};
  f32x4 sc[4][2]; float a[4], b[4], m[4];
  auto scores = [&](int j) {
#pragma unroll
    for (int u = 1; u >= 0; --u) sc[j][u] = mfma(cat(kp[0][j + u], kp[1][j + u]), cat(qp[0][j], qp[1][j]), z);
  };
  auto pick = [&](int j) {
    const float r = rq[j] * (0.17677669529663687f * 1.4426950408889634f);
    const f32x4 s0 = sc[j][0], s1 = sc[j][1];
    a[j] = sel(par, sel(ca, s1[1], s0[1]), sel(ca, s1[0], s0[0])) * (r * ra[j]);
    b[j] = sel(par, sel(cb, s1[3], s0[3]), sel(cb, s1[2], s0[2])) * (r * rb[j]);
    if (!TH && j == 0) { a[j] = sel(ca || sb, a[j], -1e9f * 1.4426950408889634f); b[j] = sel(cb || sb, b[j], -1e9f * 1.4426950408889634f); }
    m[j] = __builtin_fmaxf(a[j], b[j]);
  };
  auto expo = [&](int j) { a[j] = __builtin_amdgcn_exp2f(a[j] - m[j]); b[j] = __builtin_amdgcn_exp2f(b[j] - m[j]); m[j] = a[j] + b[j]; };
  auto pv = [&](int j) {
    const unsigned pa = pk2(a[j] * ra[j], a[j] * ra[j]), pb = pk2(b[j] * rb[j], b[j] * rb[j]);
    const bf16x8 pf = __builtin_bit_cast(bf16x8, (u32x4){pa & m0, pb & m1, pa & (m0 ^ hm), pb & (m1 ^ hm)});
#pragma unroll
    for (int c = 0; c < 2; ++c) sc[j][c] = mfma(cat(vp[c][j], vp[c][j + 1]), pf, z);
  };
  auto outp = [&](int j) { st16(ro, oo + j * 4096, pack_pair(sc[j][0] * m[j], sc[j][1] * m[j])); };
  auto soft2 = [&](int j) {
    float u, w;
    pick(j); pick(j + 1);
    swp32(m[j], m[j + 1]); u = vmax(m[j], m[j + 1]); xch16(u, u, w); u = vmax(u, w); xch32(u, m[j], m[j + 1]);
    expo(j); expo(j + 1);
    pv(j + 1); pv(j);
    swp32(m[j], m[j + 1]); u = m[j] + m[j + 1]; xch16(u, u, w); u = __builtin_amdgcn_rcpf(u + w); xch32(u, m[j], m[j + 1]);
    outp(j + 1); outp(j);
  };
  unsigned f; u32x4 xv, xk;
  auto xrd = [&]() {
    f = xf[1 - 2 * TH]; xv = *(volatile LAS u32x4*)(uintptr_t)(xb + (TH ? 1024 : 2048));
    if (TH) xk = *(volatile LAS u32x4*)(uintptr_t)xb;
  };
  auto xget = [&]() {
    sched0();
    while (__builtin_expect(__builtin_amdgcn_readfirstlane(f) != l + 1, 0)) xrd();
    vp[0][0] = (u32x2){xv[0], xv[1]}; vp[1][0] = (u32x2){xv[2], xv[3]};
    if (TH) { kp[0][0] = (u32x2){xk[0], xk[1]}; kp[1][0] = (u32x2){xk[2], xk[3]}; }
  };
  if (ATT4) {
    scores(3); scores(2);
    fqq(1);
    xrd();
    sched0();
    fk(0); fqq(0);
    if (TH) fv(3); else fk(4);
    scores(1);
    fv(2); fv(1); fv(0);
    pick(3); pick(2);
#pragma unroll
    for (int c = 0; c < 2; ++c) asm volatile("" : "+v"(qp[c][0]), "+v"(kp[c][1]), "+v"(vp[c][1]), "+v"(vp[c][2]), "+v"(vp[c][3]), "+v"(vp[c][4]), "+v"(sc[1][c]), "+v"(a[2 + c]), "+v"(b[2 + c]), "+v"(m[2 + c]));
    if (!TH) asm volatile("" : "+v"(kp[0][0]), "+v"(kp[1][0]));
    xget();
    scores(0);
    pick(1); pick(0);
    float u, w;
    swp32(m[0], m[2]); swp32(m[1], m[3]); u = m[0]; w = m[1];
    asm volatile("v_max_f32 %0, %0, %2\n\tv_max_f32 %1, %1, %3\n\ts_nop 1" : "+v"(u), "+v"(w) : "v"(m[2]), "v"(m[3]));
    swp16(u, w); u = vmax(u, w);
    xch16(u, u, w); xch32(u, m[0], m[2]); xch32(w, m[1], m[3]);
    expo(3); expo(2); expo(1); expo(0);
    pv(3); pv(2); pv(1); pv(0);
    swp32(m[0], m[2]); swp32(m[1], m[3]); u = m[0] + m[2]; w = m[1] + m[3];
    swp16(u, w); u = __builtin_amdgcn_rcpf(u + w);
    xch16(u, u, w); xch32(u, m[0], m[2]); xch32(w, m[1], m[3]);
    outp(3); outp(2); outp(1); outp(0);
  } else {
    scores(3); scores(2);
    sched0();
    if (TH) fv(3); else fk(4);
    fv(2);
    xrd();
    sched0();
    fv(1); fqq(1); fk(0); fqq(0);
    soft2(2);
    fv(0);
#pragma unroll
    for (int c = 0; c < 2; ++c) asm volatile("" : "+v"(qp[c][0]), "+v"(qp[c][1]), "+v"(kp[c][1]), "+v"(vp[c][1]));
    xget();
    scores(1); scores(0);
    soft2(0);
  }
}

extern "C" __global__ __launch_bounds__(512, 2) void mega_kernel(const u16* x0, const u16* wqkv, const u16* wo, const u16* w1, const u16* w2, const u16* b1, const u16* b2, u16* out, char* ws) {
  const int wid0 = __builtin_amdgcn_readfirstlane(threadIdx.x >> 6);
  asm volatile("s_getpc_b64 s[40:41]\n\ts_andn2_b32 s40, s40, 63\n\ts_load_dwordx2 s[100:101], s[40:41], %0 offset:124\n\ts_load_dwordx2 s[100:101], s[40:41], %0 offset:252"
               :: "s"(__builtin_amdgcn_readfirstlane(threadIdx.x) << 2) : "s40", "s41", "s100", "s101", "scc");
  sched0();
#define PHASE_VARS \
      int tid = threadIdx.x, bid = blockIdx.x, wid = wid0; \
      asm volatile("" : "+v"(tid)); asm volatile("" : "+s"(bid), "+s"(wid), "+s"(ws)); \
      const int lw = wid - 4, lt = tid - 256, lane = tid & 63, fr = lane & 15, fq = lane >> 4, wp = wid & 1, wq = wid >> 1; \
      const unsigned j = (bid >> 3) * 4 + lw; \
      int hp, sb; tile_map<16, 16, 4, 8>(bid, hp, sb);            \
      int tp, tq; tile_map<8, 32, 4, 8>(bid, tp, tq);             \
      int fp, fq1; tile_map<28, 8, 7, 4>(bid, fp, fq1);           \
      const int cw = bid < 16 ? wid + 4 * (bid >> 3) : -1; \
      (void)cw; (void)j; (void)lt; (void)wp; (void)wq; (void)lw; (void)fr; (void)fq; (void)hp; (void)sb; (void)tp; (void)tq; (void)fp; (void)fq1;
#define LOADER_VARS PHASE_VARS asm volatile("" : "+s"(wqkv), "+s"(wo), "+s"(w1), "+s"(w2));

  if (__builtin_expect(wid0 >= 4, 1)) {
    const u16* xin = x0;
    Pfc pf(wo, w1, blockIdx.x * 4 + wid0 - 4);
#pragma unroll 1
    for (int l = 0; l < NL; ++l) {
      {
        LOADER_VARS
        const int s0 = sb * 128;
        GQ::Loader L(wqkv + hp * 64, xin, lw, lane, qkv_rot(bid));
        if (l == 0) L.pre_w1<0>();
        L.seta((unsigned)(s0 * D * 2), halo_off(s0, lw, lane), lw, lane);
        if (l == 0) { L.issue_a<0>(L.ab + L.kord(0) * 128); L.pre<1, true, true>(); }
        sched0();
        const rsrc_t rs = mkrsrc(ws + WS_ST, MB);
        f32x4 c = {0.f, 0.f, 0.f, 0.f}; f32x2 s1 = {0.f, 0.f}, s2 = {0.f, 0.f}, o = {0.f, 0.f}; float pv = 0.f;
        bf16x8 ones = {0x3F80, 0x3F80, 0x3F80, 0x3F80, 0x3F80, 0x3F80, 0x3F80, 0x3F80};
        const int ct = lw & 1 ? 8 + (sb & 3) : sb & 7; const bool cs = l == 0 && lw >= 2;
        int tpf = (slow_res(wqkv) >> 2) != (bid & 1) ? 0 : lw < 2 ? 10 : 9;
        unsigned ra = GQ::AOFF + ((hp & 3) * 32 + lw * 8) * 128 + lane * 16, rb = rd_km(lane, ct);
        unsigned so = lane & 7 ? MB : ph((unsigned)(((l * S + s0 + (hp & 3) * 32 + lw * 8 + (lane >> 3)) * 4 + (hp >> 2)) * 16));
        unsigned co = fr >= 2 || !cs ? MB : ph((unsigned)(ST_CS + ((hp * 96 + ct * 8 + fq * 2 + fr) * 2 + (sb >> 3)) * 16));
        unsigned to[3], xo = x_off(tp, tq, lw, fr, fq), q2 = ((bid & 2) * 4 + slow_res(wqkv)) * 128 + ((bid >> 3) * 2 + (lw & 1)) * (3 * D * 2);
#pragma unroll
        for (int i = 0; i < 3; ++i) {
          const int r = s0 - 16 + (i == 2 && lw ? 64 : i * 64) + (lt >> 2);
          to[i] = ph((unsigned)(((l * S + (r < 0 ? 0 : r)) * 4 + (lt & 3)) * 16));
        }
        unsigned ta = (lt & 3) < (lw ? 2 : 3) ? L_TAB + ((lt & 3) * 64 + (lt >> 2)) * 4 : L_SCR, ts = ph((unsigned)(ST_CS + (hp * 192 + (lw ? lt - 64 : lt)) * 16));
        asm volatile("" : "+v"(ra), "+v"(rb), "+v"(so), "+v"(co), "+v"(to[0]), "+v"(to[1]), "+v"(to[2]), "+v"(xo), "+v"(ta), "+v"(ts), "+v"(c), "+v"(s1), "+v"(s2), "+v"(ones), "+v"(o));
        asm volatile("" : "+s"(q2), "+s"(tpf));
        const GW::Loader W(wo + tp * 128, ws, lw, lane);
        auto pfq = [&](int n) { const unsigned o = q2 + L.kord(n) * GQ::WSTEP; spf2<0>(wqkv, o); spf2<2048>(wqkv, o); spf2<4096>(wqkv, o); };
        if (l) {
          if (QBR == 3) bar();
          if (!(EW & 8)) L.pre_w();
          if (tpf) { pfq(2 + (lw >> 1)); pfq(4 + (lw >> 1)); }
          if (QBR == 4) bar();
          L.start();
        }
        unsigned tag, tg2; u32x4 gs, a[3];
        auto rowst = [&](int i) {
          const float m0 = __uint_as_float(a[i][0]), m = red4(m0) * 0.25f, q = red4(__uint_as_float(a[i][2]) + 256.f * (m0 - m) * (m0 - m));
          o = (f32x2){sel((lt & 3) == i, m, o.x), sel((lt & 3) == i, q, o.y)};
          asm volatile("" : "+v"(o));
        };
        for_c<0, 16 - GQ::H>([&](auto tc) {
          constexpr int t = tc.value;
          u32x4 v; unsigned tv;
          L.step<t, t == 5 || t == 6 ? 1 : t == 9 || t == 10 ? 4 : 0>([&](unsigned ls) {
            if (t < 4) v = *(const LAS u32x4*)(uintptr_t)(ls + ra);
            if (t == 3) tv = *(volatile LAS unsigned*)(uintptr_t)L_TAG;
          }, [&]() {
            if (t < 8 && cs) {
              constexpr int ls = 32768 + t % 3 * GQ::SB;
              bf16x4 x[4];
#pragma unroll
              for (int e = 0; e < 4; ++e) x[e] = lds_tr4(rb + (ls & ~0x7fff) + (ls & 0x7fff) + (e >> 1) * 4096 + (e & 1) * 512);
#pragma unroll
              for (int ks = 0; ks < 2; ++ks) c = mfma(__builtin_shufflevector(x[2 * ks], x[2 * ks + 1], 0, 1, 2, 3, 4, 5, 6, 7), ones, c);
            } else if (t >= 4) {
              if (t >= 10 || t >= tpf) { if (t < TC1) pf.go(); }
              else if (lw < 2) pfq(t + 4);
              else if (t == 8) { pfq(14); pfq(15); }
            }
            if (t < 4) {
              if (t == 0) pv = bflo(v[0]);
#pragma unroll
              for (int e = 0; e < 4; ++e) { const f32x2 d = (f32x2){bflo(v[e]), bfhi(v[e])} - pv; s1 += d; s2 += d * d; }
              asm volatile("" : "+v"(s1), "+v"(s2));
            }
            if (t == 3) { tag = __builtin_amdgcn_readfirstlane(tv); tg2 = tag2(tag); if (tpf == 10) pfq(7); if (l && tpf == 9) pfq(6); }
            if (t == 4) {
              const float d1 = (s1.x + s1.y) * (1.f / 32.f), ml = pv + d1, m = red8(ml) * 0.125f;
              const float q = red8(vmax((s2.x + s2.y) - 32.f * d1 * d1, 0.f) + 32.f * (ml - m) * (ml - m));
              gs = (u32x4){__float_as_uint(m), tag, __float_as_uint(q), tg2};
              asm volatile("" : "+v"(gs));
            }
            if (t == 8) { gs = (u32x4){__float_as_uint(sel(fr, c[2], c[0])), tag, __float_as_uint(sel(fr, c[3], c[1])), tg2}; asm volatile("" : "+v"(gs)); }
            if (t == 11) {
#pragma unroll
              for (int i = 0; i < 3; ++i) ld_ok(a[i]);
              for (int spin = 0;; ++spin) {
                unsigned bad = 0;
#pragma unroll
                for (int i = 0; i < 3; ++i) bad |= (a[i][1] ^ tag) | (a[i][3] ^ tg2);
                if (__builtin_expect(__all(bad == 0), 1) || spin_fail(spin, ws)) break;
                __builtin_amdgcn_s_sleep(8);
#pragma unroll
                for (int i = 0; i < 3; ++i) ld_again(a[i], rs, to[i]);
              }
              rowst(0);
            }
            if (t == 12) { rowst(1); rowst(2); }
            if (t == 13) { LAS float* p = (LAS float*)(uintptr_t)ta; p[0] = -o.x; p[144] = __builtin_amdgcn_rsqf(o.y * (1.f / D) + EPS); }
          }, [&]() {
            if (t == 4) st16(rs, so, gs);
            if (t == 8) {
              st16(rs, co, gs);
              sched0();
#pragma unroll
              for (int i = 0; i < 3; ++i) ld_raw(a[i], rs, to[i]);
            }
          });
        });
        if (l == 0) {
#pragma unroll
          for (int p = 0; p < 4; ++p) dma16((const char*)xin + (p >> 1) * 32768 + (p & 1) * 64, xo, L_X + lw * 4096 + p * 1024);
          ld_raw(gs, rs, ts);
          wait_vm<GQ::CNT + 5>();
        } else wait_vm<GQ::CNT>();
        bar();
        W.pre_w1<0>();
        wait_lgkm0();
        if (l == 0) wait_vm<9>(); else wait_vm<4>();
        bar();
        W.pre_w1<1>(); W.pre_w1<2>();
        if (l == 0) {
          wait_vm<12>();
          ld_ok(gs);
          for (int spin = 0;; ++spin) {
            if (__builtin_expect(__all(((gs[1] ^ tag) | (gs[3] ^ tg2)) == 0), 1) || spin_fail(spin, ws)) break;
            __builtin_amdgcn_s_sleep(8);
            ld_again(gs, rs, ts);
          }
          float c0 = __uint_as_float(gs[0]), c1 = __uint_as_float(gs[2]);
          c0 += dppf<0xB1>(c0); c1 += dppf<0xB1>(c1);
          *(LAS float*)(uintptr_t)(L_CS + (lw ? lt - 64 : lt) * 4) = sel(lt & 1, c1, c0);
        }
        if (l == 0 && lw < TBA) { if (fr < 14) touch4(w1 + (size_t)(((bid >> 3) * TBA + lw) * 8 + (bid & 4)) * F + (bid & 3) * 896, fq * (F * 2) + fr * 128, L_SCR + lw * 256); }
        if (l == 0) { wait_lgkm0(); bar(); }
      }
      {
        LOADER_VARS
        GW::Loader L(wo + tp * 128, ws + WS_O + (size_t)tq * 32 * 8192, lw, lane);
        const G1::Loader W(w1 + fp * 128, ws, lw, lane);
        unsigned po = ((bid & 1) * 8 + (slow_res(wo) ^ (j & 1))) * 128 + (j >> 1) * (D * 2);
#pragma unroll
        for (int k = GW::H; k < 10; ++k) spf2(wo, po + k * GW::WSTEP);
        asm volatile("" : "+s"(po));
        sched0();
        bar();
#pragma unroll
        for (int k = 10; k < 16; ++k) spf2(wo, po + k * GW::WSTEP);
        L.start();
        L.run(16 - GW::H, [&](int) { pf.go(); });
        pf.go();
        if ((EW & 2) && bid < 224) {
          wait_vm<2 * GW::CNT>(); bar(); W.pre_w1<1>();
          wait_vm<GW::CNT + 4>(); bar(); W.pre_w1<2>();
          wait_vm<8>(); bar();
          bar(); W.pre_w1<0>();
        } else { L.tail<GW::H - 1>(); bar(); }
      }
      {
        LOADER_VARS
        if (bid < 224) {
          G1::Loader L(w1 + fp * 128, ws + WS_H + (size_t)fq1 * 64 * 16384, lw, lane);
          if (!(EW & 2)) L.pre_w();
          const G2A::Loader W((const char*)(w2 + (tq & 23) * 64) + (tq & 8 ? JA * G2::WSTEP : 0), ws, lw, lane);
          const int g0 = (bid & 3) * 14, ga = (g0 + ((slow_res(w1) - g0) & 7)) ^ (j & 1), gb = ga + 8 < g0 + 14 ? ga + 8 : ga;
          const unsigned j2 = j < 32 ? 112 + (j & 15) : j;
          unsigned pa = (j >> 1) * (F * 2) + ga * 128, pb = (j >> 1) * (F * 2) + gb * 128, pd = (j2 >> 1) * (F * 2) + (j & 16 ? gb : ga) * 128;
          asm volatile("" : "+s"(pa), "+s"(pb), "+s"(pd));
          auto pfb = [&](int k) { spf2(w1, pa + k * G1::WSTEP); spf2(w1, pb + k * G1::WSTEP); spf2(w1, pd + k * G1::WSTEP); };
#pragma unroll
          for (int k = G1::H; k < G1::H + PA_1; ++k) pfb(k);
          wait_lgkm0(); pf.go();
          sched0();
          bar();
          pf.go();
          L.start();
          L.run(32 - G1::H, [&](int t) {
            if (pf.pc < 3 + NW1) pf.go();
            else if (t < 32 - G1::H - PA_1) pfb(t + G1::H + PA_1);
          });
          if (EW & 4) {
            wait_vm<2 * G1::CNT>(); bar(); W.pre_w1<1>();
            wait_vm<G1::CNT + 4>(); bar();
            wait_vm<4>(); bar(); W.pre_w1<0>();
          } else L.tail<G1::H - 1>();
          bar();
        } else {
          wait_lgkm0(); pf.go();
          bar();
          if (l == 0) {
            __builtin_amdgcn_s_sleep(24);
            while (pf.pc < 3 + NW1) { wait_lgkm0(); pf.go(); }
            spare_touch(w1, w2, bid, wid, lane);
          }
        }
        pf.pc = 99;
      }
      {
        LOADER_VARS
        constexpr int NB = F / 64 - 2 * JA;
        const char* ub = ws + WS_U + (size_t)tp * 112 * 16384;
        G2A::Loader A((const char*)(w2 + (tq & 23) * 64) + (tq & 8 ? JA * G2::WSTEP : 0), ub + (tq & 8 ? JA * 32768 : 0), lw, lane);
        if (!(EW & 4) || bid >= 224) A.pre_w();
        const GQ::Loader W(wqkv + hp * 64, ws, lw, lane, qkv_rot(bid));
        unsigned pb = ((bid & 4) * 4 + (j & 1) * 8 + slow_res(w2)) * 128 + (j >> 1) * (S * 2) + JA * G2::WSTEP, pa = pb - (bid & 2 ? 0 : JA * G2::WSTEP);
        asm volatile("" : "+s"(pa), "+s"(pb));
        auto side = [&](int n) { n += G2A::H + PA_2; if (n < JA + NB) spf2(w2, (n < JA ? pa : pb) + n * G2::WSTEP); };
#pragma unroll
        for (int k = 0; k < PA_2; ++k) side(k - PA_2);
        G2::Loader L((const char*)(w2 + tq * 64) + 2 * JA * G2::WSTEP, ub + 2 * JA * 32768, lw, lane);
        gu32* xf = (gu32*)(ws + WS_MAIL) + (l * 4 + 2) * MEDGE + (tq * 8 + tp) * 64 + 28 + lw;
        const char* xp = ws + WS_XP + (unsigned)(((bid ^ 2) * 4 + lw) << 14);
        unsigned fl = L_QKV + lw * 256, xv = ph(lane * 16);
        asm volatile("" : "+s"(xf), "+s"(xp), "+s"(fl)); asm volatile("" : "+v"(xv));
        sched0();
        bar();
        A.start();
        A.run(JA - G2A::H, [&](int t) { side(t); });
        auto stage = [&](auto s) { L.issue_w<s.value * G2::SB>(L.w + s.value * G2::WSTEP); L.issue_a<s.value * G2::SB>(L.ab + s.value * 32768); sched0(); };
        wait_vm<G2A::CNT>(); bar(); stage(IC<0>{}); side(JA - 2);
        wait_vm<G2::CNT>(); bar(); stage(IC<1>{}); side(JA - 1);
        wait_vm<G2::CNT>(); bar(); stage(IC<2>{}); stage(IC<3>{}); side(JA);
        L.t = 1; L.islot = 0;
        L.run(NB - G2::H, [&](int t) {
          if (t == NB - G2::H - FLS) asm volatile("s_mov_b32 m0, %2\n\ts_nop 0\n\tglobal_load_lds_dword %0, %1 sc1" :: "v"(0u), "s"(xf), "s"(fl) : "memory");
          side(JA + t);
        });
        auto xdma = [&]() {
#pragma unroll
          for (int i = 0; i < 4; ++i) dma16(xp + i * 2048, xv, L_X + lw * 4096 + i * 1024);
          sched0();
        };
        sched0(); wait_vm<2 * G2::CNT>(); bar();
        if (__builtin_amdgcn_readfirstlane(*(volatile LAS unsigned*)(uintptr_t)fl)) {
          xdma();
          if ((EW & 8) && l == 0) { W.pre_w1<0>(); wait_vm<G2::CNT + 10>(); bar(); W.pre_w1<1>(); wait_vm<16>(); bar(); wait_vm<12>(); }
          else { wait_vm<G2::CNT + 4>(); bar(); wait_vm<4>(); bar(); wait_vm<0>(); }
        } else {
          wait_vm<G2::CNT>(); bar(); wait_vm<0>(); bar();
          for (int spin = 0;; ++spin) {
            if (__builtin_amdgcn_readfirstlane(__hip_atomic_load(xf, RLX_AGENT)) || spin_fail(spin, ws)) break;
            __builtin_amdgcn_s_sleep(2);
          }
          xdma();
          if ((EW & 8) && l == 0) { W.pre_w(); wait_vm<12>(); } else wait_vm<0>();
        }
        bar();
        xin = (const u16*)(ws + WS_X2);
        if (NL == 1) bar();
      }
    }
    return;
  }

  {
    PHASE_VARS
    if (cw >= 0) code_warm(cw, lane, L_SCR + wid * 256);
    pfb_q(wqkv, bid, (bid >> 3) * 4 + wid, 0); pfb_q(wqkv, bid, (bid >> 3) * 4 + wid, 1);
    if (wid == 3) {
      if ((slow_res(wqkv) >> 2) == (bid & 1)) {
        __builtin_amdgcn_s_sleep(ET);
        if ((lane & 3) < 3 && lane < 40) touch4(wqkv, (unsigned)(((qkv_rot(bid) * 64 + 128 + (bid >> 3) * 10 + (lane >> 2)) & 1023) * (3 * D * 2) + (lane & 3) * (D * 2) + ((bid & 2) * 4 + slow_res(wqkv)) * 128), L_SCR + 768);
      }
    } else {
      const unsigned ep = __hip_atomic_load((gu32*)(ws + WS_EPOCH), RLX_AGENT);
      const rsrc_t rz = mkrsrc(ws + WS_XCH, 3 * MB);
      if (tid < 128) st16(rz, ph((((tid >> 6) * 256 + tq * 8 + tp) * 64 + lane) * 16), (u32x4){0u, SENT, 0u, SENT});
      else {
        const int e = (tid >> 3) & 7, line = e == 0 || e == 7 ? (2 * sb + (e & 1)) * 8 + (hp & 7) : e * 256 + bid;
        st16(rz, WS_MAIL - WS_XCH + line * 256 + (tid & 7) * 16, (u32x4){0u, 0u, 0u, 0u});
      }
      if (wid == 1 && bid < 224) touch4(b1 + fp * 128, lane * 4, L_BIAS);
      if (wid == 2) touch4(b2 + tq * 64, (lane & 31) * 4, L_BIAS + 256);
      if (wid == 0) { *(LAS unsigned*)(uintptr_t)L_TAG = ep + 1u; *(LAS u32x4*)(uintptr_t)(L_TAG + 16) = (u32x4){0u, 0u, 0u, 0u}; }
      asm volatile("s_waitcnt vmcnt(0)" :: "v"(ep) : "memory");
    }
  }
#pragma unroll 1
  for (int l = 0; l < NL; ++l) {
    {
      PHASE_VARS
      if (wid & 2) qkv_attn<1>(ws, l, wp, lane, hp, sb, cw, (qkv_rot(bid) & 15) >> 1, tp, tq);
      else qkv_attn<0>(ws, l, wp, lane, hp, sb, cw, 0, tp, tq);
    }
    {
      PHASE_VARS
      if (WSP == 0) sync_post(ws, l * 4, lane < 16 ? (sb * 16 + lane) * 64 + hp : -1, wid);
      GW::Comp<O42<false, WNE>> g; g.setup(wp, wq, lane);
      f32x4 acc[4][2];
#pragma unroll
      for (int i = 0; i < 4; ++i)
#pragma unroll
        for (int jj = 0; jj < 2; ++jj) acc[i][jj] = (f32x4){0.f, 0.f, 0.f, 0.f};
      unsigned xa = L_X + wid * 4096 + lane * 16;
      { bf16x8 s[2]; sel_frags(true, fr, fq, s); tile_add(acc, s, xa); }
      if (WSP == 1) {
#pragma unroll
        for (int i = 0; i < 4; ++i) asm volatile("" : "+v"(acc[i][0]), "+v"(acc[i][1]));
        sync_post(ws, l * 4, lane < 16 ? (sb * 16 + lane) * 64 + hp : -1, wid);
      }
      const rsrc_t rx = mkrsrc(ws + WS_XCH + l * (MB / 2), MB / 2), rh = mkrsrc(ws + WS_H, S * D * 4);
      unsigned xs = fq & 1 ? MB : ph((unsigned)(((tq * 8 + tp) * 64 + wp * 32 + wq * 16 + fr) * 16 + fq * 4)), xl = ph((unsigned)((tq * 512 + fq * 128 + wq * 16 + fr) * 16));
      const unsigned h0 = ph((unsigned)(((tp * 32 + tq) << 14) + wp * 8192 + (wq * 32 + fr) * 128 + ((pair_chunk(fq) ^ (km_f(fr) << 1)) << 4)));
      u32x4 ho = {h0, h0 ^ 64, h0 + 4096, (h0 ^ 64) + 4096};
      bf16x8 ones = {0x3F80, 0x3F80, 0x3F80, 0x3F80, 0x3F80, 0x3F80, 0x3F80, 0x3F80};
      asm volatile("" : "+v"(xa), "+v"(xs), "+v"(xl), "+v"(ho), "+v"(ones));
      g.prep(acc);
      if (WSP == 2) sync_post(ws, l * 4, lane < 16 ? (sb * 16 + lane) * 64 + hp : -1, wid);
      sync_wait<WSP == 2 ? SW0 : SWW>(ws, l * 4, tq * 8 + tp, lane, 0xffffu, (1u << GW::H) - 1, wid);
      g.run_steps<false>(acc, [&](int it, int i) { if (i == 0 && cw >= 0 && it == -2) code_warm(cw, lane, L_SCR + wid * 256); });
      bar();
      sched0();
      g.pend<0, 4>(acc);
      u32x4 pk[2][2]; float mp[2], q[2];
#pragma unroll
      for (int jj = 0; jj < 2; ++jj) {
        f32x4 sm = {0.f, 0.f, 0.f, 0.f};
#pragma unroll
        for (int i = 0; i < 2; ++i) { pk[jj][i] = cat4(pk4(acc[2 * i][jj]), pk4(acc[2 * i + 1][jj])); asm volatile("" : "+v"(pk[jj][i])); sm = mfma(ones, __builtin_bit_cast(bf16x8, pk[jj][i]), sm); }
        mp[jj] = sm[0] * (1.f / 64.f); asm volatile("" : "+v"(mp[jj]));
      }
#pragma unroll
      for (int jj = 0; jj < 2; ++jj) {
        f32x4 qv = {0.f, 0.f, 0.f, 0.f};
#pragma unroll
        for (int i = 0; i < 4; ++i) {
          const unsigned x = pk[jj][i >> 1][(i & 1) * 2], y = pk[jj][i >> 1][(i & 1) * 2 + 1];
          acc[i][jj] = (f32x4){bflo(x), bfhi(x), bflo(y), bfhi(y)} - mp[jj]; qv += acc[i][jj] * acc[i][jj];
        }
        q[jj] = (qv[0] + qv[2]) + (qv[1] + qv[3]);
      }
      {
        float u, w;
        swp32(q[0], q[1]); xch16(q[0] + q[1], u, w);
        __builtin_amdgcn_raw_buffer_store_b64((u32x2){__float_as_uint(sel(fq < 2, mp[0], mp[1])), __float_as_uint(u + w)}, rx, xs, 0, 16);
      }
      sched0();
#pragma unroll
      for (int p = 0; p < 4; ++p) *(LAS u32x4*)(uintptr_t)(xa + p * 1024) = pk[p >> 1][p & 1];
      if (XD) __builtin_amdgcn_s_sleep(XD);
      f32x4 gr[4];
      for (int spin = 0;; ++spin) {
        asm volatile("" ::: "memory");
#pragma unroll
        for (int p = 0; p < 4; ++p) gr[p] = __builtin_bit_cast(f32x4, __builtin_amdgcn_raw_buffer_load_b128(rx, xl + p * 1024, 0, 16));
        const float lo = vmin3(vmin3(gr[0][1], gr[0][3], gr[1][1]), vmin3(gr[1][3], gr[2][1], gr[2][3]), vmin3(gr[3][1], gr[3][3], gr[3][3]));
        if (!__any(lo < 0.f) || spin_fail(spin, ws)) break;
        __builtin_amdgcn_s_sleep(XS);
      }
      {
        const f32x4 t = (gr[0] + gr[1]) + (gr[2] + gr[3]);
        float m[2], u, w, v, y;
#pragma unroll
        for (int jj = 0; jj < 2; ++jj) {
          m[jj] = t[2 * jj] * 0.25f;
          float e = 0.f;
#pragma unroll
          for (int p = 0; p < 4; ++p) { const float d = gr[p][2 * jj] - m[jj]; e += d * d; }
          q[jj] = t[2 * jj + 1] + 64.f * e;
        }
        swp32(m[0], m[1]); swp32(q[0], q[1]);
        xch16((m[0] + m[1]) * 0.5f, u, w); xch16((q[0] + q[1]) + 128.f * (m[0] - m[1]) * (m[0] - m[1]), v, y);
        const float var = ((v + y) + 256.f * (u - w) * (u - w)) * (1.f / D), r1 = __builtin_amdgcn_rsqf(var + EPS);
        xch32((u + w) * 0.5f, m[0], m[1]);
        xch32(r1 * __builtin_amdgcn_rsqf(var * r1 * r1 + EPS), q[0], q[1]);
#pragma unroll
        for (int jj = 0; jj < 2; ++jj) {
          const float c = (mp[jj] - m[jj]) * q[jj];
#pragma unroll
          for (int i = 0; i < 2; ++i) st16(rh, ho[jj * 2 + i], pack_pair(acc[2 * i][jj] * q[jj] + c, acc[2 * i + 1][jj] * q[jj] + c));
        }
      }
    }
    {
      PHASE_VARS
      const int post = lane < 28 ? (tp * 28 + lane) * 64 + tq : -1;
      if (bid >= 224) {
        sync_post(ws, l * 4 + 1, post, wid);
        if (l == 0 && bid == 255 && wid == 0) { const unsigned tag = get_tag(); if (lane == 0) __hip_atomic_store((gu32*)(ws + WS_EPOCH), tag, RLX_AGENT); }
        if (l == 0) { __builtin_amdgcn_s_sleep(24); spare_touch(w1, w2, bid, wid, lane); }
      } else {
        G1::Comp<O44<false, 32>> g; g.setup(wp, wq, lane);
        f32x4 acc[4][4];
#pragma unroll
        for (int i = 0; i < 4; ++i) { const u32x2 b = *(const LAS u32x2*)(uintptr_t)(L_BIAS + (wp * 4 + i) * 32 + fq * 8);
#pragma unroll
          for (int jj = 0; jj < 4; ++jj) acc[i][jj] = (f32x4){bflo(b.x), bfhi(b.x), bflo(b.y), bfhi(b.y)}; }
        const rsrc_t ro = mkrsrc(ws + WS_U, D * F * 4);
        const unsigned u0 = ph((unsigned)(((fq1 * 56 + fp * 2 + wp) << 14) + (wq * 64 + fr) * 128 + ((pair_chunk(fq) ^ (fr >> 1)) << 4)));
        u32x4 uo = {u0, u0 + 4096, u0 + 8192, u0 + 12288}, uo2 = uo ^ 64;
        asm volatile("" : "+v"(uo), "+v"(uo2));
        g.prep(acc);
        sync_post(ws, l * 4 + 1, post, wid);
        sync_wait(ws, l * 4 + 1, fq1 * 28 + fp, lane, ~0u, (1u << G1::H) - 1, wid);
        g.run_steps<false>(acc, [&](int it, int i) { if (i == 0 && cw >= 0 && it == -2) code_warm(cw, lane, L_SCR + wid * 256); });
        bar();
        sched0();
        g.pend<0, 8>(acc);
#pragma unroll
        for (int i = 0; i < 2; ++i)
#pragma unroll
          for (int jj = 0; jj < 4; ++jj) st16(ro, (i ? uo2 : uo)[jj], pack_pair(gelu_tanh(acc[2 * i][jj]), gelu_tanh(acc[2 * i + 1][jj])));
      }
    }
    {
      PHASE_VARS
      asm volatile("" : "+s"(out));
      G2A::Comp<O44<true, ANE>> ga; ga.setup(wp, wq, lane);
#pragma unroll
      for (int jj = 0; jj < 4; ++jj) ga.qrd[0][jj] = 65536 + rd_km(lane, wq * 2 + (jj & 1)) + (((jj >> 1) ^ (tq >> 3)) & 1) * 8192;
      G2::Comp<O42<true, BNE>> g; g.setup(wp, wq, lane);
      g.setwin();
      f32x4 ac4[4][4], acc[4][2];
#pragma unroll
      for (int jj = 0; jj < 2; ++jj) { const float b = bf2f(*(const LAS u16*)(uintptr_t)(L_BIAS + 256 + (wq * 2 + jj) * 32 + fr * 2));
#pragma unroll
        for (int i = 0; i < 4; ++i) { ac4[i][jj] = (f32x4){b, b, b, b}; ac4[i][jj + 2] = (f32x4){0.f, 0.f, 0.f, 0.f}; } }
      unsigned xa = L_X + wid * 4096 + lane * 16, xo = (unsigned)((bid * 4 + wid) << 14) + ph(lane * 16), oo = x_off(tp, tq, wid, fr, fq), xo2 = xo + 4096, oo2 = oo + 32768;
      unsigned fo = lane ? MB : (((tq ^ 8) * 8 + tp) * 64 + 28 + wid) * 4u, one = 1u;
      bf16x8 s[2]; sel_frags(false, fr, fq, s); tile_add(ac4, s, xa);
      const rsrc_t rx = mkrsrc(ws + WS_XP, 16 * MB), rf = mkrsrc(ws + WS_MAIL + (l * 4 + 2) * (MEDGE * 4), MEDGE * 4), ro = mkrsrc(l ? (const void*)out : (const void*)(ws + WS_X2), S * D * 2);
      asm volatile("" : "+v"(xa), "+v"(xo), "+v"(oo), "+v"(xo2), "+v"(oo2), "+v"(fo), "+v"(one), "+v"(s[0]), "+v"(s[1]));
      ga.prep(ac4);
      sync_post(ws, l * 4 + 2, bid < 224 && lane < 32 ? (lane * 8 + fq1) * 64 + fp : -1, wid);
      static_assert(JA % 2 == 0 && G2A::H == 2, "");
      sync_wait(ws, l * 4 + 2, tq * 8 + tp, lane, 0xfffffffu, tq & 8 ? 1u << JA / 2 : 1u, wid);
      ga.run_steps<false>(ac4, [&](int it, int i) { if (i == 0 && cw >= 0 && it == -2) code_warm(cw, lane, L_SCR + wid * 256); });
      ga.pend<4, 8>(ac4);
#pragma unroll
      for (int i = 0; i < 2; ++i)
#pragma unroll
        for (int jj = 0; jj < 2; ++jj) st16(rx, (jj ? xo2 : xo) + i * 2048, cat4(pk4(ac4[2 * i][jj + 2]), pk4(ac4[2 * i + 1][jj + 2])));
#pragma unroll
      for (int i = 0; i < 4; ++i) { acc[i][0] = ac4[i][0]; acc[i][1] = ac4[i][1]; }
#pragma unroll
      for (int k = 0; k < 2; ++k) { g.fp[2][k] = ga.fp[2][k]; g.fp[3][k] = ga.fp[3][k]; g.fq[0][k] = ga.fq[0][k]; g.fq[1][k] = ga.fq[1][k]; }
      sched0();
      g.run_steps<true>(acc, [&](int it, int i) {
        if (i == PS % 4 && it == PS / 4 - (F / 64 - 2 * JA) / 4) {
          wait_vm<0>();
          __builtin_amdgcn_raw_buffer_store_b32(one, rf, fo, 0, 16);
        }
      });
      bar();
      sched0();
      g.pend<0, 4>(acc);
      tile_add(acc, s, xa);
      u32x4 xr[4];
#pragma unroll
      for (int i = 0; i < 2; ++i)
#pragma unroll
        for (int jj = 0; jj < 2; ++jj) { xr[jj * 2 + i] = pack_pair(acc[2 * i][jj], acc[2 * i + 1][jj]); st16(ro, (jj ? oo2 : oo) + i * 64, xr[jj * 2 + i]); }
      if (l == 0) {
#pragma unroll
        for (int p = 0; p < 4; ++p) *(LAS u32x4*)(uintptr_t)(xa + p * 1024) = xr[p];
        if (NL == 1) sync_post(ws, 3, -1, wid);
      }
    }
  }
}

extern "C" void kernel_launch(void* const* d_in, const int* in_sizes, int n_in,
                              void* d_out, int out_size, void* d_ws, size_t ws_size,
                              hipStream_t stream) {
  (void)hipFuncSetAttribute((const void*)mega_kernel, hipFuncAttributeMaxDynamicSharedMemorySize, 163840);
  mega_kernel<<<256, 512, 163840, stream>>>((const u16*)d_in[0], (const u16*)d_in[1], (const u16*)d_in[2], (const u16*)d_in[4], (const u16*)d_in[6], (const u16*)d_in[5], (const u16*)d_in[7], (u16*)d_out, (char*)d_ws);
}
```

```cpp
#include <hip/hip_runtime.h>
#include <hip/hip_bf16.h>
#include <stdint.h>

typedef unsigned short u16;
typedef unsigned long long u64;
typedef short bf16x8 __attribute__((ext_vector_type(8)));
typedef short bf16x4 __attribute__((ext_vector_type(4)));
typedef float f32x4 __attribute__((ext_vector_type(4)));
typedef float f32x2 __attribute__((ext_vector_type(2)));
typedef unsigned u32x4 __attribute__((ext_vector_type(4)));
typedef unsigned u32x2 __attribute__((ext_vector_type(2)));
typedef __bf16 bf16v2 __attribute__((ext_vector_type(2)));
#define LAS __attribute__((address_space(3)))
#define GAS __attribute__((address_space(1)))
#define DEVI __device__ __forceinline__
typedef __attribute__((address_space(1))) unsigned gu32;
typedef __attribute__((address_space(1))) unsigned long long gu64;

constexpr int S = 2048, D = 1024, F = 3584;
constexpr float EPS = 1e-5f;

DEVI float bf2f(u16 v) { return __uint_as_float(((unsigned)v) << 16); }
DEVI float bflo(unsigned v) { return __uint_as_float(v << 16); }
DEVI float bfhi(unsigned v) { return __uint_as_float(v & 0xffff0000u); }
DEVI unsigned pk2(float lo, float hi) { bf16v2 p; p.x = (__bf16)lo; p.y = (__bf16)hi; return __builtin_bit_cast(unsigned, p); }

DEVI const void* uni_ptr(const void* p) {
  const u64 v = (u64)p;
  return (const void*)(((u64)(unsigned)__builtin_amdgcn_readfirstlane((unsigned)(v >> 32)) << 32) | (unsigned)__builtin_amdgcn_readfirstlane((unsigned)v));
}
DEVI void dma16(const void* sbase, unsigned voff, unsigned ldsaddr) {
  ldsaddr = __builtin_amdgcn_readfirstlane(ldsaddr);
  asm volatile("s_mov_b32 m0, %2\n\ts_nop 0\n\tglobal_load_lds_dwordx4 %0, %1 sc1" :: "v"(voff), "s"(sbase), "s"(ldsaddr) : "memory");
}
template <int N, int IMM> DEVI void dma_grp(const void* sbase, const unsigned* v, unsigned lb) {
  static_assert(N == 1 || N == 2 || N == 4, "");
  if constexpr (N == 4)
    asm volatile("s_add_u32 m0, %1, %2\n\ts_nop 0\n\tglobal_load_lds_dwordx4 %3, %0 sc1\n\tglobal_load_lds_dwordx4 %4, %0 offset:1024 sc1\n\t"
                 "global_load_lds_dwordx4 %5, %0 offset:2048 sc1\n\tglobal_load_lds_dwordx4 %6, %0 offset:3072 sc1"
                 :: "s"(sbase), "s"(lb), "i"(IMM), "v"(v[0]), "v"(v[1]), "v"(v[2]), "v"(v[3]) : "memory", "scc");
  else if constexpr (N == 2)
    asm volatile("s_add_u32 m0, %1, %2\n\ts_nop 0\n\tglobal_load_lds_dwordx4 %3, %0 sc1\n\tglobal_load_lds_dwordx4 %4, %0 offset:1024 sc1"
                 :: "s"(sbase), "s"(lb), "i"(IMM), "v"(v[0]), "v"(v[1]) : "memory", "scc");
  else
    asm volatile("s_add_u32 m0, %1, %2\n\ts_nop 0\n\tglobal_load_lds_dwordx4 %3, %0 sc1" :: "s"(sbase), "s"(lb), "i"(IMM), "v"(v[0]) : "memory", "scc");
}
template <int N> DEVI void wait_vm() { asm volatile("s_waitcnt vmcnt(%0)" :: "i"(N) : "memory"); }
DEVI void wait_lgkm0() { asm volatile("" ::: "memory"); __builtin_amdgcn_s_waitcnt(0xC07F); asm volatile("" ::: "memory"); }
DEVI void mfma_ip(f32x4& c, const bf16x8& a, const bf16x8& b) { asm volatile("v_mfma_f32_16x16x32_bf16 %0, %1, %2, %0" : "+v"(c) : "v"(a), "v"(b)); }
DEVI void mfma_gap() { __builtin_amdgcn_sched_barrier(0); asm volatile("s_nop 7"); __builtin_amdgcn_sched_barrier(0); }
DEVI void bar() { asm volatile("" ::: "memory"); __builtin_amdgcn_s_barrier(); asm volatile("" ::: "memory"); }
DEVI void sched0() { __builtin_amdgcn_sched_barrier(0); }

DEVI bf16x8 lds_b128(unsigned addr) { return *(const LAS bf16x8*)(uintptr_t)addr; }
DEVI bf16x4 lds_tr4(unsigned addr) { return __builtin_amdgcn_ds_read_tr16_b64_v4i16((LAS bf16x4*)(uintptr_t)addr); }

typedef __amdgpu_buffer_rsrc_t rsrc_t;
DEVI rsrc_t mkrsrc(const void* p, unsigned bytes) { return __builtin_amdgcn_make_buffer_rsrc((void*)p, 0, bytes, 0x00020000); }
DEVI void st16(rsrc_t r, unsigned off, u32x4 v) { __builtin_amdgcn_raw_buffer_store_b128(v, r, off, 0, 16); }
DEVI void ld_raw(u32x4& v, rsrc_t r, unsigned off) { asm volatile("buffer_load_dwordx4 %0, %1, %2, 0 offen sc1 ; raw load" : "=&v"(v) : "v"(off), "s"(r) : "memory"); }
DEVI void ld_again(u32x4& v, rsrc_t r, unsigned off) { asm volatile("buffer_load_dwordx4 %0, %1, %2, 0 offen sc1\n\ts_waitcnt vmcnt(0)" : "=&v"(v) : "v"(off), "s"(r) : "memory"); }
DEVI void ld_ok(u32x4& v) { asm volatile("; raw ok %0" : "+v"(v)); }
#define RLX_AGENT __ATOMIC_RELAXED, __HIP_MEMORY_SCOPE_AGENT

template <int N> DEVI float ror16(float v) { return __int_as_float(__builtin_amdgcn_update_dpp(0, __float_as_int(v), 0x120 + N, 0xf, 0xf, false)); }
DEVI float red16(float v) { v += ror16<8>(v); v += ror16<4>(v); v += ror16<2>(v); v += ror16<1>(v); return v; }
DEVI void xch32(float v, float& a, float& b) { unsigned u = __float_as_uint(v); auto r = __builtin_amdgcn_permlane32_swap(u, u, false, false); unsigned x = r[0], y = r[1]; a = __uint_as_float(x); b = __uint_as_float(y); }
DEVI void xch16(float v, float& a, float& b) { unsigned u = __float_as_uint(v); auto r = __builtin_amdgcn_permlane16_swap(u, u, false, false); unsigned x = r[0], y = r[1]; a = __uint_as_float(x); b = __uint_as_float(y); }
DEVI void swp32(float& a, float& b) { auto r = __builtin_amdgcn_permlane32_swap(__float_as_uint(a), __float_as_uint(b), false, false); unsigned x = r[0], y = r[1]; a = __uint_as_float(x); b = __uint_as_float(y); }
DEVI void swp16(float& a, float& b) { auto r = __builtin_amdgcn_permlane16_swap(__float_as_uint(a), __float_as_uint(b), false, false); unsigned x = r[0], y = r[1]; a = __uint_as_float(x); b = __uint_as_float(y); }
DEVI float colsum4(float v) { float a, b; xch32(v, a, b); v = a + b; xch16(v, a, b); return a + b; }
DEVI float vmax(float a, float b) { float r; asm("v_max_f32 %0, %1, %2" : "=v"(r) : "v"(a), "v"(b)); return r; }
DEVI float vmin3(float a, float b, float c) { float r; asm("v_min3_f32 %0, %1, %2, %3" : "=v"(r) : "v"(a), "v"(b), "v"(c)); return r; }
DEVI float vmax3(float a, float b, float c) { float r; asm("v_max3_f32 %0, %1, %2, %3" : "=v"(r) : "v"(a), "v"(b), "v"(c)); return r; }

DEVI unsigned src_kc(int c, int ld) { int row = c >> 3, ch = (c & 7) ^ ((row >> 1) & 7); return (unsigned)(row * ld * 2 + ch * 16); }
DEVI int km_f(int k) { return ((k >> 1) & 1) | (((k >> 3) & 1) << 1); }
DEVI unsigned ph(unsigned x) { return ((x >> 7) << 8) | (x & 127u); }
DEVI unsigned rd_kc(int lane, int ks) { int r = lane & 15, g = lane >> 4; return (unsigned)(r * 128 + (((ks * 4 + g) ^ (r >> 1)) << 4)); }
DEVI unsigned rd_km(int lane, int ct) {
  int g = lane >> 4, q = (lane & 15) >> 2, p = lane & 3;
  int f = ((q >> 1) & 1) | ((g & 1) << 1);
  return (unsigned)((ct >> 2) * 8192 + (8 * g + q) * 128 + (((ct & 3) ^ f) << 5) + 8 * p);
}


constexpr unsigned CODE_BYTES = 50176;
DEVI void code_warm(int part, int lane, unsigned scratch) {
  unsigned long long pc;
  asm volatile("s_getpc_b64 s[40:41]\n\ts_add_u32 s40, s40, mega_kernel@rel32@lo+4\n\ts_addc_u32 s41, s41, mega_kernel@rel32@hi+12\n\ts_mov_b64 %0, s[40:41]" : "=s"(pc) :: "s40", "s41", "scc");
  const unsigned off = (unsigned)(part * 64 + lane) * 128u;
  scratch = __builtin_amdgcn_readfirstlane(scratch);
  if (off < CODE_BYTES) asm volatile("s_nop 3\n\ts_mov_b32 m0, %2\n\ts_nop 0\n\tglobal_load_lds_dword %0, %1" :: "v"(off), "s"(pc), "s"(scratch) : "memory");
}

DEVI unsigned src_km(int lt, int ld) { const int k = lt >> 3; return (unsigned)(k * ld * 2 + (((lt & 7) ^ (km_f(k) << 1)) << 4)); }
DEVI void touch4(const void* sbase, unsigned voff, unsigned ldsaddr) {
  ldsaddr = __builtin_amdgcn_readfirstlane(ldsaddr); sbase = uni_ptr(sbase);
  asm volatile("s_nop 3\n\ts_mov_b32 m0, %2\n\ts_nop 0\n\tglobal_load_lds_dword %0, %1" :: "v"(voff), "s"(sbase), "s"(ldsaddr) : "memory");
}

template <int IMM> DEVI void spf1(const void* base, unsigned soff) { asm volatile("s_load_dword s101, %0, %1 offset:%2" :: "s"(base), "s"(soff), "i"(IMM) : "s101"); }
template <int IMM = 0> DEVI void spf2(const void* base, unsigned soff) { asm volatile("s_load_dwordx2 s[100:101], %0, %1 offset:%2" :: "s"(base), "s"(soff), "i"(IMM + 60) : "s100", "s101"); }
template <int ST> DEVI void spf2x6(const void* base, unsigned soff) {
  asm volatile("s_load_dwordx2 s[100:101], %0, %1 offset:%2\n\ts_load_dwordx2 s[100:101], %0, %1 offset:%3\n\ts_load_dwordx2 s[100:101], %0, %1 offset:%4\n\t"
               "s_load_dwordx2 s[100:101], %0, %1 offset:%5\n\ts_load_dwordx2 s[100:101], %0, %1 offset:%6\n\ts_load_dwordx2 s[100:101], %0, %1 offset:%7"
               :: "s"(base), "s"(soff), "i"(60), "i"(60 + ST), "i"(60 + 2 * ST), "i"(60 + 3 * ST), "i"(60 + 4 * ST), "i"(60 + 5 * ST) : "s100", "s101");
}
template <int ST> DEVI void spf1x12(const void* base, unsigned soff) {
  spf1<0>(base, soff); spf1<ST>(base, soff); spf1<2 * ST>(base, soff); spf1<3 * ST>(base, soff); spf1<4 * ST>(base, soff); spf1<5 * ST>(base, soff);
  spf1<6 * ST>(base, soff); spf1<7 * ST>(base, soff); spf1<8 * ST>(base, soff); spf1<9 * ST>(base, soff); spf1<10 * ST>(base, soff); spf1<11 * ST>(base, soff);
}
DEVI int lgkm_out() { int n; asm volatile("s_getreg_b32 %0, hwreg(HW_REG_IB_STS, 8, 4)" : "=s"(n)); return n; }
DEVI int slow_res(const void* w) { return (3 - ((unsigned)(uintptr_t)w >> 7)) & 7; }

constexpr int WNE = 16, ANE = 24, BNE = 13, QNE = 46;
struct Plan { int n; signed char q[40], idx[40], k[40], h[40], g[40]; };
template <class O, bool PKM, bool QKM> constexpr Plan make_plan() {
  Plan p{};
  int nr = 0;
  for (int c = 0; c < O::NRO; ++c) nr += ((O::ro[c] >> 4) ? QKM : PKM) ? 4 : 2;
  int prev = 0;
  for (int c = 0; c < O::NRO; ++c) {
    const int q = O::ro[c] >> 4, i = O::ro[c] & 15;
    int av[2] = {0, 0}, k0 = -1;
    for (int m = 0; m < 2 * O::NP; ++m) {
      const int mm = (m + 2 * O::NPEND) % (2 * O::NP);
      if ((q ? O::pj[m >> 1] : O::pi[m >> 1]) == i && m < 2 * O::NPEND) av[((m >> 1) ^ m) & 1] = m;
      if ((q ? O::pj[mm >> 1] : O::pi[mm >> 1]) == i && k0 < 0) k0 = ((mm >> 1) ^ mm) & 1;
    }
    for (int kk = 0; kk < 2; ++kk)
      for (int h = 0; h < ((q ? QKM : PKM) ? 2 : 1); ++h) {
        const int k = k0 ^ kk;
        int g = p.n * O::NE / nr;
        if (g < av[k]) g = av[k];
        if (g < prev + (p.n && O::NE >= nr ? 1 : 0)) g = prev + (p.n && O::NE >= nr ? 1 : 0);
        p.q[p.n] = q; p.idx[p.n] = i; p.k[p.n] = k; p.h[p.n] = h; p.g[p.n] = g; prev = g; ++p.n;
      }
  }
  return p;
}
struct OBase { static constexpr int pidx(int i) { return i; } static constexpr int poff(int) { return 0; } static constexpr int qoff(int j) { return j * 2048; } static constexpr bool swp(int) { return false; } };
template <bool PF, int NE_> struct O44 : OBase {
  static constexpr int TP = 4, TQ = 4, NPA = 4, NP = 16, NPEND = 8, NRO = 8, NE = NE_;
  static constexpr signed char pi[16] = {2, 3, 3, 2, 2, 3, 3, 2, 0, 1, 1, 0, 0, 1, 1, 0}, pj[16] = {0, 0, 1, 1, 2, 2, 3, 3, 0, 0, 1, 1, 2, 2, 3, 3};
  static constexpr signed char ro[8] = {0, PF ? 1 : 16, PF ? 16 : 1, 17, 18, 19, 2, 3};
};
template <bool PF, int NE_> struct O42 : OBase {
  static constexpr int TP = 4, TQ = 2, NPA = 4, NP = 8, NPEND = 4, NRO = 6, NE = NE_;
  static constexpr signed char pi[8] = {2, 3, 3, 2, 0, 1, 1, 0}, pj[8] = {0, 0, 1, 1, 0, 0, 1, 1};
  static constexpr signed char ro[6] = {0, PF ? 1 : 16, PF ? 16 : 1, 17, 3, 2};
};
template <int TH> struct OQ {
  static constexpr int TP = 6, TQ = 5, NPA = 2, NP = 26, NPEND = 13, NRO = 11, NE = QNE;
  static constexpr signed char pi[26] = {3, 4, 5, 5, 4, 3, 3, 4, 5, 5, TH ? 5 : 3, 4, TH ? 3 : 4, 0, 1, 2, 2, 1, 0, 0, 1, 2, 2, TH ? 2 : 0, 1, TH ? 0 : 1};
  static constexpr signed char pj[26] = {0, 0, 0, 1, 1, 1, 2, 2, 2, TH ? 4 : 3, 3, 3, TH ? 3 : 4, 0, 0, 0, 1, 1, 1, 2, 2, 2, TH ? 4 : 3, 3, 3, TH ? 3 : 4};
  static constexpr signed char ro[11] = {0, 1, 16, 2, 17, 18, TH ? 20 : 19, TH ? 19 : 20, 3, 4, 5};
  static constexpr int pidx(int i) { return i / 3; }
  static constexpr int poff(int i) { return i % 3 * 8192; }
  static constexpr int qoff(int j) { return j == 4 ? 0 : (1 + 4 * TH + j) * 2048; }
  static constexpr bool swp(int i) { return i % 3 == 2; }
};
template <bool PKM, bool QKM, int PB, int QB, int XB, int NS, int NT, bool WISP, int BASE, int ROT, int WLD, int WSUB, int ASTEP, int RLX = 0, bool QF = false>
struct Gemm {
  static constexpr int SB = PB + QB + XB, CNT = (PB + QB) / 4096 + (XB ? 1 : 0);
  static constexpr int PO = QF ? QB : 0, QO = QF ? 0 : PB, WB = WISP ? PB : QB, AB = WISP ? QB : PB, WOFF = WISP ? PO : QO, AOFF = WISP ? QO + XB : PO;
  static constexpr int XL = XB / 64, H = NS - 1 - RLX, CA = AB / 4096 + (XB ? 1 : 0), WSTEP = 64 * WLD * 2;
  static_assert(H * CNT <= 63 && BASE + NS * SB <= 163840, "");
  struct Loader {
    static constexpr int NW = WB / 4096, NA = AB / 4096;
    const char* w; const char* ab; unsigned wv[NW], av[NA], xv, lwb, lab, lx; int rot, islot, t; bool xl;
    DEVI Loader(const void* w_, const void* ab_, int lw, int lane, int rot_ = 0)
        : w((const char*)w_ - 3072), ab((const char*)ab_ - 3072), lwb(BASE + WOFF + lw * (NW == 6 ? 2048 : WB / 4)), lab(BASE + AOFF + lw * (AB / 4)), lx(BASE + PB + lw * (XL * 16)),
          rot(rot_), islot((H + ROT) % NS), t(0), xl(lane < XL) {
      const unsigned w0 = (lane >> 3) * (WLD * 2), f0 = (lane & 7) ^ ((lane >> 3) & 2);
#pragma unroll
      for (int i = 0; i < NW; ++i) {
        const unsigned p = NW == 6 ? (i >> 1) * 8 + lw * 2 + (i & 1) : lw * NW + i;
        wv[i] = w0 + ((f0 ^ ((i & 1) << 2)) << 4) + (NW == 6 ? (i >> 1) * WSUB + (lw * 2 + (i & 1)) * (8 * WLD * 2) - (i & 1) * 1024 : (p >> 3) * WSUB + (p & 7) * (8 * WLD * 2) - i * 1024) + 3072;
        asm volatile("" : "+v"(wv[i]));
      }
      asm volatile("" : "+s"(w), "+s"(lwb));
      if (XB) asm volatile("" : "+s"(rot));
      else seta(0, 0, lw, lane);
    }
    DEVI void seta(unsigned a0, unsigned xv_, int lw, int lane) {
      xv = xv_ + 3072;
#pragma unroll
      for (int i = 0; i < NA; ++i) { const unsigned p = lw * NA + i; av[i] = (XB ? a0 + src_kc(p * 64 + lane, D) : p * 2048 + ph(lane * 16)) + 3072 - i * 1024; asm volatile("" : "+v"(av[i])); }
      asm volatile("" : "+s"(ab), "+s"(lab));
      if (XB) { asm volatile("" : "+v"(xv)); asm volatile("" : "+s"(lx)); }
    }
    DEVI int kord(int n) const { if constexpr (XB != 0) return (n + rot) & (NT - 1); else return n; }
    template <int SO> DEVI void issue_w(const char* p, unsigned so = 0) const {
      if constexpr (NW == 6) { dma_grp<2, SO>(p, wv, lwb + so); dma_grp<2, SO + 8192>(p, wv + 2, lwb + so); dma_grp<2, SO + 16384>(p, wv + 4, lwb + so); }
      else dma_grp<NW, SO>(p, wv, lwb + so);
    }
    template <int SO> DEVI void issue_a(const char* p, unsigned so = 0) const {
      if (XB) { if (xl) dma_grp<1, SO>(p, &xv, lx + so); }
      dma_grp<NA, SO>(p, av, lab + so);
    }
    template <int S0, bool W, bool A> DEVI void pre() const {
      if constexpr (S0 < H) {
        if (W) issue_w<((S0 + ROT) % NS) * SB>(w + (kord(0) + S0) * WSTEP);
        if (A) issue_a<((S0 + ROT) % NS) * SB>(ab + (kord(0) + S0) * ASTEP);
        pre<S0 + 1, W, A>();
      }
    }
    template <int T> DEVI void pre_w1() const { issue_w<((T + ROT) % NS) * SB>(w + (kord(0) + T) * WSTEP); sched0(); }
    DEVI void pre_w() const { pre<0, true, false>(); sched0(); }
    template <int T> DEVI void peel() const {
      if constexpr (T < H - 1) {
        wait_vm<(H - 1 - T) * CA + T * CNT>();
        bar();
        issue_w<((T + H + ROT) % NS) * SB>(w + (T + H) * WSTEP); issue_a<((T + H + ROT) % NS) * SB>(ab + (T + H) * ASTEP);
        peel<T + 1>();
      }
    }
    DEVI void start() {
      sched0();
      bar();
      pre<0, false, true>();
      sched0();
      if constexpr (H == 2) wait_vm<CA>();
      else { peel<0>(); islot = (2 * H - 1 + ROT) % NS; t = H - 1; }
    }
    template <class HK> DEVI void run(int tend, HK side) {
#pragma unroll 1
      for (; t < tend; ++t) {
        const char* wk = w + (t + H) * WSTEP; const char* ak = ab + (t + H) * ASTEP;
        unsigned lw_ = lwb + islot * SB, la_ = lab + islot * SB;
        islot = islot + 1 == NS ? 0 : islot + 1;
        asm volatile("" : "+s"(wk), "+s"(ak), "+s"(lw_), "+s"(la_));
        wait_vm<(H - 1) * CNT>();
        bar();
        dma_grp<NW, 0>(wk, wv, lw_);
        side(t);
        dma_grp<NA, 0>(ak, av, la_);
        sched0();
      }
    }
    template <int T, int X, class A, class B, class C> DEVI void step(A pre_, B side, C post) const {
      const int k = kord(T + H);
      const char* wk = w + k * WSTEP; const char* ak = ab + k * ASTEP;
      asm volatile("" : "+s"(wk), "+s"(ak));
      wait_vm<(H - 1) * CNT + X>();
      bar();
      pre_(BASE + ((T + ROT) % NS) * SB);
      issue_w<((T + H + ROT) % NS) * SB>(wk);
      sched0();
      side();
      sched0();
      issue_a<((T + H + ROT) % NS) * SB>(ak);
      post();
      sched0();
    }
    template <int I> DEVI void tail() const { if constexpr (I >= 0) { sched0(); wait_vm<I * CNT>(); bar(); tail<I - 1>(); } }
  };
  static constexpr int NWIN = (NS * SB + 65535) / 65536;
  template <class O> struct Comp {
    static constexpr int TP = O::TP, TQ = O::TQ, NM = 2 * O::NP, NPA = PKM ? O::NPA : 2, NQA = QKM ? TQ : 2;
    static constexpr Plan PL = make_plan<O, PKM, QKM>();
    unsigned prd[NWIN][NPA], qrd[NWIN][NQA];
    bf16x8 fp[TP][2], fq[TQ][2]; bf16x4 lo;
    DEVI void setwin() {
#pragma unroll
      for (int w = 1; w < NWIN; ++w) {
#pragma unroll
        for (int i = 0; i < NPA; ++i) { prd[w][i] = prd[0][i] + w * 65536; asm volatile("" : "+v"(prd[w][i])); }
#pragma unroll
        for (int i = 0; i < NQA; ++i) { qrd[w][i] = qrd[0][i] + w * 65536; asm volatile("" : "+v"(qrd[w][i])); }
      }
#pragma unroll
      for (int i = 0; i < NPA; ++i) asm volatile("" : "+v"(prd[0][i]));
#pragma unroll
      for (int i = 0; i < NQA; ++i) asm volatile("" : "+v"(qrd[0][i]));
    }
    DEVI void setup(int wp, int wq, int lane) {
      const unsigned b = BASE;
      if (PKM) {
#pragma unroll
        for (int i = 0; i < TP; ++i) prd[0][i] = b + rd_km(lane, wp * TP + i);
      } else { prd[0][0] = b + rd_kc(lane, 0) + wp * TP * 2048; prd[0][1] = b + rd_kc(lane, 1) + wp * TP * 2048; }
      if (QKM) {
#pragma unroll
        for (int j = 0; j < TQ; ++j) qrd[0][j] = b + rd_km(lane, wq * TQ + j);
      } else { qrd[0][0] = b + rd_kc(lane, 0) + wq * TQ * 2048; qrd[0][1] = b + rd_kc(lane, 1) + wq * TQ * 2048; }
    }
    template <int C, int N> DEVI static unsigned wa(const unsigned (&b)[NWIN][N], int i) { return b[C >> 16][i] + (C & 0xffff); }
    template <int SLOT, int R> DEVI void rd() {
      constexpr int q = PL.q[R], i = PL.idx[R], k = PL.k[R], h = PL.h[R];
      bf16x8& f = q ? fq[q ? i : 0][k] : fp[q ? 0 : i][k];
      if constexpr (q ? QKM : PKM) {
        unsigned a;
        if constexpr (q) a = wa<SLOT * SB + QO + k * 4096 + h * 512, NQA>(qrd, i); else a = wa<SLOT * SB + PO + O::poff(i) + k * 4096 + h * 512, NPA>(prd, O::pidx(i));
        const bf16x4 x = lds_tr4(a);
        if constexpr (h) f = __builtin_shufflevector(lo, x, 0, 1, 2, 3, 4, 5, 6, 7); else lo = x;
      } else if constexpr (q) f = lds_b128(wa<SLOT * SB + QO + O::qoff(i), 2>(qrd, k));
      else f = lds_b128(wa<SLOT * SB + PO + i * 2048, 2>(prd, k));
    }
    template <int SLOT, int M, int R> DEVI void rds() { if constexpr (R < PL.n) { if constexpr (PL.g[R] == M) rd<SLOT, R>(); if constexpr (PL.g[R] <= M) rds<SLOT, M, R + 1>(); } }
    template <int M, bool IP = false> DEVI void mm(f32x4 (&acc)[TP][TQ]) {
      constexpr int i = O::pi[M >> 1], j = O::pj[M >> 1], k = ((M >> 1) ^ M) & 1;
      if constexpr (IP) { if constexpr (O::swp(i)) mfma_ip(acc[i][j], fq[j][k], fp[i][k]); else mfma_ip(acc[i][j], fp[i][k], fq[j][k]); }
      else if constexpr (O::swp(i)) acc[i][j] = __builtin_amdgcn_mfma_f32_16x16x32_bf16(fq[j][k], fp[i][k], acc[i][j], 0, 0, 0);
      else acc[i][j] = __builtin_amdgcn_mfma_f32_16x16x32_bf16(fp[i][k], fq[j][k], acc[i][j], 0, 0, 0);
    }
    template <int A, int B> DEVI void pend(f32x4 (&acc)[TP][TQ]) { if constexpr (A < B) { mm<2 * A>(acc); mm<2 * A + 1>(acc); pend<A + 1, B>(acc); } }
    template <int SLOT, bool FIRST, int M> DEVI void body(f32x4 (&acc)[TP][TQ]) {
      if constexpr (M < NM) {
        if constexpr (!FIRST || M >= 2 * O::NPEND) { mm<M, true>(acc); sched0(); }
        rds<SLOT, M, 0>();
        sched0();
        body<SLOT, FIRST, M + 1>(acc);
      }
    }
    template <int SLOT, bool FIRST> DEVI void step(f32x4 (&acc)[TP][TQ]) { if (!RLX) wait_lgkm0(); bar(); sched0(); body<SLOT, FIRST, 0>(acc); }
    template <int I, int N, int T0, class MID> DEVI void steps(f32x4 (&acc)[TP][TQ], MID mid) {
      if constexpr (I < N) { mid(I); step<(I + T0 + ROT) % NS, false>(acc); steps<I + 1, N, T0>(acc, mid); }
    }
    DEVI void prep(f32x4 (&acc)[TP][TQ]) { setwin(); pin(acc); }
    DEVI void pin(f32x4 (&acc)[TP][TQ]) {
#pragma unroll
      for (int p = 0; p < O::NP; ++p) asm volatile("" : "+v"(acc[O::pi[p]][O::pj[p]]));
    }
    template <bool CONT, class MID> DEVI void run_steps(f32x4 (&acc)[TP][TQ], MID mid) {
      constexpr int T0 = CONT ? 0 : 1;
      if constexpr (T0) step<ROT % NS, true>(acc);
#pragma unroll 1
      for (int it = 0; it < (NT - T0) / NS; ++it) {
        steps<0, NS, T0>(acc, [&](int i) { mid(it - (NT - T0) / NS, i); });
      }
      steps<0, (NT - T0) % NS, T0>(acc, [](int) {});
      wait_lgkm0();
      mfma_gap();
    }
  };
};

DEVI f32x4 gelu_arg(f32x4 u) { return u * (-2.302208198f + -0.1029432397f * (u * u)); }
DEVI f32x4 gelu_tanh(f32x4 u) {
  f32x4 a = gelu_arg(u);
#pragma unroll
  for (int e = 0; e < 4; ++e) a[e] = __builtin_amdgcn_exp2f(a[e]);
  a += 1.f;
#pragma unroll
  for (int e = 0; e < 4; ++e) a[e] = __builtin_amdgcn_rcpf(a[e]);
  return u * a;
}

template <int tilesP, int tilesQ, int gp, int gq> DEVI void tile_map(unsigned bid, int& tp, int& tq) {
  const unsigned xcd = bid & 7, idx = bid >> 3;
  constexpr unsigned gP = tilesP / gp, gQ = tilesQ / gq;
  static_assert(gP * gQ == 8, "");
  tp = (xcd % gP) * gp + idx % gp;
  tq = (xcd / gP) * gq + idx / gp;
}

template <int V> struct IC { static constexpr int value = V; };
template <int A, int B, class FN> DEVI void for_c(FN f) { if constexpr (A < B) { f(IC<A>{}); for_c<A + 1, B>(f); } }
DEVI float sel(bool c, float a, float b) { return c ? a : b; }
DEVI u32x2 pk4(const f32x4& v) { return (u32x2){pk2(v[0], v[1]), pk2(v[2], v[3])}; }
DEVI u32x4 cat4(u32x2 a, u32x2 b) { return (u32x4){a.x, a.y, b.x, b.y}; }
DEVI bf16x8 cat(u32x2 a, u32x2 b) { return __builtin_bit_cast(bf16x8, cat4(a, b)); }
DEVI f32x4 mfma(const bf16x8& a, const bf16x8& b, const f32x4& c) { return __builtin_amdgcn_mfma_f32_16x16x32_bf16(a, b, c, 0, 0, 0); }
DEVI u32x4 pack_pair(const f32x4& a, const f32x4& b) {
  const unsigned ax = pk2(a[0], a[1]), ay = pk2(a[2], a[3]), bx = pk2(b[0], b[1]), by = pk2(b[2], b[3]);
  auto rx = __builtin_amdgcn_permlane16_swap(ax, bx, false, false);
  auto ry = __builtin_amdgcn_permlane16_swap(ay, by, false, false);
  const unsigned x0 = rx[0], x1 = rx[1], y0 = ry[0], y1 = ry[1];
  return (u32x4){x0, y0, x1, y1};
}
DEVI int pair_chunk(int fq) { return ((fq & 1) << 1) | (fq >> 1); }
DEVI void sel_frags(bool paired, int fr, int fq, bf16x8 (&s)[2]) {
  const unsigned one = fr & 1 ? 0x3F800000u : 0x3F80u;
#pragma unroll
  for (int i = 0; i < 2; ++i) {
    const bool on = paired ? (fq & 1) == i && (fr >> 3) == (fq >> 1) : (fr >> 2) == fq;
    const int d = paired ? (fr & 7) >> 1 : i * 2 + ((fr >> 1) & 1);
    u32x4 v;
#pragma unroll
    for (int e = 0; e < 4; ++e) v[e] = on && d == e ? one : 0u;
    s[i] = __builtin_bit_cast(bf16x8, v);
  }
}
template <int TQ> DEVI void tile_add(f32x4 (&acc)[4][TQ], const bf16x8 (&s)[2], unsigned a) {
  bf16x8 t[4];
#pragma unroll
  for (int p = 0; p < 4; ++p) t[p] = lds_b128(a + p * 1024);
#pragma unroll
  for (int p = 0; p < 4; ++p)
#pragma unroll
    for (int i = 0; i < 2; ++i) acc[(p & 1) * 2 + i][p >> 1] = mfma(s[i], t[p], acc[(p & 1) * 2 + i][p >> 1]);
}
template <int C> DEVI float dppf(float v) { return __int_as_float(__builtin_amdgcn_update_dpp(0, __float_as_int(v), C, 0xf, 0xf, false)); }
DEVI float red8(float v) { v += dppf<0xB1>(v); v += dppf<0x4E>(v); v += dppf<0x141>(v); return v; }
DEVI float red4(float v) { v += dppf<0xB1>(v); v += dppf<0x4E>(v); return v; }

constexpr unsigned SENT = 0xBF800000u;
constexpr int SPIN_MAX = 1 << 21;

constexpr unsigned MB = 1u << 20;
constexpr unsigned WS_O = 0, WS_H = 8 * MB, WS_U = 16 * MB, WS_X2 = 32 * MB, WS_XP = 40 * MB  ;
constexpr unsigned WS_ST = 120 * MB, ST_CS = 2 * S * 64, WS_XCH = 122 * MB  , WS_EPOCH = 123 * MB, WS_ABORT = 123 * MB + 256  ;
DEVI unsigned tag2(unsigned t) { return t * 0x9E3779B1u ^ 0x5BD1E995u; }
constexpr int MEDGE = 256 * 64;
constexpr unsigned WS_MAIL = 124 * MB;

constexpr int NL = 2, QBR = 3, FLS = 3, NPL = 6, SPON = 1, PC0 = 0, STG = 1, WSP = 1, SWW = 2, GSH = 1, GSP = 1, SSC = 1, RB = 1, SW0 = 8, SW1 = 6, XD = 3, XS = 4, JA = 22, PS = 3, TBA = 3, TC1 = 12, NW1 = 9, EW = 4, ET = 20, SPW = 1, PA_1 = 3, PA_2 = 3;
constexpr int L_X = 0, L_TAB = 16384, L_CS = L_TAB + 1152, L_TAG = L_CS + 768, L_BIAS = L_TAG + 128, L_SCR = L_BIAS + 512  , L_QKV = 20480;
static_assert(L_SCR + 1536 <= L_QKV, "");
typedef Gemm<true, false, 24576, 16384, 2048, 3, D / 64, true, 32768, 0, 3 * D, 2 * D, 128> GQ;
typedef Gemm<true, false, 16384, 8192, 0, 5, D / 64, true, 40960, 2, D, 128, 16384, 1, true> GW;
typedef Gemm<true, true, 16384, 16384, 0, 4, S / 64, true, 32768, 1, F, 128, 32768, 0, true> G1;
typedef Gemm<false, true, 16384, 16384, 0, 3, JA, false, 65536, (3 - JA % 3) % 3, S, 1024, 32768> G2A;
typedef Gemm<false, true, 16384, 8192, 0, 4, F / 64 - 2 * JA, false, 65536, 0, S, 128, 32768> G2;

DEVI unsigned get_tag() { return __builtin_amdgcn_readfirstlane(*(volatile LAS unsigned*)(uintptr_t)L_TAG); }
DEVI bool spin_fail(int spin, char* ws) {
  if ((spin & 255) != 255) return false;
  gu32* ab = (gu32*)(ws + WS_ABORT);
  const unsigned tag = get_tag();
  if (spin >= SPIN_MAX) { __hip_atomic_store(ab, tag, RLX_AGENT); return true; }
  return __builtin_amdgcn_readfirstlane(__hip_atomic_load(ab, RLX_AGENT)) == tag;
}
DEVI void sync_post(char* ws, int edge, int post, int wid) {
  unsigned o = post >= 0 && wid == 0 ? post * 4u : MB, one = 1u;
  const rsrc_t r = mkrsrc(ws + WS_MAIL + edge * (MEDGE * 4), MEDGE * 4);
  asm volatile("s_waitcnt vmcnt(0) lgkmcnt(0)" : "+v"(o), "+v"(one) : "s"(r) : "memory");
  sched0();
  bar();
  if (wid == 0) __builtin_amdgcn_raw_buffer_store_b32(one, r, o, 0, 16);
  sched0();
}
template <int DELAY = SW0> DEVI void sync_wait(char* ws, int edge, int line, int lane, unsigned all, unsigned first, int wid) {
  sched0();
  if (wid == 3) {
    gu32* pw = (gu32*)(ws + WS_MAIL) + edge * MEDGE + line * 64 + (lane & 31);
    if (DELAY) __builtin_amdgcn_s_sleep(DELAY);
    if (!STG) first = all;
    for (int spin = 0;; ++spin) {
      unsigned miss = ~(unsigned)__builtin_amdgcn_ballot_w64(__hip_atomic_load(pw, RLX_AGENT) != 0);
      if (spin_fail(spin, ws)) miss = 0;
      if (first && !(miss & first)) { bar(); first = 0; }
      if (!(miss & all)) break;
      __builtin_amdgcn_s_sleep(SW1);
    }
  } else bar();
}
DEVI unsigned halo_off(int s0, int lw, int lane) {
  const int c = lw * 32 + (lane & 31), row = c >> 3, ch = (c & 7) ^ ((row >> 1) & 7), tok = s0 - 16 + row;
  return (unsigned)((tok < 0 ? 0 : tok) * D * 2 + ch * 16);
}
DEVI int qkv_rot(int bid) { return (bid & 3) * 4 + (bid & 4) * 2; }
DEVI unsigned x_off(int tp, int tq, int w, int fr, int fq) { return (unsigned)(((tq * 64 + (w >> 1) * 32 + fr) * D + tp * 128 + (w & 1) * 64 + pair_chunk(fq) * 8) * 2); }

DEVI void pfb_q(const u16* w, int bid, int j, int n) {
  const int r = slow_res(w);
  if ((r >> 2) == (bid & 1)) {
    const unsigned o = (unsigned)(((bid & 2) * 4 + r) * 128 + (j >> 1) * (3 * D * 2) + (j & 1) * 64 + ((n + qkv_rot(bid)) & 15) * GQ::WSTEP);
    spf1<0>(w, o); spf1<2048>(w, o); spf1<4096>(w, o);
  }
}
constexpr int PFR = NPL * 128 * 1024, PF_END = TBA * 256 + NW1 * PFR / (F * 2) / 8 * 8;
static_assert(TBA * 256 * F * 2 + NW1 * PFR <= S * F * 2 && 3 * PFR >= D * D * 2, "");
struct Pfc {
  const char* p; const char* q; int pc;
  DEVI Pfc(const u16* wo, const u16* w1, unsigned g) : p((const char*)wo + g * (NPL * 128)), q((const char*)w1 + TBA * 256 * F * 2 + g * (NPL * 128)), pc(PC0) {}
  DEVI void go() {
    unsigned t, u;
    asm volatile("s_getreg_b32 %2, hwreg(HW_REG_IB_STS, 8, 4)\n\ts_cmp_lt_u32 %1, %5\n\ts_cselect_b32 %3, 0, 1\n\ts_or_b32 %2, %2, %3\n\ts_cbranch_scc1 1f\n\t"
                 "s_load_dwordx2 s[100:101], %0, 60\n\ts_load_dwordx2 s[100:101], %0, 188\n\ts_load_dwordx2 s[100:101], %0, 316\n\t"
                 "s_load_dwordx2 s[100:101], %0, 444\n\ts_load_dwordx2 s[100:101], %0, 572\n\ts_load_dwordx2 s[100:101], %0, 700\n\t"
                 ".if %8 == 7\n\ts_load_dwordx2 s[100:101], %0, 828\n\t.endif\n\t"
                 "s_mov_b32 %2, %7\n\ts_cmp_lt_u32 %1, 2\n\ts_cselect_b32 %2, %6, %2\n\ts_mov_b64 s[40:41], %0\n\ts_add_u32 s40, s40, %2\n\ts_addc_u32 s41, s41, 0\n\t"
                 "s_cmp_eq_u32 %1, 2\n\ts_cselect_b64 %0, %4, s[40:41]\n\ts_add_u32 %1, %1, 1\n1:"
                 : "+s"(p), "+s"(pc), "=&s"(t), "=&s"(u) : "s"(q), "i"(3 + NW1), "i"((D * D * 2 - PFR) / 2), "i"(PFR), "i"(NPL) : "s40", "s41", "s100", "s101", "scc");
  }
};
DEVI void spare_touch(const u16* w1, const u16* w2, int bid, int wid, int lane) {
  if (!SPON) return;
  const int j = ((bid >> 3) - 28) * 8 + wid, fr = lane & 15, fq = lane >> 4;
  const unsigned scr = L_QKV + wid * 256;
#pragma unroll 1
  for (int r = PF_END + (j * 2 + ((bid >> 2) & 1)) * 4; r < S; r += 256) { if (fr < 14) touch4(w1 + (size_t)r * F + (bid & 3) * 896, fq * (F * 2) + fr * 128, scr); wait_vm<SPW>(); }
#pragma unroll 1
  for (int r = (j * 2 + (bid & 1)) * 4; r < JA * 64; r += 256) { touch4(w2 + (size_t)(r + (bid & 2) * JA * 32) * S + (bid & 4) * 256, fq * (S * 2) + fr * 128, scr); wait_vm<SPW>(); }
#pragma unroll 1
  for (int r = JA * 128 + ((31 - j) * 2 + (bid & 1)) * 8; r < F; r += 512) { touch4(w2 + (size_t)r * S + ((bid & 7) >> 1) * 512, (lane >> 3) * (S * 2) + (lane & 7) * 128, scr); wait_vm<SPW>(); }
}

template <int TH> DEVI void qkv_attn(char* ws, int l, int h, int lane, int hp, int sb, int cw, int r0, int tp, int tq) {
  asm volatile("; half %1" : "+v"(lane) : "i"(TH));
  const int fr = lane & 15, fq = lane >> 4;
  GQ::Comp<OQ<TH>> g;
#pragma unroll
  for (int c = 0; c < 2; ++c) { g.prd[0][c] = 32768 + rd_km(lane, h * 2 + c); g.qrd[0][c] = 32768 + rd_kc(lane, c); }
  f32x4 acc[6][5];
#pragma unroll
  for (int i = 0; i < 6; ++i)
#pragma unroll
    for (int j = 0; j < 5; ++j) acc[i][j] = (f32x4){0.f, 0.f, 0.f, 0.f};
  const int par = fr & 1, da = fr - 4 * fq - par;
  const bool ca = da >= 0, cb = da >= 2;
  const unsigned tm = L_TAB + (16 + 64 * TH) * 4, tc = L_CS + h * 128;
  unsigned am = tm + fr * 4 - 320, a4 = tm + fq * 16 - 320, aa = tm + 576 + (4 * fq + par - (ca ? 0 : 16)) * 4, ab = tm + 576 + (4 * fq + par + 2 - (cb ? 0 : 16)) * 4;
  unsigned c4 = tc + fq * 16, c1 = tc + 512 + fr * 4, xb = L_QKV + h * 3072 + lane * 16, hm = par ? 0xffff0000u : 0xffffu, m0 = ca ? 0u : hm, m1 = cb ? 0u : hm;
  unsigned oo = ph((unsigned)((((sb * 2 + TH) * 16 + hp) << 13) + fr * 128 + (((h * 4 + pair_chunk(fq)) ^ (fr >> 1)) << 4)));
  asm volatile("" : "+v"(am), "+v"(a4), "+v"(aa), "+v"(ab), "+v"(c4), "+v"(c1), "+v"(xb), "+v"(hm), "+v"(m0), "+v"(m1), "+v"(oo));
  g.prep(acc);
  if (l) {
    const int sbc = (tq >> 1) + fq;
    sync_post(ws, 3, !TH && (lane < 16 || (lane < 32 && (tq & 1) && sbc < 16)) ? (sbc * 16 + fr) * 64 + (tq - 2 * sbc + 1) * 8 + tp : -1, TH * 2 + h);
  }
  if (l) sync_wait(ws, 3, sb * 16 + hp, lane, sb ? 0xffffffu : 0xffff00u, (sb ? 0x10101u : 0x10100u) << r0, TH * 2 + h);
  g.template run_steps<false>(acc, [&](int it, int i) { if (i == 0 && cw >= 0 && it == -2) code_warm(cw, lane, L_SCR + (TH * 2 + h) * 256); });
  const rsrc_t ro = mkrsrc(ws + WS_O, S * D * 4);
  if (l == 0) bar();
  float mt[5], cv[2], rq[4], ra[4], rb[4]; f32x4 m4[5], cq[2], ck[2];
#pragma unroll
  for (int c = 0; c < 2; ++c) {
    ck[c] = *(const LAS f32x4*)(uintptr_t)(c4 + 256 + c * 64); cv[c] = *(const LAS float*)(uintptr_t)(c1 + c * 64);
    cq[c] = *(const LAS f32x4*)(uintptr_t)(c4 + c * 64);
  }
#pragma unroll
  for (int j = 4; j >= 0; --j) {
    const int o = 320 + (j == 4 ? -64 - 256 * TH : j * 64);
    if (j < 4 || !TH) mt[j] = *(const LAS float*)(uintptr_t)(am + o);
    if (j < 4 || TH) m4[j] = *(const LAS f32x4*)(uintptr_t)(a4 + o);
  }
#pragma unroll
  for (int j = 0; j < 4; ++j) { rq[j] = *(const LAS float*)(uintptr_t)(am + 896 + j * 64); ra[j] = *(const LAS float*)(uintptr_t)(aa + j * 64); rb[j] = *(const LAS float*)(uintptr_t)(ab + j * 64); }
  u32x2 kp[2][5], qp[2][4], vp[2][5];
  auto fk = [&](int j) { kp[0][(j + 1) % 5] = pk4(acc[1][j]); kp[1][(j + 1) % 5] = pk4(acc[4][j] + ck[1] * mt[j]); };
  auto fv = [&](int j) { vp[0][(j + 1) % 5] = pk4(acc[2][j]); vp[1][(j + 1) % 5] = pk4(acc[5][j] + m4[j] * cv[1]); };
  auto fqq = [&](int j) { qp[0][j] = pk4(acc[0][j]); qp[1][j] = pk4(acc[3][j] + cq[1] * mt[j]); };
  g.template pend<10, 13>(acc);
#pragma unroll
  for (int i = 0; i < 6; ++i) { __builtin_amdgcn_sched_group_barrier(0x100, 3, 0); __builtin_amdgcn_sched_group_barrier(0x8, 1, 0); }
  sched0();
  for_c<0, 20>([&](auto mc) {
    constexpr int m = mc.value, n0 = m * 52 / 20, n1 = (m + 1) * 52 / 20;
    g.template mm<(18 - (m & ~1)) + (m & 1)>(acc);
    sched0();
#pragma unroll
    for (int n = n0; n < n1; ++n) {
      const int t = 12 - (n >> 2), e = n & 3, j = t == 12 ? 4 : t / 3, i = t == 12 ? 1 + TH : t % 3;
      float x = acc[i][j][e];
      asm volatile("v_fmac_f32 %0, %1, %2" : "+v"(x) : "v"(i == 2 ? m4[j][e] : i ? ck[0][e] : cq[0][e]), "v"(i == 2 ? cv[0] : mt[j]));
      acc[i][j][e] = x;
    }
    sched0();
  });
  volatile LAS unsigned* xf = (volatile LAS unsigned*)(uintptr_t)(L_TAG + 16 + (h * 2 + TH) * 4);
  if (TH) { fv(4); *(volatile LAS bf16x8*)(uintptr_t)(xb + 2048) = cat(vp[0][0], vp[1][0]); *xf = l + 1; fk(3); }
  else { fk(3); fv(3); *(volatile LAS bf16x8*)(uintptr_t)xb = cat(kp[0][4], kp[1][4]); *(volatile LAS bf16x8*)(uintptr_t)(xb + 1024) = cat(vp[0][4], vp[1][4]); *xf = l + 1; }
  sched0();
  fqq(3); fk(2); fqq(2); fk(1);
  sched0();
  const f32x4 z = {0.f, 0.f, 0.f, 0.f};
  f32x4 sc[4][2]; float a[4], b[4], m[4];
  auto scores = [&](int j) {
#pragma unroll
    for (int u = 1; u >= 0; --u) sc[j][u] = mfma(cat(kp[0][j + u], kp[1][j + u]), cat(qp[0][j], qp[1][j]), z);
  };
  auto pick = [&](int j) {
    const f32x4 s0 = sc[j][0], s1 = sc[j][1];
    a[j] *= sel(par, sel(ca, s1[1], s0[1]), sel(ca, s1[0], s0[0]));
    b[j] *= sel(par, sel(cb, s1[3], s0[3]), sel(cb, s1[2], s0[2]));
    if (!TH && j == 0) { a[j] = sel(ca || sb, a[j], -1e9f * 1.4426950408889634f); b[j] = sel(cb || sb, b[j], -1e9f * 1.4426950408889634f); }
    m[j] = __builtin_fmaxf(a[j], b[j]);
  };
  auto expo = [&](int j) { a[j] = __builtin_amdgcn_exp2f(a[j] - m[j]); b[j] = __builtin_amdgcn_exp2f(b[j] - m[j]); m[j] = a[j] + b[j]; };
  auto pv = [&](int j) {
    const unsigned pa = pk2(a[j] * ra[j], a[j] * ra[j]), pb = pk2(b[j] * rb[j], b[j] * rb[j]);
    const bf16x8 pf = __builtin_bit_cast(bf16x8, (u32x4){pa & m0, pb & m1, pa & (m0 ^ hm), pb & (m1 ^ hm)});
#pragma unroll
    for (int c = 0; c < 2; ++c) sc[j][c] = mfma(cat(vp[c][j], vp[c][j + 1]), pf, z);
  };
  auto outp = [&](int j) { st16(ro, oo + j * 4096, pack_pair(sc[j][0] * m[j], sc[j][1] * m[j])); };
  unsigned f; u32x4 xv, xk;
  auto xrd = [&]() {
    f = xf[1 - 2 * TH]; xv = *(volatile LAS u32x4*)(uintptr_t)(xb + (TH ? 1024 : 2048));
    if (TH) xk = *(volatile LAS u32x4*)(uintptr_t)xb;
  };
  auto xget = [&]() {
    sched0();
    while (__builtin_expect(__builtin_amdgcn_readfirstlane(f) != l + 1, 0)) xrd();
    vp[0][0] = (u32x2){xv[0], xv[1]}; vp[1][0] = (u32x2){xv[2], xv[3]};
    if (TH) { kp[0][0] = (u32x2){xk[0], xk[1]}; kp[1][0] = (u32x2){xk[2], xk[3]}; }
  };
  {
    fqq(1); fk(0); fqq(0);
    if (!TH) fk(4);
    xrd();
    sched0();
    if (TH) fv(3);
    fv(2); fv(1); fv(0);
#pragma unroll
    for (int c = 0; c < 2; ++c)
#pragma unroll
      for (int j = 0; j < 4; ++j) asm volatile("" : "+v"(qp[c][j]), "+v"(kp[c][j + 1]), "+v"(vp[c][j + 1]));
    if (!TH) asm volatile("" : "+v"(kp[0][0]), "+v"(kp[1][0]));
    xget();
    scores(3); scores(2); scores(1); scores(0);
#pragma unroll
    for (int j = 0; j < 4; ++j) { const float r = rq[j] * (0.17677669529663687f * 1.4426950408889634f); a[j] = r * ra[j]; b[j] = r * rb[j]; }
#pragma unroll
    for (int i = 0; i < 8; ++i) { __builtin_amdgcn_sched_group_barrier(0x8, 1, 0); __builtin_amdgcn_sched_group_barrier(0x2, 2, 0); }
    sched0();
    pick(3); pick(2); pick(1); pick(0);
    float u, w;
    swp32(m[0], m[2]); swp32(m[1], m[3]); u = m[0]; w = m[1];
    asm volatile("v_max_f32 %0, %0, %2\n\tv_max_f32 %1, %1, %3\n\ts_nop 1" : "+v"(u), "+v"(w) : "v"(m[2]), "v"(m[3]));
    swp16(u, w); u = vmax(u, w);
    xch16(u, u, w); xch32(u, m[0], m[2]); xch32(w, m[1], m[3]);
    expo(3); expo(2); expo(1); expo(0);
    pv(3); pv(2); pv(1); pv(0);
    swp32(m[0], m[2]); swp32(m[1], m[3]); u = m[0] + m[2]; w = m[1] + m[3];
    swp16(u, w); u = __builtin_amdgcn_rcpf(u + w);
    xch16(u, u, w); xch32(u, m[0], m[2]); xch32(w, m[1], m[3]);
    outp(3); outp(2); outp(1); outp(0);
  }
}

extern "C" __global__ __launch_bounds__(512, 2) void mega_kernel(const u16* x0, const u16* wqkv, const u16* wo, const u16* w1, const u16* w2, const u16* b1, const u16* b2, u16* out, char* ws) {
  const int wid0 = __builtin_amdgcn_readfirstlane(threadIdx.x >> 6);
  asm volatile("s_getpc_b64 s[40:41]\n\ts_andn2_b32 s40, s40, 63\n\ts_load_dwordx2 s[100:101], s[40:41], %0 offset:124\n\ts_load_dwordx2 s[100:101], s[40:41], %0 offset:252"
               :: "s"(__builtin_amdgcn_readfirstlane(threadIdx.x) << 2) : "s40", "s41", "s100", "s101", "scc");
  sched0();
#define PHASE_VARS \
      int tid = threadIdx.x, bid = blockIdx.x, wid = wid0; \
      asm volatile("" : "+v"(tid)); asm volatile("" : "+s"(bid), "+s"(wid), "+s"(ws)); \
      const int lw = wid - 4, lt = tid - 256, lane = tid & 63, fr = lane & 15, fq = lane >> 4, wp = wid & 1, wq = wid >> 1; \
      const unsigned j = (bid >> 3) * 4 + lw; \
      int hp, sb; tile_map<16, 16, 4, 8>(bid, hp, sb);            \
      int tp, tq; tile_map<8, 32, 4, 8>(bid, tp, tq);             \
      int fp, fq1; tile_map<28, 8, 7, 4>(bid, fp, fq1);           \
      const int cw = bid < 16 ? wid + 4 * (bid >> 3) : -1; \
      (void)cw; (void)j; (void)lt; (void)wp; (void)wq; (void)lw; (void)fr; (void)fq; (void)hp; (void)sb; (void)tp; (void)tq; (void)fp; (void)fq1;
#define LOADER_VARS PHASE_VARS asm volatile("" : "+s"(wqkv), "+s"(wo), "+s"(w1), "+s"(w2));

  if (__builtin_expect(wid0 >= 4, 1)) {
    const u16* xin = x0;
    Pfc pf(wo, w1, blockIdx.x * 4 + wid0 - 4);
#pragma unroll 1
    for (int l = 0; l < NL; ++l) {
      {
        LOADER_VARS
        const int s0 = sb * 128;
        GQ::Loader L(wqkv + hp * 64, xin, lw, lane, qkv_rot(bid));
        if (l == 0) L.pre_w1<0>();
        L.seta((unsigned)(s0 * D * 2), halo_off(s0, lw, lane), lw, lane);
        if (l == 0) { L.issue_a<0>(L.ab + L.kord(0) * 128); L.pre<1, true, true>(); }
        sched0();
        const rsrc_t rs = mkrsrc(ws + WS_ST, MB);
        f32x4 c = {0.f, 0.f, 0.f, 0.f}; f32x2 s1 = {0.f, 0.f}, s2 = {0.f, 0.f}, o = {0.f, 0.f}; float pv = 0.f;
        bf16x8 ones = {0x3F80, 0x3F80, 0x3F80, 0x3F80, 0x3F80, 0x3F80, 0x3F80, 0x3F80};
        const int ct = lw & 1 ? 8 + (sb & 3) : sb & 7; const bool cs = l == 0 && lw >= 2;
        int tpf = (slow_res(wqkv) >> 2) != (bid & 1) ? 0 : lw < 2 ? 10 : 9;
        unsigned ra = GQ::AOFF + ((hp & 3) * 32 + lw * 8) * 128 + lane * 16, rb = rd_km(lane, ct);
        unsigned so = lane & 7 ? MB : ph((unsigned)(((l * S + s0 + (hp & 3) * 32 + lw * 8 + (lane >> 3)) * 4 + (hp >> 2)) * 16));
        unsigned co = fr >= 2 || !cs ? MB : ph((unsigned)(ST_CS + ((hp * 96 + ct * 8 + fq * 2 + fr) * 2 + (sb >> 3)) * 16));
        unsigned to[3], xo = x_off(tp, tq, lw, fr, fq), q2 = ((bid & 2) * 4 + slow_res(wqkv)) * 128 + ((bid >> 3) * 2 + (lw & 1)) * (3 * D * 2);
#pragma unroll
        for (int i = 0; i < 3; ++i) {
          const int r = s0 - 16 + (i == 2 && lw ? 64 : i * 64) + (lt >> 2);
          to[i] = ph((unsigned)(((l * S + (r < 0 ? 0 : r)) * 4 + (lt & 3)) * 16));
        }
        unsigned ta = (lt & 3) < (lw ? 2 : 3) ? L_TAB + ((lt & 3) * 64 + (lt >> 2)) * 4 : L_SCR, ts = ph((unsigned)(ST_CS + (hp * 192 + (lw ? lt - 64 : lt)) * 16));
        asm volatile("" : "+v"(ra), "+v"(rb), "+v"(so), "+v"(co), "+v"(to[0]), "+v"(to[1]), "+v"(to[2]), "+v"(xo), "+v"(ta), "+v"(ts), "+v"(c), "+v"(s1), "+v"(s2), "+v"(ones), "+v"(o));
        asm volatile("" : "+s"(q2), "+s"(tpf));
        const GW::Loader W(wo + tp * 128, ws, lw, lane);
        auto pfq = [&](int n) { const unsigned o = q2 + L.kord(n) * GQ::WSTEP; spf2<0>(wqkv, o); spf2<2048>(wqkv, o); spf2<4096>(wqkv, o); };
        if (l) {
          if (QBR == 3) bar();
          if (!(EW & 8)) L.pre_w();
          if (tpf) { pfq(2 + (lw >> 1)); pfq(4 + (lw >> 1)); }
          if (QBR == 4) bar();
          L.start();
        }
        unsigned tag, tg2; u32x4 gs, a[3];
        auto rowst = [&](int i) {
          const float m0 = __uint_as_float(a[i][0]), m = red4(m0) * 0.25f, q = red4(__uint_as_float(a[i][2]) + 256.f * (m0 - m) * (m0 - m));
          o = (f32x2){sel((lt & 3) == i, m, o.x), sel((lt & 3) == i, q, o.y)};
          asm volatile("" : "+v"(o));
        };
        for_c<0, 16 - GQ::H>([&](auto tc) {
          constexpr int t = tc.value;
          u32x4 v; unsigned tv;
          L.step<t, t == 5 || t == 6 ? 1 : t == 9 || t == 10 ? 4 : 0>([&](unsigned ls) {
            if (t < 4) v = *(const LAS u32x4*)(uintptr_t)(ls + ra);
            if (t == 3) tv = *(volatile LAS unsigned*)(uintptr_t)L_TAG;
          }, [&]() {
            if (t < 8 && cs) {
              constexpr int ls = 32768 + t % 3 * GQ::SB;
              bf16x4 x[4];
#pragma unroll
              for (int e = 0; e < 4; ++e) x[e] = lds_tr4(rb + (ls & ~0x7fff) + (ls & 0x7fff) + (e >> 1) * 4096 + (e & 1) * 512);
#pragma unroll
              for (int ks = 0; ks < 2; ++ks) c = mfma(__builtin_shufflevector(x[2 * ks], x[2 * ks + 1], 0, 1, 2, 3, 4, 5, 6, 7), ones, c);
            } else if (t >= 4) {
              if (t >= 10 || t >= tpf) { if (t < TC1) pf.go(); }
              else if (lw < 2) pfq(t + 4);
              else if (t == 8) { pfq(14); pfq(15); }
            }
            if (t < 4) {
              if (t == 0) { pv = bflo(v[0]); asm volatile("" : "+v"(pv)); }
#pragma unroll
              for (int e = 0; e < 4; ++e) {
                if (SSC) {
                  float x = bflo(v[e]), y = bfhi(v[e]), a0 = s1.x, a1 = s1.y, b0 = s2.x, b1 = s2.y;
                  asm volatile("v_sub_f32 %0, %0, %6\n\tv_sub_f32 %1, %1, %6\n\tv_add_f32 %2, %2, %0\n\tv_add_f32 %3, %3, %1\n\tv_fmac_f32 %4, %0, %0\n\tv_fmac_f32 %5, %1, %1"
                               : "+v"(x), "+v"(y), "+v"(a0), "+v"(a1), "+v"(b0), "+v"(b1) : "v"(pv));
                  s1 = (f32x2){a0, a1}; s2 = (f32x2){b0, b1};
                } else { const f32x2 d = (f32x2){bflo(v[e]), bfhi(v[e])} - pv; s1 += d; s2 += d * d; }
              }
              asm volatile("" : "+v"(s1), "+v"(s2));
            }
            if (t == 3) { tag = __builtin_amdgcn_readfirstlane(tv); tg2 = tag2(tag); if (tpf == 10) pfq(7); if (l && tpf == 9) pfq(6); }
            if (t == 4) {
              const float d1 = (s1.x + s1.y) * (1.f / 32.f), ml = pv + d1, m = red8(ml) * 0.125f;
              const float q = red8(vmax((s2.x + s2.y) - 32.f * d1 * d1, 0.f) + 32.f * (ml - m) * (ml - m));
              gs = (u32x4){__float_as_uint(m), tag, __float_as_uint(q), tg2};
              asm volatile("" : "+v"(gs));
            }
            if (t == 8) { gs = (u32x4){__float_as_uint(sel(fr, c[2], c[0])), tag, __float_as_uint(sel(fr, c[3], c[1])), tg2}; asm volatile("" : "+v"(gs)); }
            if (t == (RB ? 12 : 11)) {
#pragma unroll
              for (int i = 0; i < 3; ++i) ld_ok(a[i]);
              for (int spin = 0;; ++spin) {
                unsigned bad = 0;
#pragma unroll
                for (int i = 0; i < 3; ++i) bad |= (a[i][1] ^ tag) | (a[i][3] ^ tg2);
                if (__builtin_expect(__all(bad == 0), 1) || spin_fail(spin, ws)) break;
                __builtin_amdgcn_s_sleep(8);
#pragma unroll
                for (int i = 0; i < 3; ++i) ld_again(a[i], rs, to[i]);
              }
              if (!RB) rowst(0);
            }
            if (t == 12 && !RB) { rowst(1); rowst(2); }
            if (t == 13 && RB == 2) rowst(0);
            if (t == 13 && !RB) { LAS float* p = (LAS float*)(uintptr_t)ta; p[0] = -o.x; p[144] = __builtin_amdgcn_rsqf(o.y * (1.f / D) + EPS); }
          }, [&]() {
            if (t == 4) st16(rs, so, gs);
            if (t == 8) {
              st16(rs, co, gs);
              sched0();
#pragma unroll
              for (int i = 0; i < 3; ++i) ld_raw(a[i], rs, to[i]);
            }
          });
        });
        if (l == 0) {
#pragma unroll
          for (int p = 0; p < 4; ++p) dma16((const char*)xin + (p >> 1) * 32768 + (p & 1) * 64, xo, L_X + lw * 4096 + p * 1024);
          ld_raw(gs, rs, ts);
          wait_vm<GQ::CNT + 5>();
        } else wait_vm<GQ::CNT>();
        bar();
        W.pre_w1<0>();
        if (RB) { if (RB == 1) rowst(0); rowst(1); rowst(2); LAS float* p = (LAS float*)(uintptr_t)ta; p[0] = -o.x; p[144] = __builtin_amdgcn_rsqf(o.y * (1.f / D) + EPS); }
        wait_lgkm0();
        if (l == 0) wait_vm<9>(); else wait_vm<4>();
        bar();
        W.pre_w1<1>(); W.pre_w1<2>();
        if (l == 0) {
          wait_vm<12>();
          ld_ok(gs);
          for (int spin = 0;; ++spin) {
            if (__builtin_expect(__all(((gs[1] ^ tag) | (gs[3] ^ tg2)) == 0), 1) || spin_fail(spin, ws)) break;
            __builtin_amdgcn_s_sleep(8);
            ld_again(gs, rs, ts);
          }
          float c0 = __uint_as_float(gs[0]), c1 = __uint_as_float(gs[2]);
          c0 += dppf<0xB1>(c0); c1 += dppf<0xB1>(c1);
          *(LAS float*)(uintptr_t)(L_CS + (lw ? lt - 64 : lt) * 4) = sel(lt & 1, c1, c0);
        }
        if (l == 0 && lw < TBA) { if (fr < 14) touch4(w1 + (size_t)(((bid >> 3) * TBA + lw) * 8 + (bid & 4)) * F + (bid & 3) * 896, fq * (F * 2) + fr * 128, L_SCR + lw * 256); }
        if (l == 0) { wait_lgkm0(); bar(); }
      }
      {
        LOADER_VARS
        GW::Loader L(wo + tp * 128, ws + WS_O + (size_t)tq * 32 * 8192, lw, lane);
        unsigned po = ((bid & 1) * 8 + (slow_res(wo) ^ (j & 1))) * 128 + (j >> 1) * (D * 2);
#pragma unroll
        for (int k = GW::H; k < 10; ++k) spf2(wo, po + k * GW::WSTEP);
        asm volatile("" : "+s"(po));
        sched0();
        bar();
#pragma unroll
        for (int k = 10; k < 16; ++k) spf2(wo, po + k * GW::WSTEP);
        L.start();
        L.run(16 - GW::H, [&](int) { pf.go(); });
        pf.go();
        L.tail<GW::H - 1>(); bar();
      }
      {
        LOADER_VARS
        if (bid < 224) {
          G1::Loader L(w1 + fp * 128, ws + WS_H + (size_t)fq1 * 64 * 16384, lw, lane);
          L.pre_w();
          const G2A::Loader W((const char*)(w2 + (tq & 23) * 64) + (tq & 8 ? JA * G2::WSTEP : 0), ws, lw, lane);
          const int g0 = (bid & 3) * 14, ga = (g0 + ((slow_res(w1) - g0) & 7)) ^ (j & 1), gb = ga + 8 < g0 + 14 ? ga + 8 : ga;
          const unsigned j2 = j < 32 ? 112 + (j & 15) : j;
          unsigned pa = (j >> 1) * (F * 2) + ga * 128, pb = (j >> 1) * (F * 2) + gb * 128, pd = (j2 >> 1) * (F * 2) + (j & 16 ? gb : ga) * 128;
          asm volatile("" : "+s"(pa), "+s"(pb), "+s"(pd));
          auto pfb = [&](int k) { spf2(w1, pa + k * G1::WSTEP); spf2(w1, pb + k * G1::WSTEP); spf2(w1, pd + k * G1::WSTEP); };
#pragma unroll
          for (int k = G1::H; k < G1::H + PA_1; ++k) pfb(k);
          wait_lgkm0(); pf.go();
          sched0();
          bar();
          pf.go();
          L.start();
          L.run(32 - G1::H, [&](int t) {
            if (pf.pc < 3 + NW1) pf.go();
            else if (t < 32 - G1::H - PA_1) pfb(t + G1::H + PA_1);
          });
          if (EW & 4) {
            wait_vm<2 * G1::CNT>(); bar(); W.pre_w1<1>();
            wait_vm<G1::CNT + 4>(); bar();
            wait_vm<4>(); bar(); W.pre_w1<0>();
          } else L.tail<G1::H - 1>();
          bar();
        } else {
          wait_lgkm0(); pf.go();
          bar();
          if (l == 0) {
            __builtin_amdgcn_s_sleep(24);
            while (pf.pc < 3 + NW1) { wait_lgkm0(); pf.go(); }
            spare_touch(w1, w2, bid, wid, lane);
          }
        }
        pf.pc = 99;
      }
      {
        LOADER_VARS
        constexpr int NB = F / 64 - 2 * JA;
        const char* ub = ws + WS_U + (size_t)tp * 112 * 16384;
        G2A::Loader A((const char*)(w2 + (tq & 23) * 64) + (tq & 8 ? JA * G2::WSTEP : 0), ub + (tq & 8 ? JA * 32768 : 0), lw, lane);
        if (!(EW & 4) || bid >= 224) A.pre_w();
        const GQ::Loader W(wqkv + hp * 64, ws, lw, lane, qkv_rot(bid));
        unsigned pb = ((bid & 4) * 4 + (j & 1) * 8 + slow_res(w2)) * 128 + (j >> 1) * (S * 2) + JA * G2::WSTEP, pa = pb - (bid & 2 ? 0 : JA * G2::WSTEP);
        asm volatile("" : "+s"(pa), "+s"(pb));
        auto side = [&](int n) { n += G2A::H + PA_2; if (n < JA + NB) spf2(w2, (n < JA ? pa : pb) + n * G2::WSTEP); };
#pragma unroll
        for (int k = 0; k < PA_2; ++k) side(k - PA_2);
        G2::Loader L((const char*)(w2 + tq * 64) + 2 * JA * G2::WSTEP, ub + 2 * JA * 32768, lw, lane);
        gu32* xf = (gu32*)(ws + WS_MAIL) + (l * 4 + 2) * MEDGE + (tq * 8 + tp) * 64 + 28 + lw;
        const char* xp = ws + WS_XP + (unsigned)(((bid ^ 2) * 4 + lw) << 14);
        unsigned fl = L_QKV + lw * 256, xv = ph(lane * 16);
        asm volatile("" : "+s"(xf), "+s"(xp), "+s"(fl)); asm volatile("" : "+v"(xv));
        sched0();
        bar();
        A.start();
        A.run(JA - G2A::H, [&](int t) { side(t); });
        auto stage = [&](auto s) { L.issue_w<s.value * G2::SB>(L.w + s.value * G2::WSTEP); L.issue_a<s.value * G2::SB>(L.ab + s.value * 32768); sched0(); };
        wait_vm<G2A::CNT>(); bar(); stage(IC<0>{}); side(JA - 2);
        wait_vm<G2::CNT>(); bar(); stage(IC<1>{}); side(JA - 1);
        wait_vm<G2::CNT>(); bar(); stage(IC<2>{}); stage(IC<3>{}); side(JA);
        L.t = 1; L.islot = 0;
        L.run(NB - G2::H, [&](int t) {
          if (t == NB - G2::H - FLS) asm volatile("s_mov_b32 m0, %2\n\ts_nop 0\n\tglobal_load_lds_dword %0, %1 sc1" :: "v"(0u), "s"(xf), "s"(fl) : "memory");
          side(JA + t);
        });
        auto xdma = [&]() {
#pragma unroll
          for (int i = 0; i < 4; ++i) dma16(xp + i * 2048, xv, L_X + lw * 4096 + i * 1024);
          sched0();
        };
        sched0(); wait_vm<2 * G2::CNT>(); bar();
        if (__builtin_amdgcn_readfirstlane(*(volatile LAS unsigned*)(uintptr_t)fl)) {
          xdma();
          if ((EW & 8) && l == 0) { W.pre_w1<0>(); wait_vm<G2::CNT + 10>(); bar(); W.pre_w1<1>(); wait_vm<16>(); bar(); wait_vm<12>(); }
          else { wait_vm<G2::CNT + 4>(); bar(); wait_vm<4>(); bar(); wait_vm<0>(); }
        } else {
          wait_vm<G2::CNT>(); bar(); wait_vm<0>(); bar();
          for (int spin = 0;; ++spin) {
            if (__builtin_amdgcn_readfirstlane(__hip_atomic_load(xf, RLX_AGENT)) || spin_fail(spin, ws)) break;
            __builtin_amdgcn_s_sleep(2);
          }
          xdma();
          if ((EW & 8) && l == 0) { W.pre_w(); wait_vm<12>(); } else wait_vm<0>();
        }
        bar();
        xin = (const u16*)(ws + WS_X2);
        if (NL == 1) bar();
      }
    }
    return;
  }

  {
    PHASE_VARS
    if (cw >= 0) code_warm(cw, lane, L_SCR + wid * 256);
    pfb_q(wqkv, bid, (bid >> 3) * 4 + wid, 0); pfb_q(wqkv, bid, (bid >> 3) * 4 + wid, 1);
    if (wid == 3) {
      if ((slow_res(wqkv) >> 2) == (bid & 1)) {
        __builtin_amdgcn_s_sleep(ET);
        if ((lane & 3) < 3 && lane < 40) touch4(wqkv, (unsigned)(((qkv_rot(bid) * 64 + 128 + (bid >> 3) * 10 + (lane >> 2)) & 1023) * (3 * D * 2) + (lane & 3) * (D * 2) + ((bid & 2) * 4 + slow_res(wqkv)) * 128), L_SCR + 768);
      }
    } else {
      const unsigned ep = __hip_atomic_load((gu32*)(ws + WS_EPOCH), RLX_AGENT);
      const rsrc_t rz = mkrsrc(ws + WS_XCH, 3 * MB);
      if (tid < 128) st16(rz, ph((((tid >> 6) * 256 + tq * 8 + tp) * 64 + lane) * 16), (u32x4){0u, SENT, 0u, SENT});
      else {
        const int e = (tid >> 3) & 7, line = e == 0 || e == 7 ? (2 * sb + (e & 1)) * 8 + (hp & 7) : e * 256 + bid;
        st16(rz, WS_MAIL - WS_XCH + line * 256 + (tid & 7) * 16, (u32x4){0u, 0u, 0u, 0u});
      }
      if (wid == 1 && bid < 224) touch4(b1 + fp * 128, lane * 4, L_BIAS);
      if (wid == 2) touch4(b2 + tq * 64, (lane & 31) * 4, L_BIAS + 256);
      if (wid == 0) { *(LAS unsigned*)(uintptr_t)L_TAG = ep + 1u; *(LAS u32x4*)(uintptr_t)(L_TAG + 16) = (u32x4){0u, 0u, 0u, 0u}; }
      asm volatile("s_waitcnt vmcnt(0)" :: "v"(ep) : "memory");
    }
  }
#pragma unroll 1
  for (int l = 0; l < NL; ++l) {
    {
      PHASE_VARS
      if (wid & 2) qkv_attn<1>(ws, l, wp, lane, hp, sb, cw, (qkv_rot(bid) & 15) >> 1, tp, tq);
      else qkv_attn<0>(ws, l, wp, lane, hp, sb, cw, 0, tp, tq);
    }
    {
      PHASE_VARS
      if (WSP == 0) sync_post(ws, l * 4, lane < 16 ? (sb * 16 + lane) * 64 + hp : -1, wid);
      GW::Comp<O42<false, WNE>> g; g.setup(wp, wq, lane);
      f32x4 acc[4][2];
#pragma unroll
      for (int i = 0; i < 4; ++i)
#pragma unroll
        for (int jj = 0; jj < 2; ++jj) acc[i][jj] = (f32x4){0.f, 0.f, 0.f, 0.f};
      unsigned xa = L_X + wid * 4096 + lane * 16;
      { bf16x8 s[2]; sel_frags(true, fr, fq, s); tile_add(acc, s, xa); }
      if (WSP == 1) {
#pragma unroll
        for (int i = 0; i < 4; ++i) asm volatile("" : "+v"(acc[i][0]), "+v"(acc[i][1]));
        sync_post(ws, l * 4, lane < 16 ? (sb * 16 + lane) * 64 + hp : -1, wid);
      }
      const rsrc_t rx = mkrsrc(ws + WS_XCH + l * (MB / 2), MB / 2), rh = mkrsrc(ws + WS_H, S * D * 4);
      unsigned xs = fq != fr >> 2 ? MB : ph((unsigned)(((tq * 8 + tp) * 64 + wp * 32 + wq * 16 + fr) * 16)), xl = ph((unsigned)((tq * 512 + fq * 128 + wq * 16 + fr) * 16));
      const unsigned h0 = ph((unsigned)(((tp * 32 + tq) << 14) + wp * 8192 + (wq * 32 + fr) * 128 + ((pair_chunk(fq) ^ (km_f(fr) << 1)) << 4)));
      u32x4 ho = {h0, h0 ^ 64, h0 + 4096, (h0 ^ 64) + 4096};
      bf16x8 ones = {0x3F80, 0x3F80, 0x3F80, 0x3F80, 0x3F80, 0x3F80, 0x3F80, 0x3F80};
      asm volatile("" : "+v"(xa), "+v"(xs), "+v"(xl), "+v"(ho), "+v"(ones));
      g.prep(acc);
      if (WSP == 2) sync_post(ws, l * 4, lane < 16 ? (sb * 16 + lane) * 64 + hp : -1, wid);
      sync_wait<WSP == 2 ? SW0 : SWW>(ws, l * 4, tq * 8 + tp, lane, 0xffffu, (1u << GW::H) - 1, wid);
      g.run_steps<false>(acc, [&](int it, int i) { if (i == 0 && cw >= 0 && it == -2) code_warm(cw, lane, L_SCR + wid * 256); });
      bar();
      sched0();
      g.pend<0, 4>(acc);
      u32x4 pk[2][2]; float mp[2];
      {
        f32x4 sm[2], gm[2];
#pragma unroll
        for (int i = 0; i < 2; ++i)
#pragma unroll
          for (int jj = 0; jj < 2; ++jj) {
            pk[jj][i] = cat4(pk4(acc[2 * i][jj]), pk4(acc[2 * i + 1][jj])); asm volatile("" : "+v"(pk[jj][i]));
            const bf16x8 x = __builtin_bit_cast(bf16x8, pk[jj][i]);
            gm[jj] = mfma(x, x, i ? gm[jj] : (f32x4){0.f, 0.f, 0.f, 0.f}); sm[jj] = mfma(ones, x, i ? sm[jj] : (f32x4){0.f, 0.f, 0.f, 0.f});
          }
        u32x4 st;
#pragma unroll
        for (int jj = 0; jj < 2; ++jj) {
          const f32x4 gv = gm[jj];
          st[2 * jj] = __float_as_uint(sm[jj][0]); st[2 * jj + 1] = __float_as_uint(sel(fr & 2, sel(fr & 1, gv[3], gv[2]), sel(fr & 1, gv[1], gv[0])));
          mp[jj] = sm[jj][0] * (1.f / 64.f);
        }
        st16(rx, xs, st);
      }
      sched0();
#pragma unroll
      for (int p = 0; p < 4; ++p) *(LAS u32x4*)(uintptr_t)(xa + p * 1024) = pk[p >> 1][p & 1];
      asm volatile("" : "+v"(mp[0]), "+v"(mp[1]));
#pragma unroll
      for (int jj = 0; jj < 2; ++jj)
#pragma unroll
        for (int i = 0; i < 2; ++i) {
          const auto rx_ = __builtin_amdgcn_permlane16_swap(pk[jj][i][0], pk[jj][i][2], false, false), ry_ = __builtin_amdgcn_permlane16_swap(pk[jj][i][1], pk[jj][i][3], false, false);
          const unsigned x0 = rx_[0], x1 = rx_[1], y0 = ry_[0], y1 = ry_[1];
          acc[2 * i][jj] = (f32x4){bflo(x0), bfhi(x0), bflo(y0), bfhi(y0)} - mp[jj]; acc[2 * i + 1][jj] = (f32x4){bflo(x1), bfhi(x1), bflo(y1), bfhi(y1)} - mp[jj];
          asm volatile("" : "+v"(acc[2 * i][jj]), "+v"(acc[2 * i + 1][jj]));
        }
      if (XD) __builtin_amdgcn_s_sleep(XD);
      f32x4 gr[4];
      for (int spin = 0;; ++spin) {
        asm volatile("" ::: "memory");
#pragma unroll
        for (int p = 0; p < 4; ++p) gr[p] = __builtin_bit_cast(f32x4, __builtin_amdgcn_raw_buffer_load_b128(rx, xl + p * 1024, 0, 16));
        const float lo = vmin3(vmin3(gr[0][1], gr[0][3], gr[1][1]), vmin3(gr[1][3], gr[2][1], gr[2][3]), vmin3(gr[3][1], gr[3][3], gr[3][3]));
        if (!__any(lo < 0.f) || spin_fail(spin, ws)) break;
        __builtin_amdgcn_s_sleep(XS);
      }
      {
        const f32x4 t = (gr[0] + gr[1]) + (gr[2] + gr[3]);
        float m[2] = {t[0], t[2]}, q[2] = {t[1], t[3]}, u, w, v, y;
        swp32(m[0], m[1]); swp32(q[0], q[1]);
        xch16(m[0] + m[1], u, w); xch16(q[0] + q[1], v, y);
        const float mean = (u + w) * (1.f / D), var = __builtin_fmaxf((v + y) * (1.f / D) - mean * mean, 0.f), r1 = __builtin_amdgcn_rsqf(var + EPS);
        xch32(mean, m[0], m[1]);
        xch32(r1 * __builtin_amdgcn_rsqf(var * r1 * r1 + EPS), q[0], q[1]);
#pragma unroll
        for (int jj = 0; jj < 2; ++jj) {
          const float c = (mp[jj] - m[jj]) * q[jj];
#pragma unroll
          for (int i = 0; i < 2; ++i) st16(rh, ho[jj * 2 + i], cat4(pk4(acc[2 * i][jj] * q[jj] + c), pk4(acc[2 * i + 1][jj] * q[jj] + c)));
        }
      }
    }
    {
      PHASE_VARS
      const int post = lane < 28 ? (tp * 28 + lane) * 64 + tq : -1;
      if (bid >= 224) {
        sync_post(ws, l * 4 + 1, post, wid);
        if (l == 0 && bid == 255 && wid == 0) { const unsigned tag = get_tag(); if (lane == 0) __hip_atomic_store((gu32*)(ws + WS_EPOCH), tag, RLX_AGENT); }
        if (l == 0) { __builtin_amdgcn_s_sleep(24); spare_touch(w1, w2, bid, wid, lane); }
      } else {
        G1::Comp<O44<false, 32>> g; g.setup(wp, wq, lane);
        f32x4 acc[4][4];
#pragma unroll
        for (int i = 0; i < 4; ++i) { const u32x2 b = *(const LAS u32x2*)(uintptr_t)(L_BIAS + (wp * 4 + i) * 32 + fq * 8);
#pragma unroll
          for (int jj = 0; jj < 4; ++jj) acc[i][jj] = (f32x4){bflo(b.x), bfhi(b.x), bflo(b.y), bfhi(b.y)}; }
        const rsrc_t ro = mkrsrc(ws + WS_U, D * F * 4);
        const unsigned u0 = ph((unsigned)(((fq1 * 56 + fp * 2 + wp) << 14) + (wq * 64 + fr) * 128 + ((pair_chunk(fq) ^ (fr >> 1)) << 4)));
        u32x4 uo = {u0, u0 + 4096, u0 + 8192, u0 + 12288}, uo2 = uo ^ 64;
        asm volatile("" : "+v"(uo), "+v"(uo2));
        g.prep(acc);
        sync_post(ws, l * 4 + 1, post, wid);
        sync_wait(ws, l * 4 + 1, fq1 * 28 + fp, lane, ~0u, (1u << G1::H) - 1, wid);
        g.run_steps<false>(acc, [&](int it, int i) { if (i == 0 && cw >= 0 && it == -2) code_warm(cw, lane, L_SCR + wid * 256); });
        bar();
        sched0();
        if (GSH) {
          for_c<0, 2>([&](auto jc) {
            constexpr int jj = jc.value;
            f32x4 a[2];
#pragma unroll
            for (int i = 0; i < 2; ++i) { a[i] = gelu_arg(acc[i][jj]); asm volatile("" : "+v"(a[i])); }
            sched0();
            for_c<0, 4>([&](auto ec) { constexpr int e = ec.value; g.mm<jj * 8 + e>(acc); sched0(); a[0][e] = __builtin_amdgcn_exp2f(a[0][e]); a[1][e] = __builtin_amdgcn_exp2f(a[1][e]); asm volatile("" : "+v"(a[0]), "+v"(a[1])); sched0(); });
            a[0] += 1.f; a[1] += 1.f; asm volatile("" : "+v"(a[0]), "+v"(a[1]));
            sched0();
            for_c<0, 4>([&](auto ec) { constexpr int e = ec.value; g.mm<jj * 8 + 4 + e>(acc); sched0(); a[0][e] = __builtin_amdgcn_rcpf(a[0][e]); a[1][e] = __builtin_amdgcn_rcpf(a[1][e]); asm volatile("" : "+v"(a[0]), "+v"(a[1])); sched0(); });
            st16(ro, uo[jj], pack_pair(acc[0][jj] * a[0], acc[1][jj] * a[1]));
          });
        } else g.pend<0, 8>(acc);
#pragma unroll
        for (int n = GSH ? 2 : 0; n < 8; ++n) { const int i = n >> 2, jj = n & 3; st16(ro, (i ? uo2 : uo)[jj], pack_pair(gelu_tanh(acc[2 * i][jj]), gelu_tanh(acc[2 * i + 1][jj]))); }
      }
    }
    {
      PHASE_VARS
      asm volatile("" : "+s"(out));
      G2A::Comp<O44<true, ANE>> ga; ga.setup(wp, wq, lane);
#pragma unroll
      for (int jj = 0; jj < 4; ++jj) ga.qrd[0][jj] = 65536 + rd_km(lane, wq * 2 + (jj & 1)) + (((jj >> 1) ^ (tq >> 3)) & 1) * 8192;
      G2::Comp<O42<true, BNE>> g; g.setup(wp, wq, lane);
      g.setwin();
      ga.setwin();
      if (GSP == 1) sync_post(ws, l * 4 + 2, bid < 224 && lane < 32 ? (lane * 8 + fq1) * 64 + fp : -1, wid);
      f32x4 ac4[4][4], acc[4][2];
#pragma unroll
      for (int jj = 0; jj < 2; ++jj) { const float b = bf2f(*(const LAS u16*)(uintptr_t)(L_BIAS + 256 + (wq * 2 + jj) * 32 + fr * 2));
#pragma unroll
        for (int i = 0; i < 4; ++i) { ac4[i][jj] = (f32x4){b, b, b, b}; ac4[i][jj + 2] = (f32x4){0.f, 0.f, 0.f, 0.f}; } }
      unsigned xa = L_X + wid * 4096 + lane * 16, xo = (unsigned)((bid * 4 + wid) << 14) + ph(lane * 16), oo = x_off(tp, tq, wid, fr, fq), xo2 = xo + 4096, oo2 = oo + 32768;
      unsigned fo = lane ? MB : (((tq ^ 8) * 8 + tp) * 64 + 28 + wid) * 4u, one = 1u;
      bf16x8 s[2]; sel_frags(false, fr, fq, s); tile_add(ac4, s, xa);
      const rsrc_t rx = mkrsrc(ws + WS_XP, 16 * MB), rf = mkrsrc(ws + WS_MAIL + (l * 4 + 2) * (MEDGE * 4), MEDGE * 4), ro = mkrsrc(l ? (const void*)out : (const void*)(ws + WS_X2), S * D * 2);
      asm volatile("" : "+v"(xa), "+v"(xo), "+v"(oo), "+v"(xo2), "+v"(oo2), "+v"(fo), "+v"(one), "+v"(s[0]), "+v"(s[1]));
      ga.pin(ac4);
      if (GSP == 2) sync_post(ws, l * 4 + 2, bid < 224 && lane < 32 ? (lane * 8 + fq1) * 64 + fp : -1, wid);
      static_assert(JA % 2 == 0 && G2A::H == 2, "");
      sync_wait<GSP == 2 ? SW0 : SWW>(ws, l * 4 + 2, tq * 8 + tp, lane, 0xfffffffu, tq & 8 ? 1u << JA / 2 : 1u, wid);
      ga.run_steps<false>(ac4, [&](int it, int i) { if (i == 0 && cw >= 0 && it == -2) code_warm(cw, lane, L_SCR + wid * 256); });
      ga.pend<4, 8>(ac4);
#pragma unroll
      for (int i = 0; i < 2; ++i)
#pragma unroll
        for (int jj = 0; jj < 2; ++jj) st16(rx, (jj ? xo2 : xo) + i * 2048, cat4(pk4(ac4[2 * i][jj + 2]), pk4(ac4[2 * i + 1][jj + 2])));
#pragma unroll
      for (int i = 0; i < 4; ++i) { acc[i][0] = ac4[i][0]; acc[i][1] = ac4[i][1]; }
#pragma unroll
      for (int k = 0; k < 2; ++k) { g.fp[2][k] = ga.fp[2][k]; g.fp[3][k] = ga.fp[3][k]; g.fq[0][k] = ga.fq[0][k]; g.fq[1][k] = ga.fq[1][k]; }
      sched0();
      g.run_steps<true>(acc, [&](int it, int i) {
        if (i == PS % 4 && it == PS / 4 - (F / 64 - 2 * JA) / 4) {
          wait_vm<0>();
          __builtin_amdgcn_raw_buffer_store_b32(one, rf, fo, 0, 16);
        }
      });
      bar();
      sched0();
      g.pend<0, 4>(acc);
      tile_add(acc, s, xa);
      u32x4 xr[4];
#pragma unroll
      for (int i = 0; i < 2; ++i)
#pragma unroll
        for (int jj = 0; jj < 2; ++jj) { xr[jj * 2 + i] = pack_pair(acc[2 * i][jj], acc[2 * i + 1][jj]); st16(ro, (jj ? oo2 : oo) + i * 64, xr[jj * 2 + i]); }
      if (l == 0) {
#pragma unroll
        for (int p = 0; p < 4; ++p) *(LAS u32x4*)(uintptr_t)(xa + p * 1024) = xr[p];
        if (NL == 1) sync_post(ws, 3, -1, wid);
      }
    }
  }
}

extern "C" void kernel_launch(void* const* d_in, const int* in_sizes, int n_in,
                              void* d_out, int out_size, void* d_ws, size_t ws_size,
                              hipStream_t stream) {
  (void)hipFuncSetAttribute((const void*)mega_kernel, hipFuncAttributeMaxDynamicSharedMemorySize, 163840);
  mega_kernel<<<256, 512, 163840, stream>>>((const u16*)d_in[0], (const u16*)d_in[1], (const u16*)d_in[2], (const u16*)d_in[4], (const u16*)d_in[6], (const u16*)d_in[5], (const u16*)d_in[7], (u16*)d_out, (char*)d_ws);
}
```

```cpp
#include <hip/hip_runtime.h>
#include <hip/hip_bf16.h>
#include <stdint.h>

typedef unsigned short u16;
typedef unsigned long long u64;
typedef short bf16x8 __attribute__((ext_vector_type(8)));
typedef short bf16x4 __attribute__((ext_vector_type(4)));
typedef float f32x4 __attribute__((ext_vector_type(4)));
typedef float f32x2 __attribute__((ext_vector_type(2)));
typedef unsigned u32x4 __attribute__((ext_vector_type(4)));
typedef unsigned u32x2 __attribute__((ext_vector_type(2)));
typedef __bf16 bf16v2 __attribute__((ext_vector_type(2)));
#define LAS __attribute__((address_space(3)))
#define GAS __attribute__((address_space(1)))
#define DEVI __device__ __forceinline__
typedef __attribute__((address_space(1))) unsigned gu32;
typedef __attribute__((address_space(1))) unsigned long long gu64;

constexpr int S = 2048, D = 1024, F = 3584;
constexpr float EPS = 1e-5f;

DEVI float bf2f(u16 v) { return __uint_as_float(((unsigned)v) << 16); }
DEVI float bflo(unsigned v) { return __uint_as_float(v << 16); }
DEVI float bfhi(unsigned v) { return __uint_as_float(v & 0xffff0000u); }
DEVI unsigned pk2(float lo, float hi) { bf16v2 p; p.x = (__bf16)lo; p.y = (__bf16)hi; return __builtin_bit_cast(unsigned, p); }

DEVI const void* uni_ptr(const void* p) {
  const u64 v = (u64)p;
  return (const void*)(((u64)(unsigned)__builtin_amdgcn_readfirstlane((unsigned)(v >> 32)) << 32) | (unsigned)__builtin_amdgcn_readfirstlane((unsigned)v));
}
DEVI void dma16(const void* sbase, unsigned voff, unsigned ldsaddr) {
  ldsaddr = __builtin_amdgcn_readfirstlane(ldsaddr);
  asm volatile("s_mov_b32 m0, %2\n\ts_nop 0\n\tglobal_load_lds_dwordx4 %0, %1 sc1" :: "v"(voff), "s"(sbase), "s"(ldsaddr) : "memory");
}
template <int N, int IMM> DEVI void dma_grp(const void* sbase, const unsigned* v, unsigned lb) {
  static_assert(N == 1 || N == 2 || N == 4, "");
  if constexpr (N == 4)
    asm volatile("s_add_u32 m0, %1, %2\n\ts_nop 0\n\tglobal_load_lds_dwordx4 %3, %0 sc1\n\tglobal_load_lds_dwordx4 %4, %0 offset:1024 sc1\n\t"
                 "global_load_lds_dwordx4 %5, %0 offset:2048 sc1\n\tglobal_load_lds_dwordx4 %6, %0 offset:3072 sc1"
                 :: "s"(sbase), "s"(lb), "i"(IMM), "v"(v[0]), "v"(v[1]), "v"(v[2]), "v"(v[3]) : "memory", "scc");
  else if constexpr (N == 2)
    asm volatile("s_add_u32 m0, %1, %2\n\ts_nop 0\n\tglobal_load_lds_dwordx4 %3, %0 sc1\n\tglobal_load_lds_dwordx4 %4, %0 offset:1024 sc1"
                 :: "s"(sbase), "s"(lb), "i"(IMM), "v"(v[0]), "v"(v[1]) : "memory", "scc");
  else
    asm volatile("s_add_u32 m0, %1, %2\n\ts_nop 0\n\tglobal_load_lds_dwordx4 %3, %0 sc1" :: "s"(sbase), "s"(lb), "i"(IMM), "v"(v[0]) : "memory", "scc");
}
template <int N> DEVI void wait_vm() { asm volatile("s_waitcnt vmcnt(%0)" :: "i"(N) : "memory"); }
DEVI void wait_lgkm0() { asm volatile("" ::: "memory"); __builtin_amdgcn_s_waitcnt(0xC07F); asm volatile("" ::: "memory"); }
DEVI void mfma_ip(f32x4& c, const bf16x8& a, const bf16x8& b) { asm volatile("v_mfma_f32_16x16x32_bf16 %0, %1, %2, %0" : "+v"(c) : "v"(a), "v"(b)); }
DEVI void mfma_gap() { __builtin_amdgcn_sched_barrier(0); asm volatile("s_nop 7"); __builtin_amdgcn_sched_barrier(0); }
DEVI void bar() { asm volatile("" ::: "memory"); __builtin_amdgcn_s_barrier(); asm volatile("" ::: "memory"); }
DEVI void sched0() { __builtin_amdgcn_sched_barrier(0); }

DEVI bf16x8 lds_b128(unsigned addr) { return *(const LAS bf16x8*)(uintptr_t)addr; }
DEVI bf16x4 lds_tr4(unsigned addr) { return __builtin_amdgcn_ds_read_tr16_b64_v4i16((LAS bf16x4*)(uintptr_t)addr); }

typedef __amdgpu_buffer_rsrc_t rsrc_t;
DEVI rsrc_t mkrsrc(const void* p, unsigned bytes) { return __builtin_amdgcn_make_buffer_rsrc((void*)p, 0, bytes, 0x00020000); }
DEVI void st16(rsrc_t r, unsigned off, u32x4 v) { __builtin_amdgcn_raw_buffer_store_b128(v, r, off, 0, 16); }
DEVI void ld_raw(u32x4& v, rsrc_t r, unsigned off) { asm volatile("buffer_load_dwordx4 %0, %1, %2, 0 offen sc1 ; raw load" : "=&v"(v) : "v"(off), "s"(r) : "memory"); }
DEVI void ld_again(u32x4& v, rsrc_t r, unsigned off) { asm volatile("buffer_load_dwordx4 %0, %1, %2, 0 offen sc1\n\ts_waitcnt vmcnt(0)" : "=&v"(v) : "v"(off), "s"(r) : "memory"); }
DEVI void ld_ok(u32x4& v) { asm volatile("; raw ok %0" : "+v"(v)); }
#define RLX_AGENT __ATOMIC_RELAXED, __HIP_MEMORY_SCOPE_AGENT

template <int N> DEVI float ror16(float v) { return __int_as_float(__builtin_amdgcn_update_dpp(0, __float_as_int(v), 0x120 + N, 0xf, 0xf, false)); }
DEVI float red16(float v) { v += ror16<8>(v); v += ror16<4>(v); v += ror16<2>(v); v += ror16<1>(v); return v; }
DEVI void xch32(float v, float& a, float& b) { unsigned u = __float_as_uint(v); auto r = __builtin_amdgcn_permlane32_swap(u, u, false, false); unsigned x = r[0], y = r[1]; a = __uint_as_float(x); b = __uint_as_float(y); }
DEVI void xch16(float v, float& a, float& b) { unsigned u = __float_as_uint(v); auto r = __builtin_amdgcn_permlane16_swap(u, u, false, false); unsigned x = r[0], y = r[1]; a = __uint_as_float(x); b = __uint_as_float(y); }
DEVI void swp32(float& a, float& b) { auto r = __builtin_amdgcn_permlane32_swap(__float_as_uint(a), __float_as_uint(b), false, false); unsigned x = r[0], y = r[1]; a = __uint_as_float(x); b = __uint_as_float(y); }
DEVI void swp16(float& a, float& b) { auto r = __builtin_amdgcn_permlane16_swap(__float_as_uint(a), __float_as_uint(b), false, false); unsigned x = r[0], y = r[1]; a = __uint_as_float(x); b = __uint_as_float(y); }
DEVI float colsum4(float v) { float a, b; xch32(v, a, b); v = a + b; xch16(v, a, b); return a + b; }
DEVI float vmax(float a, float b) { float r; asm("v_max_f32 %0, %1, %2" : "=v"(r) : "v"(a), "v"(b)); return r; }
DEVI float vmin3(float a, float b, float c) { float r; asm("v_min3_f32 %0, %1, %2, %3" : "=v"(r) : "v"(a), "v"(b), "v"(c)); return r; }
DEVI float vmax3(float a, float b, float c) { float r; asm("v_max3_f32 %0, %1, %2, %3" : "=v"(r) : "v"(a), "v"(b), "v"(c)); return r; }

DEVI unsigned src_kc(int c, int ld) { int row = c >> 3, ch = (c & 7) ^ ((row >> 1) & 7); return (unsigned)(row * ld * 2 + ch * 16); }
DEVI int km_f(int k) { return ((k >> 1) & 1) | (((k >> 3) & 1) << 1); }
DEVI unsigned ph(unsigned x) { return ((x >> 7) << 8) | (x & 127u); }
DEVI unsigned rd_kc(int lane, int ks) { int r = lane & 15, g = lane >> 4; return (unsigned)(r * 128 + (((ks * 4 + g) ^ (r >> 1)) << 4)); }
DEVI unsigned rd_km(int lane, int ct) {
  int g = lane >> 4, q = (lane & 15) >> 2, p = lane & 3;
  int f = ((q >> 1) & 1) | ((g & 1) << 1);
  return (unsigned)((ct >> 2) * 8192 + (8 * g + q) * 128 + (((ct & 3) ^ f) << 5) + 8 * p);
}


constexpr unsigned CODE_BYTES = 50176;
DEVI void code_warm(int part, int lane, unsigned scratch) {
  unsigned long long pc;
  asm volatile("s_getpc_b64 s[40:41]\n\ts_add_u32 s40, s40, mega_kernel@rel32@lo+4\n\ts_addc_u32 s41, s41, mega_kernel@rel32@hi+12\n\ts_mov_b64 %0, s[40:41]" : "=s"(pc) :: "s40", "s41", "scc");
  const unsigned off = (unsigned)(part * 64 + lane) * 128u;
  scratch = __builtin_amdgcn_readfirstlane(scratch);
  if (off < CODE_BYTES) asm volatile("s_nop 3\n\ts_mov_b32 m0, %2\n\ts_nop 0\n\tglobal_load_lds_dword %0, %1" :: "v"(off), "s"(pc), "s"(scratch) : "memory");
}

DEVI unsigned src_km(int lt, int ld) { const int k = lt >> 3; return (unsigned)(k * ld * 2 + (((lt & 7) ^ (km_f(k) << 1)) << 4)); }
DEVI void touch4(const void* sbase, unsigned voff, unsigned ldsaddr) {
  ldsaddr = __builtin_amdgcn_readfirstlane(ldsaddr); sbase = uni_ptr(sbase);
  asm volatile("s_nop 3\n\ts_mov_b32 m0, %2\n\ts_nop 0\n\tglobal_load_lds_dword %0, %1" :: "v"(voff), "s"(sbase), "s"(ldsaddr) : "memory");
}

template <int IMM> DEVI void spf1(const void* base, unsigned soff) { asm volatile("s_load_dword s101, %0, %1 offset:%2" :: "s"(base), "s"(soff), "i"(IMM) : "s101"); }
template <int IMM = 0> DEVI void spf2(const void* base, unsigned soff) { asm volatile("s_load_dwordx2 s[100:101], %0, %1 offset:%2" :: "s"(base), "s"(soff), "i"(IMM + 60) : "s100", "s101"); }
template <int ST> DEVI void spf2x6(const void* base, unsigned soff) {
  asm volatile("s_load_dwordx2 s[100:101], %0, %1 offset:%2\n\ts_load_dwordx2 s[100:101], %0, %1 offset:%3\n\ts_load_dwordx2 s[100:101], %0, %1 offset:%4\n\t"
               "s_load_dwordx2 s[100:101], %0, %1 offset:%5\n\ts_load_dwordx2 s[100:101], %0, %1 offset:%6\n\ts_load_dwordx2 s[100:101], %0, %1 offset:%7"
               :: "s"(base), "s"(soff), "i"(60), "i"(60 + ST), "i"(60 + 2 * ST), "i"(60 + 3 * ST), "i"(60 + 4 * ST), "i"(60 + 5 * ST) : "s100", "s101");
}
template <int ST> DEVI void spf1x12(const void* base, unsigned soff) {
  spf1<0>(base, soff); spf1<ST>(base, soff); spf1<2 * ST>(base, soff); spf1<3 * ST>(base, soff); spf1<4 * ST>(base, soff); spf1<5 * ST>(base, soff);
  spf1<6 * ST>(base, soff); spf1<7 * ST>(base, soff); spf1<8 * ST>(base, soff); spf1<9 * ST>(base, soff); spf1<10 * ST>(base, soff); spf1<11 * ST>(base, soff);
}
DEVI int lgkm_out() { int n; asm volatile("s_getreg_b32 %0, hwreg(HW_REG_IB_STS, 8, 4)" : "=s"(n)); return n; }
DEVI int slow_res(const void* w) { return (3 - ((unsigned)(uintptr_t)w >> 7)) & 7; }

constexpr int WNE = 16, ANE = 24, BNE = 13, QNE = 46;
struct Plan { int n; signed char q[40], idx[40], k[40], h[40], g[40]; };
template <class O, bool PKM, bool QKM> constexpr Plan make_plan() {
  Plan p{};
  int nr = 0;
  for (int c = 0; c < O::NRO; ++c) nr += ((O::ro[c] >> 4) ? QKM : PKM) ? 4 : 2;
  int prev = 0;
  for (int c = 0; c < O::NRO; ++c) {
    const int q = O::ro[c] >> 4, i = O::ro[c] & 15;
    int av[2] = {0, 0}, k0 = -1;
    for (int m = 0; m < 2 * O::NP; ++m) {
      const int mm = (m + 2 * O::NPEND) % (2 * O::NP);
      if ((q ? O::pj[m >> 1] : O::pi[m >> 1]) == i && m < 2 * O::NPEND) av[((m >> 1) ^ m) & 1] = m;
      if ((q ? O::pj[mm >> 1] : O::pi[mm >> 1]) == i && k0 < 0) k0 = ((mm >> 1) ^ mm) & 1;
    }
    for (int kk = 0; kk < 2; ++kk)
      for (int h = 0; h < ((q ? QKM : PKM) ? 2 : 1); ++h) {
        const int k = k0 ^ kk;
        int g = p.n * O::NE / nr;
        if (g < av[k]) g = av[k];
        if (g < prev + (p.n && O::NE >= nr ? 1 : 0)) g = prev + (p.n && O::NE >= nr ? 1 : 0);
        p.q[p.n] = q; p.idx[p.n] = i; p.k[p.n] = k; p.h[p.n] = h; p.g[p.n] = g; prev = g; ++p.n;
      }
  }
  return p;
}
struct OBase { static constexpr int pidx(int i) { return i; } static constexpr int poff(int) { return 0; } static constexpr int qoff(int j) { return j * 2048; } static constexpr bool swp(int) { return false; } };
template <bool PF, int NE_> struct O44 : OBase {
  static constexpr int TP = 4, TQ = 4, NPA = 4, NP = 16, NPEND = 8, NRO = 8, NE = NE_;
  static constexpr signed char pi[16] = {2, 3, 3, 2, 2, 3, 3, 2, 0, 1, 1, 0, 0, 1, 1, 0}, pj[16] = {0, 0, 1, 1, 2, 2, 3, 3, 0, 0, 1, 1, 2, 2, 3, 3};
  static constexpr signed char ro[8] = {0, PF ? 1 : 16, PF ? 16 : 1, 17, 18, 19, 2, 3};
};
template <bool PF, int NE_> struct O42 : OBase {
  static constexpr int TP = 4, TQ = 2, NPA = 4, NP = 8, NPEND = 4, NRO = 6, NE = NE_;
  static constexpr signed char pi[8] = {2, 3, 3, 2, 0, 1, 1, 0}, pj[8] = {0, 0, 1, 1, 0, 0, 1, 1};
  static constexpr signed char ro[6] = {0, PF ? 1 : 16, PF ? 16 : 1, 17, 3, 2};
};
template <int TH> struct OQ {
  static constexpr int TP = 6, TQ = 5, NPA = 2, NP = 26, NPEND = 13, NRO = 11, NE = QNE;
  static constexpr signed char pi[26] = {3, 4, 5, 5, 4, 3, 3, 4, 5, 5, TH ? 5 : 3, 4, TH ? 3 : 4, 0, 1, 2, 2, 1, 0, 0, 1, 2, 2, TH ? 2 : 0, 1, TH ? 0 : 1};
  static constexpr signed char pj[26] = {0, 0, 0, 1, 1, 1, 2, 2, 2, TH ? 4 : 3, 3, 3, TH ? 3 : 4, 0, 0, 0, 1, 1, 1, 2, 2, 2, TH ? 4 : 3, 3, 3, TH ? 3 : 4};
  static constexpr signed char ro[11] = {0, 1, 16, 2, 17, 18, TH ? 20 : 19, TH ? 19 : 20, 3, 4, 5};
  static constexpr int pidx(int i) { return i / 3; }
  static constexpr int poff(int i) { return i % 3 * 8192; }
  static constexpr int qoff(int j) { return j == 4 ? 0 : (1 + 4 * TH + j) * 2048; }
  static constexpr bool swp(int i) { return i % 3 == 2; }
};
template <bool PKM, bool QKM, int PB, int QB, int XB, int NS, int NT, bool WISP, int BASE, int ROT, int WLD, int WSUB, int ASTEP, int RLX = 0, bool QF = false>
struct Gemm {
  static constexpr int SB = PB + QB + XB, CNT = (PB + QB) / 4096 + (XB ? 1 : 0);
  static constexpr int PO = QF ? QB : 0, QO = QF ? 0 : PB, WB = WISP ? PB : QB, AB = WISP ? QB : PB, WOFF = WISP ? PO : QO, AOFF = WISP ? QO + XB : PO;
  static constexpr int XL = XB / 64, H = NS - 1 - RLX, CA = AB / 4096 + (XB ? 1 : 0), WSTEP = 64 * WLD * 2;
  static_assert(H * CNT <= 63 && BASE + NS * SB <= 163840, "");
  struct Loader {
    static constexpr int NW = WB / 4096, NA = AB / 4096;
    const char* w; const char* ab; unsigned wv[NW], av[NA], xv, lwb, lab, lx; int rot, islot, t; bool xl;
    DEVI Loader(const void* w_, const void* ab_, int lw, int lane, int rot_ = 0)
        : w((const char*)w_ - 3072), ab((const char*)ab_ - 3072), lwb(BASE + WOFF + lw * (NW == 6 ? 2048 : WB / 4)), lab(BASE + AOFF + lw * (AB / 4)), lx(BASE + PB + lw * (XL * 16)),
          rot(rot_), islot((H + ROT) % NS), t(0), xl(lane < XL) {
      const unsigned w0 = (lane >> 3) * (WLD * 2), f0 = (lane & 7) ^ ((lane >> 3) & 2);
#pragma unroll
      for (int i = 0; i < NW; ++i) {
        const unsigned p = NW == 6 ? (i >> 1) * 8 + lw * 2 + (i & 1) : lw * NW + i;
        wv[i] = w0 + ((f0 ^ ((i & 1) << 2)) << 4) + (NW == 6 ? (i >> 1) * WSUB + (lw * 2 + (i & 1)) * (8 * WLD * 2) - (i & 1) * 1024 : (p >> 3) * WSUB + (p & 7) * (8 * WLD * 2) - i * 1024) + 3072;
        asm volatile("" : "+v"(wv[i]));
      }
      asm volatile("" : "+s"(w), "+s"(lwb));
      if (XB) asm volatile("" : "+s"(rot));
      else seta(0, 0, lw, lane);
    }
    DEVI void seta(unsigned a0, unsigned xv_, int lw, int lane) {
      xv = xv_ + 3072;
#pragma unroll
      for (int i = 0; i < NA; ++i) { const unsigned p = lw * NA + i; av[i] = (XB ? a0 + src_kc(p * 64 + lane, D) : p * 2048 + ph(lane * 16)) + 3072 - i * 1024; asm volatile("" : "+v"(av[i])); }
      asm volatile("" : "+s"(ab), "+s"(lab));
      if (XB) { asm volatile("" : "+v"(xv)); asm volatile("" : "+s"(lx)); }
    }
    DEVI int kord(int n) const { if constexpr (XB != 0) return (n + rot) & (NT - 1); else return n; }
    template <int SO> DEVI void issue_w(const char* p, unsigned so = 0) const {
      if constexpr (NW == 6) { dma_grp<2, SO>(p, wv, lwb + so); dma_grp<2, SO + 8192>(p, wv + 2, lwb + so); dma_grp<2, SO + 16384>(p, wv + 4, lwb + so); }
      else dma_grp<NW, SO>(p, wv, lwb + so);
    }
    template <int SO> DEVI void issue_a(const char* p, unsigned so = 0) const {
      if (XB) { if (xl) dma_grp<1, SO>(p, &xv, lx + so); }
      dma_grp<NA, SO>(p, av, lab + so);
    }
    template <int S0, bool W, bool A> DEVI void pre() const {
      if constexpr (S0 < H) {
        if (W) issue_w<((S0 + ROT) % NS) * SB>(w + (kord(0) + S0) * WSTEP);
        if (A) issue_a<((S0 + ROT) % NS) * SB>(ab + (kord(0) + S0) * ASTEP);
        pre<S0 + 1, W, A>();
      }
    }
    template <int T> DEVI void pre_w1() const { issue_w<((T + ROT) % NS) * SB>(w + (kord(0) + T) * WSTEP); sched0(); }
    DEVI void pre_w() const { pre<0, true, false>(); sched0(); }
    template <int T> DEVI void peel() const {
      if constexpr (T < H - 1) {
        wait_vm<(H - 1 - T) * CA + T * CNT>();
        bar();
        issue_w<((T + H + ROT) % NS) * SB>(w + (T + H) * WSTEP); issue_a<((T + H + ROT) % NS) * SB>(ab + (T + H) * ASTEP);
        peel<T + 1>();
      }
    }
    DEVI void start() {
      sched0();
      bar();
      pre<0, false, true>();
      sched0();
      if constexpr (H == 2) wait_vm<CA>();
      else { peel<0>(); islot = (2 * H - 1 + ROT) % NS; t = H - 1; }
    }
    template <class HK> DEVI void run(int tend, HK side) {
#pragma unroll 1
      for (; t < tend; ++t) {
        const char* wk = w + (t + H) * WSTEP; const char* ak = ab + (t + H) * ASTEP;
        unsigned lw_ = lwb + islot * SB, la_ = lab + islot * SB;
        islot = islot + 1 == NS ? 0 : islot + 1;
        asm volatile("" : "+s"(wk), "+s"(ak), "+s"(lw_), "+s"(la_));
        wait_vm<(H - 1) * CNT>();
        bar();
        dma_grp<NW, 0>(wk, wv, lw_);
        side(t);
        dma_grp<NA, 0>(ak, av, la_);
        sched0();
      }
    }
    template <int T, int X, class A, class B, class C> DEVI void step(A pre_, B side, C post) const {
      const int k = kord(T + H);
      const char* wk = w + k * WSTEP; const char* ak = ab + k * ASTEP;
      asm volatile("" : "+s"(wk), "+s"(ak));
      wait_vm<(H - 1) * CNT + X>();
      bar();
      pre_(BASE + ((T + ROT) % NS) * SB);
      issue_w<((T + H + ROT) % NS) * SB>(wk);
      sched0();
      side();
      sched0();
      issue_a<((T + H + ROT) % NS) * SB>(ak);
      post();
      sched0();
    }
    template <int I> DEVI void tail() const { if constexpr (I >= 0) { sched0(); wait_vm<I * CNT>(); bar(); tail<I - 1>(); } }
  };
  static constexpr int NWIN = (NS * SB + 65535) / 65536;
  template <class O> struct Comp {
    static constexpr int TP = O::TP, TQ = O::TQ, NM = 2 * O::NP, NPA = PKM ? O::NPA : 2, NQA = QKM ? TQ : 2;
    static constexpr Plan PL = make_plan<O, PKM, QKM>();
    unsigned prd[NWIN][NPA], qrd[NWIN][NQA];
    bf16x8 fp[TP][2], fq[TQ][2]; bf16x4 lo;
    DEVI void setwin() {
#pragma unroll
      for (int w = 1; w < NWIN; ++w) {
#pragma unroll
        for (int i = 0; i < NPA; ++i) { prd[w][i] = prd[0][i] + w * 65536; asm volatile("" : "+v"(prd[w][i])); }
#pragma unroll
        for (int i = 0; i < NQA; ++i) { qrd[w][i] = qrd[0][i] + w * 65536; asm volatile("" : "+v"(qrd[w][i])); }
      }
#pragma unroll
      for (int i = 0; i < NPA; ++i) asm volatile("" : "+v"(prd[0][i]));
#pragma unroll
      for (int i = 0; i < NQA; ++i) asm volatile("" : "+v"(qrd[0][i]));
    }
    DEVI void setup(int wp, int wq, int lane) {
      const unsigned b = BASE;
      if (PKM) {
#pragma unroll
        for (int i = 0; i < TP; ++i) prd[0][i] = b + rd_km(lane, wp * TP + i);
      } else { prd[0][0] = b + rd_kc(lane, 0) + wp * TP * 2048; prd[0][1] = b + rd_kc(lane, 1) + wp * TP * 2048; }
      if (QKM) {
#pragma unroll
        for (int j = 0; j < TQ; ++j) qrd[0][j] = b + rd_km(lane, wq * TQ + j);
      } else { qrd[0][0] = b + rd_kc(lane, 0) + wq * TQ * 2048; qrd[0][1] = b + rd_kc(lane, 1) + wq * TQ * 2048; }
    }
    template <int C, int N> DEVI static unsigned wa(const unsigned (&b)[NWIN][N], int i) { return b[C >> 16][i] + (C & 0xffff); }
    template <int SLOT, int R> DEVI void rd() {
      constexpr int q = PL.q[R], i = PL.idx[R], k = PL.k[R], h = PL.h[R];
      bf16x8& f = q ? fq[q ? i : 0][k] : fp[q ? 0 : i][k];
      if constexpr (q ? QKM : PKM) {
        unsigned a;
        if constexpr (q) a = wa<SLOT * SB + QO + k * 4096 + h * 512, NQA>(qrd, i); else a = wa<SLOT * SB + PO + O::poff(i) + k * 4096 + h * 512, NPA>(prd, O::pidx(i));
        const bf16x4 x = lds_tr4(a);
        if constexpr (h) f = __builtin_shufflevector(lo, x, 0, 1, 2, 3, 4, 5, 6, 7); else lo = x;
      } else if constexpr (q) f = lds_b128(wa<SLOT * SB + QO + O::qoff(i), 2>(qrd, k));
      else f = lds_b128(wa<SLOT * SB + PO + i * 2048, 2>(prd, k));
    }
    template <int SLOT, int M, int R> DEVI void rds() { if constexpr (R < PL.n) { if constexpr (PL.g[R] == M) rd<SLOT, R>(); if constexpr (PL.g[R] <= M) rds<SLOT, M, R + 1>(); } }
    template <int M, bool IP = false> DEVI void mm(f32x4 (&acc)[TP][TQ]) {
      constexpr int i = O::pi[M >> 1], j = O::pj[M >> 1], k = ((M >> 1) ^ M) & 1;
      if constexpr (IP) { if constexpr (O::swp(i)) mfma_ip(acc[i][j], fq[j][k], fp[i][k]); else mfma_ip(acc[i][j], fp[i][k], fq[j][k]); }
      else if constexpr (O::swp(i)) acc[i][j] = __builtin_amdgcn_mfma_f32_16x16x32_bf16(fq[j][k], fp[i][k], acc[i][j], 0, 0, 0);
      else acc[i][j] = __builtin_amdgcn_mfma_f32_16x16x32_bf16(fp[i][k], fq[j][k], acc[i][j], 0, 0, 0);
    }
    template <int A, int B> DEVI void pend(f32x4 (&acc)[TP][TQ]) { if constexpr (A < B) { mm<2 * A>(acc); mm<2 * A + 1>(acc); pend<A + 1, B>(acc); } }
    template <int SLOT, bool FIRST, int M> DEVI void body(f32x4 (&acc)[TP][TQ]) {
      if constexpr (M < NM) {
        if constexpr (!FIRST || M >= 2 * O::NPEND) { mm<M, true>(acc); sched0(); }
        rds<SLOT, M, 0>();
        sched0();
        body<SLOT, FIRST, M + 1>(acc);
      }
    }
    template <int SLOT, bool FIRST> DEVI void step(f32x4 (&acc)[TP][TQ]) { if (!RLX) wait_lgkm0(); bar(); sched0(); body<SLOT, FIRST, 0>(acc); }
    template <int I, int N, int T0, class MID> DEVI void steps(f32x4 (&acc)[TP][TQ], MID mid) {
      if constexpr (I < N) { mid(I); step<(I + T0 + ROT) % NS, false>(acc); steps<I + 1, N, T0>(acc, mid); }
    }
    DEVI void prep(f32x4 (&acc)[TP][TQ]) { setwin(); pin(acc); }
    DEVI void pin(f32x4 (&acc)[TP][TQ]) {
#pragma unroll
      for (int p = 0; p < O::NP; ++p) asm volatile("" : "+v"(acc[O::pi[p]][O::pj[p]]));
    }
    template <bool CONT, class MID> DEVI void run_steps(f32x4 (&acc)[TP][TQ], MID mid) {
      constexpr int T0 = CONT ? 0 : 1;
      if constexpr (T0) step<ROT % NS, true>(acc);
#pragma unroll 1
      for (int it = 0; it < (NT - T0) / NS; ++it) {
        steps<0, NS, T0>(acc, [&](int i) { mid(it - (NT - T0) / NS, i); });
      }
      steps<0, (NT - T0) % NS, T0>(acc, [](int) {});
      wait_lgkm0();
      mfma_gap();
    }
  };
};

DEVI f32x4 gelu_arg(f32x4 u) { return u * (-2.302208198f + -0.1029432397f * (u * u)); }
DEVI f32x4 gelu_tanh(f32x4 u) {
  f32x4 a = gelu_arg(u);
#pragma unroll
  for (int e = 0; e < 4; ++e) a[e] = __builtin_amdgcn_exp2f(a[e]);
  a += 1.f;
#pragma unroll
  for (int e = 0; e < 4; ++e) a[e] = __builtin_amdgcn_rcpf(a[e]);
  return u * a;
}

template <int tilesP, int tilesQ, int gp, int gq> DEVI void tile_map(unsigned bid, int& tp, int& tq) {
  const unsigned xcd = bid & 7, idx = bid >> 3;
  constexpr unsigned gP = tilesP / gp, gQ = tilesQ / gq;
  static_assert(gP * gQ == 8, "");
  tp = (xcd % gP) * gp + idx % gp;
  tq = (xcd / gP) * gq + idx / gp;
}

template <int V> struct IC { static constexpr int value = V; };
template <int A, int B, class FN> DEVI void for_c(FN f) { if constexpr (A < B) { f(IC<A>{}); for_c<A + 1, B>(f); } }
DEVI float sel(bool c, float a, float b) { return c ? a : b; }
DEVI u32x2 pk4(const f32x4& v) { return (u32x2){pk2(v[0], v[1]), pk2(v[2], v[3])}; }
DEVI u32x4 cat4(u32x2 a, u32x2 b) { return (u32x4){a.x, a.y, b.x, b.y}; }
DEVI bf16x8 cat(u32x2 a, u32x2 b) { return __builtin_bit_cast(bf16x8, cat4(a, b)); }
DEVI f32x4 mfma(const bf16x8& a, const bf16x8& b, const f32x4& c) { return __builtin_amdgcn_mfma_f32_16x16x32_bf16(a, b, c, 0, 0, 0); }
DEVI u32x4 pack_pair(const f32x4& a, const f32x4& b) {
  const unsigned ax = pk2(a[0], a[1]), ay = pk2(a[2], a[3]), bx = pk2(b[0], b[1]), by = pk2(b[2], b[3]);
  auto rx = __builtin_amdgcn_permlane16_swap(ax, bx, false, false);
  auto ry = __builtin_amdgcn_permlane16_swap(ay, by, false, false);
  const unsigned x0 = rx[0], x1 = rx[1], y0 = ry[0], y1 = ry[1];
  return (u32x4){x0, y0, x1, y1};
}
DEVI int pair_chunk(int fq) { return ((fq & 1) << 1) | (fq >> 1); }
DEVI void sel_frags(bool paired, int fr, int fq, bf16x8 (&s)[2]) {
  const unsigned one = fr & 1 ? 0x3F800000u : 0x3F80u;
#pragma unroll
  for (int i = 0; i < 2; ++i) {
    const bool on = paired ? (fq & 1) == i && (fr >> 3) == (fq >> 1) : (fr >> 2) == fq;
    const int d = paired ? (fr & 7) >> 1 : i * 2 + ((fr >> 1) & 1);
    u32x4 v;
#pragma unroll
    for (int e = 0; e < 4; ++e) v[e] = on && d == e ? one : 0u;
    s[i] = __builtin_bit_cast(bf16x8, v);
  }
}
template <int TQ> DEVI void tile_add(f32x4 (&acc)[4][TQ], const bf16x8 (&s)[2], unsigned a) {
  bf16x8 t[4];
#pragma unroll
  for (int p = 0; p < 4; ++p) t[p] = lds_b128(a + p * 1024);
#pragma unroll
  for (int p = 0; p < 4; ++p)
#pragma unroll
    for (int i = 0; i < 2; ++i) acc[(p & 1) * 2 + i][p >> 1] = mfma(s[i], t[p], acc[(p & 1) * 2 + i][p >> 1]);
}
template <int C> DEVI float dppf(float v) { return __int_as_float(__builtin_amdgcn_update_dpp(0, __float_as_int(v), C, 0xf, 0xf, false)); }
DEVI float red8(float v) { v += dppf<0xB1>(v); v += dppf<0x4E>(v); v += dppf<0x141>(v); return v; }
DEVI float red4(float v) { v += dppf<0xB1>(v); v += dppf<0x4E>(v); return v; }

constexpr unsigned SENT = 0xBF800000u;
constexpr int SPIN_MAX = 1 << 21;

constexpr unsigned MB = 1u << 20;
constexpr unsigned WS_O = 0, WS_H = 8 * MB, WS_U = 16 * MB, WS_X2 = 32 * MB, WS_XP = 40 * MB  ;
constexpr unsigned WS_ST = 120 * MB, ST_CS = 2 * S * 64, WS_XCH = 122 * MB  , WS_EPOCH = 123 * MB, WS_ABORT = 123 * MB + 256  ;
DEVI unsigned tag2(unsigned t) { return t * 0x9E3779B1u ^ 0x5BD1E995u; }
constexpr int MEDGE = 256 * 64;
constexpr unsigned WS_MAIL = 124 * MB;

constexpr int NL = 2, QBR = 3, FLS = 3, NPL = 6, SPON = 1, PC0 = 0, STG = 1, WSP = 1, SWW = 2, GSH = 1, GSP = 1, SSC = 1, RB = 1, SI = 2, LPR = 1, CPR = 0, SW0 = 8, SW1 = 6, XD = 3, XS = 4, JA = 22, PS = 3, TBA = 3, TC1 = 12, NW1 = 9, EW = 4, ET = 8, SPW = 1, PA_1 = 3, PA_2 = 3;
constexpr int L_X = 0, L_TAB = 16384, L_CS = L_TAB + 1152, L_TAG = L_CS + 768, L_BIAS = L_TAG + 128, L_SCR = L_BIAS + 512  , L_QKV = 20480;
static_assert(L_SCR + 1536 <= L_QKV, "");
typedef Gemm<true, false, 24576, 16384, 2048, 3, D / 64, true, 32768, 0, 3 * D, 2 * D, 128> GQ;
typedef Gemm<true, false, 16384, 8192, 0, 5, D / 64, true, 40960, 2, D, 128, 16384, 1, true> GW;
typedef Gemm<true, true, 16384, 16384, 0, 4, S / 64, true, 32768, 1, F, 128, 32768, 0, true> G1;
typedef Gemm<false, true, 16384, 16384, 0, 3, JA, false, 65536, (3 - JA % 3) % 3, S, 1024, 32768> G2A;
typedef Gemm<false, true, 16384, 8192, 0, 4, F / 64 - 2 * JA, false, 65536, 0, S, 128, 32768> G2;

DEVI unsigned get_tag() { return __builtin_amdgcn_readfirstlane(*(volatile LAS unsigned*)(uintptr_t)L_TAG) + 1u; }
DEVI bool spin_fail(int spin, char* ws) {
  if ((spin & 255) != 255) return false;
  gu32* ab = (gu32*)(ws + WS_ABORT);
  const unsigned tag = get_tag();
  if (spin >= SPIN_MAX) { __hip_atomic_store(ab, tag, RLX_AGENT); return true; }
  return __builtin_amdgcn_readfirstlane(__hip_atomic_load(ab, RLX_AGENT)) == tag;
}
DEVI void sync_post(char* ws, int edge, int post, int wid) {
  unsigned o = post >= 0 && wid == 0 ? post * 4u : MB, one = 1u;
  const rsrc_t r = mkrsrc(ws + WS_MAIL + edge * (MEDGE * 4), MEDGE * 4);
  asm volatile("s_waitcnt vmcnt(0) lgkmcnt(0)" : "+v"(o), "+v"(one) : "s"(r) : "memory");
  sched0();
  bar();
  if (wid == 0) __builtin_amdgcn_raw_buffer_store_b32(one, r, o, 0, 16);
  sched0();
}
template <int DELAY = SW0> DEVI void sync_wait(char* ws, int edge, int line, int lane, unsigned all, unsigned first, int wid) {
  sched0();
  if (wid == 3) {
    gu32* pw = (gu32*)(ws + WS_MAIL) + edge * MEDGE + line * 64 + (lane & 31);
    if (DELAY) __builtin_amdgcn_s_sleep(DELAY);
    if (!STG) first = all;
    for (int spin = 0;; ++spin) {
      unsigned miss = ~(unsigned)__builtin_amdgcn_ballot_w64(__hip_atomic_load(pw, RLX_AGENT) != 0);
      if (spin_fail(spin, ws)) miss = 0;
      if (first && !(miss & first)) { bar(); first = 0; }
      if (!(miss & all)) break;
      __builtin_amdgcn_s_sleep(SW1);
    }
  } else bar();
}
DEVI unsigned halo_off(int s0, int lw, int lane) {
  const int c = lw * 32 + (lane & 31), row = c >> 3, ch = (c & 7) ^ ((row >> 1) & 7), tok = s0 - 16 + row;
  return (unsigned)((tok < 0 ? 0 : tok) * D * 2 + ch * 16);
}
DEVI int qkv_rot(int bid) { return (bid & 3) * 4 + (bid & 4) * 2; }
DEVI unsigned x_off(int tp, int tq, int w, int fr, int fq) { return (unsigned)(((tq * 64 + (w >> 1) * 32 + fr) * D + tp * 128 + (w & 1) * 64 + pair_chunk(fq) * 8) * 2); }

DEVI void pfb_q(const u16* w, int bid, int j, int n) {
  const int r = slow_res(w);
  if ((r >> 2) == (bid & 1)) {
    const unsigned o = (unsigned)(((bid & 2) * 4 + r) * 128 + (j >> 1) * (3 * D * 2) + (j & 1) * 64 + ((n + qkv_rot(bid)) & 15) * GQ::WSTEP);
    spf1<0>(w, o); spf1<2048>(w, o); spf1<4096>(w, o);
  }
}
constexpr int PFR = NPL * 128 * 1024, PF_END = TBA * 256 + NW1 * PFR / (F * 2) / 8 * 8;
static_assert(TBA * 256 * F * 2 + NW1 * PFR <= S * F * 2 && 3 * PFR >= D * D * 2, "");
struct Pfc {
  const char* p; const char* q; int pc;
  DEVI Pfc(const u16* wo, const u16* w1, unsigned g) : p((const char*)wo + g * (NPL * 128)), q((const char*)w1 + TBA * 256 * F * 2 + g * (NPL * 128)), pc(PC0) {}
  DEVI void go() {
    unsigned t, u;
    asm volatile("s_getreg_b32 %2, hwreg(HW_REG_IB_STS, 8, 4)\n\ts_cmp_lt_u32 %1, %5\n\ts_cselect_b32 %3, 0, 1\n\ts_or_b32 %2, %2, %3\n\ts_cbranch_scc1 1f\n\t"
                 "s_load_dwordx2 s[100:101], %0, 60\n\ts_load_dwordx2 s[100:101], %0, 188\n\ts_load_dwordx2 s[100:101], %0, 316\n\t"
                 "s_load_dwordx2 s[100:101], %0, 444\n\ts_load_dwordx2 s[100:101], %0, 572\n\ts_load_dwordx2 s[100:101], %0, 700\n\t"
                 ".if %8 == 7\n\ts_load_dwordx2 s[100:101], %0, 828\n\t.endif\n\t"
                 "s_mov_b32 %2, %7\n\ts_cmp_lt_u32 %1, 2\n\ts_cselect_b32 %2, %6, %2\n\ts_mov_b64 s[40:41], %0\n\ts_add_u32 s40, s40, %2\n\ts_addc_u32 s41, s41, 0\n\t"
                 "s_cmp_eq_u32 %1, 2\n\ts_cselect_b64 %0, %4, s[40:41]\n\ts_add_u32 %1, %1, 1\n1:"
                 : "+s"(p), "+s"(pc), "=&s"(t), "=&s"(u) : "s"(q), "i"(3 + NW1), "i"((D * D * 2 - PFR) / 2), "i"(PFR), "i"(NPL) : "s40", "s41", "s100", "s101", "scc");
  }
};
DEVI void spare_touch(const u16* w1, const u16* w2, int bid, int wid, int lane) {
  if (!SPON) return;
  const int j = ((bid >> 3) - 28) * 8 + wid, fr = lane & 15, fq = lane >> 4;
  const unsigned scr = L_QKV + wid * 256;
#pragma unroll 1
  for (int r = PF_END + (j * 2 + ((bid >> 2) & 1)) * 4; r < S; r += 256) { if (fr < 14) touch4(w1 + (size_t)r * F + (bid & 3) * 896, fq * (F * 2) + fr * 128, scr); wait_vm<SPW>(); }
#pragma unroll 1
  for (int r = (j * 2 + (bid & 1)) * 4; r < JA * 64; r += 256) { touch4(w2 + (size_t)(r + (bid & 2) * JA * 32) * S + (bid & 4) * 256, fq * (S * 2) + fr * 128, scr); wait_vm<SPW>(); }
#pragma unroll 1
  for (int r = JA * 128 + ((31 - j) * 2 + (bid & 1)) * 8; r < F; r += 512) { touch4(w2 + (size_t)r * S + ((bid & 7) >> 1) * 512, (lane >> 3) * (S * 2) + (lane & 7) * 128, scr); wait_vm<SPW>(); }
}

template <int TH> DEVI void qkv_attn(char* ws, int l, int h, int lane, int hp, int sb, int cw, int bid, int tp, int tq, const u16* wqkv) {
  asm volatile("; half %1" : "+v"(lane) : "i"(TH));
  const int fr = lane & 15, fq = lane >> 4;
  GQ::Comp<OQ<TH>> g;
#pragma unroll
  for (int c = 0; c < 2; ++c) { g.prd[0][c] = 32768 + rd_km(lane, h * 2 + c); g.qrd[0][c] = 32768 + rd_kc(lane, c); }
  f32x4 acc[6][5];
#pragma unroll
  for (int i = 0; i < 6; ++i)
#pragma unroll
    for (int j = 0; j < 5; ++j) acc[i][j] = (f32x4){0.f, 0.f, 0.f, 0.f};
  const int par = fr & 1, da = fr - 4 * fq - par;
  const bool ca = da >= 0, cb = da >= 2;
  const unsigned tm = L_TAB + (16 + 64 * TH) * 4, tc = L_CS + h * 128;
  unsigned am = tm + fr * 4 - 320, a4 = tm + fq * 16 - 320, aa = tm + 576 + (4 * fq + par - (ca ? 0 : 16)) * 4, ab = tm + 576 + (4 * fq + par + 2 - (cb ? 0 : 16)) * 4;
  unsigned c4 = tc + fq * 16, c1 = tc + 512 + fr * 4, xb = L_QKV + h * 3072 + lane * 16, hm = par ? 0xffff0000u : 0xffffu, m0 = ca ? 0u : hm, m1 = cb ? 0u : hm;
  unsigned oo = ph((unsigned)((((sb * 2 + TH) * 16 + hp) << 13) + fr * 128 + (((h * 4 + pair_chunk(fq)) ^ (fr >> 1)) << 4)));
  asm volatile("" : "+v"(am), "+v"(a4), "+v"(aa), "+v"(ab), "+v"(c4), "+v"(c1), "+v"(xb), "+v"(hm), "+v"(m0), "+v"(m1), "+v"(oo));
  g.prep(acc);
  if (l) {
    const int sbc = (tq >> 1) + fq;
    sync_post(ws, 3, !TH && (lane < 16 || (lane < 32 && (tq & 1) && sbc < 16)) ? (sbc * 16 + fr) * 64 + (tq - 2 * sbc + 1) * 8 + tp : -1, TH * 2 + h);
  }
  if (l) sync_wait(ws, 3, sb * 16 + hp, lane, sb ? 0xffffffu : 0xffff00u, (sb ? 0x10101u : 0x10100u) << ((qkv_rot(bid) & 15) >> 1), TH * 2 + h);
  else if (!TH || !h) wait_vm<0>();
  else if ((slow_res(wqkv) >> 2) == (bid & 1)) {
    if (ET) __builtin_amdgcn_s_sleep(ET);
    if ((lane & 3) < 3 && lane < 40) touch4(wqkv, (unsigned)(((qkv_rot(bid) * 64 + 128 + (bid >> 3) * 10 + (lane >> 2)) & 1023) * (3 * D * 2) + (lane & 3) * (D * 2) + ((bid & 2) * 4 + slow_res(wqkv)) * 128), L_SCR + 768);
  }
  g.template run_steps<false>(acc, [&](int it, int i) { if (i == 0 && cw >= 0 && it == -2) code_warm(cw, lane, L_SCR + (TH * 2 + h) * 256); });
  const rsrc_t ro = mkrsrc(ws + WS_O, S * D * 4);
  if (l == 0) bar();
  float mt[5], cv[2], rq[4], ra[4], rb[4]; f32x4 m4[5], cq[2], ck[2];
#pragma unroll
  for (int c = 0; c < 2; ++c) {
    ck[c] = *(const LAS f32x4*)(uintptr_t)(c4 + 256 + c * 64); cv[c] = *(const LAS float*)(uintptr_t)(c1 + c * 64);
    cq[c] = *(const LAS f32x4*)(uintptr_t)(c4 + c * 64);
  }
#pragma unroll
  for (int j = 4; j >= 0; --j) {
    const int o = 320 + (j == 4 ? -64 - 256 * TH : j * 64);
    if (j < 4 || !TH) mt[j] = *(const LAS float*)(uintptr_t)(am + o);
    if (j < 4 || TH) m4[j] = *(const LAS f32x4*)(uintptr_t)(a4 + o);
  }
#pragma unroll
  for (int j = 0; j < 4; ++j) { rq[j] = *(const LAS float*)(uintptr_t)(am + 896 + j * 64); ra[j] = *(const LAS float*)(uintptr_t)(aa + j * 64); rb[j] = *(const LAS float*)(uintptr_t)(ab + j * 64); }
  u32x2 kp[2][5], qp[2][4], vp[2][5];
  auto fk = [&](int j) { kp[0][(j + 1) % 5] = pk4(acc[1][j]); kp[1][(j + 1) % 5] = pk4(acc[4][j] + ck[1] * mt[j]); };
  auto fv = [&](int j) { vp[0][(j + 1) % 5] = pk4(acc[2][j]); vp[1][(j + 1) % 5] = pk4(acc[5][j] + m4[j] * cv[1]); };
  auto fqq = [&](int j) { qp[0][j] = pk4(acc[0][j]); qp[1][j] = pk4(acc[3][j] + cq[1] * mt[j]); };
  g.template pend<10, 13>(acc);
#pragma unroll
  for (int i = 0; i < 6; ++i) { __builtin_amdgcn_sched_group_barrier(0x100, 3, 0); __builtin_amdgcn_sched_group_barrier(0x8, 1, 0); }
  sched0();
  for_c<0, 20>([&](auto mc) {
    constexpr int m = mc.value, n0 = m * 52 / 20, n1 = (m + 1) * 52 / 20;
    g.template mm<(18 - (m & ~1)) + (m & 1)>(acc);
    sched0();
#pragma unroll
    for (int n = n0; n < n1; ++n) {
      const int t = 12 - (n >> 2), e = n & 3, j = t == 12 ? 4 : t / 3, i = t == 12 ? 1 + TH : t % 3;
      float x = acc[i][j][e];
      asm volatile("v_fmac_f32 %0, %1, %2" : "+v"(x) : "v"(i == 2 ? m4[j][e] : i ? ck[0][e] : cq[0][e]), "v"(i == 2 ? cv[0] : mt[j]));
      acc[i][j][e] = x;
    }
    sched0();
  });
  volatile LAS unsigned* xf = (volatile LAS unsigned*)(uintptr_t)(L_TAG + 16 + (h * 2 + TH) * 4);
  if (TH) { fv(4); *(volatile LAS bf16x8*)(uintptr_t)(xb + 2048) = cat(vp[0][0], vp[1][0]); *xf = l + 1; fk(3); }
  else { fk(3); fv(3); *(volatile LAS bf16x8*)(uintptr_t)xb = cat(kp[0][4], kp[1][4]); *(volatile LAS bf16x8*)(uintptr_t)(xb + 1024) = cat(vp[0][4], vp[1][4]); *xf = l + 1; }
  sched0();
  fqq(3); fk(2); fqq(2); fk(1);
  sched0();
  const f32x4 z = {0.f, 0.f, 0.f, 0.f};
  f32x4 sc[4][2]; float a[4], b[4], m[4];
  auto scores = [&](int j) {
#pragma unroll
    for (int u = 1; u >= 0; --u) sc[j][u] = mfma(cat(kp[0][j + u], kp[1][j + u]), cat(qp[0][j], qp[1][j]), z);
  };
  auto pick = [&](int j) {
    const f32x4 s0 = sc[j][0], s1 = sc[j][1];
    a[j] *= sel(par, sel(ca, s1[1], s0[1]), sel(ca, s1[0], s0[0]));
    b[j] *= sel(par, sel(cb, s1[3], s0[3]), sel(cb, s1[2], s0[2]));
    if (!TH && j == 0) { a[j] = sel(ca || sb, a[j], -1e9f * 1.4426950408889634f); b[j] = sel(cb || sb, b[j], -1e9f * 1.4426950408889634f); }
    m[j] = __builtin_fmaxf(a[j], b[j]);
  };
  auto expo = [&](int j) { a[j] = __builtin_amdgcn_exp2f(a[j] - m[j]); b[j] = __builtin_amdgcn_exp2f(b[j] - m[j]); m[j] = a[j] + b[j]; };
  auto pv = [&](int j) {
    const unsigned pa = pk2(a[j] * ra[j], a[j] * ra[j]), pb = pk2(b[j] * rb[j], b[j] * rb[j]);
    const bf16x8 pf = __builtin_bit_cast(bf16x8, (u32x4){pa & m0, pb & m1, pa & (m0 ^ hm), pb & (m1 ^ hm)});
#pragma unroll
    for (int c = 0; c < 2; ++c) sc[j][c] = mfma(cat(vp[c][j], vp[c][j + 1]), pf, z);
  };
  auto outp = [&](int j) { st16(ro, oo + j * 4096, pack_pair(sc[j][0] * m[j], sc[j][1] * m[j])); };
  unsigned f; u32x4 xv, xk;
  auto xrd = [&]() {
    f = xf[1 - 2 * TH]; xv = *(volatile LAS u32x4*)(uintptr_t)(xb + (TH ? 1024 : 2048));
    if (TH) xk = *(volatile LAS u32x4*)(uintptr_t)xb;
  };
  auto xget = [&]() {
    sched0();
    while (__builtin_expect(__builtin_amdgcn_readfirstlane(f) != l + 1, 0)) xrd();
    vp[0][0] = (u32x2){xv[0], xv[1]}; vp[1][0] = (u32x2){xv[2], xv[3]};
    if (TH) { kp[0][0] = (u32x2){xk[0], xk[1]}; kp[1][0] = (u32x2){xk[2], xk[3]}; }
  };
  {
    fqq(1); fk(0); fqq(0);
    if (!TH) fk(4);
    xrd();
    sched0();
    if (TH) fv(3);
    fv(2); fv(1); fv(0);
#pragma unroll
    for (int c = 0; c < 2; ++c)
#pragma unroll
      for (int j = 0; j < 4; ++j) asm volatile("" : "+v"(qp[c][j]), "+v"(kp[c][j + 1]), "+v"(vp[c][j + 1]));
    if (!TH) asm volatile("" : "+v"(kp[0][0]), "+v"(kp[1][0]));
    xget();
    scores(3); scores(2); scores(1); scores(0);
#pragma unroll
    for (int j = 0; j < 4; ++j) { const float r = rq[j] * (0.17677669529663687f * 1.4426950408889634f); a[j] = r * ra[j]; b[j] = r * rb[j]; }
#pragma unroll
    for (int i = 0; i < 8; ++i) { __builtin_amdgcn_sched_group_barrier(0x8, 1, 0); __builtin_amdgcn_sched_group_barrier(0x2, 2, 0); }
    sched0();
    pick(3); pick(2); pick(1); pick(0);
    float u, w;
    swp32(m[0], m[2]); swp32(m[1], m[3]); u = m[0]; w = m[1];
    asm volatile("v_max_f32 %0, %0, %2\n\tv_max_f32 %1, %1, %3\n\ts_nop 1" : "+v"(u), "+v"(w) : "v"(m[2]), "v"(m[3]));
    swp16(u, w); u = vmax(u, w);
    xch16(u, u, w); xch32(u, m[0], m[2]); xch32(w, m[1], m[3]);
    expo(3); expo(2); expo(1); expo(0);
    pv(3); pv(2); pv(1); pv(0);
    swp32(m[0], m[2]); swp32(m[1], m[3]); u = m[0] + m[2]; w = m[1] + m[3];
    swp16(u, w); u = __builtin_amdgcn_rcpf(u + w);
    xch16(u, u, w); xch32(u, m[0], m[2]); xch32(w, m[1], m[3]);
    outp(3); outp(2); outp(1); outp(0);
  }
}

extern "C" __global__ __launch_bounds__(512, 2) void mega_kernel(const u16* x0, const u16* wqkv, const u16* wo, const u16* w1, const u16* w2, const u16* b1, const u16* b2, u16* out, char* ws) {
  const int wid0 = __builtin_amdgcn_readfirstlane(threadIdx.x >> 6);
  asm volatile("s_getpc_b64 s[40:41]\n\ts_andn2_b32 s40, s40, 63\n\ts_load_dwordx2 s[100:101], s[40:41], %0 offset:124\n\ts_load_dwordx2 s[100:101], s[40:41], %0 offset:252"
               :: "s"(__builtin_amdgcn_readfirstlane(threadIdx.x) << 2) : "s40", "s41", "s100", "s101", "scc");
  sched0();
#define PHASE_VARS \
      int tid = threadIdx.x, bid = blockIdx.x, wid = wid0; \
      asm volatile("" : "+v"(tid)); asm volatile("" : "+s"(bid), "+s"(wid), "+s"(ws)); \
      const int lw = wid - 4, lt = tid - 256, lane = tid & 63, fr = lane & 15, fq = lane >> 4, wp = wid & 1, wq = wid >> 1; \
      const unsigned j = (bid >> 3) * 4 + lw; \
      int hp, sb; tile_map<16, 16, 4, 8>(bid, hp, sb);            \
      int tp, tq; tile_map<8, 32, 4, 8>(bid, tp, tq);             \
      int fp, fq1; tile_map<28, 8, 7, 4>(bid, fp, fq1);           \
      const int cw = bid < 16 ? wid + 4 * (bid >> 3) : -1; \
      (void)cw; (void)j; (void)lt; (void)wp; (void)wq; (void)lw; (void)fr; (void)fq; (void)hp; (void)sb; (void)tp; (void)tq; (void)fp; (void)fq1;
#define LOADER_VARS PHASE_VARS asm volatile("" : "+s"(wqkv), "+s"(wo), "+s"(w1), "+s"(w2));

  if (__builtin_expect(wid0 >= 4, 1)) {
    if (LPR) __builtin_amdgcn_s_setprio(3);
    const u16* xin = x0;
    Pfc pf(wo, w1, blockIdx.x * 4 + wid0 - 4);
#pragma unroll 1
    for (int l = 0; l < NL; ++l) {
      {
        LOADER_VARS
        const int s0 = sb * 128;
        GQ::Loader L(wqkv + hp * 64, xin, lw, lane, qkv_rot(bid));
        if (l == 0) L.pre_w1<0>();
        L.seta((unsigned)(s0 * D * 2), halo_off(s0, lw, lane), lw, lane);
        if (l == 0) { L.issue_a<0>(L.ab + L.kord(0) * 128); if (!SI) L.pre<1, true, true>(); }
        sched0();
        const rsrc_t rs = mkrsrc(ws + WS_ST, MB);
        f32x4 c = {0.f, 0.f, 0.f, 0.f}; f32x2 s1 = {0.f, 0.f}, s2 = {0.f, 0.f}, o = {0.f, 0.f}; float pv = 0.f;
        bf16x8 ones = {0x3F80, 0x3F80, 0x3F80, 0x3F80, 0x3F80, 0x3F80, 0x3F80, 0x3F80};
        const int ct = lw & 1 ? 8 + (sb & 3) : sb & 7; const bool cs = l == 0 && lw >= 2;
        int tpf = (slow_res(wqkv) >> 2) != (bid & 1) ? 0 : lw < 2 ? 10 : 9;
        unsigned ra = GQ::AOFF + ((hp & 3) * 32 + lw * 8) * 128 + lane * 16, rb = rd_km(lane, ct);
        unsigned so = lane & 7 ? MB : ph((unsigned)(((l * S + s0 + (hp & 3) * 32 + lw * 8 + (lane >> 3)) * 4 + (hp >> 2)) * 16));
        unsigned co = fr >= 2 || !cs ? MB : ph((unsigned)(ST_CS + ((hp * 96 + ct * 8 + fq * 2 + fr) * 2 + (sb >> 3)) * 16));
        asm volatile("" : "+v"(ra), "+v"(rb), "+v"(so), "+v"(co), "+v"(c), "+v"(s1), "+v"(s2), "+v"(ones), "+v"(o));
        asm volatile("" : "+s"(tpf));
        if (SI && l == 0) L.pre_w1<1>();
        sched0();
        unsigned to[3], xo = x_off(tp, tq, lw, fr, fq), q2 = ((bid & 2) * 4 + slow_res(wqkv)) * 128 + ((bid >> 3) * 2 + (lw & 1)) * (3 * D * 2);
#pragma unroll
        for (int i = 0; i < 3; ++i) {
          const int r = s0 - 16 + (i == 2 && lw ? 64 : i * 64) + (lt >> 2);
          to[i] = ph((unsigned)(((l * S + (r < 0 ? 0 : r)) * 4 + (lt & 3)) * 16));
        }
        unsigned ta = (lt & 3) < (lw ? 2 : 3) ? L_TAB + ((lt & 3) * 64 + (lt >> 2)) * 4 : L_SCR, ts = ph((unsigned)(ST_CS + (hp * 192 + (lw ? lt - 64 : lt)) * 16));
        asm volatile("" : "+v"(to[0]), "+v"(to[1]), "+v"(to[2]), "+v"(xo), "+v"(ta), "+v"(ts));
        asm volatile("" : "+s"(q2));
        if (SI == 1 && l == 0) L.pre<1, false, true>();
        sched0();
        const GW::Loader W(wo + tp * 128, ws, lw, lane);
        if (SI == 2 && l == 0) L.pre<1, false, true>();
        sched0();
        auto pfq = [&](int n) { const unsigned o = q2 + L.kord(n) * GQ::WSTEP; spf2<0>(wqkv, o); spf2<2048>(wqkv, o); spf2<4096>(wqkv, o); };
        if (l) {
          if (QBR == 3) bar();
          if (!(EW & 8)) L.pre_w();
          if (tpf) { pfq(2 + (lw >> 1)); pfq(4 + (lw >> 1)); }
          if (QBR == 4) bar();
          L.start();
        }
        if (LPR == 1) __builtin_amdgcn_s_setprio(0);
        unsigned tag, tg2; u32x4 gs, a[3];
        auto rowst = [&](int i) {
          const float m0 = __uint_as_float(a[i][0]), m = red4(m0) * 0.25f, q = red4(__uint_as_float(a[i][2]) + 256.f * (m0 - m) * (m0 - m));
          o = (f32x2){sel((lt & 3) == i, m, o.x), sel((lt & 3) == i, q, o.y)};
          asm volatile("" : "+v"(o));
        };
        for_c<0, 16 - GQ::H>([&](auto tc) {
          constexpr int t = tc.value;
          u32x4 v; unsigned tv;
          L.step<t, t == 5 || t == 6 ? 1 : t == 9 || t == 10 ? 4 : 0>([&](unsigned ls) {
            if (t < 4) v = *(const LAS u32x4*)(uintptr_t)(ls + ra);
            if (t == 3) tv = *(volatile LAS unsigned*)(uintptr_t)L_TAG;
          }, [&]() {
            if (t < 8 && cs) {
              constexpr int ls = 32768 + t % 3 * GQ::SB;
              bf16x4 x[4];
#pragma unroll
              for (int e = 0; e < 4; ++e) x[e] = lds_tr4(rb + (ls & ~0x7fff) + (ls & 0x7fff) + (e >> 1) * 4096 + (e & 1) * 512);
#pragma unroll
              for (int ks = 0; ks < 2; ++ks) c = mfma(__builtin_shufflevector(x[2 * ks], x[2 * ks + 1], 0, 1, 2, 3, 4, 5, 6, 7), ones, c);
            } else if (t >= 4) {
              if (t >= 10 || t >= tpf) { if (t < TC1) pf.go(); }
              else if (lw < 2) pfq(t + 4);
              else if (t == 8) { pfq(14); pfq(15); }
            }
            if (t < 4) {
              if (t == 0) { pv = bflo(v[0]); asm volatile("" : "+v"(pv)); }
#pragma unroll
              for (int e = 0; e < 4; ++e) {
                if (SSC) {
                  float x = bflo(v[e]), y = bfhi(v[e]), a0 = s1.x, a1 = s1.y, b0 = s2.x, b1 = s2.y;
                  asm volatile("v_sub_f32 %0, %0, %6\n\tv_sub_f32 %1, %1, %6\n\tv_add_f32 %2, %2, %0\n\tv_add_f32 %3, %3, %1\n\tv_fmac_f32 %4, %0, %0\n\tv_fmac_f32 %5, %1, %1"
                               : "+v"(x), "+v"(y), "+v"(a0), "+v"(a1), "+v"(b0), "+v"(b1) : "v"(pv));
                  s1 = (f32x2){a0, a1}; s2 = (f32x2){b0, b1};
                } else { const f32x2 d = (f32x2){bflo(v[e]), bfhi(v[e])} - pv; s1 += d; s2 += d * d; }
              }
              asm volatile("" : "+v"(s1), "+v"(s2));
            }
            if (t == 3) { tag = __builtin_amdgcn_readfirstlane(tv) + 1u; tg2 = tag2(tag); if (tpf == 10) pfq(7); if (l && tpf == 9) pfq(6); }
            if (t == 4) {
              const float d1 = (s1.x + s1.y) * (1.f / 32.f), ml = pv + d1, m = red8(ml) * 0.125f;
              const float q = red8(vmax((s2.x + s2.y) - 32.f * d1 * d1, 0.f) + 32.f * (ml - m) * (ml - m));
              gs = (u32x4){__float_as_uint(m), tag, __float_as_uint(q), tg2};
              asm volatile("" : "+v"(gs));
            }
            if (t == 8) { gs = (u32x4){__float_as_uint(sel(fr, c[2], c[0])), tag, __float_as_uint(sel(fr, c[3], c[1])), tg2}; asm volatile("" : "+v"(gs)); }
            if (t == (RB ? 12 : 11)) {
#pragma unroll
              for (int i = 0; i < 3; ++i) ld_ok(a[i]);
              for (int spin = 0;; ++spin) {
                unsigned bad = 0;
#pragma unroll
                for (int i = 0; i < 3; ++i) bad |= (a[i][1] ^ tag) | (a[i][3] ^ tg2);
                if (__builtin_expect(__all(bad == 0), 1) || spin_fail(spin, ws)) break;
                __builtin_amdgcn_s_sleep(8);
#pragma unroll
                for (int i = 0; i < 3; ++i) ld_again(a[i], rs, to[i]);
              }
              if (!RB) rowst(0);
            }
            if (t == 12 && !RB) { rowst(1); rowst(2); }
            if (t == 13 && RB == 2) rowst(0);
            if (t == 13 && !RB) { LAS float* p = (LAS float*)(uintptr_t)ta; p[0] = -o.x; p[144] = __builtin_amdgcn_rsqf(o.y * (1.f / D) + EPS); }
          }, [&]() {
            if (t == 4) st16(rs, so, gs);
            if (t == 8) {
              st16(rs, co, gs);
              sched0();
#pragma unroll
              for (int i = 0; i < 3; ++i) ld_raw(a[i], rs, to[i]);
            }
          });
        });
        if (l == 0) {
#pragma unroll
          for (int p = 0; p < 4; ++p) dma16((const char*)xin + (p >> 1) * 32768 + (p & 1) * 64, xo, L_X + lw * 4096 + p * 1024);
          ld_raw(gs, rs, ts);
          wait_vm<GQ::CNT + 5>();
        } else wait_vm<GQ::CNT>();
        bar();
        W.pre_w1<0>();
        if (RB) { if (RB == 1) rowst(0); rowst(1); rowst(2); LAS float* p = (LAS float*)(uintptr_t)ta; p[0] = -o.x; p[144] = __builtin_amdgcn_rsqf(o.y * (1.f / D) + EPS); }
        wait_lgkm0();
        if (l == 0) wait_vm<9>(); else wait_vm<4>();
        bar();
        W.pre_w1<1>(); W.pre_w1<2>();
        if (l == 0) {
          wait_vm<12>();
          ld_ok(gs);
          for (int spin = 0;; ++spin) {
            if (__builtin_expect(__all(((gs[1] ^ tag) | (gs[3] ^ tg2)) == 0), 1) || spin_fail(spin, ws)) break;
            __builtin_amdgcn_s_sleep(8);
            ld_again(gs, rs, ts);
          }
          float c0 = __uint_as_float(gs[0]), c1 = __uint_as_float(gs[2]);
          c0 += dppf<0xB1>(c0); c1 += dppf<0xB1>(c1);
          *(LAS float*)(uintptr_t)(L_CS + (lw ? lt - 64 : lt) * 4) = sel(lt & 1, c1, c0);
        }
        if (l == 0 && lw < TBA) { if (fr < 14) touch4(w1 + (size_t)(((bid >> 3) * TBA + lw) * 8 + (bid & 4)) * F + (bid & 3) * 896, fq * (F * 2) + fr * 128, L_SCR + lw * 256); }
        if (l == 0) { wait_lgkm0(); bar(); }
      }
      {
        LOADER_VARS
        GW::Loader L(wo + tp * 128, ws + WS_O + (size_t)tq * 32 * 8192, lw, lane);
        unsigned po = ((bid & 1) * 8 + (slow_res(wo) ^ (j & 1))) * 128 + (j >> 1) * (D * 2);
#pragma unroll
        for (int k = GW::H; k < 10; ++k) spf2(wo, po + k * GW::WSTEP);
        asm volatile("" : "+s"(po));
        sched0();
        bar();
#pragma unroll
        for (int k = 10; k < 16; ++k) spf2(wo, po + k * GW::WSTEP);
        L.start();
        L.run(16 - GW::H, [&](int) { pf.go(); });
        pf.go();
        L.tail<GW::H - 1>(); bar();
      }
      {
        LOADER_VARS
        if (bid < 224) {
          G1::Loader L(w1 + fp * 128, ws + WS_H + (size_t)fq1 * 64 * 16384, lw, lane);
          L.pre_w();
          const G2A::Loader W((const char*)(w2 + (tq & 23) * 64) + (tq & 8 ? JA * G2::WSTEP : 0), ws, lw, lane);
          const int g0 = (bid & 3) * 14, ga = (g0 + ((slow_res(w1) - g0) & 7)) ^ (j & 1), gb = ga + 8 < g0 + 14 ? ga + 8 : ga;
          const unsigned j2 = j < 32 ? 112 + (j & 15) : j;
          unsigned pa = (j >> 1) * (F * 2) + ga * 128, pb = (j >> 1) * (F * 2) + gb * 128, pd = (j2 >> 1) * (F * 2) + (j & 16 ? gb : ga) * 128;
          asm volatile("" : "+s"(pa), "+s"(pb), "+s"(pd));
          auto pfb = [&](int k) { spf2(w1, pa + k * G1::WSTEP); spf2(w1, pb + k * G1::WSTEP); spf2(w1, pd + k * G1::WSTEP); };
#pragma unroll
          for (int k = G1::H; k < G1::H + PA_1; ++k) pfb(k);
          wait_lgkm0(); pf.go();
          sched0();
          bar();
          pf.go();
          L.start();
          L.run(32 - G1::H, [&](int t) {
            if (pf.pc < 3 + NW1) pf.go();
            else if (t < 32 - G1::H - PA_1) pfb(t + G1::H + PA_1);
          });
          if (EW & 4) {
            wait_vm<2 * G1::CNT>(); bar(); W.pre_w1<1>();
            wait_vm<G1::CNT + 4>(); bar();
            wait_vm<4>(); bar(); W.pre_w1<0>();
          } else L.tail<G1::H - 1>();
          bar();
        } else {
          wait_lgkm0(); pf.go();
          bar();
          if (l == 0) {
            __builtin_amdgcn_s_sleep(24);
            while (pf.pc < 3 + NW1) { wait_lgkm0(); pf.go(); }
            spare_touch(w1, w2, bid, wid, lane);
          }
        }
        pf.pc = 99;
      }
      {
        LOADER_VARS
        constexpr int NB = F / 64 - 2 * JA;
        const char* ub = ws + WS_U + (size_t)tp * 112 * 16384;
        G2A::Loader A((const char*)(w2 + (tq & 23) * 64) + (tq & 8 ? JA * G2::WSTEP : 0), ub + (tq & 8 ? JA * 32768 : 0), lw, lane);
        if (!(EW & 4) || bid >= 224) A.pre_w();
        const GQ::Loader W(wqkv + hp * 64, ws, lw, lane, qkv_rot(bid));
        unsigned pb = ((bid & 4) * 4 + (j & 1) * 8 + slow_res(w2)) * 128 + (j >> 1) * (S * 2) + JA * G2::WSTEP, pa = pb - (bid & 2 ? 0 : JA * G2::WSTEP);
        asm volatile("" : "+s"(pa), "+s"(pb));
        auto side = [&](int n) { n += G2A::H + PA_2; if (n < JA + NB) spf2(w2, (n < JA ? pa : pb) + n * G2::WSTEP); };
#pragma unroll
        for (int k = 0; k < PA_2; ++k) side(k - PA_2);
        G2::Loader L((const char*)(w2 + tq * 64) + 2 * JA * G2::WSTEP, ub + 2 * JA * 32768, lw, lane);
        gu32* xf = (gu32*)(ws + WS_MAIL) + (l * 4 + 2) * MEDGE + (tq * 8 + tp) * 64 + 28 + lw;
        const char* xp = ws + WS_XP + (unsigned)(((bid ^ 2) * 4 + lw) << 14);
        unsigned fl = L_QKV + lw * 256, xv = ph(lane * 16);
        asm volatile("" : "+s"(xf), "+s"(xp), "+s"(fl)); asm volatile("" : "+v"(xv));
        sched0();
        bar();
        A.start();
        A.run(JA - G2A::H, [&](int t) { side(t); });
        auto stage = [&](auto s) { L.issue_w<s.value * G2::SB>(L.w + s.value * G2::WSTEP); L.issue_a<s.value * G2::SB>(L.ab + s.value * 32768); sched0(); };
        wait_vm<G2A::CNT>(); bar(); stage(IC<0>{}); side(JA - 2);
        wait_vm<G2::CNT>(); bar(); stage(IC<1>{}); side(JA - 1);
        wait_vm<G2::CNT>(); bar(); stage(IC<2>{}); stage(IC<3>{}); side(JA);
        L.t = 1; L.islot = 0;
        L.run(NB - G2::H, [&](int t) {
          if (t == NB - G2::H - FLS) asm volatile("s_mov_b32 m0, %2\n\ts_nop 0\n\tglobal_load_lds_dword %0, %1 sc1" :: "v"(0u), "s"(xf), "s"(fl) : "memory");
          side(JA + t);
        });
        auto xdma = [&]() {
#pragma unroll
          for (int i = 0; i < 4; ++i) dma16(xp + i * 2048, xv, L_X + lw * 4096 + i * 1024);
          sched0();
        };
        sched0(); wait_vm<2 * G2::CNT>(); bar();
        if (__builtin_amdgcn_readfirstlane(*(volatile LAS unsigned*)(uintptr_t)fl)) {
          xdma();
          if ((EW & 8) && l == 0) { W.pre_w1<0>(); wait_vm<G2::CNT + 10>(); bar(); W.pre_w1<1>(); wait_vm<16>(); bar(); wait_vm<12>(); }
          else { wait_vm<G2::CNT + 4>(); bar(); wait_vm<4>(); bar(); wait_vm<0>(); }
        } else {
          wait_vm<G2::CNT>(); bar(); wait_vm<0>(); bar();
          for (int spin = 0;; ++spin) {
            if (__builtin_amdgcn_readfirstlane(__hip_atomic_load(xf, RLX_AGENT)) || spin_fail(spin, ws)) break;
            __builtin_amdgcn_s_sleep(2);
          }
          xdma();
          if ((EW & 8) && l == 0) { W.pre_w(); wait_vm<12>(); } else wait_vm<0>();
        }
        bar();
        xin = (const u16*)(ws + WS_X2);
        if (NL == 1) bar();
      }
    }
    return;
  }

  {
    PHASE_VARS
    if (cw >= 0) code_warm(cw, lane, L_SCR + wid * 256);
    pfb_q(wqkv, bid, (bid >> 3) * 4 + wid, 0); pfb_q(wqkv, bid, (bid >> 3) * 4 + wid, 1);
    if (wid < 3) {
      if (tid == 0) asm volatile("s_mov_b32 m0, %2\n\ts_nop 0\n\tglobal_load_lds_dword %0, %1 sc1" :: "v"(WS_EPOCH), "s"(ws), "s"(L_TAG) : "memory");
      const rsrc_t rz = mkrsrc(ws + WS_XCH, 3 * MB);
      if (tid < 128) st16(rz, ph((((tid >> 6) * 256 + tq * 8 + tp) * 64 + lane) * 16), (u32x4){0u, SENT, 0u, SENT});
      else {
        const int e = (tid >> 3) & 7, line = e == 0 || e == 7 ? (2 * sb + (e & 1)) * 8 + (hp & 7) : e * 256 + bid;
        st16(rz, WS_MAIL - WS_XCH + line * 256 + (tid & 7) * 16, (u32x4){0u, 0u, 0u, 0u});
      }
      if (wid == 1 && bid < 224) touch4(b1 + fp * 128, lane * 4, L_BIAS);
      if (wid == 2) touch4(b2 + tq * 64, (lane & 31) * 4, L_BIAS + 256);
      if (wid == 0) *(LAS u32x4*)(uintptr_t)(L_TAG + 16) = (u32x4){0u, 0u, 0u, 0u};
    }
    if (CPR) __builtin_amdgcn_s_setprio(CPR);
  }
#pragma unroll 1
  for (int l = 0; l < NL; ++l) {
    {
      PHASE_VARS
      if (wid & 2) qkv_attn<1>(ws, l, wp, lane, hp, sb, cw, bid, tp, tq, wqkv);
      else qkv_attn<0>(ws, l, wp, lane, hp, sb, cw, bid, tp, tq, wqkv);
    }
    {
      PHASE_VARS
      if (WSP == 0) sync_post(ws, l * 4, lane < 16 ? (sb * 16 + lane) * 64 + hp : -1, wid);
      GW::Comp<O42<false, WNE>> g; g.setup(wp, wq, lane);
      f32x4 acc[4][2];
#pragma unroll
      for (int i = 0; i < 4; ++i)
#pragma unroll
        for (int jj = 0; jj < 2; ++jj) acc[i][jj] = (f32x4){0.f, 0.f, 0.f, 0.f};
      unsigned xa = L_X + wid * 4096 + lane * 16;
      { bf16x8 s[2]; sel_frags(true, fr, fq, s); tile_add(acc, s, xa); }
      if (WSP == 1) {
#pragma unroll
        for (int i = 0; i < 4; ++i) asm volatile("" : "+v"(acc[i][0]), "+v"(acc[i][1]));
        sync_post(ws, l * 4, lane < 16 ? (sb * 16 + lane) * 64 + hp : -1, wid);
      }
      const rsrc_t rx = mkrsrc(ws + WS_XCH + l * (MB / 2), MB / 2), rh = mkrsrc(ws + WS_H, S * D * 4);
      unsigned xs = fq != fr >> 2 ? MB : ph((unsigned)(((tq * 8 + tp) * 64 + wp * 32 + wq * 16 + fr) * 16)), xl = ph((unsigned)((tq * 512 + fq * 128 + wq * 16 + fr) * 16));
      const unsigned h0 = ph((unsigned)(((tp * 32 + tq) << 14) + wp * 8192 + (wq * 32 + fr) * 128 + ((pair_chunk(fq) ^ (km_f(fr) << 1)) << 4)));
      u32x4 ho = {h0, h0 ^ 64, h0 + 4096, (h0 ^ 64) + 4096};
      bf16x8 ones = {0x3F80, 0x3F80, 0x3F80, 0x3F80, 0x3F80, 0x3F80, 0x3F80, 0x3F80};
      asm volatile("" : "+v"(xa), "+v"(xs), "+v"(xl), "+v"(ho), "+v"(ones));
      g.prep(acc);
      if (WSP == 2) sync_post(ws, l * 4, lane < 16 ? (sb * 16 + lane) * 64 + hp : -1, wid);
      sync_wait<WSP == 2 ? SW0 : SWW>(ws, l * 4, tq * 8 + tp, lane, 0xffffu, (1u << GW::H) - 1, wid);
      g.run_steps<false>(acc, [&](int it, int i) { if (i == 0 && cw >= 0 && it == -2) code_warm(cw, lane, L_SCR + wid * 256); });
      bar();
      sched0();
      g.pend<0, 4>(acc);
      u32x4 pk[2][2]; float mp[2];
      {
        f32x4 sm[2], gm[2];
#pragma unroll
        for (int i = 0; i < 2; ++i)
#pragma unroll
          for (int jj = 0; jj < 2; ++jj) {
            pk[jj][i] = cat4(pk4(acc[2 * i][jj]), pk4(acc[2 * i + 1][jj])); asm volatile("" : "+v"(pk[jj][i]));
            const bf16x8 x = __builtin_bit_cast(bf16x8, pk[jj][i]);
            gm[jj] = mfma(x, x, i ? gm[jj] : (f32x4){0.f, 0.f, 0.f, 0.f}); sm[jj] = mfma(ones, x, i ? sm[jj] : (f32x4){0.f, 0.f, 0.f, 0.f});
          }
        u32x4 st;
#pragma unroll
        for (int jj = 0; jj < 2; ++jj) {
          const f32x4 gv = gm[jj];
          st[2 * jj] = __float_as_uint(sm[jj][0]); st[2 * jj + 1] = __float_as_uint(sel(fr & 2, sel(fr & 1, gv[3], gv[2]), sel(fr & 1, gv[1], gv[0])));
          mp[jj] = sm[jj][0] * (1.f / 64.f);
        }
        st16(rx, xs, st);
      }
      sched0();
#pragma unroll
      for (int p = 0; p < 4; ++p) *(LAS u32x4*)(uintptr_t)(xa + p * 1024) = pk[p >> 1][p & 1];
      asm volatile("" : "+v"(mp[0]), "+v"(mp[1]));
#pragma unroll
      for (int jj = 0; jj < 2; ++jj)
#pragma unroll
        for (int i = 0; i < 2; ++i) {
          const auto rx_ = __builtin_amdgcn_permlane16_swap(pk[jj][i][0], pk[jj][i][2], false, false), ry_ = __builtin_amdgcn_permlane16_swap(pk[jj][i][1], pk[jj][i][3], false, false);
          const unsigned x0 = rx_[0], x1 = rx_[1], y0 = ry_[0], y1 = ry_[1];
          acc[2 * i][jj] = (f32x4){bflo(x0), bfhi(x0), bflo(y0), bfhi(y0)} - mp[jj]; acc[2 * i + 1][jj] = (f32x4){bflo(x1), bfhi(x1), bflo(y1), bfhi(y1)} - mp[jj];
          asm volatile("" : "+v"(acc[2 * i][jj]), "+v"(acc[2 * i + 1][jj]));
        }
      if (XD) __builtin_amdgcn_s_sleep(XD);
      f32x4 gr[4];
      for (int spin = 0;; ++spin) {
        asm volatile("" ::: "memory");
#pragma unroll
        for (int p = 0; p < 4; ++p) gr[p] = __builtin_bit_cast(f32x4, __builtin_amdgcn_raw_buffer_load_b128(rx, xl + p * 1024, 0, 16));
        const float lo = vmin3(vmin3(gr[0][1], gr[0][3], gr[1][1]), vmin3(gr[1][3], gr[2][1], gr[2][3]), vmin3(gr[3][1], gr[3][3], gr[3][3]));
        if (!__any(lo < 0.f) || spin_fail(spin, ws)) break;
        __builtin_amdgcn_s_sleep(XS);
      }
      {
        const f32x4 t = (gr[0] + gr[1]) + (gr[2] + gr[3]);
        float m[2] = {t[0], t[2]}, q[2] = {t[1], t[3]}, u, w, v, y;
        swp32(m[0], m[1]); swp32(q[0], q[1]);
        xch16(m[0] + m[1], u, w); xch16(q[0] + q[1], v, y);
        const float mean = (u + w) * (1.f / D), var = __builtin_fmaxf((v + y) * (1.f / D) - mean * mean, 0.f), r1 = __builtin_amdgcn_rsqf(var + EPS);
        xch32(mean, m[0], m[1]);
        xch32(r1 * __builtin_amdgcn_rsqf(var * r1 * r1 + EPS), q[0], q[1]);
#pragma unroll
        for (int jj = 0; jj < 2; ++jj) {
          const float c = (mp[jj] - m[jj]) * q[jj];
#pragma unroll
          for (int i = 0; i < 2; ++i) st16(rh, ho[jj * 2 + i], cat4(pk4(acc[2 * i][jj] * q[jj] + c), pk4(acc[2 * i + 1][jj] * q[jj] + c)));
        }
      }
    }
    {
      PHASE_VARS
      const int post = lane < 28 ? (tp * 28 + lane) * 64 + tq : -1;
      if (bid >= 224) {
        sync_post(ws, l * 4 + 1, post, wid);
        if (l == 0 && bid == 255 && wid == 0) { const unsigned tag = get_tag(); if (lane == 0) __hip_atomic_store((gu32*)(ws + WS_EPOCH), tag, RLX_AGENT); }
        if (l == 0) { __builtin_amdgcn_s_sleep(24); spare_touch(w1, w2, bid, wid, lane); }
      } else {
        G1::Comp<O44<false, 32>> g; g.setup(wp, wq, lane);
        f32x4 acc[4][4];
#pragma unroll
        for (int i = 0; i < 4; ++i) { const u32x2 b = *(const LAS u32x2*)(uintptr_t)(L_BIAS + (wp * 4 + i) * 32 + fq * 8);
#pragma unroll
          for (int jj = 0; jj < 4; ++jj) acc[i][jj] = (f32x4){bflo(b.x), bfhi(b.x), bflo(b.y), bfhi(b.y)}; }
        const rsrc_t ro = mkrsrc(ws + WS_U, D * F * 4);
        const unsigned u0 = ph((unsigned)(((fq1 * 56 + fp * 2 + wp) << 14) + (wq * 64 + fr) * 128 + ((pair_chunk(fq) ^ (fr >> 1)) << 4)));
        u32x4 uo = {u0, u0 + 4096, u0 + 8192, u0 + 12288}, uo2 = uo ^ 64;
        asm volatile("" : "+v"(uo), "+v"(uo2));
        g.prep(acc);
        sync_post(ws, l * 4 + 1, post, wid);
        sync_wait(ws, l * 4 + 1, fq1 * 28 + fp, lane, ~0u, (1u << G1::H) - 1, wid);
        g.run_steps<false>(acc, [&](int it, int i) { if (i == 0 && cw >= 0 && it == -2) code_warm(cw, lane, L_SCR + wid * 256); });
        bar();
        sched0();
        if (GSH) {
          for_c<0, 2>([&](auto jc) {
            constexpr int jj = jc.value;
            f32x4 a[2];
#pragma unroll
            for (int i = 0; i < 2; ++i) { a[i] = gelu_arg(acc[i][jj]); asm volatile("" : "+v"(a[i])); }
            sched0();
            for_c<0, 4>([&](auto ec) { constexpr int e = ec.value; g.mm<jj * 8 + e>(acc); sched0(); a[0][e] = __builtin_amdgcn_exp2f(a[0][e]); a[1][e] = __builtin_amdgcn_exp2f(a[1][e]); asm volatile("" : "+v"(a[0]), "+v"(a[1])); sched0(); });
            a[0] += 1.f; a[1] += 1.f; asm volatile("" : "+v"(a[0]), "+v"(a[1]));
            sched0();
            for_c<0, 4>([&](auto ec) { constexpr int e = ec.value; g.mm<jj * 8 + 4 + e>(acc); sched0(); a[0][e] = __builtin_amdgcn_rcpf(a[0][e]); a[1][e] = __builtin_amdgcn_rcpf(a[1][e]); asm volatile("" : "+v"(a[0]), "+v"(a[1])); sched0(); });
            st16(ro, uo[jj], pack_pair(acc[0][jj] * a[0], acc[1][jj] * a[1]));
          });
        } else g.pend<0, 8>(acc);
#pragma unroll
        for (int n = GSH ? 2 : 0; n < 8; ++n) { const int i = n >> 2, jj = n & 3; st16(ro, (i ? uo2 : uo)[jj], pack_pair(gelu_tanh(acc[2 * i][jj]), gelu_tanh(acc[2 * i + 1][jj]))); }
      }
    }
    {
      PHASE_VARS
      asm volatile("" : "+s"(out));
      G2A::Comp<O44<true, ANE>> ga; ga.setup(wp, wq, lane);
#pragma unroll
      for (int jj = 0; jj < 4; ++jj) ga.qrd[0][jj] = 65536 + rd_km(lane, wq * 2 + (jj & 1)) + (((jj >> 1) ^ (tq >> 3)) & 1) * 8192;
      G2::Comp<O42<true, BNE>> g; g.setup(wp, wq, lane);
      g.setwin();
      ga.setwin();
      if (GSP == 1) sync_post(ws, l * 4 + 2, bid < 224 && lane < 32 ? (lane * 8 + fq1) * 64 + fp : -1, wid);
      f32x4 ac4[4][4], acc[4][2];
#pragma unroll
      for (int jj = 0; jj < 2; ++jj) { const float b = bf2f(*(const LAS u16*)(uintptr_t)(L_BIAS + 256 + (wq * 2 + jj) * 32 + fr * 2));
#pragma unroll
        for (int i = 0; i < 4; ++i) { ac4[i][jj] = (f32x4){b, b, b, b}; ac4[i][jj + 2] = (f32x4){0.f, 0.f, 0.f, 0.f}; } }
      unsigned xa = L_X + wid * 4096 + lane * 16, xo = (unsigned)((bid * 4 + wid) << 14) + ph(lane * 16), oo = x_off(tp, tq, wid, fr, fq), xo2 = xo + 4096, oo2 = oo + 32768;
      unsigned fo = lane ? MB : (((tq ^ 8) * 8 + tp) * 64 + 28 + wid) * 4u, one = 1u;
      bf16x8 s[2]; sel_frags(false, fr, fq, s); tile_add(ac4, s, xa);
      const rsrc_t rx = mkrsrc(ws + WS_XP, 16 * MB), rf = mkrsrc(ws + WS_MAIL + (l * 4 + 2) * (MEDGE * 4), MEDGE * 4), ro = mkrsrc(l ? (const void*)out : (const void*)(ws + WS_X2), S * D * 2);
      asm volatile("" : "+v"(xa), "+v"(xo), "+v"(oo), "+v"(xo2), "+v"(oo2), "+v"(fo), "+v"(one), "+v"(s[0]), "+v"(s[1]));
      ga.pin(ac4);
      if (GSP == 2) sync_post(ws, l * 4 + 2, bid < 224 && lane < 32 ? (lane * 8 + fq1) * 64 + fp : -1, wid);
      static_assert(JA % 2 == 0 && G2A::H == 2, "");
      sync_wait<GSP == 2 ? SW0 : SWW>(ws, l * 4 + 2, tq * 8 + tp, lane, 0xfffffffu, tq & 8 ? 1u << JA / 2 : 1u, wid);
      ga.run_steps<false>(ac4, [&](int it, int i) { if (i == 0 && cw >= 0 && it == -2) code_warm(cw, lane, L_SCR + wid * 256); });
      ga.pend<4, 8>(ac4);
#pragma unroll
      for (int i = 0; i < 2; ++i)
#pragma unroll
        for (int jj = 0; jj < 2; ++jj) st16(rx, (jj ? xo2 : xo) + i * 2048, cat4(pk4(ac4[2 * i][jj + 2]), pk4(ac4[2 * i + 1][jj + 2])));
#pragma unroll
      for (int i = 0; i < 4; ++i) { acc[i][0] = ac4[i][0]; acc[i][1] = ac4[i][1]; }
#pragma unroll
      for (int k = 0; k < 2; ++k) { g.fp[2][k] = ga.fp[2][k]; g.fp[3][k] = ga.fp[3][k]; g.fq[0][k] = ga.fq[0][k]; g.fq[1][k] = ga.fq[1][k]; }
      sched0();
      g.run_steps<true>(acc, [&](int it, int i) {
        if (i == PS % 4 && it == PS / 4 - (F / 64 - 2 * JA) / 4) {
          wait_vm<0>();
          __builtin_amdgcn_raw_buffer_store_b32(one, rf, fo, 0, 16);
        }
      });
      bar();
      sched0();
      g.pend<0, 4>(acc);
      tile_add(acc, s, xa);
      u32x4 xr[4];
#pragma unroll
      for (int i = 0; i < 2; ++i)
#pragma unroll
        for (int jj = 0; jj < 2; ++jj) { xr[jj * 2 + i] = pack_pair(acc[2 * i][jj], acc[2 * i + 1][jj]); st16(ro, (jj ? oo2 : oo) + i * 64, xr[jj * 2 + i]); }
      if (l == 0) {
#pragma unroll
        for (int p = 0; p < 4; ++p) *(LAS u32x4*)(uintptr_t)(xa + p * 1024) = xr[p];
        if (NL == 1) sync_post(ws, 3, -1, wid);
      }
    }
  }
}

extern "C" void kernel_launch(void* const* d_in, const int* in_sizes, int n_in,
                              void* d_out, int out_size, void* d_ws, size_t ws_size,
                              hipStream_t stream) {
  (void)hipFuncSetAttribute((const void*)mega_kernel, hipFuncAttributeMaxDynamicSharedMemorySize, 163840);
  mega_kernel<<<256, 512, 163840, stream>>>((const u16*)d_in[0], (const u16*)d_in[1], (const u16*)d_in[2], (const u16*)d_in[4], (const u16*)d_in[6], (const u16*)d_in[5], (const u16*)d_in[7], (u16*)d_out, (char*)d_ws);
}
```

```cpp
#define LEAN
#define STAMP
#define PROBE 35
#define PROBE0 34
#define PROBEF 16
#include <hip/hip_runtime.h>
#include <hip/hip_bf16.h>
#include <stdint.h>

typedef unsigned short u16;
typedef unsigned long long u64;
typedef short bf16x8 __attribute__((ext_vector_type(8)));
typedef short bf16x4 __attribute__((ext_vector_type(4)));
typedef float f32x4 __attribute__((ext_vector_type(4)));
typedef float f32x2 __attribute__((ext_vector_type(2)));
typedef unsigned u32x4 __attribute__((ext_vector_type(4)));
typedef unsigned u32x2 __attribute__((ext_vector_type(2)));
typedef __bf16 bf16v2 __attribute__((ext_vector_type(2)));
#define LAS __attribute__((address_space(3)))
#define GAS __attribute__((address_space(1)))
#define DEVI __device__ __forceinline__
typedef __attribute__((address_space(1))) unsigned gu32;
typedef __attribute__((address_space(1))) unsigned long long gu64;

constexpr int S = 2048, D = 1024, F = 3584;
constexpr float EPS = 1e-5f;

DEVI float bf2f(u16 v) { return __uint_as_float(((unsigned)v) << 16); }
DEVI float bflo(unsigned v) { return __uint_as_float(v << 16); }
DEVI float bfhi(unsigned v) { return __uint_as_float(v & 0xffff0000u); }
DEVI unsigned pk2(float lo, float hi) { bf16v2 p; p.x = (__bf16)lo; p.y = (__bf16)hi; return __builtin_bit_cast(unsigned, p); }

DEVI const void* uni_ptr(const void* p) {
  const u64 v = (u64)p;
  return (const void*)(((u64)(unsigned)__builtin_amdgcn_readfirstlane((unsigned)(v >> 32)) << 32) | (unsigned)__builtin_amdgcn_readfirstlane((unsigned)v));
}
DEVI void dma16(const void* sbase, unsigned voff, unsigned ldsaddr) {
  ldsaddr = __builtin_amdgcn_readfirstlane(ldsaddr);
  asm volatile("s_mov_b32 m0, %2\n\ts_nop 0\n\tglobal_load_lds_dwordx4 %0, %1 sc1" :: "v"(voff), "s"(sbase), "s"(ldsaddr) : "memory");
}
template <int N, int IMM> DEVI void dma_grp(const void* sbase, const unsigned* v, unsigned lb) {
  static_assert(N == 1 || N == 2 || N == 4, "");
  if constexpr (N == 4)
    asm volatile("s_add_u32 m0, %1, %2\n\ts_nop 0\n\tglobal_load_lds_dwordx4 %3, %0 sc1\n\tglobal_load_lds_dwordx4 %4, %0 offset:1024 sc1\n\t"
                 "global_load_lds_dwordx4 %5, %0 offset:2048 sc1\n\tglobal_load_lds_dwordx4 %6, %0 offset:3072 sc1"
                 :: "s"(sbase), "s"(lb), "i"(IMM), "v"(v[0]), "v"(v[1]), "v"(v[2]), "v"(v[3]) : "memory", "scc");
  else if constexpr (N == 2)
    asm volatile("s_add_u32 m0, %1, %2\n\ts_nop 0\n\tglobal_load_lds_dwordx4 %3, %0 sc1\n\tglobal_load_lds_dwordx4 %4, %0 offset:1024 sc1"
                 :: "s"(sbase), "s"(lb), "i"(IMM), "v"(v[0]), "v"(v[1]) : "memory", "scc");
  else
    asm volatile("s_add_u32 m0, %1, %2\n\ts_nop 0\n\tglobal_load_lds_dwordx4 %3, %0 sc1" :: "s"(sbase), "s"(lb), "i"(IMM), "v"(v[0]) : "memory", "scc");
}
template <int N> DEVI void wait_vm() { asm volatile("s_waitcnt vmcnt(%0)" :: "i"(N) : "memory"); }
DEVI void wait_lgkm0() { asm volatile("" ::: "memory"); __builtin_amdgcn_s_waitcnt(0xC07F); asm volatile("" ::: "memory"); }
DEVI void mfma_ip(f32x4& c, const bf16x8& a, const bf16x8& b) { asm volatile("v_mfma_f32_16x16x32_bf16 %0, %1, %2, %0" : "+v"(c) : "v"(a), "v"(b)); }
DEVI void mfma_gap() { __builtin_amdgcn_sched_barrier(0); asm volatile("s_nop 7"); __builtin_amdgcn_sched_barrier(0); }
DEVI void bar() { asm volatile("" ::: "memory"); __builtin_amdgcn_s_barrier(); asm volatile("" ::: "memory"); }
DEVI void sched0() { __builtin_amdgcn_sched_barrier(0); }

DEVI bf16x8 lds_b128(unsigned addr) { return *(const LAS bf16x8*)(uintptr_t)addr; }
DEVI bf16x4 lds_tr4(unsigned addr) { return __builtin_amdgcn_ds_read_tr16_b64_v4i16((LAS bf16x4*)(uintptr_t)addr); }

typedef __amdgpu_buffer_rsrc_t rsrc_t;
DEVI rsrc_t mkrsrc(const void* p, unsigned bytes) { return __builtin_amdgcn_make_buffer_rsrc((void*)p, 0, bytes, 0x00020000); }
DEVI void st16(rsrc_t r, unsigned off, u32x4 v) { __builtin_amdgcn_raw_buffer_store_b128(v, r, off, 0, 16); }
DEVI void ld_raw(u32x4& v, rsrc_t r, unsigned off) { asm volatile("buffer_load_dwordx4 %0, %1, %2, 0 offen sc1 ; raw load" : "=&v"(v) : "v"(off), "s"(r) : "memory"); }
DEVI void ld_again(u32x4& v, rsrc_t r, unsigned off) { asm volatile("buffer_load_dwordx4 %0, %1, %2, 0 offen sc1\n\ts_waitcnt vmcnt(0)" : "=&v"(v) : "v"(off), "s"(r) : "memory"); }
DEVI void ld_ok(u32x4& v) { asm volatile("; raw ok %0" : "+v"(v)); }
#define RLX_AGENT __ATOMIC_RELAXED, __HIP_MEMORY_SCOPE_AGENT

template <int N> DEVI float ror16(float v) { return __int_as_float(__builtin_amdgcn_update_dpp(0, __float_as_int(v), 0x120 + N, 0xf, 0xf, false)); }
DEVI float red16(float v) { v += ror16<8>(v); v += ror16<4>(v); v += ror16<2>(v); v += ror16<1>(v); return v; }
DEVI void xch32(float v, float& a, float& b) { unsigned u = __float_as_uint(v); auto r = __builtin_amdgcn_permlane32_swap(u, u, false, false); unsigned x = r[0], y = r[1]; a = __uint_as_float(x); b = __uint_as_float(y); }
DEVI void xch16(float v, float& a, float& b) { unsigned u = __float_as_uint(v); auto r = __builtin_amdgcn_permlane16_swap(u, u, false, false); unsigned x = r[0], y = r[1]; a = __uint_as_float(x); b = __uint_as_float(y); }
DEVI void swp32(float& a, float& b) { auto r = __builtin_amdgcn_permlane32_swap(__float_as_uint(a), __float_as_uint(b), false, false); unsigned x = r[0], y = r[1]; a = __uint_as_float(x); b = __uint_as_float(y); }
DEVI void swp16(float& a, float& b) { auto r = __builtin_amdgcn_permlane16_swap(__float_as_uint(a), __float_as_uint(b), false, false); unsigned x = r[0], y = r[1]; a = __uint_as_float(x); b = __uint_as_float(y); }
DEVI float colsum4(float v) { float a, b; xch32(v, a, b); v = a + b; xch16(v, a, b); return a + b; }
DEVI float vmax(float a, float b) { float r; asm("v_max_f32 %0, %1, %2" : "=v"(r) : "v"(a), "v"(b)); return r; }
DEVI float vmin3(float a, float b, float c) { float r; asm("v_min3_f32 %0, %1, %2, %3" : "=v"(r) : "v"(a), "v"(b), "v"(c)); return r; }
DEVI float vmax3(float a, float b, float c) { float r; asm("v_max3_f32 %0, %1, %2, %3" : "=v"(r) : "v"(a), "v"(b), "v"(c)); return r; }

DEVI unsigned src_kc(int c, int ld) { int row = c >> 3, ch = (c & 7) ^ ((row >> 1) & 7); return (unsigned)(row * ld * 2 + ch * 16); }
DEVI int km_f(int k) { return ((k >> 1) & 1) | (((k >> 3) & 1) << 1); }
DEVI unsigned ph(unsigned x) { return ((x >> 7) << 8) | (x & 127u); }
DEVI unsigned rd_kc(int lane, int ks) { int r = lane & 15, g = lane >> 4; return (unsigned)(r * 128 + (((ks * 4 + g) ^ (r >> 1)) << 4)); }
DEVI unsigned rd_km(int lane, int ct) {
  int g = lane >> 4, q = (lane & 15) >> 2, p = lane & 3;
  int f = ((q >> 1) & 1) | ((g & 1) << 1);
  return (unsigned)((ct >> 2) * 8192 + (8 * g + q) * 128 + (((ct & 3) ^ f) << 5) + 8 * p);
}

DEVI void stamp(unsigned a) { unsigned x, y;
  asm volatile("s_memrealtime s[98:99]\n\tv_mov_b32 %1, %2\n\ts_waitcnt lgkmcnt(0)\n\tv_mov_b32 %0, s98\n\tds_write_b32 %1, %0" : "=&v"(x), "=&v"(y) : "s"(a) : "s98", "s99"); }
#define ST(k, i) do { if (__builtin_amdgcn_readfirstlane(threadIdx.x) == 0) stamp(19968 + ((k) * 8 + (i)) * 4); } while (0)
#define STL(k, i) do { if (__builtin_amdgcn_readfirstlane(threadIdx.x) == 256) stamp(19968 + ((k) * 8 + (i)) * 4); } while (0)

constexpr unsigned CODE_BYTES = 50176;
DEVI void code_warm(int part, int lane, unsigned scratch) {
  unsigned long long pc;
  asm volatile("s_getpc_b64 s[40:41]\n\ts_add_u32 s40, s40, mega_kernel@rel32@lo+4\n\ts_addc_u32 s41, s41, mega_kernel@rel32@hi+12\n\ts_mov_b64 %0, s[40:41]" : "=s"(pc) :: "s40", "s41", "scc");
  const unsigned off = (unsigned)(part * 64 + lane) * 128u;
  scratch = __builtin_amdgcn_readfirstlane(scratch);
  if (off < CODE_BYTES) asm volatile("s_nop 3\n\ts_mov_b32 m0, %2\n\ts_nop 0\n\tglobal_load_lds_dword %0, %1" :: "v"(off), "s"(pc), "s"(scratch) : "memory");
}

DEVI unsigned src_km(int lt, int ld) { const int k = lt >> 3; return (unsigned)(k * ld * 2 + (((lt & 7) ^ (km_f(k) << 1)) << 4)); }
DEVI void touch4(const void* sbase, unsigned voff, unsigned ldsaddr) {
  ldsaddr = __builtin_amdgcn_readfirstlane(ldsaddr); sbase = uni_ptr(sbase);
  asm volatile("s_nop 3\n\ts_mov_b32 m0, %2\n\ts_nop 0\n\tglobal_load_lds_dword %0, %1" :: "v"(voff), "s"(sbase), "s"(ldsaddr) : "memory");
}

template <int IMM> DEVI void spf1(const void* base, unsigned soff) { asm volatile("s_load_dword s101, %0, %1 offset:%2" :: "s"(base), "s"(soff), "i"(IMM) : "s101"); }
template <int IMM = 0> DEVI void spf2(const void* base, unsigned soff) { asm volatile("s_load_dwordx2 s[100:101], %0, %1 offset:%2" :: "s"(base), "s"(soff), "i"(IMM + 60) : "s100", "s101"); }
template <int ST> DEVI void spf2x6(const void* base, unsigned soff) {
  asm volatile("s_load_dwordx2 s[100:101], %0, %1 offset:%2\n\ts_load_dwordx2 s[100:101], %0, %1 offset:%3\n\ts_load_dwordx2 s[100:101], %0, %1 offset:%4\n\t"
               "s_load_dwordx2 s[100:101], %0, %1 offset:%5\n\ts_load_dwordx2 s[100:101], %0, %1 offset:%6\n\ts_load_dwordx2 s[100:101], %0, %1 offset:%7"
               :: "s"(base), "s"(soff), "i"(60), "i"(60 + ST), "i"(60 + 2 * ST), "i"(60 + 3 * ST), "i"(60 + 4 * ST), "i"(60 + 5 * ST) : "s100", "s101");
}
template <int ST> DEVI void spf1x12(const void* base, unsigned soff) {
  spf1<0>(base, soff); spf1<ST>(base, soff); spf1<2 * ST>(base, soff); spf1<3 * ST>(base, soff); spf1<4 * ST>(base, soff); spf1<5 * ST>(base, soff);
  spf1<6 * ST>(base, soff); spf1<7 * ST>(base, soff); spf1<8 * ST>(base, soff); spf1<9 * ST>(base, soff); spf1<10 * ST>(base, soff); spf1<11 * ST>(base, soff);
}
DEVI int lgkm_out() { int n; asm volatile("s_getreg_b32 %0, hwreg(HW_REG_IB_STS, 8, 4)" : "=s"(n)); return n; }
DEVI int slow_res(const void* w) { return (3 - ((unsigned)(uintptr_t)w >> 7)) & 7; }

#define MTD
#define MT(i)
#define MTOUT(k, w)
#define CSP
#define CSA
#define WNE 16
#define ANE 24
#define BNE 13
#define QNE 46
struct Plan { int n; signed char q[40], idx[40], k[40], h[40], g[40]; };
template <class O, bool PKM, bool QKM> constexpr Plan make_plan() {
  Plan p{};
  int nr = 0;
  for (int c = 0; c < O::NRO; ++c) nr += ((O::ro[c] >> 4) ? QKM : PKM) ? 4 : 2;
  int prev = 0;
  for (int c = 0; c < O::NRO; ++c) {
    const int q = O::ro[c] >> 4, i = O::ro[c] & 15;
    int av[2] = {0, 0}, k0 = -1;
    for (int m = 0; m < 2 * O::NP; ++m) {
      const int mm = (m + 2 * O::NPEND) % (2 * O::NP);
      if ((q ? O::pj[m >> 1] : O::pi[m >> 1]) == i && m < 2 * O::NPEND) av[((m >> 1) ^ m) & 1] = m;
      if ((q ? O::pj[mm >> 1] : O::pi[mm >> 1]) == i && k0 < 0) k0 = ((mm >> 1) ^ mm) & 1;
    }
    for (int kk = 0; kk < 2; ++kk)
      for (int h = 0; h < ((q ? QKM : PKM) ? 2 : 1); ++h) {
        const int k = k0 ^ kk;
        int g = p.n * O::NE / nr;
        if (g < av[k]) g = av[k];
        if (g < prev + (p.n && O::NE >= nr ? 1 : 0)) g = prev + (p.n && O::NE >= nr ? 1 : 0);
        p.q[p.n] = q; p.idx[p.n] = i; p.k[p.n] = k; p.h[p.n] = h; p.g[p.n] = g; prev = g; ++p.n;
      }
  }
  return p;
}
struct OBase { static constexpr int pidx(int i) { return i; } static constexpr int poff(int) { return 0; } static constexpr int qoff(int j) { return j * 2048; } static constexpr bool swp(int) { return false; } };
template <bool PF, int NE_> struct O44 : OBase {
  static constexpr int TP = 4, TQ = 4, NPA = 4, NP = 16, NPEND = 8, NRO = 8, NE = NE_;
  static constexpr signed char pi[16] = {2, 3, 3, 2, 2, 3, 3, 2, 0, 1, 1, 0, 0, 1, 1, 0}, pj[16] = {0, 0, 1, 1, 2, 2, 3, 3, 0, 0, 1, 1, 2, 2, 3, 3};
  static constexpr signed char ro[8] = {0, PF ? 1 : 16, PF ? 16 : 1, 17, 18, 19, 2, 3};
};
template <bool PF, int NE_> struct O42 : OBase {
  static constexpr int TP = 4, TQ = 2, NPA = 4, NP = 8, NPEND = 4, NRO = 6, NE = NE_;
  static constexpr signed char pi[8] = {2, 3, 3, 2, 0, 1, 1, 0}, pj[8] = {0, 0, 1, 1, 0, 0, 1, 1};
  static constexpr signed char ro[6] = {0, PF ? 1 : 16, PF ? 16 : 1, 17, 3, 2};
};
template <int TH> struct OQ {
  static constexpr int TP = 6, TQ = 5, NPA = 2, NP = 26, NPEND = 13, NRO = 11, NE = QNE;
  static constexpr signed char pi[26] = {3, 4, 5, 5, 4, 3, 3, 4, 5, 5, TH ? 5 : 3, 4, TH ? 3 : 4, 0, 1, 2, 2, 1, 0, 0, 1, 2, 2, TH ? 2 : 0, 1, TH ? 0 : 1};
  static constexpr signed char pj[26] = {0, 0, 0, 1, 1, 1, 2, 2, 2, TH ? 4 : 3, 3, 3, TH ? 3 : 4, 0, 0, 0, 1, 1, 1, 2, 2, 2, TH ? 4 : 3, 3, 3, TH ? 3 : 4};
  static constexpr signed char ro[11] = {0, 1, 16, 2, 17, 18, TH ? 20 : 19, TH ? 19 : 20, 3, 4, 5};
  static constexpr int pidx(int i) { return i / 3; }
  static constexpr int poff(int i) { return i % 3 * 8192; }
  static constexpr int qoff(int j) { return j == 4 ? 0 : (1 + 4 * TH + j) * 2048; }
  static constexpr bool swp(int i) { return i % 3 == 2; }
};
template <bool PKM, bool QKM, int PB, int QB, int XB, int NS, int NT, bool WISP, int BASE, int ROT, int WLD, int WSUB, int ASTEP, int RLX = 0, bool QF = false>
struct Gemm {
  static constexpr int SB = PB + QB + XB, CNT = (PB + QB) / 4096 + (XB ? 1 : 0);
  static constexpr int PO = QF ? QB : 0, QO = QF ? 0 : PB, WB = WISP ? PB : QB, AB = WISP ? QB : PB, WOFF = WISP ? PO : QO, AOFF = WISP ? QO + XB : PO;
  static constexpr int XL = XB / 64, H = NS - 1 - RLX, CA = AB / 4096 + (XB ? 1 : 0), WSTEP = 64 * WLD * 2;
  static_assert(H * CNT <= 63 && BASE + NS * SB <= 163840, "");
  struct Loader {
    static constexpr int NW = WB / 4096, NA = AB / 4096;
    const char* w; const char* ab; unsigned wv[NW], av[NA], xv, lwb, lab, lx; int rot, islot, t; bool xl;
    DEVI Loader(const void* w_, const void* ab_, int lw, int lane, int rot_ = 0)
        : w((const char*)w_ - 3072), ab((const char*)ab_ - 3072), lwb(BASE + WOFF + lw * (NW == 6 ? 2048 : WB / 4)), lab(BASE + AOFF + lw * (AB / 4)), lx(BASE + PB + lw * (XL * 16)),
          rot(rot_), islot((H + ROT) % NS), t(0), xl(lane < XL) {
      const unsigned w0 = (lane >> 3) * (WLD * 2), f0 = (lane & 7) ^ ((lane >> 3) & 2);
#pragma unroll
      for (int i = 0; i < NW; ++i) {
        const unsigned p = NW == 6 ? (i >> 1) * 8 + lw * 2 + (i & 1) : lw * NW + i;
        wv[i] = w0 + ((f0 ^ ((i & 1) << 2)) << 4) + (NW == 6 ? (i >> 1) * WSUB + (lw * 2 + (i & 1)) * (8 * WLD * 2) - (i & 1) * 1024 : (p >> 3) * WSUB + (p & 7) * (8 * WLD * 2) - i * 1024) + 3072;
        asm volatile("" : "+v"(wv[i]));
      }
      asm volatile("" : "+s"(w), "+s"(lwb));
      if (XB) asm volatile("" : "+s"(rot));
      else seta(0, 0, lw, lane);
    }
    DEVI void seta(unsigned a0, unsigned xv_, int lw, int lane) {
      xv = xv_ + 3072;
#pragma unroll
      for (int i = 0; i < NA; ++i) { const unsigned p = lw * NA + i; av[i] = (XB ? a0 + src_kc(p * 64 + lane, D) : p * 2048 + ph(lane * 16)) + 3072 - i * 1024; asm volatile("" : "+v"(av[i])); }
      asm volatile("" : "+s"(ab), "+s"(lab));
      if (XB) { asm volatile("" : "+v"(xv)); asm volatile("" : "+s"(lx)); }
    }
    DEVI int kord(int n) const { if constexpr (XB != 0) return (n + rot) & (NT - 1); else return n; }
    template <int SO> DEVI void issue_w(const char* p, unsigned so = 0) const {
      if constexpr (NW == 6) { dma_grp<2, SO>(p, wv, lwb + so); dma_grp<2, SO + 8192>(p, wv + 2, lwb + so); dma_grp<2, SO + 16384>(p, wv + 4, lwb + so); }
      else dma_grp<NW, SO>(p, wv, lwb + so);
    }
    template <int SO> DEVI void issue_a(const char* p, unsigned so = 0) const {
      if (XB) { if (xl) dma_grp<1, SO>(p, &xv, lx + so); }
      dma_grp<NA, SO>(p, av, lab + so);
    }
    template <int S0, bool W, bool A> DEVI void pre() const {
      if constexpr (S0 < H) {
        if (W) issue_w<((S0 + ROT) % NS) * SB>(w + (kord(0) + S0) * WSTEP);
        if (A) issue_a<((S0 + ROT) % NS) * SB>(ab + (kord(0) + S0) * ASTEP);
        pre<S0 + 1, W, A>();
      }
    }
    template <int T> DEVI void pre_w1() const { issue_w<((T + ROT) % NS) * SB>(w + (kord(0) + T) * WSTEP); sched0(); }
    DEVI void pre_w() const { pre<0, true, false>(); sched0(); }
    template <int T> DEVI void peel() const {
      if constexpr (T < H - 1) {
        wait_vm<(H - 1 - T) * CA + T * CNT>();
        bar();
        issue_w<((T + H + ROT) % NS) * SB>(w + (T + H) * WSTEP); issue_a<((T + H + ROT) % NS) * SB>(ab + (T + H) * ASTEP);
        peel<T + 1>();
      }
    }
    DEVI void start() {
      sched0();
      bar();
      pre<0, false, true>();
      sched0();
      if constexpr (H == 2) wait_vm<CA>();
      else { peel<0>(); islot = (2 * H - 1 + ROT) % NS; t = H - 1; }
    }
    template <class HK> DEVI void run(int tend, HK side) {
#pragma unroll 1
      for (; t < tend; ++t) {
        const char* wk = w + (t + H) * WSTEP; const char* ak = ab + (t + H) * ASTEP;
        unsigned lw_ = lwb + islot * SB, la_ = lab + islot * SB;
        islot = islot + 1 == NS ? 0 : islot + 1;
        asm volatile("" : "+s"(wk), "+s"(ak), "+s"(lw_), "+s"(la_));
        wait_vm<(H - 1) * CNT>();
        bar();
        dma_grp<NW, 0>(wk, wv, lw_);
        side(t);
        dma_grp<NA, 0>(ak, av, la_);
        sched0();
      }
    }
    template <int T, int X, class A, class B, class C> DEVI void step(A pre_, B side, C post) const {
      const int k = kord(T + H);
      const char* wk = w + k * WSTEP; const char* ak = ab + k * ASTEP;
      asm volatile("" : "+s"(wk), "+s"(ak));
      wait_vm<(H - 1) * CNT + X>();
      bar();
      pre_(BASE + ((T + ROT) % NS) * SB);
      issue_w<((T + H + ROT) % NS) * SB>(wk);
      sched0();
      side();
      sched0();
      issue_a<((T + H + ROT) % NS) * SB>(ak);
      post();
      sched0();
    }
    template <int I> DEVI void tail() const { if constexpr (I >= 0) { sched0(); wait_vm<I * CNT>(); bar(); tail<I - 1>(); } }
  };
  static constexpr int NWIN = (NS * SB + 65535) / 65536;
  template <class O> struct Comp {
    static constexpr int TP = O::TP, TQ = O::TQ, NM = 2 * O::NP, NPA = PKM ? O::NPA : 2, NQA = QKM ? TQ : 2;
    static constexpr Plan PL = make_plan<O, PKM, QKM>();
    unsigned prd[NWIN][NPA], qrd[NWIN][NQA];
    bf16x8 fp[TP][2], fq[TQ][2]; bf16x4 lo;
    DEVI void setwin() {
#pragma unroll
      for (int w = 1; w < NWIN; ++w) {
#pragma unroll
        for (int i = 0; i < NPA; ++i) { prd[w][i] = prd[0][i] + w * 65536; asm volatile("" : "+v"(prd[w][i])); }
#pragma unroll
        for (int i = 0; i < NQA; ++i) { qrd[w][i] = qrd[0][i] + w * 65536; asm volatile("" : "+v"(qrd[w][i])); }
      }
#pragma unroll
      for (int i = 0; i < NPA; ++i) asm volatile("" : "+v"(prd[0][i]));
#pragma unroll
      for (int i = 0; i < NQA; ++i) asm volatile("" : "+v"(qrd[0][i]));
    }
    DEVI void setup(int wp, int wq, int lane) {
      const unsigned b = BASE;
      if (PKM) {
#pragma unroll
        for (int i = 0; i < TP; ++i) prd[0][i] = b + rd_km(lane, wp * TP + i);
      } else { prd[0][0] = b + rd_kc(lane, 0) + wp * TP * 2048; prd[0][1] = b + rd_kc(lane, 1) + wp * TP * 2048; }
      if (QKM) {
#pragma unroll
        for (int j = 0; j < TQ; ++j) qrd[0][j] = b + rd_km(lane, wq * TQ + j);
      } else { qrd[0][0] = b + rd_kc(lane, 0) + wq * TQ * 2048; qrd[0][1] = b + rd_kc(lane, 1) + wq * TQ * 2048; }
    }
    template <int C, int N> DEVI static unsigned wa(const unsigned (&b)[NWIN][N], int i) { return b[C >> 16][i] + (C & 0xffff); }
    template <int SLOT, int R> DEVI void rd() {
      constexpr int q = PL.q[R], i = PL.idx[R], k = PL.k[R], h = PL.h[R];
      bf16x8& f = q ? fq[q ? i : 0][k] : fp[q ? 0 : i][k];
      if constexpr (q ? QKM : PKM) {
        unsigned a;
        if constexpr (q) a = wa<SLOT * SB + QO + k * 4096 + h * 512, NQA>(qrd, i); else a = wa<SLOT * SB + PO + O::poff(i) + k * 4096 + h * 512, NPA>(prd, O::pidx(i));
        const bf16x4 x = lds_tr4(a);
        if constexpr (h) f = __builtin_shufflevector(lo, x, 0, 1, 2, 3, 4, 5, 6, 7); else lo = x;
      } else if constexpr (q) f = lds_b128(wa<SLOT * SB + QO + O::qoff(i), 2>(qrd, k));
      else f = lds_b128(wa<SLOT * SB + PO + i * 2048, 2>(prd, k));
    }
    template <int SLOT, int M, int R> DEVI void rds() { if constexpr (R < PL.n) { if constexpr (PL.g[R] == M) rd<SLOT, R>(); if constexpr (PL.g[R] <= M) rds<SLOT, M, R + 1>(); } }
    template <int M, bool IP = false> DEVI void mm(f32x4 (&acc)[TP][TQ]) {
      constexpr int i = O::pi[M >> 1], j = O::pj[M >> 1], k = ((M >> 1) ^ M) & 1;
      if constexpr (IP) { if constexpr (O::swp(i)) mfma_ip(acc[i][j], fq[j][k], fp[i][k]); else mfma_ip(acc[i][j], fp[i][k], fq[j][k]); }
      else if constexpr (O::swp(i)) acc[i][j] = __builtin_amdgcn_mfma_f32_16x16x32_bf16(fq[j][k], fp[i][k], acc[i][j], 0, 0, 0);
      else acc[i][j] = __builtin_amdgcn_mfma_f32_16x16x32_bf16(fp[i][k], fq[j][k], acc[i][j], 0, 0, 0);
    }
    template <int A, int B> DEVI void pend(f32x4 (&acc)[TP][TQ]) { if constexpr (A < B) { mm<2 * A>(acc); mm<2 * A + 1>(acc); pend<A + 1, B>(acc); } }
    template <int SLOT, bool FIRST, int M> DEVI void body(f32x4 (&acc)[TP][TQ]) {
      if constexpr (M < NM) {
        if constexpr (!FIRST || M >= 2 * O::NPEND) { mm<M, true>(acc); sched0(); }
        rds<SLOT, M, 0>();
        sched0();
        body<SLOT, FIRST, M + 1>(acc);
      }
    }
    template <int SLOT, bool FIRST> DEVI void step(f32x4 (&acc)[TP][TQ]) { if (!RLX) wait_lgkm0(); bar(); sched0(); body<SLOT, FIRST, 0>(acc); }
    template <int I, int N, int T0, class MID> DEVI void steps(f32x4 (&acc)[TP][TQ], MID mid) {
      if constexpr (I < N) { mid(I); step<(I + T0 + ROT) % NS, false>(acc); steps<I + 1, N, T0>(acc, mid); }
    }
    DEVI void prep(f32x4 (&acc)[TP][TQ]) { setwin(); pin(acc); }
    DEVI void pin(f32x4 (&acc)[TP][TQ]) {
#pragma unroll
      for (int p = 0; p < O::NP; ++p) asm volatile("" : "+v"(acc[O::pi[p]][O::pj[p]]));
    }
    template <bool CONT, class MID> DEVI void run_steps(f32x4 (&acc)[TP][TQ], MID mid CSP) {
      constexpr int T0 = CONT ? 0 : 1;
      if constexpr (T0) step<ROT % NS, true>(acc);
#pragma unroll 1
      for (int it = 0; it < (NT - T0) / NS; ++it) {
        steps<0, NS, T0>(acc, [&](int i) { mid(it - (NT - T0) / NS, i); });
      }
      steps<0, (NT - T0) % NS, T0>(acc, [](int) {});
      wait_lgkm0();
      mfma_gap();
    }
  };
};

DEVI f32x4 gelu_arg(f32x4 u) { return u * (-2.302208198f + -0.1029432397f * (u * u)); }
DEVI f32x4 gelu_tanh(f32x4 u) {
  f32x4 a = gelu_arg(u);
#pragma unroll
  for (int e = 0; e < 4; ++e) a[e] = __builtin_amdgcn_exp2f(a[e]);
  a += 1.f;
#pragma unroll
  for (int e = 0; e < 4; ++e) a[e] = __builtin_amdgcn_rcpf(a[e]);
  return u * a;
}

template <int tilesP, int tilesQ, int gp, int gq> DEVI void tile_map(unsigned bid, int& tp, int& tq) {
  const unsigned xcd = bid & 7, idx = bid >> 3;
  constexpr unsigned gP = tilesP / gp, gQ = tilesQ / gq;
  static_assert(gP * gQ == 8, "");
  tp = (xcd % gP) * gp + idx % gp;
  tq = (xcd / gP) * gq + idx / gp;
}

template <int V> struct IC { static constexpr int value = V; };
template <int A, int B, class FN> DEVI void for_c(FN f) { if constexpr (A < B) { f(IC<A>{}); for_c<A + 1, B>(f); } }
DEVI float sel(bool c, float a, float b) { return c ? a : b; }
DEVI u32x2 pk4(const f32x4& v) { return (u32x2){pk2(v[0], v[1]), pk2(v[2], v[3])}; }
DEVI u32x4 cat4(u32x2 a, u32x2 b) { return (u32x4){a.x, a.y, b.x, b.y}; }
DEVI bf16x8 cat(u32x2 a, u32x2 b) { return __builtin_bit_cast(bf16x8, cat4(a, b)); }
DEVI f32x4 mfma(const bf16x8& a, const bf16x8& b, const f32x4& c) { return __builtin_amdgcn_mfma_f32_16x16x32_bf16(a, b, c, 0, 0, 0); }
DEVI u32x4 pack_pair(const f32x4& a, const f32x4& b) {
  const unsigned ax = pk2(a[0], a[1]), ay = pk2(a[2], a[3]), bx = pk2(b[0], b[1]), by = pk2(b[2], b[3]);
  auto rx = __builtin_amdgcn_permlane16_swap(ax, bx, false, false);
  auto ry = __builtin_amdgcn_permlane16_swap(ay, by, false, false);
  const unsigned x0 = rx[0], x1 = rx[1], y0 = ry[0], y1 = ry[1];
  return (u32x4){x0, y0, x1, y1};
}
DEVI int pair_chunk(int fq) { return ((fq & 1) << 1) | (fq >> 1); }
DEVI void sel_frags(bool paired, int fr, int fq, bf16x8 (&s)[2]) {
  const unsigned one = fr & 1 ? 0x3F800000u : 0x3F80u;
#pragma unroll
  for (int i = 0; i < 2; ++i) {
    const bool on = paired ? (fq & 1) == i && (fr >> 3) == (fq >> 1) : (fr >> 2) == fq;
    const int d = paired ? (fr & 7) >> 1 : i * 2 + ((fr >> 1) & 1);
    u32x4 v;
#pragma unroll
    for (int e = 0; e < 4; ++e) v[e] = on && d == e ? one : 0u;
    s[i] = __builtin_bit_cast(bf16x8, v);
  }
}
template <int TQ> DEVI void tile_add(f32x4 (&acc)[4][TQ], const bf16x8 (&s)[2], unsigned a) {
  bf16x8 t[4];
#pragma unroll
  for (int p = 0; p < 4; ++p) t[p] = lds_b128(a + p * 1024);
#pragma unroll
  for (int p = 0; p < 4; ++p)
#pragma unroll
    for (int i = 0; i < 2; ++i) acc[(p & 1) * 2 + i][p >> 1] = mfma(s[i], t[p], acc[(p & 1) * 2 + i][p >> 1]);
}
template <int C> DEVI float dppf(float v) { return __int_as_float(__builtin_amdgcn_update_dpp(0, __float_as_int(v), C, 0xf, 0xf, false)); }
DEVI float red8(float v) { v += dppf<0xB1>(v); v += dppf<0x4E>(v); v += dppf<0x141>(v); return v; }
DEVI float red4(float v) { v += dppf<0xB1>(v); v += dppf<0x4E>(v); return v; }

constexpr unsigned SENT = 0xBF800000u;
constexpr int SPIN_MAX = 1 << 21;

constexpr unsigned MB = 1u << 20;
constexpr unsigned WS_O = 0, WS_H = 8 * MB, WS_U = 16 * MB, WS_X2 = 32 * MB, WS_XP = 40 * MB  ;
constexpr unsigned WS_ST = 120 * MB, ST_CS = 2 * S * 64, WS_XCH = 122 * MB  , WS_EPOCH = 123 * MB, WS_ABORT = 123 * MB + 256  ;
DEVI unsigned tag2(unsigned t) { return t * 0x9E3779B1u ^ 0x5BD1E995u; }
constexpr int MEDGE = 256 * 64;
constexpr unsigned WS_MAIL = 124 * MB;

#define NL 2
#define QBR 3
#define FLS 3
#define NPL 6
#define SPON 1
#define PC0 0
#define STG 1
#define WSP 1
#define SWW 2
#define GSH 1
#define GSP 1
#define SSC 1
#define RB 1
#define SI 2
#define LPR 1
#define CPR 0
#define SW0 8
#define SW1 6
#define XD 3
#define XS 4
#define JA 22
#define PS 3
#define TBA 3
#define TC1 12
#define NW1 9
#define EW 4
#define ET 8
#define SPW 1
#define PA_1 3
#define PA_2 3
constexpr int L_X = 0, L_TAB = 16384, L_CS = L_TAB + 1152, L_TAG = L_CS + 768, L_BIAS = L_TAG + 128, L_SCR = L_BIAS + 512  , L_QKV = 20480;
static_assert(L_SCR + 1536 <= L_QKV, "");
typedef Gemm<true, false, 24576, 16384, 2048, 3, D / 64, true, 32768, 0, 3 * D, 2 * D, 128> GQ;
typedef Gemm<true, false, 16384, 8192, 0, 5, D / 64, true, 40960, 2, D, 128, 16384, 1, true> GW;
typedef Gemm<true, true, 16384, 16384, 0, 4, S / 64, true, 32768, 1, F, 128, 32768, 0, true> G1;
typedef Gemm<false, true, 16384, 16384, 0, 3, JA, false, 65536, (3 - JA % 3) % 3, S, 1024, 32768> G2A;
typedef Gemm<false, true, 16384, 8192, 0, 4, F / 64 - 2 * JA, false, 65536, 0, S, 128, 32768> G2;

DEVI unsigned get_tag() { return __builtin_amdgcn_readfirstlane(*(volatile LAS unsigned*)(uintptr_t)L_TAG) + 1u; }
DEVI bool spin_fail(int spin, char* ws) {
  if ((spin & 255) != 255) return false;
  gu32* ab = (gu32*)(ws + WS_ABORT);
  const unsigned tag = get_tag();
  if (spin >= SPIN_MAX) { __hip_atomic_store(ab, tag, RLX_AGENT); return true; }
  return __builtin_amdgcn_readfirstlane(__hip_atomic_load(ab, RLX_AGENT)) == tag;
}
DEVI void sync_post(char* ws, int edge, int post, int wid) {
  unsigned o = post >= 0 && wid == 0 ? post * 4u : MB, one = 1u;
  const rsrc_t r = mkrsrc(ws + WS_MAIL + edge * (MEDGE * 4), MEDGE * 4);
  asm volatile("s_waitcnt vmcnt(0) lgkmcnt(0)" : "+v"(o), "+v"(one) : "s"(r) : "memory");
  sched0();
  bar();
  ST(edge + 1, 6);
  if (wid == 0) __builtin_amdgcn_raw_buffer_store_b32(one, r, o, 0, 16);
  sched0();
}
template <int DELAY = SW0> DEVI void sync_wait(char* ws, int edge, int line, int lane, unsigned all, unsigned first, int wid) {
  sched0();
  if (wid == 3) {
    gu32* pw = (gu32*)(ws + WS_MAIL) + edge * MEDGE + line * 64 + (lane & 31);
    if (DELAY) __builtin_amdgcn_s_sleep(DELAY);
    if (!STG) first = all;
    for (int spin = 0;; ++spin) {
      unsigned miss = ~(unsigned)__builtin_amdgcn_ballot_w64(__hip_atomic_load(pw, RLX_AGENT) != 0);
      if (spin_fail(spin, ws)) miss = 0;
      if (first && !(miss & first)) { bar(); first = 0; }
      if (!(miss & all)) break;
      __builtin_amdgcn_s_sleep(SW1);
    }
  } else bar();
  ST(edge + 1, 4);
  ST(edge + 2, 0);
}
DEVI unsigned halo_off(int s0, int lw, int lane) {
  const int c = lw * 32 + (lane & 31), row = c >> 3, ch = (c & 7) ^ ((row >> 1) & 7), tok = s0 - 16 + row;
  return (unsigned)((tok < 0 ? 0 : tok) * D * 2 + ch * 16);
}
DEVI int qkv_rot(int bid) { return (bid & 3) * 4 + (bid & 4) * 2; }
DEVI unsigned x_off(int tp, int tq, int w, int fr, int fq) { return (unsigned)(((tq * 64 + (w >> 1) * 32 + fr) * D + tp * 128 + (w & 1) * 64 + pair_chunk(fq) * 8) * 2); }

DEVI void pfb_q(const u16* w, int bid, int j, int n) {
  const int r = slow_res(w);
  if ((r >> 2) == (bid & 1)) {
    const unsigned o = (unsigned)(((bid & 2) * 4 + r) * 128 + (j >> 1) * (3 * D * 2) + (j & 1) * 64 + ((n + qkv_rot(bid)) & 15) * GQ::WSTEP);
    spf1<0>(w, o); spf1<2048>(w, o); spf1<4096>(w, o);
  }
}
constexpr int PFR = NPL * 128 * 1024, PF_END = TBA * 256 + NW1 * PFR / (F * 2) / 8 * 8;
static_assert(TBA * 256 * F * 2 + NW1 * PFR <= S * F * 2 && 3 * PFR >= D * D * 2, "");
struct Pfc {
  const char* p; const char* q; int pc;
  DEVI Pfc(const u16* wo, const u16* w1, unsigned g) : p((const char*)wo + g * (NPL * 128)), q((const char*)w1 + TBA * 256 * F * 2 + g * (NPL * 128)), pc(PC0) {}
  DEVI void go() {
    unsigned t, u;
    asm volatile("s_getreg_b32 %2, hwreg(HW_REG_IB_STS, 8, 4)\n\ts_cmp_lt_u32 %1, %5\n\ts_cselect_b32 %3, 0, 1\n\ts_or_b32 %2, %2, %3\n\ts_cbranch_scc1 1f\n\t"
                 "s_load_dwordx2 s[100:101], %0, 60\n\ts_load_dwordx2 s[100:101], %0, 188\n\ts_load_dwordx2 s[100:101], %0, 316\n\t"
                 "s_load_dwordx2 s[100:101], %0, 444\n\ts_load_dwordx2 s[100:101], %0, 572\n\ts_load_dwordx2 s[100:101], %0, 700\n\t"
                 ".if %8 == 7\n\ts_load_dwordx2 s[100:101], %0, 828\n\t.endif\n\t"
                 "s_mov_b32 %2, %7\n\ts_cmp_lt_u32 %1, 2\n\ts_cselect_b32 %2, %6, %2\n\ts_mov_b64 s[40:41], %0\n\ts_add_u32 s40, s40, %2\n\ts_addc_u32 s41, s41, 0\n\t"
                 "s_cmp_eq_u32 %1, 2\n\ts_cselect_b64 %0, %4, s[40:41]\n\ts_add_u32 %1, %1, 1\n1:"
                 : "+s"(p), "+s"(pc), "=&s"(t), "=&s"(u) : "s"(q), "i"(3 + NW1), "i"((D * D * 2 - PFR) / 2), "i"(PFR), "i"(NPL) : "s40", "s41", "s100", "s101", "scc");
  }
};
DEVI void spare_touch(const u16* w1, const u16* w2, int bid, int wid, int lane) {
  if (!SPON) return;
  const int j = ((bid >> 3) - 28) * 8 + wid, fr = lane & 15, fq = lane >> 4;
  const unsigned scr = L_QKV + wid * 256;
#pragma unroll 1
  for (int r = PF_END + (j * 2 + ((bid >> 2) & 1)) * 4; r < S; r += 256) { if (fr < 14) touch4(w1 + (size_t)r * F + (bid & 3) * 896, fq * (F * 2) + fr * 128, scr); wait_vm<SPW>(); }
#pragma unroll 1
  for (int r = (j * 2 + (bid & 1)) * 4; r < JA * 64; r += 256) { touch4(w2 + (size_t)(r + (bid & 2) * JA * 32) * S + (bid & 4) * 256, fq * (S * 2) + fr * 128, scr); wait_vm<SPW>(); }
#pragma unroll 1
  for (int r = JA * 128 + ((31 - j) * 2 + (bid & 1)) * 8; r < F; r += 512) { touch4(w2 + (size_t)r * S + ((bid & 7) >> 1) * 512, (lane >> 3) * (S * 2) + (lane & 7) * 128, scr); wait_vm<SPW>(); }
}

template <int TH> DEVI void qkv_attn(char* ws, int l, int h, int lane, int hp, int sb, int cw, int bid, int tp, int tq, const u16* wqkv) {
  asm volatile("; half %1" : "+v"(lane) : "i"(TH));
  const int fr = lane & 15, fq = lane >> 4;
  GQ::Comp<OQ<TH>> g;
#pragma unroll
  for (int c = 0; c < 2; ++c) { g.prd[0][c] = 32768 + rd_km(lane, h * 2 + c); g.qrd[0][c] = 32768 + rd_kc(lane, c); }
  f32x4 acc[6][5];
#pragma unroll
  for (int i = 0; i < 6; ++i)
#pragma unroll
    for (int j = 0; j < 5; ++j) acc[i][j] = (f32x4){0.f, 0.f, 0.f, 0.f};
  const int par = fr & 1, da = fr - 4 * fq - par;
  const bool ca = da >= 0, cb = da >= 2;
  const unsigned tm = L_TAB + (16 + 64 * TH) * 4, tc = L_CS + h * 128;
  unsigned am = tm + fr * 4 - 320, a4 = tm + fq * 16 - 320, aa = tm + 576 + (4 * fq + par - (ca ? 0 : 16)) * 4, ab = tm + 576 + (4 * fq + par + 2 - (cb ? 0 : 16)) * 4;
  unsigned c4 = tc + fq * 16, c1 = tc + 512 + fr * 4, xb = L_QKV + h * 3072 + lane * 16, hm = par ? 0xffff0000u : 0xffffu, m0 = ca ? 0u : hm, m1 = cb ? 0u : hm;
  unsigned oo = ph((unsigned)((((sb * 2 + TH) * 16 + hp) << 13) + fr * 128 + (((h * 4 + pair_chunk(fq)) ^ (fr >> 1)) << 4)));
  asm volatile("" : "+v"(am), "+v"(a4), "+v"(aa), "+v"(ab), "+v"(c4), "+v"(c1), "+v"(xb), "+v"(hm), "+v"(m0), "+v"(m1), "+v"(oo));
  g.prep(acc);
  if (l) {
    const int sbc = (tq >> 1) + fq;
    sync_post(ws, 3, !TH && (lane < 16 || (lane < 32 && (tq & 1) && sbc < 16)) ? (sbc * 16 + fr) * 64 + (tq - 2 * sbc + 1) * 8 + tp : -1, TH * 2 + h);
  }
  if (l) sync_wait(ws, 3, sb * 16 + hp, lane, sb ? 0xffffffu : 0xffff00u, (sb ? 0x10101u : 0x10100u) << ((qkv_rot(bid) & 15) >> 1), TH * 2 + h);
  else if (!TH || !h) wait_vm<0>();
  else if ((slow_res(wqkv) >> 2) == (bid & 1)) {
    if (ET) __builtin_amdgcn_s_sleep(ET);
    if ((lane & 3) < 3 && lane < 40) touch4(wqkv, (unsigned)(((qkv_rot(bid) * 64 + 128 + (bid >> 3) * 10 + (lane >> 2)) & 1023) * (3 * D * 2) + (lane & 3) * (D * 2) + ((bid & 2) * 4 + slow_res(wqkv)) * 128), L_SCR + 768);
  }
  g.template run_steps<false>(acc, [&](int it, int i) { if (i == 0 && cw >= 0 && it == -2) code_warm(cw, lane, L_SCR + (TH * 2 + h) * 256); } CSA);
  const rsrc_t ro = mkrsrc(ws + WS_O, S * D * 4);
  MTD;
  MT(0);
  if (l == 0) bar();
  ST(1 + l * 4, 2);
  float mt[5], cv[2], rq[4], ra[4], rb[4]; f32x4 m4[5], cq[2], ck[2];
#pragma unroll
  for (int c = 0; c < 2; ++c) {
    ck[c] = *(const LAS f32x4*)(uintptr_t)(c4 + 256 + c * 64); cv[c] = *(const LAS float*)(uintptr_t)(c1 + c * 64);
    cq[c] = *(const LAS f32x4*)(uintptr_t)(c4 + c * 64);
  }
#pragma unroll
  for (int j = 4; j >= 0; --j) {
    const int o = 320 + (j == 4 ? -64 - 256 * TH : j * 64);
    if (j < 4 || !TH) mt[j] = *(const LAS float*)(uintptr_t)(am + o);
    if (j < 4 || TH) m4[j] = *(const LAS f32x4*)(uintptr_t)(a4 + o);
  }
#pragma unroll
  for (int j = 0; j < 4; ++j) { rq[j] = *(const LAS float*)(uintptr_t)(am + 896 + j * 64); ra[j] = *(const LAS float*)(uintptr_t)(aa + j * 64); rb[j] = *(const LAS float*)(uintptr_t)(ab + j * 64); }
  u32x2 kp[2][5], qp[2][4], vp[2][5];
  auto fk = [&](int j) { kp[0][(j + 1) % 5] = pk4(acc[1][j]); kp[1][(j + 1) % 5] = pk4(acc[4][j] + ck[1] * mt[j]); };
  auto fv = [&](int j) { vp[0][(j + 1) % 5] = pk4(acc[2][j]); vp[1][(j + 1) % 5] = pk4(acc[5][j] + m4[j] * cv[1]); };
  auto fqq = [&](int j) { qp[0][j] = pk4(acc[0][j]); qp[1][j] = pk4(acc[3][j] + cq[1] * mt[j]); };
  g.template pend<10, 13>(acc);
#pragma unroll
  for (int i = 0; i < 6; ++i) { __builtin_amdgcn_sched_group_barrier(0x100, 3, 0); __builtin_amdgcn_sched_group_barrier(0x8, 1, 0); }
  sched0();
  MT(2);
  for_c<0, 20>([&](auto mc) {
    constexpr int m = mc.value, n0 = m * 52 / 20, n1 = (m + 1) * 52 / 20;
    g.template mm<(18 - (m & ~1)) + (m & 1)>(acc);
    sched0();
#pragma unroll
    for (int n = n0; n < n1; ++n) {
      const int t = 12 - (n >> 2), e = n & 3, j = t == 12 ? 4 : t / 3, i = t == 12 ? 1 + TH : t % 3;
      float x = acc[i][j][e];
      asm volatile("v_fmac_f32 %0, %1, %2" : "+v"(x) : "v"(i == 2 ? m4[j][e] : i ? ck[0][e] : cq[0][e]), "v"(i == 2 ? cv[0] : mt[j]));
      acc[i][j][e] = x;
    }
    sched0();
  });
  volatile LAS unsigned* xf = (volatile LAS unsigned*)(uintptr_t)(L_TAG + 16 + (h * 2 + TH) * 4);
  if (TH) { fv(4); *(volatile LAS bf16x8*)(uintptr_t)(xb + 2048) = cat(vp[0][0], vp[1][0]); *xf = l + 1; fk(3); }
  else { fk(3); fv(3); *(volatile LAS bf16x8*)(uintptr_t)xb = cat(kp[0][4], kp[1][4]); *(volatile LAS bf16x8*)(uintptr_t)(xb + 1024) = cat(vp[0][4], vp[1][4]); *xf = l + 1; }
  sched0();
  fqq(3); fk(2); fqq(2); fk(1);
  sched0();
  const f32x4 z = {0.f, 0.f, 0.f, 0.f};
  f32x4 sc[4][2]; float a[4], b[4], m[4];
  auto scores = [&](int j) {
#pragma unroll
    for (int u = 1; u >= 0; --u) sc[j][u] = mfma(cat(kp[0][j + u], kp[1][j + u]), cat(qp[0][j], qp[1][j]), z);
  };
  auto pick = [&](int j) {
    const f32x4 s0 = sc[j][0], s1 = sc[j][1];
    a[j] *= sel(par, sel(ca, s1[1], s0[1]), sel(ca, s1[0], s0[0]));
    b[j] *= sel(par, sel(cb, s1[3], s0[3]), sel(cb, s1[2], s0[2]));
    if (!TH && j == 0) { a[j] = sel(ca || sb, a[j], -1e9f * 1.4426950408889634f); b[j] = sel(cb || sb, b[j], -1e9f * 1.4426950408889634f); }
    m[j] = __builtin_fmaxf(a[j], b[j]);
  };
  auto expo = [&](int j) { a[j] = __builtin_amdgcn_exp2f(a[j] - m[j]); b[j] = __builtin_amdgcn_exp2f(b[j] - m[j]); m[j] = a[j] + b[j]; };
  auto pv = [&](int j) {
    const unsigned pa = pk2(a[j] * ra[j], a[j] * ra[j]), pb = pk2(b[j] * rb[j], b[j] * rb[j]);
    const bf16x8 pf = __builtin_bit_cast(bf16x8, (u32x4){pa & m0, pb & m1, pa & (m0 ^ hm), pb & (m1 ^ hm)});
#pragma unroll
    for (int c = 0; c < 2; ++c) sc[j][c] = mfma(cat(vp[c][j], vp[c][j + 1]), pf, z);
  };
  auto outp = [&](int j) { st16(ro, oo + j * 4096, pack_pair(sc[j][0] * m[j], sc[j][1] * m[j])); };
  unsigned f; u32x4 xv, xk;
  auto xrd = [&]() {
    f = xf[1 - 2 * TH]; xv = *(volatile LAS u32x4*)(uintptr_t)(xb + (TH ? 1024 : 2048));
    if (TH) xk = *(volatile LAS u32x4*)(uintptr_t)xb;
  };
  auto xget = [&]() {
    sched0();
    while (__builtin_expect(__builtin_amdgcn_readfirstlane(f) != l + 1, 0)) xrd();
    vp[0][0] = (u32x2){xv[0], xv[1]}; vp[1][0] = (u32x2){xv[2], xv[3]};
    if (TH) { kp[0][0] = (u32x2){xk[0], xk[1]}; kp[1][0] = (u32x2){xk[2], xk[3]}; }
  };
  MT(3);
  {
    fqq(1); fk(0); fqq(0);
    if (!TH) fk(4);
    xrd();
    sched0();
    if (TH) fv(3);
    fv(2); fv(1); fv(0);
#pragma unroll
    for (int c = 0; c < 2; ++c)
#pragma unroll
      for (int j = 0; j < 4; ++j) asm volatile("" : "+v"(qp[c][j]), "+v"(kp[c][j + 1]), "+v"(vp[c][j + 1]));
    if (!TH) asm volatile("" : "+v"(kp[0][0]), "+v"(kp[1][0]));
    MT(4);
    xget();
    scores(3); scores(2); scores(1); scores(0);
#pragma unroll
    for (int j = 0; j < 4; ++j) { const float r = rq[j] * (0.17677669529663687f * 1.4426950408889634f); a[j] = r * ra[j]; b[j] = r * rb[j]; }
#pragma unroll
    for (int i = 0; i < 8; ++i) { __builtin_amdgcn_sched_group_barrier(0x8, 1, 0); __builtin_amdgcn_sched_group_barrier(0x2, 2, 0); }
    sched0();
    pick(3); pick(2); pick(1); pick(0);
    float u, w;
    swp32(m[0], m[2]); swp32(m[1], m[3]); u = m[0]; w = m[1];
    asm volatile("v_max_f32 %0, %0, %2\n\tv_max_f32 %1, %1, %3\n\ts_nop 1" : "+v"(u), "+v"(w) : "v"(m[2]), "v"(m[3]));
    swp16(u, w); u = vmax(u, w);
    xch16(u, u, w); xch32(u, m[0], m[2]); xch32(w, m[1], m[3]);
    MT(5);
    expo(3); expo(2); expo(1); expo(0);
    pv(3); pv(2); pv(1); pv(0);
    swp32(m[0], m[2]); swp32(m[1], m[3]); u = m[0] + m[2]; w = m[1] + m[3];
    swp16(u, w); u = __builtin_amdgcn_rcpf(u + w);
    xch16(u, u, w); xch32(u, m[0], m[2]); xch32(w, m[1], m[3]);
    MT(6);
    outp(3); outp(2); outp(1); outp(0);
  }
  MT(7);
  MTOUT(10 + TH, TH * 2);
}

extern "C" __global__ __launch_bounds__(512, 2) void mega_kernel(const u16* x0, const u16* wqkv, const u16* wo, const u16* w1, const u16* w2, const u16* b1, const u16* b2, u16* out, char* ws) {
  const int wid0 = __builtin_amdgcn_readfirstlane(threadIdx.x >> 6);
  ST(0, 7);
  STL(0, 6);
  asm volatile("s_getpc_b64 s[40:41]\n\ts_andn2_b32 s40, s40, 63\n\ts_load_dwordx2 s[100:101], s[40:41], %0 offset:124\n\ts_load_dwordx2 s[100:101], s[40:41], %0 offset:252"
               :: "s"(__builtin_amdgcn_readfirstlane(threadIdx.x) << 2) : "s40", "s41", "s100", "s101", "scc");
  sched0();
#define PHASE_VARS \
      int tid = threadIdx.x, bid = blockIdx.x, wid = wid0; \
      asm volatile("" : "+v"(tid)); asm volatile("" : "+s"(bid), "+s"(wid), "+s"(ws)); \
      const int lw = wid - 4, lt = tid - 256, lane = tid & 63, fr = lane & 15, fq = lane >> 4, wp = wid & 1, wq = wid >> 1; \
      const unsigned j = (bid >> 3) * 4 + lw; \
      int hp, sb; tile_map<16, 16, 4, 8>(bid, hp, sb);            \
      int tp, tq; tile_map<8, 32, 4, 8>(bid, tp, tq);             \
      int fp, fq1; tile_map<28, 8, 7, 4>(bid, fp, fq1);           \
      const int cw = bid < 16 ? wid + 4 * (bid >> 3) : -1; \
      (void)cw; (void)j; (void)lt; (void)wp; (void)wq; (void)lw; (void)fr; (void)fq; (void)hp; (void)sb; (void)tp; (void)tq; (void)fp; (void)fq1;
#define LOADER_VARS PHASE_VARS asm volatile("" : "+s"(wqkv), "+s"(wo), "+s"(w1), "+s"(w2));

  if (__builtin_expect(wid0 >= 4, 1)) {
    if (LPR) __builtin_amdgcn_s_setprio(3);
    const u16* xin = x0;
    Pfc pf(wo, w1, blockIdx.x * 4 + wid0 - 4);
#pragma unroll 1
    for (int l = 0; l < NL; ++l) {
      {
        LOADER_VARS
        const int s0 = sb * 128;
        GQ::Loader L(wqkv + hp * 64, xin, lw, lane, qkv_rot(bid));
        if (l == 0) L.pre_w1<0>();
        L.seta((unsigned)(s0 * D * 2), halo_off(s0, lw, lane), lw, lane);
        if (l == 0) { L.issue_a<0>(L.ab + L.kord(0) * 128); if (!SI) L.pre<1, true, true>(); }
        sched0();
        const rsrc_t rs = mkrsrc(ws + WS_ST, MB);
        f32x4 c = {0.f, 0.f, 0.f, 0.f}; f32x2 s1 = {0.f, 0.f}, s2 = {0.f, 0.f}, o = {0.f, 0.f}; float pv = 0.f;
        bf16x8 ones = {0x3F80, 0x3F80, 0x3F80, 0x3F80, 0x3F80, 0x3F80, 0x3F80, 0x3F80};
        const int ct = lw & 1 ? 8 + (sb & 3) : sb & 7; const bool cs = l == 0 && lw >= 2;
        int tpf = (slow_res(wqkv) >> 2) != (bid & 1) ? 0 : lw < 2 ? 10 : 9;
        unsigned ra = GQ::AOFF + ((hp & 3) * 32 + lw * 8) * 128 + lane * 16, rb = rd_km(lane, ct);
        unsigned so = lane & 7 ? MB : ph((unsigned)(((l * S + s0 + (hp & 3) * 32 + lw * 8 + (lane >> 3)) * 4 + (hp >> 2)) * 16));
        unsigned co = fr >= 2 || !cs ? MB : ph((unsigned)(ST_CS + ((hp * 96 + ct * 8 + fq * 2 + fr) * 2 + (sb >> 3)) * 16));
        asm volatile("" : "+v"(ra), "+v"(rb), "+v"(so), "+v"(co), "+v"(c), "+v"(s1), "+v"(s2), "+v"(ones), "+v"(o));
        asm volatile("" : "+s"(tpf));
        if (SI && l == 0) L.pre_w1<1>();
        sched0();
        unsigned to[3], xo = x_off(tp, tq, lw, fr, fq), q2 = ((bid & 2) * 4 + slow_res(wqkv)) * 128 + ((bid >> 3) * 2 + (lw & 1)) * (3 * D * 2);
#pragma unroll
        for (int i = 0; i < 3; ++i) {
          const int r = s0 - 16 + (i == 2 && lw ? 64 : i * 64) + (lt >> 2);
          to[i] = ph((unsigned)(((l * S + (r < 0 ? 0 : r)) * 4 + (lt & 3)) * 16));
        }
        unsigned ta = (lt & 3) < (lw ? 2 : 3) ? L_TAB + ((lt & 3) * 64 + (lt >> 2)) * 4 : L_SCR, ts = ph((unsigned)(ST_CS + (hp * 192 + (lw ? lt - 64 : lt)) * 16));
        asm volatile("" : "+v"(to[0]), "+v"(to[1]), "+v"(to[2]), "+v"(xo), "+v"(ta), "+v"(ts));
        asm volatile("" : "+s"(q2));
        if (SI == 1 && l == 0) L.pre<1, false, true>();
        sched0();
        const GW::Loader W(wo + tp * 128, ws, lw, lane);
        if (SI == 2 && l == 0) L.pre<1, false, true>();
        sched0();
        auto pfq = [&](int n) { const unsigned o = q2 + L.kord(n) * GQ::WSTEP; spf2<0>(wqkv, o); spf2<2048>(wqkv, o); spf2<4096>(wqkv, o); };
        if (l) {
          if (QBR == 3) bar();
          if (!(EW & 8)) L.pre_w();
          if (tpf) { pfq(2 + (lw >> 1)); pfq(4 + (lw >> 1)); }
          if (QBR == 4) bar();
          L.start();
        }
        else STL(0, 2);
        if (LPR == 1) __builtin_amdgcn_s_setprio(0);
        unsigned tag, tg2; u32x4 gs, a[3];
        auto rowst = [&](int i) {
          const float m0 = __uint_as_float(a[i][0]), m = red4(m0) * 0.25f, q = red4(__uint_as_float(a[i][2]) + 256.f * (m0 - m) * (m0 - m));
          o = (f32x2){sel((lt & 3) == i, m, o.x), sel((lt & 3) == i, q, o.y)};
          asm volatile("" : "+v"(o));
        };
        for_c<0, 16 - GQ::H>([&](auto tc) {
          constexpr int t = tc.value;
          u32x4 v; unsigned tv;
          L.step<t, t == 5 || t == 6 ? 1 : t == 9 || t == 10 ? 4 : 0>([&](unsigned ls) {
            if (t < 4) v = *(const LAS u32x4*)(uintptr_t)(ls + ra);
            if (t == 3) tv = *(volatile LAS unsigned*)(uintptr_t)L_TAG;
          }, [&]() {
            if (t < 8 && cs) {
              constexpr int ls = 32768 + t % 3 * GQ::SB;
              bf16x4 x[4];
#pragma unroll
              for (int e = 0; e < 4; ++e) x[e] = lds_tr4(rb + (ls & ~0x7fff) + (ls & 0x7fff) + (e >> 1) * 4096 + (e & 1) * 512);
#pragma unroll
              for (int ks = 0; ks < 2; ++ks) c = mfma(__builtin_shufflevector(x[2 * ks], x[2 * ks + 1], 0, 1, 2, 3, 4, 5, 6, 7), ones, c);
            } else if (t >= 4) {
              if (t >= 10 || t >= tpf) { if (t < TC1) pf.go(); }
              else if (lw < 2) pfq(t + 4);
              else if (t == 8) { pfq(14); pfq(15); }
            }
            if (t < 4) {
              if (t == 0) { pv = bflo(v[0]); asm volatile("" : "+v"(pv)); }
#pragma unroll
              for (int e = 0; e < 4; ++e) {
                if (SSC) {
                  float x = bflo(v[e]), y = bfhi(v[e]), a0 = s1.x, a1 = s1.y, b0 = s2.x, b1 = s2.y;
                  asm volatile("v_sub_f32 %0, %0, %6\n\tv_sub_f32 %1, %1, %6\n\tv_add_f32 %2, %2, %0\n\tv_add_f32 %3, %3, %1\n\tv_fmac_f32 %4, %0, %0\n\tv_fmac_f32 %5, %1, %1"
                               : "+v"(x), "+v"(y), "+v"(a0), "+v"(a1), "+v"(b0), "+v"(b1) : "v"(pv));
                  s1 = (f32x2){a0, a1}; s2 = (f32x2){b0, b1};
                } else { const f32x2 d = (f32x2){bflo(v[e]), bfhi(v[e])} - pv; s1 += d; s2 += d * d; }
              }
              asm volatile("" : "+v"(s1), "+v"(s2));
            }
            if (t == 3) { tag = __builtin_amdgcn_readfirstlane(tv) + 1u; tg2 = tag2(tag); if (tpf == 10) pfq(7); if (l && tpf == 9) pfq(6); }
            if (t == 4) {
              const float d1 = (s1.x + s1.y) * (1.f / 32.f), ml = pv + d1, m = red8(ml) * 0.125f;
              const float q = red8(vmax((s2.x + s2.y) - 32.f * d1 * d1, 0.f) + 32.f * (ml - m) * (ml - m));
              gs = (u32x4){__float_as_uint(m), tag, __float_as_uint(q), tg2};
              asm volatile("" : "+v"(gs));
            }
            if (t == 8) { gs = (u32x4){__float_as_uint(sel(fr, c[2], c[0])), tag, __float_as_uint(sel(fr, c[3], c[1])), tg2}; asm volatile("" : "+v"(gs)); }
            if (t == (RB ? 12 : 11)) {
#pragma unroll
              for (int i = 0; i < 3; ++i) ld_ok(a[i]);
              for (int spin = 0;; ++spin) {
                unsigned bad = 0;
#pragma unroll
                for (int i = 0; i < 3; ++i) bad |= (a[i][1] ^ tag) | (a[i][3] ^ tg2);
                if (__builtin_expect(__all(bad == 0), 1) || spin_fail(spin, ws)) break;
                __builtin_amdgcn_s_sleep(8);
#pragma unroll
                for (int i = 0; i < 3; ++i) ld_again(a[i], rs, to[i]);
              }
              if (!RB) rowst(0);
            }
            if (t == 12 && !RB) { rowst(1); rowst(2); }
            if (t == 13 && RB == 2) rowst(0);
            if (t == 13 && !RB) { LAS float* p = (LAS float*)(uintptr_t)ta; p[0] = -o.x; p[144] = __builtin_amdgcn_rsqf(o.y * (1.f / D) + EPS); }
          }, [&]() {
            if (t == 4) st16(rs, so, gs);
            if (t == 8) {
              st16(rs, co, gs);
              sched0();
#pragma unroll
              for (int i = 0; i < 3; ++i) ld_raw(a[i], rs, to[i]);
            }
          });
        });
        if (l == 0) {
#pragma unroll
          for (int p = 0; p < 4; ++p) dma16((const char*)xin + (p >> 1) * 32768 + (p & 1) * 64, xo, L_X + lw * 4096 + p * 1024);
          ld_raw(gs, rs, ts);
          wait_vm<GQ::CNT + 5>();
        } else wait_vm<GQ::CNT>();
        bar();
        W.pre_w1<0>();
        if (RB) { if (RB == 1) rowst(0); rowst(1); rowst(2); LAS float* p = (LAS float*)(uintptr_t)ta; p[0] = -o.x; p[144] = __builtin_amdgcn_rsqf(o.y * (1.f / D) + EPS); }
        wait_lgkm0();
        if (l == 0) wait_vm<9>(); else wait_vm<4>();
        bar();
        W.pre_w1<1>(); W.pre_w1<2>();
        if (l == 0) {
          wait_vm<12>();
          ld_ok(gs);
          for (int spin = 0;; ++spin) {
            if (__builtin_expect(__all(((gs[1] ^ tag) | (gs[3] ^ tg2)) == 0), 1) || spin_fail(spin, ws)) break;
            __builtin_amdgcn_s_sleep(8);
            ld_again(gs, rs, ts);
          }
          float c0 = __uint_as_float(gs[0]), c1 = __uint_as_float(gs[2]);
          c0 += dppf<0xB1>(c0); c1 += dppf<0xB1>(c1);
          *(LAS float*)(uintptr_t)(L_CS + (lw ? lt - 64 : lt) * 4) = sel(lt & 1, c1, c0);
        }
        if (l == 0 && lw < TBA) { if (fr < 14) touch4(w1 + (size_t)(((bid >> 3) * TBA + lw) * 8 + (bid & 4)) * F + (bid & 3) * 896, fq * (F * 2) + fr * 128, L_SCR + lw * 256); }
        if (l == 0) { wait_lgkm0(); bar(); }
      }
      {
        LOADER_VARS
        GW::Loader L(wo + tp * 128, ws + WS_O + (size_t)tq * 32 * 8192, lw, lane);
        unsigned po = ((bid & 1) * 8 + (slow_res(wo) ^ (j & 1))) * 128 + (j >> 1) * (D * 2);
#pragma unroll
        for (int k = GW::H; k < 10; ++k) spf2(wo, po + k * GW::WSTEP);
        asm volatile("" : "+s"(po));
        sched0();
        bar();
#pragma unroll
        for (int k = 10; k < 16; ++k) spf2(wo, po + k * GW::WSTEP);
        L.start();
        L.run(16 - GW::H, [&](int) { pf.go(); });
        pf.go();
        L.tail<GW::H - 1>(); bar();
      }
      {
        LOADER_VARS
        if (bid < 224) {
          G1::Loader L(w1 + fp * 128, ws + WS_H + (size_t)fq1 * 64 * 16384, lw, lane);
          L.pre_w();
          const G2A::Loader W((const char*)(w2 + (tq & 23) * 64) + (tq & 8 ? JA * G2::WSTEP : 0), ws, lw, lane);
          const int g0 = (bid & 3) * 14, ga = (g0 + ((slow_res(w1) - g0) & 7)) ^ (j & 1), gb = ga + 8 < g0 + 14 ? ga + 8 : ga;
          const unsigned j2 = j < 32 ? 112 + (j & 15) : j;
          unsigned pa = (j >> 1) * (F * 2) + ga * 128, pb = (j >> 1) * (F * 2) + gb * 128, pd = (j2 >> 1) * (F * 2) + (j & 16 ? gb : ga) * 128;
          asm volatile("" : "+s"(pa), "+s"(pb), "+s"(pd));
          auto pfb = [&](int k) { spf2(w1, pa + k * G1::WSTEP); spf2(w1, pb + k * G1::WSTEP); spf2(w1, pd + k * G1::WSTEP); };
#pragma unroll
          for (int k = G1::H; k < G1::H + PA_1; ++k) pfb(k);
          wait_lgkm0(); pf.go();
          sched0();
          bar();
          pf.go();
          L.start();
          L.run(32 - G1::H, [&](int t) {
            if (pf.pc < 3 + NW1) pf.go();
            else if (t < 32 - G1::H - PA_1) pfb(t + G1::H + PA_1);
          });
          if (EW & 4) {
            wait_vm<2 * G1::CNT>(); bar(); W.pre_w1<1>();
            wait_vm<G1::CNT + 4>(); bar();
            wait_vm<4>(); bar(); W.pre_w1<0>();
          } else L.tail<G1::H - 1>();
          bar();
        } else {
          wait_lgkm0(); pf.go();
          bar();
          if (l == 0) {
            __builtin_amdgcn_s_sleep(24);
            while (pf.pc < 3 + NW1) { wait_lgkm0(); pf.go(); }
            spare_touch(w1, w2, bid, wid, lane);
          }
        }
        pf.pc = 99;
      }
      {
        LOADER_VARS
        constexpr int NB = F / 64 - 2 * JA;
        const char* ub = ws + WS_U + (size_t)tp * 112 * 16384;
        G2A::Loader A((const char*)(w2 + (tq & 23) * 64) + (tq & 8 ? JA * G2::WSTEP : 0), ub + (tq & 8 ? JA * 32768 : 0), lw, lane);
        if (!(EW & 4) || bid >= 224) A.pre_w();
        const GQ::Loader W(wqkv + hp * 64, ws, lw, lane, qkv_rot(bid));
        unsigned pb = ((bid & 4) * 4 + (j & 1) * 8 + slow_res(w2)) * 128 + (j >> 1) * (S * 2) + JA * G2::WSTEP, pa = pb - (bid & 2 ? 0 : JA * G2::WSTEP);
        asm volatile("" : "+s"(pa), "+s"(pb));
        auto side = [&](int n) { n += G2A::H + PA_2; if (n < JA + NB) spf2(w2, (n < JA ? pa : pb) + n * G2::WSTEP); };
#pragma unroll
        for (int k = 0; k < PA_2; ++k) side(k - PA_2);
        G2::Loader L((const char*)(w2 + tq * 64) + 2 * JA * G2::WSTEP, ub + 2 * JA * 32768, lw, lane);
        gu32* xf = (gu32*)(ws + WS_MAIL) + (l * 4 + 2) * MEDGE + (tq * 8 + tp) * 64 + 28 + lw;
        const char* xp = ws + WS_XP + (unsigned)(((bid ^ 2) * 4 + lw) << 14);
        unsigned fl = L_QKV + lw * 256, xv = ph(lane * 16);
        asm volatile("" : "+s"(xf), "+s"(xp), "+s"(fl)); asm volatile("" : "+v"(xv));
        sched0();
        bar();
        A.start();
        A.run(JA - G2A::H, [&](int t) { side(t); });
        auto stage = [&](auto s) { L.issue_w<s.value * G2::SB>(L.w + s.value * G2::WSTEP); L.issue_a<s.value * G2::SB>(L.ab + s.value * 32768); sched0(); };
        wait_vm<G2A::CNT>(); bar(); stage(IC<0>{}); side(JA - 2);
        wait_vm<G2::CNT>(); bar(); stage(IC<1>{}); side(JA - 1);
        wait_vm<G2::CNT>(); bar(); stage(IC<2>{}); stage(IC<3>{}); side(JA);
        L.t = 1; L.islot = 0;
        L.run(NB - G2::H, [&](int t) {
          if (t == NB - G2::H - FLS) asm volatile("s_mov_b32 m0, %2\n\ts_nop 0\n\tglobal_load_lds_dword %0, %1 sc1" :: "v"(0u), "s"(xf), "s"(fl) : "memory");
          side(JA + t);
        });
        auto xdma = [&]() {
#pragma unroll
          for (int i = 0; i < 4; ++i) dma16(xp + i * 2048, xv, L_X + lw * 4096 + i * 1024);
          sched0();
        };
        sched0(); wait_vm<2 * G2::CNT>(); bar();
        if (__builtin_amdgcn_readfirstlane(*(volatile LAS unsigned*)(uintptr_t)fl)) {
          xdma();
          if ((EW & 8) && l == 0) { W.pre_w1<0>(); wait_vm<G2::CNT + 10>(); bar(); W.pre_w1<1>(); wait_vm<16>(); bar(); wait_vm<12>(); }
          else { wait_vm<G2::CNT + 4>(); bar(); wait_vm<4>(); bar(); wait_vm<0>(); }
        } else {
          wait_vm<G2::CNT>(); bar(); wait_vm<0>(); bar();
          for (int spin = 0;; ++spin) {
            if (__builtin_amdgcn_readfirstlane(__hip_atomic_load(xf, RLX_AGENT)) || spin_fail(spin, ws)) break;
            __builtin_amdgcn_s_sleep(2);
          }
          STL(4 + l * 4, 7);
          xdma();
          if ((EW & 8) && l == 0) { W.pre_w(); wait_vm<12>(); } else wait_vm<0>();
        }
        bar();
        xin = (const u16*)(ws + WS_X2);
        if (NL == 1) bar();
      }
    }
    return;
  }

  {
    ST(0, 0);
    PHASE_VARS
    if (cw >= 0) code_warm(cw, lane, L_SCR + wid * 256);
    pfb_q(wqkv, bid, (bid >> 3) * 4 + wid, 0); pfb_q(wqkv, bid, (bid >> 3) * 4 + wid, 1);
    if (wid < 3) {
      if (tid == 0) asm volatile("s_mov_b32 m0, %2\n\ts_nop 0\n\tglobal_load_lds_dword %0, %1 sc1" :: "v"(WS_EPOCH), "s"(ws), "s"(L_TAG) : "memory");
      const rsrc_t rz = mkrsrc(ws + WS_XCH, 3 * MB);
      if (tid < 128) st16(rz, ph((((tid >> 6) * 256 + tq * 8 + tp) * 64 + lane) * 16), (u32x4){0u, SENT, 0u, SENT});
      else {
        const int e = (tid >> 3) & 7, line = e == 0 || e == 7 ? (2 * sb + (e & 1)) * 8 + (hp & 7) : e * 256 + bid;
        st16(rz, WS_MAIL - WS_XCH + line * 256 + (tid & 7) * 16, (u32x4){0u, 0u, 0u, 0u});
      }
      if (wid == 1 && bid < 224) touch4(b1 + fp * 128, lane * 4, L_BIAS);
      if (wid == 2) touch4(b2 + tq * 64, (lane & 31) * 4, L_BIAS + 256);
      if (wid == 0) *(LAS u32x4*)(uintptr_t)(L_TAG + 16) = (u32x4){0u, 0u, 0u, 0u};
    }
    if (CPR) __builtin_amdgcn_s_setprio(CPR);
    ST(0, 4);
    ST(1, 0);
  }
#pragma unroll 1
  for (int l = 0; l < NL; ++l) {
    {
      PHASE_VARS
      if (wid & 2) qkv_attn<1>(ws, l, wp, lane, hp, sb, cw, bid, tp, tq, wqkv);
      else qkv_attn<0>(ws, l, wp, lane, hp, sb, cw, bid, tp, tq, wqkv);
      ST(1 + l * 4, 5);
    }
    {
      PHASE_VARS
      if (WSP == 0) sync_post(ws, l * 4, lane < 16 ? (sb * 16 + lane) * 64 + hp : -1, wid);
      GW::Comp<O42<false, WNE>> g; g.setup(wp, wq, lane);
      f32x4 acc[4][2];
#pragma unroll
      for (int i = 0; i < 4; ++i)
#pragma unroll
        for (int jj = 0; jj < 2; ++jj) acc[i][jj] = (f32x4){0.f, 0.f, 0.f, 0.f};
      unsigned xa = L_X + wid * 4096 + lane * 16;
      { bf16x8 s[2]; sel_frags(true, fr, fq, s); tile_add(acc, s, xa); }
      if (WSP == 1) {
#pragma unroll
        for (int i = 0; i < 4; ++i) asm volatile("" : "+v"(acc[i][0]), "+v"(acc[i][1]));
        sync_post(ws, l * 4, lane < 16 ? (sb * 16 + lane) * 64 + hp : -1, wid);
      }
      const rsrc_t rx = mkrsrc(ws + WS_XCH + l * (MB / 2), MB / 2), rh = mkrsrc(ws + WS_H, S * D * 4);
      unsigned xs = fq != fr >> 2 ? MB : ph((unsigned)(((tq * 8 + tp) * 64 + wp * 32 + wq * 16 + fr) * 16)), xl = ph((unsigned)((tq * 512 + fq * 128 + wq * 16 + fr) * 16));
      const unsigned h0 = ph((unsigned)(((tp * 32 + tq) << 14) + wp * 8192 + (wq * 32 + fr) * 128 + ((pair_chunk(fq) ^ (km_f(fr) << 1)) << 4)));
      u32x4 ho = {h0, h0 ^ 64, h0 + 4096, (h0 ^ 64) + 4096};
      bf16x8 ones = {0x3F80, 0x3F80, 0x3F80, 0x3F80, 0x3F80, 0x3F80, 0x3F80, 0x3F80};
      asm volatile("" : "+v"(xa), "+v"(xs), "+v"(xl), "+v"(ho), "+v"(ones));
      g.prep(acc);
      if (WSP == 2) sync_post(ws, l * 4, lane < 16 ? (sb * 16 + lane) * 64 + hp : -1, wid);
      sync_wait<WSP == 2 ? SW0 : SWW>(ws, l * 4, tq * 8 + tp, lane, 0xffffu, (1u << GW::H) - 1, wid);
      g.run_steps<false>(acc, [&](int it, int i) { if (i == 0 && cw >= 0 && it == -2) code_warm(cw, lane, L_SCR + wid * 256); } CSA);
      MTD;
      MT(0);
      bar();
      sched0();
      MT(1);
      g.pend<0, 4>(acc);
      ST(2 + l * 4, 2);
      u32x4 pk[2][2]; float mp[2];
      {
        f32x4 sm[2], gm[2];
#pragma unroll
        for (int i = 0; i < 2; ++i)
#pragma unroll
          for (int jj = 0; jj < 2; ++jj) {
            pk[jj][i] = cat4(pk4(acc[2 * i][jj]), pk4(acc[2 * i + 1][jj])); asm volatile("" : "+v"(pk[jj][i]));
            const bf16x8 x = __builtin_bit_cast(bf16x8, pk[jj][i]);
            gm[jj] = mfma(x, x, i ? gm[jj] : (f32x4){0.f, 0.f, 0.f, 0.f}); sm[jj] = mfma(ones, x, i ? sm[jj] : (f32x4){0.f, 0.f, 0.f, 0.f});
          }
        u32x4 st;
#pragma unroll
        for (int jj = 0; jj < 2; ++jj) {
          const f32x4 gv = gm[jj];
          st[2 * jj] = __float_as_uint(sm[jj][0]); st[2 * jj + 1] = __float_as_uint(sel(fr & 2, sel(fr & 1, gv[3], gv[2]), sel(fr & 1, gv[1], gv[0])));
          mp[jj] = sm[jj][0] * (1.f / 64.f);
        }
        st16(rx, xs, st);
      }
      sched0();
      MT(2);
#pragma unroll
      for (int p = 0; p < 4; ++p) *(LAS u32x4*)(uintptr_t)(xa + p * 1024) = pk[p >> 1][p & 1];
      asm volatile("" : "+v"(mp[0]), "+v"(mp[1]));
#pragma unroll
      for (int jj = 0; jj < 2; ++jj)
#pragma unroll
        for (int i = 0; i < 2; ++i) {
          const auto rx_ = __builtin_amdgcn_permlane16_swap(pk[jj][i][0], pk[jj][i][2], false, false), ry_ = __builtin_amdgcn_permlane16_swap(pk[jj][i][1], pk[jj][i][3], false, false);
          const unsigned x0 = rx_[0], x1 = rx_[1], y0 = ry_[0], y1 = ry_[1];
          acc[2 * i][jj] = (f32x4){bflo(x0), bfhi(x0), bflo(y0), bfhi(y0)} - mp[jj]; acc[2 * i + 1][jj] = (f32x4){bflo(x1), bfhi(x1), bflo(y1), bfhi(y1)} - mp[jj];
          asm volatile("" : "+v"(acc[2 * i][jj]), "+v"(acc[2 * i + 1][jj]));
        }
      if (XD) __builtin_amdgcn_s_sleep(XD);
      MT(3);
      f32x4 gr[4];
      for (int spin = 0;; ++spin) {
        asm volatile("" ::: "memory");
#pragma unroll
        for (int p = 0; p < 4; ++p) gr[p] = __builtin_bit_cast(f32x4, __builtin_amdgcn_raw_buffer_load_b128(rx, xl + p * 1024, 0, 16));
        const float lo = vmin3(vmin3(gr[0][1], gr[0][3], gr[1][1]), vmin3(gr[1][3], gr[2][1], gr[2][3]), vmin3(gr[3][1], gr[3][3], gr[3][3]));
        if (!__any(lo < 0.f) || spin_fail(spin, ws)) break;
        __builtin_amdgcn_s_sleep(XS);
      }
      ST(2 + l * 4, 5);
      MT(4);
      {
        const f32x4 t = (gr[0] + gr[1]) + (gr[2] + gr[3]);
        float m[2] = {t[0], t[2]}, q[2] = {t[1], t[3]}, u, w, v, y;
        swp32(m[0], m[1]); swp32(q[0], q[1]);
        xch16(m[0] + m[1], u, w); xch16(q[0] + q[1], v, y);
        const float mean = (u + w) * (1.f / D), var = __builtin_fmaxf((v + y) * (1.f / D) - mean * mean, 0.f), r1 = __builtin_amdgcn_rsqf(var + EPS);
        xch32(mean, m[0], m[1]);
        xch32(r1 * __builtin_amdgcn_rsqf(var * r1 * r1 + EPS), q[0], q[1]);
        ST(2 + l * 4, 3);
        MT(5);
#pragma unroll
        for (int jj = 0; jj < 2; ++jj) {
          const float c = (mp[jj] - m[jj]) * q[jj];
#pragma unroll
          for (int i = 0; i < 2; ++i) st16(rh, ho[jj * 2 + i], cat4(pk4(acc[2 * i][jj] * q[jj] + c), pk4(acc[2 * i + 1][jj] * q[jj] + c)));
        }
      }
      MT(6);
      MT(7);
      MTOUT(12, 0);
    }
    {
      PHASE_VARS
      const int post = lane < 28 ? (tp * 28 + lane) * 64 + tq : -1;
      if (bid >= 224) {
        sync_post(ws, l * 4 + 1, post, wid);
        if (l == 0 && bid == 255 && wid == 0) { const unsigned tag = get_tag(); if (lane == 0) __hip_atomic_store((gu32*)(ws + WS_EPOCH), tag, RLX_AGENT); }
        ST(2 + l * 4, 4);
        ST(3 + l * 4, 0);
        if (l == 0) { __builtin_amdgcn_s_sleep(24); spare_touch(w1, w2, bid, wid, lane); }
      } else {
        G1::Comp<O44<false, 32>> g; g.setup(wp, wq, lane);
        f32x4 acc[4][4];
#pragma unroll
        for (int i = 0; i < 4; ++i) { const u32x2 b = *(const LAS u32x2*)(uintptr_t)(L_BIAS + (wp * 4 + i) * 32 + fq * 8);
#pragma unroll
          for (int jj = 0; jj < 4; ++jj) acc[i][jj] = (f32x4){bflo(b.x), bfhi(b.x), bflo(b.y), bfhi(b.y)}; }
        const rsrc_t ro = mkrsrc(ws + WS_U, D * F * 4);
        const unsigned u0 = ph((unsigned)(((fq1 * 56 + fp * 2 + wp) << 14) + (wq * 64 + fr) * 128 + ((pair_chunk(fq) ^ (fr >> 1)) << 4)));
        u32x4 uo = {u0, u0 + 4096, u0 + 8192, u0 + 12288}, uo2 = uo ^ 64;
        asm volatile("" : "+v"(uo), "+v"(uo2));
        g.prep(acc);
        sync_post(ws, l * 4 + 1, post, wid);
        sync_wait(ws, l * 4 + 1, fq1 * 28 + fp, lane, ~0u, (1u << G1::H) - 1, wid);
        g.run_steps<false>(acc, [&](int it, int i) { if (i == 0 && cw >= 0 && it == -2) code_warm(cw, lane, L_SCR + wid * 256); } CSA);
        bar();
        sched0();
        ST(3 + l * 4, 2);
        if (GSH) {
          for_c<0, 2>([&](auto jc) {
            constexpr int jj = jc.value;
            f32x4 a[2];
#pragma unroll
            for (int i = 0; i < 2; ++i) { a[i] = gelu_arg(acc[i][jj]); asm volatile("" : "+v"(a[i])); }
            sched0();
            for_c<0, 4>([&](auto ec) { constexpr int e = ec.value; g.mm<jj * 8 + e>(acc); sched0(); a[0][e] = __builtin_amdgcn_exp2f(a[0][e]); a[1][e] = __builtin_amdgcn_exp2f(a[1][e]); asm volatile("" : "+v"(a[0]), "+v"(a[1])); sched0(); });
            a[0] += 1.f; a[1] += 1.f; asm volatile("" : "+v"(a[0]), "+v"(a[1]));
            sched0();
            for_c<0, 4>([&](auto ec) { constexpr int e = ec.value; g.mm<jj * 8 + 4 + e>(acc); sched0(); a[0][e] = __builtin_amdgcn_rcpf(a[0][e]); a[1][e] = __builtin_amdgcn_rcpf(a[1][e]); asm volatile("" : "+v"(a[0]), "+v"(a[1])); sched0(); });
            st16(ro, uo[jj], pack_pair(acc[0][jj] * a[0], acc[1][jj] * a[1]));
          });
        } else g.pend<0, 8>(acc);
#pragma unroll
        for (int n = GSH ? 2 : 0; n < 8; ++n) { const int i = n >> 2, jj = n & 3; st16(ro, (i ? uo2 : uo)[jj], pack_pair(gelu_tanh(acc[2 * i][jj]), gelu_tanh(acc[2 * i + 1][jj]))); }
        ST(3 + l * 4, 3);
      }
    }
    {
      PHASE_VARS
      asm volatile("" : "+s"(out));
      G2A::Comp<O44<true, ANE>> ga; ga.setup(wp, wq, lane);
#pragma unroll
      for (int jj = 0; jj < 4; ++jj) ga.qrd[0][jj] = 65536 + rd_km(lane, wq * 2 + (jj & 1)) + (((jj >> 1) ^ (tq >> 3)) & 1) * 8192;
      G2::Comp<O42<true, BNE>> g; g.setup(wp, wq, lane);
      g.setwin();
      ga.setwin();
      if (GSP == 1) sync_post(ws, l * 4 + 2, bid < 224 && lane < 32 ? (lane * 8 + fq1) * 64 + fp : -1, wid);
      f32x4 ac4[4][4], acc[4][2];
#pragma unroll
      for (int jj = 0; jj < 2; ++jj) { const float b = bf2f(*(const LAS u16*)(uintptr_t)(L_BIAS + 256 + (wq * 2 + jj) * 32 + fr * 2));
#pragma unroll
        for (int i = 0; i < 4; ++i) { ac4[i][jj] = (f32x4){b, b, b, b}; ac4[i][jj + 2] = (f32x4){0.f, 0.f, 0.f, 0.f}; } }
      unsigned xa = L_X + wid * 4096 + lane * 16, xo = (unsigned)((bid * 4 + wid) << 14) + ph(lane * 16), oo = x_off(tp, tq, wid, fr, fq), xo2 = xo + 4096, oo2 = oo + 32768;
      unsigned fo = lane ? MB : (((tq ^ 8) * 8 + tp) * 64 + 28 + wid) * 4u, one = 1u;
      bf16x8 s[2]; sel_frags(false, fr, fq, s); tile_add(ac4, s, xa);
      const rsrc_t rx = mkrsrc(ws + WS_XP, 16 * MB), rf = mkrsrc(ws + WS_MAIL + (l * 4 + 2) * (MEDGE * 4), MEDGE * 4), ro = mkrsrc(l ? (const void*)out : (const void*)(ws + WS_X2), S * D * 2);
      asm volatile("" : "+v"(xa), "+v"(xo), "+v"(oo), "+v"(xo2), "+v"(oo2), "+v"(fo), "+v"(one), "+v"(s[0]), "+v"(s[1]));
      ga.pin(ac4);
      if (GSP == 2) sync_post(ws, l * 4 + 2, bid < 224 && lane < 32 ? (lane * 8 + fq1) * 64 + fp : -1, wid);
      static_assert(JA % 2 == 0 && G2A::H == 2, "");
      sync_wait<GSP == 2 ? SW0 : SWW>(ws, l * 4 + 2, tq * 8 + tp, lane, 0xfffffffu, tq & 8 ? 1u << JA / 2 : 1u, wid);
      ga.run_steps<false>(ac4, [&](int it, int i) { if (i == 0 && cw >= 0 && it == -2) code_warm(cw, lane, L_SCR + wid * 256); } CSA);
      ga.pend<4, 8>(ac4);
      ST(4 + l * 4, 1);
#pragma unroll
      for (int i = 0; i < 2; ++i)
#pragma unroll
        for (int jj = 0; jj < 2; ++jj) st16(rx, (jj ? xo2 : xo) + i * 2048, cat4(pk4(ac4[2 * i][jj + 2]), pk4(ac4[2 * i + 1][jj + 2])));
#pragma unroll
      for (int i = 0; i < 4; ++i) { acc[i][0] = ac4[i][0]; acc[i][1] = ac4[i][1]; }
#pragma unroll
      for (int k = 0; k < 2; ++k) { g.fp[2][k] = ga.fp[2][k]; g.fp[3][k] = ga.fp[3][k]; g.fq[0][k] = ga.fq[0][k]; g.fq[1][k] = ga.fq[1][k]; }
      sched0();
      g.run_steps<true>(acc, [&](int it, int i) {
        if (i == PS % 4 && it == PS / 4 - (F / 64 - 2 * JA) / 4) {
          wait_vm<0>();
          ST(4 + l * 4, 5);
          __builtin_amdgcn_raw_buffer_store_b32(one, rf, fo, 0, 16);
        }
      } CSA);
      bar();
      sched0();
      g.pend<0, 4>(acc);
      ST(4 + l * 4, 2);
      tile_add(acc, s, xa);
      u32x4 xr[4];
#pragma unroll
      for (int i = 0; i < 2; ++i)
#pragma unroll
        for (int jj = 0; jj < 2; ++jj) { xr[jj * 2 + i] = pack_pair(acc[2 * i][jj], acc[2 * i + 1][jj]); st16(ro, (jj ? oo2 : oo) + i * 64, xr[jj * 2 + i]); }
      ST(4 + l * 4, 3);
      if (l == 0) {
#pragma unroll
        for (int p = 0; p < 4; ++p) *(LAS u32x4*)(uintptr_t)(xa + p * 1024) = xr[p];
        if (NL == 1) sync_post(ws, 3, -1, wid);
      } else ST(8, 4);
    }
  }
  if (wid0 == 0 && blockIdx.x == 45) {
    wait_lgkm0();
    unsigned d = *(volatile LAS unsigned*)(uintptr_t)(19968 + PROBE * 4) - *(volatile LAS unsigned*)(uintptr_t)(19968 + PROBE0 * 4);
    const u64 e = __builtin_amdgcn_s_memrealtime() + PROBEF * (d < 20000 ? d : 0);
    while (__builtin_amdgcn_s_memrealtime() < e) __builtin_amdgcn_s_sleep(1);
  }
  return;
  if (wid0 == 0) {
    const u64 hi = __builtin_amdgcn_s_memrealtime() & ~0xffffffffull;
    wait_lgkm0();
    for (int e = threadIdx.x; e < 128; e += 64) ((gu64*)(ws + (200u << 20)))[((e >> 3) * 1024 + blockIdx.x) * 8 + (e & 7)] = hi | *(volatile LAS unsigned*)(uintptr_t)(19968 + e * 4);
  }
}

extern "C" void kernel_launch(void* const* d_in, const int* in_sizes, int n_in,
                              void* d_out, int out_size, void* d_ws, size_t ws_size,
                              hipStream_t stream) {
  (void)hipFuncSetAttribute((const void*)mega_kernel, hipFuncAttributeMaxDynamicSharedMemorySize, 163840);
  mega_kernel<<<256, 512, 163840, stream>>>((const u16*)d_in[0], (const u16*)d_in[1], (const u16*)d_in[2], (const u16*)d_in[4], (const u16*)d_in[6], (const u16*)d_in[5], (const u16*)d_in[7], (u16*)d_out, (char*)d_ws);
}
```

```cpp
#define LEAN
#define STAMP
#define PROBE 13
#define PROBE0 10
#define PROBEF 16
#define NOPO 1
#define TBA 0
#include <hip/hip_runtime.h>
#include <hip/hip_bf16.h>
#include <stdint.h>

typedef unsigned short u16;
typedef unsigned long long u64;
typedef short bf16x8 __attribute__((ext_vector_type(8)));
typedef short bf16x4 __attribute__((ext_vector_type(4)));
typedef float f32x4 __attribute__((ext_vector_type(4)));
typedef float f32x2 __attribute__((ext_vector_type(2)));
typedef unsigned u32x4 __attribute__((ext_vector_type(4)));
typedef unsigned u32x2 __attribute__((ext_vector_type(2)));
typedef __bf16 bf16v2 __attribute__((ext_vector_type(2)));
#define LAS __attribute__((address_space(3)))
#define GAS __attribute__((address_space(1)))
#define DEVI __device__ __forceinline__
typedef __attribute__((address_space(1))) unsigned gu32;
typedef __attribute__((address_space(1))) unsigned long long gu64;

constexpr int S = 2048, D = 1024, F = 3584;
constexpr float EPS = 1e-5f;

DEVI float bf2f(u16 v) { return __uint_as_float(((unsigned)v) << 16); }
DEVI float bflo(unsigned v) { return __uint_as_float(v << 16); }
DEVI float bfhi(unsigned v) { return __uint_as_float(v & 0xffff0000u); }
DEVI unsigned pk2(float lo, float hi) { bf16v2 p; p.x = (__bf16)lo; p.y = (__bf16)hi; return __builtin_bit_cast(unsigned, p); }

DEVI const void* uni_ptr(const void* p) {
  const u64 v = (u64)p;
  return (const void*)(((u64)(unsigned)__builtin_amdgcn_readfirstlane((unsigned)(v >> 32)) << 32) | (unsigned)__builtin_amdgcn_readfirstlane((unsigned)v));
}
DEVI void dma16(const void* sbase, unsigned voff, unsigned ldsaddr) {
  ldsaddr = __builtin_amdgcn_readfirstlane(ldsaddr);
  asm volatile("s_mov_b32 m0, %2\n\ts_nop 0\n\tglobal_load_lds_dwordx4 %0, %1 sc1" :: "v"(voff), "s"(sbase), "s"(ldsaddr) : "memory");
}
template <int N, int IMM> DEVI void dma_grp(const void* sbase, const unsigned* v, unsigned lb) {
  static_assert(N == 1 || N == 2 || N == 4, "");
  if constexpr (N == 4)
    asm volatile("s_add_u32 m0, %1, %2\n\ts_nop 0\n\tglobal_load_lds_dwordx4 %3, %0 sc1\n\tglobal_load_lds_dwordx4 %4, %0 offset:1024 sc1\n\t"
                 "global_load_lds_dwordx4 %5, %0 offset:2048 sc1\n\tglobal_load_lds_dwordx4 %6, %0 offset:3072 sc1"
                 :: "s"(sbase), "s"(lb), "i"(IMM), "v"(v[0]), "v"(v[1]), "v"(v[2]), "v"(v[3]) : "memory", "scc");
  else if constexpr (N == 2)
    asm volatile("s_add_u32 m0, %1, %2\n\ts_nop 0\n\tglobal_load_lds_dwordx4 %3, %0 sc1\n\tglobal_load_lds_dwordx4 %4, %0 offset:1024 sc1"
                 :: "s"(sbase), "s"(lb), "i"(IMM), "v"(v[0]), "v"(v[1]) : "memory", "scc");
  else
    asm volatile("s_add_u32 m0, %1, %2\n\ts_nop 0\n\tglobal_load_lds_dwordx4 %3, %0 sc1" :: "s"(sbase), "s"(lb), "i"(IMM), "v"(v[0]) : "memory", "scc");
}
template <int N> DEVI void wait_vm() { asm volatile("s_waitcnt vmcnt(%0)" :: "i"(N) : "memory"); }
DEVI void wait_lgkm0() { asm volatile("" ::: "memory"); __builtin_amdgcn_s_waitcnt(0xC07F); asm volatile("" ::: "memory"); }
DEVI void mfma_ip(f32x4& c, const bf16x8& a, const bf16x8& b) { asm volatile("v_mfma_f32_16x16x32_bf16 %0, %1, %2, %0" : "+v"(c) : "v"(a), "v"(b)); }
DEVI void mfma_gap() { __builtin_amdgcn_sched_barrier(0); asm volatile("s_nop 7"); __builtin_amdgcn_sched_barrier(0); }
DEVI void bar() { asm volatile("" ::: "memory"); __builtin_amdgcn_s_barrier(); asm volatile("" ::: "memory"); }
DEVI void sched0() { __builtin_amdgcn_sched_barrier(0); }

DEVI bf16x8 lds_b128(unsigned addr) { return *(const LAS bf16x8*)(uintptr_t)addr; }
DEVI bf16x4 lds_tr4(unsigned addr) { return __builtin_amdgcn_ds_read_tr16_b64_v4i16((LAS bf16x4*)(uintptr_t)addr); }

typedef __amdgpu_buffer_rsrc_t rsrc_t;
DEVI rsrc_t mkrsrc(const void* p, unsigned bytes) { return __builtin_amdgcn_make_buffer_rsrc((void*)p, 0, bytes, 0x00020000); }
DEVI void st16(rsrc_t r, unsigned off, u32x4 v) { __builtin_amdgcn_raw_buffer_store_b128(v, r, off, 0, 16); }
DEVI void ld_raw(u32x4& v, rsrc_t r, unsigned off) { asm volatile("buffer_load_dwordx4 %0, %1, %2, 0 offen sc1 ; raw load" : "=&v"(v) : "v"(off), "s"(r) : "memory"); }
DEVI void ld_again(u32x4& v, rsrc_t r, unsigned off) { asm volatile("buffer_load_dwordx4 %0, %1, %2, 0 offen sc1\n\ts_waitcnt vmcnt(0)" : "=&v"(v) : "v"(off), "s"(r) : "memory"); }
DEVI void ld_ok(u32x4& v) { asm volatile("; raw ok %0" : "+v"(v)); }
#define RLX_AGENT __ATOMIC_RELAXED, __HIP_MEMORY_SCOPE_AGENT

template <int N> DEVI float ror16(float v) { return __int_as_float(__builtin_amdgcn_update_dpp(0, __float_as_int(v), 0x120 + N, 0xf, 0xf, false)); }
DEVI float red16(float v) { v += ror16<8>(v); v += ror16<4>(v); v += ror16<2>(v); v += ror16<1>(v); return v; }
DEVI void xch32(float v, float& a, float& b) { unsigned u = __float_as_uint(v); auto r = __builtin_amdgcn_permlane32_swap(u, u, false, false); unsigned x = r[0], y = r[1]; a = __uint_as_float(x); b = __uint_as_float(y); }
DEVI void xch16(float v, float& a, float& b) { unsigned u = __float_as_uint(v); auto r = __builtin_amdgcn_permlane16_swap(u, u, false, false); unsigned x = r[0], y = r[1]; a = __uint_as_float(x); b = __uint_as_float(y); }
DEVI void swp32(float& a, float& b) { auto r = __builtin_amdgcn_permlane32_swap(__float_as_uint(a), __float_as_uint(b), false, false); unsigned x = r[0], y = r[1]; a = __uint_as_float(x); b = __uint_as_float(y); }
DEVI void swp16(float& a, float& b) { auto r = __builtin_amdgcn_permlane16_swap(__float_as_uint(a), __float_as_uint(b), false, false); unsigned x = r[0], y = r[1]; a = __uint_as_float(x); b = __uint_as_float(y); }
DEVI float colsum4(float v) { float a, b; xch32(v, a, b); v = a + b; xch16(v, a, b); return a + b; }
DEVI float vmax(float a, float b) { float r; asm("v_max_f32 %0, %1, %2" : "=v"(r) : "v"(a), "v"(b)); return r; }
DEVI float vmin3(float a, float b, float c) { float r; asm("v_min3_f32 %0, %1, %2, %3" : "=v"(r) : "v"(a), "v"(b), "v"(c)); return r; }
DEVI float vmax3(float a, float b, float c) { float r; asm("v_max3_f32 %0, %1, %2, %3" : "=v"(r) : "v"(a), "v"(b), "v"(c)); return r; }

DEVI unsigned src_kc(int c, int ld) { int row = c >> 3, ch = (c & 7) ^ ((row >> 1) & 7); return (unsigned)(row * ld * 2 + ch * 16); }
DEVI int km_f(int k) { return ((k >> 1) & 1) | (((k >> 3) & 1) << 1); }
DEVI unsigned ph(unsigned x) { return ((x >> 7) << 8) | (x & 127u); }
DEVI unsigned rd_kc(int lane, int ks) { int r = lane & 15, g = lane >> 4; return (unsigned)(r * 128 + (((ks * 4 + g) ^ (r >> 1)) << 4)); }
DEVI unsigned rd_km(int lane, int ct) {
  int g = lane >> 4, q = (lane & 15) >> 2, p = lane & 3;
  int f = ((q >> 1) & 1) | ((g & 1) << 1);
  return (unsigned)((ct >> 2) * 8192 + (8 * g + q) * 128 + (((ct & 3) ^ f) << 5) + 8 * p);
}

DEVI void stamp(unsigned a) { unsigned x, y;
  asm volatile("s_memrealtime s[98:99]\n\tv_mov_b32 %1, %2\n\ts_waitcnt lgkmcnt(0)\n\tv_mov_b32 %0, s98\n\tds_write_b32 %1, %0" : "=&v"(x), "=&v"(y) : "s"(a) : "s98", "s99"); }
#define ST(k, i) do { if (__builtin_amdgcn_readfirstlane(threadIdx.x) == 0) stamp(19968 + ((k) * 8 + (i)) * 4); } while (0)
#define STL(k, i) do { if (__builtin_amdgcn_readfirstlane(threadIdx.x) == 256) stamp(19968 + ((k) * 8 + (i)) * 4); } while (0)

constexpr unsigned CODE_BYTES = 50176;
DEVI void code_warm(int part, int lane, unsigned scratch) {
  unsigned long long pc;
  asm volatile("s_getpc_b64 s[40:41]\n\ts_add_u32 s40, s40, mega_kernel@rel32@lo+4\n\ts_addc_u32 s41, s41, mega_kernel@rel32@hi+12\n\ts_mov_b64 %0, s[40:41]" : "=s"(pc) :: "s40", "s41", "scc");
  const unsigned off = (unsigned)(part * 64 + lane) * 128u;
  scratch = __builtin_amdgcn_readfirstlane(scratch);
  if (off < CODE_BYTES) asm volatile("s_nop 3\n\ts_mov_b32 m0, %2\n\ts_nop 0\n\tglobal_load_lds_dword %0, %1" :: "v"(off), "s"(pc), "s"(scratch) : "memory");
}

DEVI unsigned src_km(int lt, int ld) { const int k = lt >> 3; return (unsigned)(k * ld * 2 + (((lt & 7) ^ (km_f(k) << 1)) << 4)); }
DEVI void touch4(const void* sbase, unsigned voff, unsigned ldsaddr) {
  ldsaddr = __builtin_amdgcn_readfirstlane(ldsaddr); sbase = uni_ptr(sbase);
  asm volatile("s_nop 3\n\ts_mov_b32 m0, %2\n\ts_nop 0\n\tglobal_load_lds_dword %0, %1" :: "v"(voff), "s"(sbase), "s"(ldsaddr) : "memory");
}

template <int IMM> DEVI void spf1(const void* base, unsigned soff) { asm volatile("s_load_dword s101, %0, %1 offset:%2" :: "s"(base), "s"(soff), "i"(IMM) : "s101"); }
template <int IMM = 0> DEVI void spf2(const void* base, unsigned soff) { asm volatile("s_load_dwordx2 s[100:101], %0, %1 offset:%2" :: "s"(base), "s"(soff), "i"(IMM + 60) : "s100", "s101"); }
template <int ST> DEVI void spf2x6(const void* base, unsigned soff) {
  asm volatile("s_load_dwordx2 s[100:101], %0, %1 offset:%2\n\ts_load_dwordx2 s[100:101], %0, %1 offset:%3\n\ts_load_dwordx2 s[100:101], %0, %1 offset:%4\n\t"
               "s_load_dwordx2 s[100:101], %0, %1 offset:%5\n\ts_load_dwordx2 s[100:101], %0, %1 offset:%6\n\ts_load_dwordx2 s[100:101], %0, %1 offset:%7"
               :: "s"(base), "s"(soff), "i"(60), "i"(60 + ST), "i"(60 + 2 * ST), "i"(60 + 3 * ST), "i"(60 + 4 * ST), "i"(60 + 5 * ST) : "s100", "s101");
}
template <int ST> DEVI void spf1x12(const void* base, unsigned soff) {
  spf1<0>(base, soff); spf1<ST>(base, soff); spf1<2 * ST>(base, soff); spf1<3 * ST>(base, soff); spf1<4 * ST>(base, soff); spf1<5 * ST>(base, soff);
  spf1<6 * ST>(base, soff); spf1<7 * ST>(base, soff); spf1<8 * ST>(base, soff); spf1<9 * ST>(base, soff); spf1<10 * ST>(base, soff); spf1<11 * ST>(base, soff);
}
DEVI int lgkm_out() { int n; asm volatile("s_getreg_b32 %0, hwreg(HW_REG_IB_STS, 8, 4)" : "=s"(n)); return n; }
DEVI int slow_res(const void* w) { return (3 - ((unsigned)(uintptr_t)w >> 7)) & 7; }

#define MTD
#define MT(i)
#define MTOUT(k, w)
#define CSP
#define CSA
#define WNE 16
#define ANE 24
#define BNE 13
#define QNE 46
struct Plan { int n; signed char q[40], idx[40], k[40], h[40], g[40]; };
template <class O, bool PKM, bool QKM> constexpr Plan make_plan() {
  Plan p{};
  int nr = 0;
  for (int c = 0; c < O::NRO; ++c) nr += ((O::ro[c] >> 4) ? QKM : PKM) ? 4 : 2;
  int prev = 0;
  for (int c = 0; c < O::NRO; ++c) {
    const int q = O::ro[c] >> 4, i = O::ro[c] & 15;
    int av[2] = {0, 0}, k0 = -1;
    for (int m = 0; m < 2 * O::NP; ++m) {
      const int mm = (m + 2 * O::NPEND) % (2 * O::NP);
      if ((q ? O::pj[m >> 1] : O::pi[m >> 1]) == i && m < 2 * O::NPEND) av[((m >> 1) ^ m) & 1] = m;
      if ((q ? O::pj[mm >> 1] : O::pi[mm >> 1]) == i && k0 < 0) k0 = ((mm >> 1) ^ mm) & 1;
    }
    for (int kk = 0; kk < 2; ++kk)
      for (int h = 0; h < ((q ? QKM : PKM) ? 2 : 1); ++h) {
        const int k = k0 ^ kk;
        int g = p.n * O::NE / nr;
        if (g < av[k]) g = av[k];
        if (g < prev + (p.n && O::NE >= nr ? 1 : 0)) g = prev + (p.n && O::NE >= nr ? 1 : 0);
        p.q[p.n] = q; p.idx[p.n] = i; p.k[p.n] = k; p.h[p.n] = h; p.g[p.n] = g; prev = g; ++p.n;
      }
  }
  return p;
}
struct OBase { static constexpr int pidx(int i) { return i; } static constexpr int poff(int) { return 0; } static constexpr int qoff(int j) { return j * 2048; } static constexpr bool swp(int) { return false; } };
template <bool PF, int NE_> struct O44 : OBase {
  static constexpr int TP = 4, TQ = 4, NPA = 4, NP = 16, NPEND = 8, NRO = 8, NE = NE_;
  static constexpr signed char pi[16] = {2, 3, 3, 2, 2, 3, 3, 2, 0, 1, 1, 0, 0, 1, 1, 0}, pj[16] = {0, 0, 1, 1, 2, 2, 3, 3, 0, 0, 1, 1, 2, 2, 3, 3};
  static constexpr signed char ro[8] = {0, PF ? 1 : 16, PF ? 16 : 1, 17, 18, 19, 2, 3};
};
template <bool PF, int NE_> struct O42 : OBase {
  static constexpr int TP = 4, TQ = 2, NPA = 4, NP = 8, NPEND = 4, NRO = 6, NE = NE_;
  static constexpr signed char pi[8] = {2, 3, 3, 2, 0, 1, 1, 0}, pj[8] = {0, 0, 1, 1, 0, 0, 1, 1};
  static constexpr signed char ro[6] = {0, PF ? 1 : 16, PF ? 16 : 1, 17, 3, 2};
};
template <int TH> struct OQ {
  static constexpr int TP = 6, TQ = 5, NPA = 2, NP = 26, NPEND = 13, NRO = 11, NE = QNE;
  static constexpr signed char pi[26] = {3, 4, 5, 5, 4, 3, 3, 4, 5, 5, TH ? 5 : 3, 4, TH ? 3 : 4, 0, 1, 2, 2, 1, 0, 0, 1, 2, 2, TH ? 2 : 0, 1, TH ? 0 : 1};
  static constexpr signed char pj[26] = {0, 0, 0, 1, 1, 1, 2, 2, 2, TH ? 4 : 3, 3, 3, TH ? 3 : 4, 0, 0, 0, 1, 1, 1, 2, 2, 2, TH ? 4 : 3, 3, 3, TH ? 3 : 4};
  static constexpr signed char ro[11] = {0, 1, 16, 2, 17, 18, TH ? 20 : 19, TH ? 19 : 20, 3, 4, 5};
  static constexpr int pidx(int i) { return i / 3; }
  static constexpr int poff(int i) { return i % 3 * 8192; }
  static constexpr int qoff(int j) { return j == 4 ? 0 : (1 + 4 * TH + j) * 2048; }
  static constexpr bool swp(int i) { return i % 3 == 2; }
};
template <bool PKM, bool QKM, int PB, int QB, int XB, int NS, int NT, bool WISP, int BASE, int ROT, int WLD, int WSUB, int ASTEP, int RLX = 0, bool QF = false>
struct Gemm {
  static constexpr int SB = PB + QB + XB, CNT = (PB + QB) / 4096 + (XB ? 1 : 0);
  static constexpr int PO = QF ? QB : 0, QO = QF ? 0 : PB, WB = WISP ? PB : QB, AB = WISP ? QB : PB, WOFF = WISP ? PO : QO, AOFF = WISP ? QO + XB : PO;
  static constexpr int XL = XB / 64, H = NS - 1 - RLX, CA = AB / 4096 + (XB ? 1 : 0), WSTEP = 64 * WLD * 2;
  static_assert(H * CNT <= 63 && BASE + NS * SB <= 163840, "");
  struct Loader {
    static constexpr int NW = WB / 4096, NA = AB / 4096;
    const char* w; const char* ab; unsigned wv[NW], av[NA], xv, lwb, lab, lx; int rot, islot, t; bool xl;
    DEVI Loader(const void* w_, const void* ab_, int lw, int lane, int rot_ = 0)
        : w((const char*)w_ - 3072), ab((const char*)ab_ - 3072), lwb(BASE + WOFF + lw * (NW == 6 ? 2048 : WB / 4)), lab(BASE + AOFF + lw * (AB / 4)), lx(BASE + PB + lw * (XL * 16)),
          rot(rot_), islot((H + ROT) % NS), t(0), xl(lane < XL) {
      const unsigned w0 = (lane >> 3) * (WLD * 2), f0 = (lane & 7) ^ ((lane >> 3) & 2);
#pragma unroll
      for (int i = 0; i < NW; ++i) {
        const unsigned p = NW == 6 ? (i >> 1) * 8 + lw * 2 + (i & 1) : lw * NW + i;
        wv[i] = w0 + ((f0 ^ ((i & 1) << 2)) << 4) + (NW == 6 ? (i >> 1) * WSUB + (lw * 2 + (i & 1)) * (8 * WLD * 2) - (i & 1) * 1024 : (p >> 3) * WSUB + (p & 7) * (8 * WLD * 2) - i * 1024) + 3072;
        asm volatile("" : "+v"(wv[i]));
      }
      asm volatile("" : "+s"(w), "+s"(lwb));
      if (XB) asm volatile("" : "+s"(rot));
      else seta(0, 0, lw, lane);
    }
    DEVI void seta(unsigned a0, unsigned xv_, int lw, int lane) {
      xv = xv_ + 3072;
#pragma unroll
      for (int i = 0; i < NA; ++i) { const unsigned p = lw * NA + i; av[i] = (XB ? a0 + src_kc(p * 64 + lane, D) : p * 2048 + ph(lane * 16)) + 3072 - i * 1024; asm volatile("" : "+v"(av[i])); }
      asm volatile("" : "+s"(ab), "+s"(lab));
      if (XB) { asm volatile("" : "+v"(xv)); asm volatile("" : "+s"(lx)); }
    }
    DEVI int kord(int n) const { if constexpr (XB != 0) return (n + rot) & (NT - 1); else return n; }
    template <int SO> DEVI void issue_w(const char* p, unsigned so = 0) const {
      if constexpr (NW == 6) { dma_grp<2, SO>(p, wv, lwb + so); dma_grp<2, SO + 8192>(p, wv + 2, lwb + so); dma_grp<2, SO + 16384>(p, wv + 4, lwb + so); }
      else dma_grp<NW, SO>(p, wv, lwb + so);
    }
    template <int SO> DEVI void issue_a(const char* p, unsigned so = 0) const {
      if (XB) { if (xl) dma_grp<1, SO>(p, &xv, lx + so); }
      dma_grp<NA, SO>(p, av, lab + so);
    }
    template <int S0, bool W, bool A> DEVI void pre() const {
      if constexpr (S0 < H) {
        if (W) issue_w<((S0 + ROT) % NS) * SB>(w + (kord(0) + S0) * WSTEP);
        if (A) issue_a<((S0 + ROT) % NS) * SB>(ab + (kord(0) + S0) * ASTEP);
        pre<S0 + 1, W, A>();
      }
    }
    template <int T> DEVI void pre_w1() const { issue_w<((T + ROT) % NS) * SB>(w + (kord(0) + T) * WSTEP); sched0(); }
    DEVI void pre_w() const { pre<0, true, false>(); sched0(); }
    template <int T> DEVI void peel() const {
      if constexpr (T < H - 1) {
        wait_vm<(H - 1 - T) * CA + T * CNT>();
        bar();
        issue_w<((T + H + ROT) % NS) * SB>(w + (T + H) * WSTEP); issue_a<((T + H + ROT) % NS) * SB>(ab + (T + H) * ASTEP);
        peel<T + 1>();
      }
    }
    DEVI void start() {
      sched0();
      bar();
      pre<0, false, true>();
      sched0();
      if constexpr (H == 2) wait_vm<CA>();
      else { peel<0>(); islot = (2 * H - 1 + ROT) % NS; t = H - 1; }
    }
    template <class HK> DEVI void run(int tend, HK side) {
#pragma unroll 1
      for (; t < tend; ++t) {
        const char* wk = w + (t + H) * WSTEP; const char* ak = ab + (t + H) * ASTEP;
        unsigned lw_ = lwb + islot * SB, la_ = lab + islot * SB;
        islot = islot + 1 == NS ? 0 : islot + 1;
        asm volatile("" : "+s"(wk), "+s"(ak), "+s"(lw_), "+s"(la_));
        wait_vm<(H - 1) * CNT>();
        bar();
        dma_grp<NW, 0>(wk, wv, lw_);
        side(t);
        dma_grp<NA, 0>(ak, av, la_);
        sched0();
      }
    }
    template <int T, int X, class A, class B, class C> DEVI void step(A pre_, B side, C post) const {
      const int k = kord(T + H);
      const char* wk = w + k * WSTEP; const char* ak = ab + k * ASTEP;
      asm volatile("" : "+s"(wk), "+s"(ak));
      wait_vm<(H - 1) * CNT + X>();
      bar();
      pre_(BASE + ((T + ROT) % NS) * SB);
      issue_w<((T + H + ROT) % NS) * SB>(wk);
      sched0();
      side();
      sched0();
      issue_a<((T + H + ROT) % NS) * SB>(ak);
      post();
      sched0();
    }
    template <int I> DEVI void tail() const { if constexpr (I >= 0) { sched0(); wait_vm<I * CNT>(); bar(); tail<I - 1>(); } }
  };
  static constexpr int NWIN = (NS * SB + 65535) / 65536;
  template <class O> struct Comp {
    static constexpr int TP = O::TP, TQ = O::TQ, NM = 2 * O::NP, NPA = PKM ? O::NPA : 2, NQA = QKM ? TQ : 2;
    static constexpr Plan PL = make_plan<O, PKM, QKM>();
    unsigned prd[NWIN][NPA], qrd[NWIN][NQA];
    bf16x8 fp[TP][2], fq[TQ][2]; bf16x4 lo;
    DEVI void setwin() {
#pragma unroll
      for (int w = 1; w < NWIN; ++w) {
#pragma unroll
        for (int i = 0; i < NPA; ++i) { prd[w][i] = prd[0][i] + w * 65536; asm volatile("" : "+v"(prd[w][i])); }
#pragma unroll
        for (int i = 0; i < NQA; ++i) { qrd[w][i] = qrd[0][i] + w * 65536; asm volatile("" : "+v"(qrd[w][i])); }
      }
#pragma unroll
      for (int i = 0; i < NPA; ++i) asm volatile("" : "+v"(prd[0][i]));
#pragma unroll
      for (int i = 0; i < NQA; ++i) asm volatile("" : "+v"(qrd[0][i]));
    }
    DEVI void setup(int wp, int wq, int lane) {
      const unsigned b = BASE;
      if (PKM) {
#pragma unroll
        for (int i = 0; i < TP; ++i) prd[0][i] = b + rd_km(lane, wp * TP + i);
      } else { prd[0][0] = b + rd_kc(lane, 0) + wp * TP * 2048; prd[0][1] = b + rd_kc(lane, 1) + wp * TP * 2048; }
      if (QKM) {
#pragma unroll
        for (int j = 0; j < TQ; ++j) qrd[0][j] = b + rd_km(lane, wq * TQ + j);
      } else { qrd[0][0] = b + rd_kc(lane, 0) + wq * TQ * 2048; qrd[0][1] = b + rd_kc(lane, 1) + wq * TQ * 2048; }
    }
    template <int C, int N> DEVI static unsigned wa(const unsigned (&b)[NWIN][N], int i) { return b[C >> 16][i] + (C & 0xffff); }
    template <int SLOT, int R> DEVI void rd() {
      constexpr int q = PL.q[R], i = PL.idx[R], k = PL.k[R], h = PL.h[R];
      bf16x8& f = q ? fq[q ? i : 0][k] : fp[q ? 0 : i][k];
      if constexpr (q ? QKM : PKM) {
        unsigned a;
        if constexpr (q) a = wa<SLOT * SB + QO + k * 4096 + h * 512, NQA>(qrd, i); else a = wa<SLOT * SB + PO + O::poff(i) + k * 4096 + h * 512, NPA>(prd, O::pidx(i));
        const bf16x4 x = lds_tr4(a);
        if constexpr (h) f = __builtin_shufflevector(lo, x, 0, 1, 2, 3, 4, 5, 6, 7); else lo = x;
      } else if constexpr (q) f = lds_b128(wa<SLOT * SB + QO + O::qoff(i), 2>(qrd, k));
      else f = lds_b128(wa<SLOT * SB + PO + i * 2048, 2>(prd, k));
    }
    template <int SLOT, int M, int R> DEVI void rds() { if constexpr (R < PL.n) { if constexpr (PL.g[R] == M) rd<SLOT, R>(); if constexpr (PL.g[R] <= M) rds<SLOT, M, R + 1>(); } }
    template <int M, bool IP = false> DEVI void mm(f32x4 (&acc)[TP][TQ]) {
      constexpr int i = O::pi[M >> 1], j = O::pj[M >> 1], k = ((M >> 1) ^ M) & 1;
      if constexpr (IP) { if constexpr (O::swp(i)) mfma_ip(acc[i][j], fq[j][k], fp[i][k]); else mfma_ip(acc[i][j], fp[i][k], fq[j][k]); }
      else if constexpr (O::swp(i)) acc[i][j] = __builtin_amdgcn_mfma_f32_16x16x32_bf16(fq[j][k], fp[i][k], acc[i][j], 0, 0, 0);
      else acc[i][j] = __builtin_amdgcn_mfma_f32_16x16x32_bf16(fp[i][k], fq[j][k], acc[i][j], 0, 0, 0);
    }
    template <int A, int B> DEVI void pend(f32x4 (&acc)[TP][TQ]) { if constexpr (A < B) { mm<2 * A>(acc); mm<2 * A + 1>(acc); pend<A + 1, B>(acc); } }
    template <int SLOT, bool FIRST, int M> DEVI void body(f32x4 (&acc)[TP][TQ]) {
      if constexpr (M < NM) {
        if constexpr (!FIRST || M >= 2 * O::NPEND) { mm<M, true>(acc); sched0(); }
        rds<SLOT, M, 0>();
        sched0();
        body<SLOT, FIRST, M + 1>(acc);
      }
    }
    template <int SLOT, bool FIRST> DEVI void step(f32x4 (&acc)[TP][TQ]) { if (!RLX) wait_lgkm0(); bar(); sched0(); body<SLOT, FIRST, 0>(acc); }
    template <int I, int N, int T0, class MID> DEVI void steps(f32x4 (&acc)[TP][TQ], MID mid) {
      if constexpr (I < N) { mid(I); step<(I + T0 + ROT) % NS, false>(acc); steps<I + 1, N, T0>(acc, mid); }
    }
    DEVI void prep(f32x4 (&acc)[TP][TQ]) { setwin(); pin(acc); }
    DEVI void pin(f32x4 (&acc)[TP][TQ]) {
#pragma unroll
      for (int p = 0; p < O::NP; ++p) asm volatile("" : "+v"(acc[O::pi[p]][O::pj[p]]));
    }
    template <bool CONT, class MID> DEVI void run_steps(f32x4 (&acc)[TP][TQ], MID mid CSP) {
      constexpr int T0 = CONT ? 0 : 1;
      if constexpr (T0) step<ROT % NS, true>(acc);
#pragma unroll 1
      for (int it = 0; it < (NT - T0) / NS; ++it) {
        steps<0, NS, T0>(acc, [&](int i) { mid(it - (NT - T0) / NS, i); });
      }
      steps<0, (NT - T0) % NS, T0>(acc, [](int) {});
      wait_lgkm0();
      mfma_gap();
    }
  };
};

DEVI f32x4 gelu_arg(f32x4 u) { return u * (-2.302208198f + -0.1029432397f * (u * u)); }
DEVI f32x4 gelu_tanh(f32x4 u) {
  f32x4 a = gelu_arg(u);
#pragma unroll
  for (int e = 0; e < 4; ++e) a[e] = __builtin_amdgcn_exp2f(a[e]);
  a += 1.f;
#pragma unroll
  for (int e = 0; e < 4; ++e) a[e] = __builtin_amdgcn_rcpf(a[e]);
  return u * a;
}

template <int tilesP, int tilesQ, int gp, int gq> DEVI void tile_map(unsigned bid, int& tp, int& tq) {
  const unsigned xcd = bid & 7, idx = bid >> 3;
  constexpr unsigned gP = tilesP / gp, gQ = tilesQ / gq;
  static_assert(gP * gQ == 8, "");
  tp = (xcd % gP) * gp + idx % gp;
  tq = (xcd / gP) * gq + idx / gp;
}

template <int V> struct IC { static constexpr int value = V; };
template <int A, int B, class FN> DEVI void for_c(FN f) { if constexpr (A < B) { f(IC<A>{}); for_c<A + 1, B>(f); } }
DEVI float sel(bool c, float a, float b) { return c ? a : b; }
DEVI u32x2 pk4(const f32x4& v) { return (u32x2){pk2(v[0], v[1]), pk2(v[2], v[3])}; }
DEVI u32x4 cat4(u32x2 a, u32x2 b) { return (u32x4){a.x, a.y, b.x, b.y}; }
DEVI bf16x8 cat(u32x2 a, u32x2 b) { return __builtin_bit_cast(bf16x8, cat4(a, b)); }
DEVI f32x4 mfma(const bf16x8& a, const bf16x8& b, const f32x4& c) { return __builtin_amdgcn_mfma_f32_16x16x32_bf16(a, b, c, 0, 0, 0); }
DEVI u32x4 pack_pair(const f32x4& a, const f32x4& b) {
  const unsigned ax = pk2(a[0], a[1]), ay = pk2(a[2], a[3]), bx = pk2(b[0], b[1]), by = pk2(b[2], b[3]);
  auto rx = __builtin_amdgcn_permlane16_swap(ax, bx, false, false);
  auto ry = __builtin_amdgcn_permlane16_swap(ay, by, false, false);
  const unsigned x0 = rx[0], x1 = rx[1], y0 = ry[0], y1 = ry[1];
  return (u32x4){x0, y0, x1, y1};
}
DEVI int pair_chunk(int fq) { return ((fq & 1) << 1) | (fq >> 1); }
DEVI void sel_frags(bool paired, int fr, int fq, bf16x8 (&s)[2]) {
  const unsigned one = fr & 1 ? 0x3F800000u : 0x3F80u;
#pragma unroll
  for (int i = 0; i < 2; ++i) {
    const bool on = paired ? (fq & 1) == i && (fr >> 3) == (fq >> 1) : (fr >> 2) == fq;
    const int d = paired ? (fr & 7) >> 1 : i * 2 + ((fr >> 1) & 1);
    u32x4 v;
#pragma unroll
    for (int e = 0; e < 4; ++e) v[e] = on && d == e ? one : 0u;
    s[i] = __builtin_bit_cast(bf16x8, v);
  }
}
template <int TQ> DEVI void tile_add(f32x4 (&acc)[4][TQ], const bf16x8 (&s)[2], unsigned a) {
  bf16x8 t[4];
#pragma unroll
  for (int p = 0; p < 4; ++p) t[p] = lds_b128(a + p * 1024);
#pragma unroll
  for (int p = 0; p < 4; ++p)
#pragma unroll
    for (int i = 0; i < 2; ++i) acc[(p & 1) * 2 + i][p >> 1] = mfma(s[i], t[p], acc[(p & 1) * 2 + i][p >> 1]);
}
template <int C> DEVI float dppf(float v) { return __int_as_float(__builtin_amdgcn_update_dpp(0, __float_as_int(v), C, 0xf, 0xf, false)); }
DEVI float red8(float v) { v += dppf<0xB1>(v); v += dppf<0x4E>(v); v += dppf<0x141>(v); return v; }
DEVI float red4(float v) { v += dppf<0xB1>(v); v += dppf<0x4E>(v); return v; }

constexpr unsigned SENT = 0xBF800000u;
constexpr int SPIN_MAX = 1 << 21;

constexpr unsigned MB = 1u << 20;
constexpr unsigned WS_O = 0, WS_H = 8 * MB, WS_U = 16 * MB, WS_X2 = 32 * MB, WS_XP = 40 * MB  ;
constexpr unsigned WS_ST = 120 * MB, ST_CS = 2 * S * 64, WS_XCH = 122 * MB  , WS_EPOCH = 123 * MB, WS_ABORT = 123 * MB + 256  ;
DEVI unsigned tag2(unsigned t) { return t * 0x9E3779B1u ^ 0x5BD1E995u; }
constexpr int MEDGE = 256 * 64;
constexpr unsigned WS_MAIL = 124 * MB;

#define NL 2
#define QBR 3
#define FLS 3
#define NPL 6
#define SPON 1
#define PC0 0
#define STG 1
#define WSP 1
#define SWW 2
#define GSH 1
#define GSP 1
#define SSC 1
#define RB 1
#define SI 2
#define LPR 1
#define CPR 0
#define SW0 8
#define SW1 6
#define XD 3
#define XS 4
#define JA 22
#define PS 3
#define TC1 12
#define NW1 9
#define EW 4
#define ET 8
#define SPW 1
#define PA_1 3
#define PA_2 3
constexpr int L_X = 0, L_TAB = 16384, L_CS = L_TAB + 1152, L_TAG = L_CS + 768, L_BIAS = L_TAG + 128, L_SCR = L_BIAS + 512  , L_QKV = 20480;
static_assert(L_SCR + 1536 <= L_QKV, "");
typedef Gemm<true, false, 24576, 16384, 2048, 3, D / 64, true, 32768, 0, 3 * D, 2 * D, 128> GQ;
typedef Gemm<true, false, 16384, 8192, 0, 5, D / 64, true, 40960, 2, D, 128, 16384, 1, true> GW;
typedef Gemm<true, true, 16384, 16384, 0, 4, S / 64, true, 32768, 1, F, 128, 32768, 0, true> G1;
typedef Gemm<false, true, 16384, 16384, 0, 3, JA, false, 65536, (3 - JA % 3) % 3, S, 1024, 32768> G2A;
typedef Gemm<false, true, 16384, 8192, 0, 4, F / 64 - 2 * JA, false, 65536, 0, S, 128, 32768> G2;

DEVI unsigned get_tag() { return __builtin_amdgcn_readfirstlane(*(volatile LAS unsigned*)(uintptr_t)L_TAG) + 1u; }
DEVI bool spin_fail(int spin, char* ws) {
  if ((spin & 255) != 255) return false;
  gu32* ab = (gu32*)(ws + WS_ABORT);
  const unsigned tag = get_tag();
  if (spin >= SPIN_MAX) { __hip_atomic_store(ab, tag, RLX_AGENT); return true; }
  return __builtin_amdgcn_readfirstlane(__hip_atomic_load(ab, RLX_AGENT)) == tag;
}
DEVI void sync_post(char* ws, int edge, int post, int wid) {
  unsigned o = post >= 0 && wid == 0 ? post * 4u : MB, one = 1u;
  const rsrc_t r = mkrsrc(ws + WS_MAIL + edge * (MEDGE * 4), MEDGE * 4);
  asm volatile("s_waitcnt vmcnt(0) lgkmcnt(0)" : "+v"(o), "+v"(one) : "s"(r) : "memory");
  sched0();
  bar();
  ST(edge + 1, 6);
  if (wid == 0) __builtin_amdgcn_raw_buffer_store_b32(one, r, o, 0, 16);
  sched0();
}
template <int DELAY = SW0> DEVI void sync_wait(char* ws, int edge, int line, int lane, unsigned all, unsigned first, int wid) {
  sched0();
  if (wid == 3) {
    gu32* pw = (gu32*)(ws + WS_MAIL) + edge * MEDGE + line * 64 + (lane & 31);
    if (DELAY) __builtin_amdgcn_s_sleep(DELAY);
    if (!STG) first = all;
    for (int spin = 0;; ++spin) {
      unsigned miss = ~(unsigned)__builtin_amdgcn_ballot_w64(__hip_atomic_load(pw, RLX_AGENT) != 0);
      if (spin_fail(spin, ws)) miss = 0;
      if (first && !(miss & first)) { bar(); first = 0; }
      if (!(miss & all)) break;
      __builtin_amdgcn_s_sleep(SW1);
    }
  } else bar();
  ST(edge + 1, 4);
  ST(edge + 2, 0);
}
DEVI unsigned halo_off(int s0, int lw, int lane) {
  const int c = lw * 32 + (lane & 31), row = c >> 3, ch = (c & 7) ^ ((row >> 1) & 7), tok = s0 - 16 + row;
  return (unsigned)((tok < 0 ? 0 : tok) * D * 2 + ch * 16);
}
DEVI int qkv_rot(int bid) { return (bid & 3) * 4 + (bid & 4) * 2; }
DEVI unsigned x_off(int tp, int tq, int w, int fr, int fq) { return (unsigned)(((tq * 64 + (w >> 1) * 32 + fr) * D + tp * 128 + (w & 1) * 64 + pair_chunk(fq) * 8) * 2); }

DEVI void pfb_q(const u16* w, int bid, int j, int n) {
  const int r = slow_res(w);
  if ((r >> 2) == (bid & 1)) {
    const unsigned o = (unsigned)(((bid & 2) * 4 + r) * 128 + (j >> 1) * (3 * D * 2) + (j & 1) * 64 + ((n + qkv_rot(bid)) & 15) * GQ::WSTEP);
    spf1<0>(w, o); spf1<2048>(w, o); spf1<4096>(w, o);
  }
}
constexpr int PFR = NPL * 128 * 1024, PF_END = TBA * 256 + NW1 * PFR / (F * 2) / 8 * 8;
static_assert(TBA * 256 * F * 2 + NW1 * PFR <= S * F * 2 && 3 * PFR >= D * D * 2, "");
struct Pfc {
  const char* p; const char* q; int pc;
  DEVI Pfc(const u16* wo, const u16* w1, unsigned g) : p((const char*)wo + g * (NPL * 128)), q((const char*)w1 + TBA * 256 * F * 2 + g * (NPL * 128)), pc(PC0) {}
  DEVI void go() {
    unsigned t, u;
    asm volatile("s_getreg_b32 %2, hwreg(HW_REG_IB_STS, 8, 4)\n\ts_cmp_lt_u32 %1, %5\n\ts_cselect_b32 %3, 0, 1\n\ts_or_b32 %2, %2, %3\n\ts_cbranch_scc1 1f\n\t"
                 "s_load_dwordx2 s[100:101], %0, 60\n\ts_load_dwordx2 s[100:101], %0, 188\n\ts_load_dwordx2 s[100:101], %0, 316\n\t"
                 "s_load_dwordx2 s[100:101], %0, 444\n\ts_load_dwordx2 s[100:101], %0, 572\n\ts_load_dwordx2 s[100:101], %0, 700\n\t"
                 ".if %8 == 7\n\ts_load_dwordx2 s[100:101], %0, 828\n\t.endif\n\t"
                 "s_mov_b32 %2, %7\n\ts_cmp_lt_u32 %1, 2\n\ts_cselect_b32 %2, %6, %2\n\ts_mov_b64 s[40:41], %0\n\ts_add_u32 s40, s40, %2\n\ts_addc_u32 s41, s41, 0\n\t"
                 "s_cmp_eq_u32 %1, 2\n\ts_cselect_b64 %0, %4, s[40:41]\n\ts_add_u32 %1, %1, 1\n1:"
                 : "+s"(p), "+s"(pc), "=&s"(t), "=&s"(u) : "s"(q), "i"(3 + NW1), "i"((D * D * 2 - PFR) / 2), "i"(PFR), "i"(NPL) : "s40", "s41", "s100", "s101", "scc");
  }
};
DEVI void spare_touch(const u16* w1, const u16* w2, int bid, int wid, int lane) {
  if (!SPON) return;
  const int j = ((bid >> 3) - 28) * 8 + wid, fr = lane & 15, fq = lane >> 4;
  const unsigned scr = L_QKV + wid * 256;
#pragma unroll 1
  for (int r = PF_END + (j * 2 + ((bid >> 2) & 1)) * 4; r < S; r += 256) { if (fr < 14) touch4(w1 + (size_t)r * F + (bid & 3) * 896, fq * (F * 2) + fr * 128, scr); wait_vm<SPW>(); }
#pragma unroll 1
  for (int r = (j * 2 + (bid & 1)) * 4; r < JA * 64; r += 256) { touch4(w2 + (size_t)(r + (bid & 2) * JA * 32) * S + (bid & 4) * 256, fq * (S * 2) + fr * 128, scr); wait_vm<SPW>(); }
#pragma unroll 1
  for (int r = JA * 128 + ((31 - j) * 2 + (bid & 1)) * 8; r < F; r += 512) { touch4(w2 + (size_t)r * S + ((bid & 7) >> 1) * 512, (lane >> 3) * (S * 2) + (lane & 7) * 128, scr); wait_vm<SPW>(); }
}

template <int TH> DEVI void qkv_attn(char* ws, int l, int h, int lane, int hp, int sb, int cw, int bid, int tp, int tq, const u16* wqkv) {
  asm volatile("; half %1" : "+v"(lane) : "i"(TH));
  const int fr = lane & 15, fq = lane >> 4;
  GQ::Comp<OQ<TH>> g;
#pragma unroll
  for (int c = 0; c < 2; ++c) { g.prd[0][c] = 32768 + rd_km(lane, h * 2 + c); g.qrd[0][c] = 32768 + rd_kc(lane, c); }
  f32x4 acc[6][5];
#pragma unroll
  for (int i = 0; i < 6; ++i)
#pragma unroll
    for (int j = 0; j < 5; ++j) acc[i][j] = (f32x4){0.f, 0.f, 0.f, 0.f};
  const int par = fr & 1, da = fr - 4 * fq - par;
  const bool ca = da >= 0, cb = da >= 2;
  const unsigned tm = L_TAB + (16 + 64 * TH) * 4, tc = L_CS + h * 128;
  unsigned am = tm + fr * 4 - 320, a4 = tm + fq * 16 - 320, aa = tm + 576 + (4 * fq + par - (ca ? 0 : 16)) * 4, ab = tm + 576 + (4 * fq + par + 2 - (cb ? 0 : 16)) * 4;
  unsigned c4 = tc + fq * 16, c1 = tc + 512 + fr * 4, xb = L_QKV + h * 3072 + lane * 16, hm = par ? 0xffff0000u : 0xffffu, m0 = ca ? 0u : hm, m1 = cb ? 0u : hm;
  unsigned oo = ph((unsigned)((((sb * 2 + TH) * 16 + hp) << 13) + fr * 128 + (((h * 4 + pair_chunk(fq)) ^ (fr >> 1)) << 4)));
  asm volatile("" : "+v"(am), "+v"(a4), "+v"(aa), "+v"(ab), "+v"(c4), "+v"(c1), "+v"(xb), "+v"(hm), "+v"(m0), "+v"(m1), "+v"(oo));
  g.prep(acc);
  if (l) {
    const int sbc = (tq >> 1) + fq;
    sync_post(ws, 3, !TH && (lane < 16 || (lane < 32 && (tq & 1) && sbc < 16)) ? (sbc * 16 + fr) * 64 + (tq - 2 * sbc + 1) * 8 + tp : -1, TH * 2 + h);
  }
  if (l) sync_wait(ws, 3, sb * 16 + hp, lane, sb ? 0xffffffu : 0xffff00u, (sb ? 0x10101u : 0x10100u) << ((qkv_rot(bid) & 15) >> 1), TH * 2 + h);
  else if (!TH || !h) wait_vm<0>();
  else if ((slow_res(wqkv) >> 2) == (bid & 1)) {
    if (ET) __builtin_amdgcn_s_sleep(ET);
    if ((lane & 3) < 3 && lane < 40) touch4(wqkv, (unsigned)(((qkv_rot(bid) * 64 + 128 + (bid >> 3) * 10 + (lane >> 2)) & 1023) * (3 * D * 2) + (lane & 3) * (D * 2) + ((bid & 2) * 4 + slow_res(wqkv)) * 128), L_SCR + 768);
  }
  g.template run_steps<false>(acc, [&](int it, int i) { if (i == 0 && cw >= 0 && it == -2) code_warm(cw, lane, L_SCR + (TH * 2 + h) * 256); } CSA);
  const rsrc_t ro = mkrsrc(ws + WS_O, S * D * 4);
  MTD;
  MT(0);
  if (l == 0) bar();
  ST(1 + l * 4, 2);
  float mt[5], cv[2], rq[4], ra[4], rb[4]; f32x4 m4[5], cq[2], ck[2];
#pragma unroll
  for (int c = 0; c < 2; ++c) {
    ck[c] = *(const LAS f32x4*)(uintptr_t)(c4 + 256 + c * 64); cv[c] = *(const LAS float*)(uintptr_t)(c1 + c * 64);
    cq[c] = *(const LAS f32x4*)(uintptr_t)(c4 + c * 64);
  }
#pragma unroll
  for (int j = 4; j >= 0; --j) {
    const int o = 320 + (j == 4 ? -64 - 256 * TH : j * 64);
    if (j < 4 || !TH) mt[j] = *(const LAS float*)(uintptr_t)(am + o);
    if (j < 4 || TH) m4[j] = *(const LAS f32x4*)(uintptr_t)(a4 + o);
  }
#pragma unroll
  for (int j = 0; j < 4; ++j) { rq[j] = *(const LAS float*)(uintptr_t)(am + 896 + j * 64); ra[j] = *(const LAS float*)(uintptr_t)(aa + j * 64); rb[j] = *(const LAS float*)(uintptr_t)(ab + j * 64); }
  u32x2 kp[2][5], qp[2][4], vp[2][5];
  auto fk = [&](int j) { kp[0][(j + 1) % 5] = pk4(acc[1][j]); kp[1][(j + 1) % 5] = pk4(acc[4][j] + ck[1] * mt[j]); };
  auto fv = [&](int j) { vp[0][(j + 1) % 5] = pk4(acc[2][j]); vp[1][(j + 1) % 5] = pk4(acc[5][j] + m4[j] * cv[1]); };
  auto fqq = [&](int j) { qp[0][j] = pk4(acc[0][j]); qp[1][j] = pk4(acc[3][j] + cq[1] * mt[j]); };
  g.template pend<10, 13>(acc);
#pragma unroll
  for (int i = 0; i < 6; ++i) { __builtin_amdgcn_sched_group_barrier(0x100, 3, 0); __builtin_amdgcn_sched_group_barrier(0x8, 1, 0); }
  sched0();
  MT(2);
  for_c<0, 20>([&](auto mc) {
    constexpr int m = mc.value, n0 = m * 52 / 20, n1 = (m + 1) * 52 / 20;
    g.template mm<(18 - (m & ~1)) + (m & 1)>(acc);
    sched0();
#pragma unroll
    for (int n = n0; n < n1; ++n) {
      const int t = 12 - (n >> 2), e = n & 3, j = t == 12 ? 4 : t / 3, i = t == 12 ? 1 + TH : t % 3;
      float x = acc[i][j][e];
      asm volatile("v_fmac_f32 %0, %1, %2" : "+v"(x) : "v"(i == 2 ? m4[j][e] : i ? ck[0][e] : cq[0][e]), "v"(i == 2 ? cv[0] : mt[j]));
      acc[i][j][e] = x;
    }
    sched0();
  });
  volatile LAS unsigned* xf = (volatile LAS unsigned*)(uintptr_t)(L_TAG + 16 + (h * 2 + TH) * 4);
  if (TH) { fv(4); *(volatile LAS bf16x8*)(uintptr_t)(xb + 2048) = cat(vp[0][0], vp[1][0]); *xf = l + 1; fk(3); }
  else { fk(3); fv(3); *(volatile LAS bf16x8*)(uintptr_t)xb = cat(kp[0][4], kp[1][4]); *(volatile LAS bf16x8*)(uintptr_t)(xb + 1024) = cat(vp[0][4], vp[1][4]); *xf = l + 1; }
  sched0();
  fqq(3); fk(2); fqq(2); fk(1);
  sched0();
  const f32x4 z = {0.f, 0.f, 0.f, 0.f};
  f32x4 sc[4][2]; float a[4], b[4], m[4];
  auto scores = [&](int j) {
#pragma unroll
    for (int u = 1; u >= 0; --u) sc[j][u] = mfma(cat(kp[0][j + u], kp[1][j + u]), cat(qp[0][j], qp[1][j]), z);
  };
  auto pick = [&](int j) {
    const f32x4 s0 = sc[j][0], s1 = sc[j][1];
    a[j] *= sel(par, sel(ca, s1[1], s0[1]), sel(ca, s1[0], s0[0]));
    b[j] *= sel(par, sel(cb, s1[3], s0[3]), sel(cb, s1[2], s0[2]));
    if (!TH && j == 0) { a[j] = sel(ca || sb, a[j], -1e9f * 1.4426950408889634f); b[j] = sel(cb || sb, b[j], -1e9f * 1.4426950408889634f); }
    m[j] = __builtin_fmaxf(a[j], b[j]);
  };
  auto expo = [&](int j) { a[j] = __builtin_amdgcn_exp2f(a[j] - m[j]); b[j] = __builtin_amdgcn_exp2f(b[j] - m[j]); m[j] = a[j] + b[j]; };
  auto pv = [&](int j) {
    const unsigned pa = pk2(a[j] * ra[j], a[j] * ra[j]), pb = pk2(b[j] * rb[j], b[j] * rb[j]);
    const bf16x8 pf = __builtin_bit_cast(bf16x8, (u32x4){pa & m0, pb & m1, pa & (m0 ^ hm), pb & (m1 ^ hm)});
#pragma unroll
    for (int c = 0; c < 2; ++c) sc[j][c] = mfma(cat(vp[c][j], vp[c][j + 1]), pf, z);
  };
  auto outp = [&](int j) { st16(ro, oo + j * 4096, pack_pair(sc[j][0] * m[j], sc[j][1] * m[j])); };
  unsigned f; u32x4 xv, xk;
  auto xrd = [&]() {
    f = xf[1 - 2 * TH]; xv = *(volatile LAS u32x4*)(uintptr_t)(xb + (TH ? 1024 : 2048));
    if (TH) xk = *(volatile LAS u32x4*)(uintptr_t)xb;
  };
  auto xget = [&]() {
    sched0();
    while (__builtin_expect(__builtin_amdgcn_readfirstlane(f) != l + 1, 0)) xrd();
    vp[0][0] = (u32x2){xv[0], xv[1]}; vp[1][0] = (u32x2){xv[2], xv[3]};
    if (TH) { kp[0][0] = (u32x2){xk[0], xk[1]}; kp[1][0] = (u32x2){xk[2], xk[3]}; }
  };
  MT(3);
  {
    fqq(1); fk(0); fqq(0);
    if (!TH) fk(4);
    xrd();
    sched0();
    if (TH) fv(3);
    fv(2); fv(1); fv(0);
#pragma unroll
    for (int c = 0; c < 2; ++c)
#pragma unroll
      for (int j = 0; j < 4; ++j) asm volatile("" : "+v"(qp[c][j]), "+v"(kp[c][j + 1]), "+v"(vp[c][j + 1]));
    if (!TH) asm volatile("" : "+v"(kp[0][0]), "+v"(kp[1][0]));
    MT(4);
    xget();
    scores(3); scores(2); scores(1); scores(0);
#pragma unroll
    for (int j = 0; j < 4; ++j) { const float r = rq[j] * (0.17677669529663687f * 1.4426950408889634f); a[j] = r * ra[j]; b[j] = r * rb[j]; }
#pragma unroll
    for (int i = 0; i < 8; ++i) { __builtin_amdgcn_sched_group_barrier(0x8, 1, 0); __builtin_amdgcn_sched_group_barrier(0x2, 2, 0); }
    sched0();
    pick(3); pick(2); pick(1); pick(0);
    float u, w;
    swp32(m[0], m[2]); swp32(m[1], m[3]); u = m[0]; w = m[1];
    asm volatile("v_max_f32 %0, %0, %2\n\tv_max_f32 %1, %1, %3\n\ts_nop 1" : "+v"(u), "+v"(w) : "v"(m[2]), "v"(m[3]));
    swp16(u, w); u = vmax(u, w);
    xch16(u, u, w); xch32(u, m[0], m[2]); xch32(w, m[1], m[3]);
    MT(5);
    expo(3); expo(2); expo(1); expo(0);
    pv(3); pv(2); pv(1); pv(0);
    swp32(m[0], m[2]); swp32(m[1], m[3]); u = m[0] + m[2]; w = m[1] + m[3];
    swp16(u, w); u = __builtin_amdgcn_rcpf(u + w);
    xch16(u, u, w); xch32(u, m[0], m[2]); xch32(w, m[1], m[3]);
    MT(6);
    outp(3); outp(2); outp(1); outp(0);
  }
  MT(7);
  MTOUT(10 + TH, TH * 2);
}

extern "C" __global__ __launch_bounds__(512, 2) void mega_kernel(const u16* x0, const u16* wqkv, const u16* wo, const u16* w1, const u16* w2, const u16* b1, const u16* b2, u16* out, char* ws) {
  const int wid0 = __builtin_amdgcn_readfirstlane(threadIdx.x >> 6);
  ST(0, 7);
  STL(0, 6);
  asm volatile("s_getpc_b64 s[40:41]\n\ts_andn2_b32 s40, s40, 63\n\ts_load_dwordx2 s[100:101], s[40:41], %0 offset:124\n\ts_load_dwordx2 s[100:101], s[40:41], %0 offset:252"
               :: "s"(__builtin_amdgcn_readfirstlane(threadIdx.x) << 2) : "s40", "s41", "s100", "s101", "scc");
  sched0();
#define PHASE_VARS \
      int tid = threadIdx.x, bid = blockIdx.x, wid = wid0; \
      asm volatile("" : "+v"(tid)); asm volatile("" : "+s"(bid), "+s"(wid), "+s"(ws)); \
      const int lw = wid - 4, lt = tid - 256, lane = tid & 63, fr = lane & 15, fq = lane >> 4, wp = wid & 1, wq = wid >> 1; \
      const unsigned j = (bid >> 3) * 4 + lw; \
      int hp, sb; tile_map<16, 16, 4, 8>(bid, hp, sb);            \
      int tp, tq; tile_map<8, 32, 4, 8>(bid, tp, tq);             \
      int fp, fq1; tile_map<28, 8, 7, 4>(bid, fp, fq1);           \
      const int cw = bid < 16 ? wid + 4 * (bid >> 3) : -1; \
      (void)cw; (void)j; (void)lt; (void)wp; (void)wq; (void)lw; (void)fr; (void)fq; (void)hp; (void)sb; (void)tp; (void)tq; (void)fp; (void)fq1;
#define LOADER_VARS PHASE_VARS asm volatile("" : "+s"(wqkv), "+s"(wo), "+s"(w1), "+s"(w2));

  if (__builtin_expect(wid0 >= 4, 1)) {
    if (LPR) __builtin_amdgcn_s_setprio(3);
    const u16* xin = x0;
    Pfc pf(wo, w1, blockIdx.x * 4 + wid0 - 4);
#pragma unroll 1
    for (int l = 0; l < NL; ++l) {
      {
        LOADER_VARS
        const int s0 = sb * 128;
        GQ::Loader L(wqkv + hp * 64, xin, lw, lane, qkv_rot(bid));
        if (l == 0) L.pre_w1<0>();
        L.seta((unsigned)(s0 * D * 2), halo_off(s0, lw, lane), lw, lane);
        if (l == 0) { L.issue_a<0>(L.ab + L.kord(0) * 128); if (!SI) L.pre<1, true, true>(); }
        sched0();
        const rsrc_t rs = mkrsrc(ws + WS_ST, MB);
        f32x4 c = {0.f, 0.f, 0.f, 0.f}; f32x2 s1 = {0.f, 0.f}, s2 = {0.f, 0.f}, o = {0.f, 0.f}; float pv = 0.f;
        bf16x8 ones = {0x3F80, 0x3F80, 0x3F80, 0x3F80, 0x3F80, 0x3F80, 0x3F80, 0x3F80};
        const int ct = lw & 1 ? 8 + (sb & 3) : sb & 7; const bool cs = l == 0 && lw >= 2;
        int tpf = (slow_res(wqkv) >> 2) != (bid & 1) ? 0 : lw < 2 ? 10 : 9;
        unsigned ra = GQ::AOFF + ((hp & 3) * 32 + lw * 8) * 128 + lane * 16, rb = rd_km(lane, ct);
        unsigned so = lane & 7 ? MB : ph((unsigned)(((l * S + s0 + (hp & 3) * 32 + lw * 8 + (lane >> 3)) * 4 + (hp >> 2)) * 16));
        unsigned co = fr >= 2 || !cs ? MB : ph((unsigned)(ST_CS + ((hp * 96 + ct * 8 + fq * 2 + fr) * 2 + (sb >> 3)) * 16));
        asm volatile("" : "+v"(ra), "+v"(rb), "+v"(so), "+v"(co), "+v"(c), "+v"(s1), "+v"(s2), "+v"(ones), "+v"(o));
        asm volatile("" : "+s"(tpf));
        if (SI && l == 0) L.pre_w1<1>();
        sched0();
        unsigned to[3], xo = x_off(tp, tq, lw, fr, fq), q2 = ((bid & 2) * 4 + slow_res(wqkv)) * 128 + ((bid >> 3) * 2 + (lw & 1)) * (3 * D * 2);
#pragma unroll
        for (int i = 0; i < 3; ++i) {
          const int r = s0 - 16 + (i == 2 && lw ? 64 : i * 64) + (lt >> 2);
          to[i] = ph((unsigned)(((l * S + (r < 0 ? 0 : r)) * 4 + (lt & 3)) * 16));
        }
        unsigned ta = (lt & 3) < (lw ? 2 : 3) ? L_TAB + ((lt & 3) * 64 + (lt >> 2)) * 4 : L_SCR, ts = ph((unsigned)(ST_CS + (hp * 192 + (lw ? lt - 64 : lt)) * 16));
        asm volatile("" : "+v"(to[0]), "+v"(to[1]), "+v"(to[2]), "+v"(xo), "+v"(ta), "+v"(ts));
        asm volatile("" : "+s"(q2));
        if (SI == 1 && l == 0) L.pre<1, false, true>();
        sched0();
        const GW::Loader W(wo + tp * 128, ws, lw, lane);
        if (SI == 2 && l == 0) L.pre<1, false, true>();
        sched0();
        auto pfq = [&](int n) { const unsigned o = q2 + L.kord(n) * GQ::WSTEP; spf2<0>(wqkv, o); spf2<2048>(wqkv, o); spf2<4096>(wqkv, o); };
        if (l) {
          if (QBR == 3) bar();
          if (!(EW & 8)) L.pre_w();
          if (tpf) { pfq(2 + (lw >> 1)); pfq(4 + (lw >> 1)); }
          if (QBR == 4) bar();
          L.start();
        }
        else STL(0, 2);
        if (LPR == 1) __builtin_amdgcn_s_setprio(0);
        unsigned tag, tg2; u32x4 gs, a[3];
        auto rowst = [&](int i) {
          const float m0 = __uint_as_float(a[i][0]), m = red4(m0) * 0.25f, q = red4(__uint_as_float(a[i][2]) + 256.f * (m0 - m) * (m0 - m));
          o = (f32x2){sel((lt & 3) == i, m, o.x), sel((lt & 3) == i, q, o.y)};
          asm volatile("" : "+v"(o));
        };
        for_c<0, 16 - GQ::H>([&](auto tc) {
          constexpr int t = tc.value;
          u32x4 v; unsigned tv;
          L.step<t, t == 5 || t == 6 ? 1 : t == 9 || t == 10 ? 4 : 0>([&](unsigned ls) {
            if (t < 4) v = *(const LAS u32x4*)(uintptr_t)(ls + ra);
            if (t == 3) tv = *(volatile LAS unsigned*)(uintptr_t)L_TAG;
          }, [&]() {
            if (t < 8 && cs) {
              constexpr int ls = 32768 + t % 3 * GQ::SB;
              bf16x4 x[4];
#pragma unroll
              for (int e = 0; e < 4; ++e) x[e] = lds_tr4(rb + (ls & ~0x7fff) + (ls & 0x7fff) + (e >> 1) * 4096 + (e & 1) * 512);
#pragma unroll
              for (int ks = 0; ks < 2; ++ks) c = mfma(__builtin_shufflevector(x[2 * ks], x[2 * ks + 1], 0, 1, 2, 3, 4, 5, 6, 7), ones, c);
            } else if (t >= 4) {
              if (t >= 10 || t >= tpf) { if (t < TC1) pf.go(); }
              else if (lw < 2) pfq(t + 4);
              else if (t == 8) { pfq(14); pfq(15); }
            }
            if (t < 4) {
              if (t == 0) { pv = bflo(v[0]); asm volatile("" : "+v"(pv)); }
#pragma unroll
              for (int e = 0; e < 4; ++e) {
                if (SSC) {
                  float x = bflo(v[e]), y = bfhi(v[e]), a0 = s1.x, a1 = s1.y, b0 = s2.x, b1 = s2.y;
                  asm volatile("v_sub_f32 %0, %0, %6\n\tv_sub_f32 %1, %1, %6\n\tv_add_f32 %2, %2, %0\n\tv_add_f32 %3, %3, %1\n\tv_fmac_f32 %4, %0, %0\n\tv_fmac_f32 %5, %1, %1"
                               : "+v"(x), "+v"(y), "+v"(a0), "+v"(a1), "+v"(b0), "+v"(b1) : "v"(pv));
                  s1 = (f32x2){a0, a1}; s2 = (f32x2){b0, b1};
                } else { const f32x2 d = (f32x2){bflo(v[e]), bfhi(v[e])} - pv; s1 += d; s2 += d * d; }
              }
              asm volatile("" : "+v"(s1), "+v"(s2));
            }
            if (t == 3) { tag = __builtin_amdgcn_readfirstlane(tv) + 1u; tg2 = tag2(tag); if (tpf == 10) pfq(7); if (l && tpf == 9) pfq(6); }
            if (t == 4) {
              const float d1 = (s1.x + s1.y) * (1.f / 32.f), ml = pv + d1, m = red8(ml) * 0.125f;
              const float q = red8(vmax((s2.x + s2.y) - 32.f * d1 * d1, 0.f) + 32.f * (ml - m) * (ml - m));
              gs = (u32x4){__float_as_uint(m), tag, __float_as_uint(q), tg2};
              asm volatile("" : "+v"(gs));
            }
            if (t == 8) { gs = (u32x4){__float_as_uint(sel(fr, c[2], c[0])), tag, __float_as_uint(sel(fr, c[3], c[1])), tg2}; asm volatile("" : "+v"(gs)); }
            if (t == (RB ? 12 : 11)) {
#pragma unroll
              for (int i = 0; i < 3; ++i) ld_ok(a[i]);
              for (int spin = 0;; ++spin) {
                unsigned bad = 0;
#pragma unroll
                for (int i = 0; i < 3; ++i) bad |= (a[i][1] ^ tag) | (a[i][3] ^ tg2);
                if (__builtin_expect(__all(bad == 0), 1) || spin_fail(spin, ws)) break;
                __builtin_amdgcn_s_sleep(8);
#pragma unroll
                for (int i = 0; i < 3; ++i) ld_again(a[i], rs, to[i]);
              }
              if (!RB) rowst(0);
            }
            if (t == 12 && !RB) { rowst(1); rowst(2); }
            if (t == 13 && RB == 2) rowst(0);
            if (t == 13 && !RB) { LAS float* p = (LAS float*)(uintptr_t)ta; p[0] = -o.x; p[144] = __builtin_amdgcn_rsqf(o.y * (1.f / D) + EPS); }
          }, [&]() {
            if (t == 4) st16(rs, so, gs);
            if (t == 8) {
              st16(rs, co, gs);
              sched0();
#pragma unroll
              for (int i = 0; i < 3; ++i) ld_raw(a[i], rs, to[i]);
            }
          });
        });
        if (l == 0) {
#pragma unroll
          for (int p = 0; p < 4; ++p) dma16((const char*)xin + (p >> 1) * 32768 + (p & 1) * 64, xo, L_X + lw * 4096 + p * 1024);
          ld_raw(gs, rs, ts);
          wait_vm<GQ::CNT + 5>();
        } else wait_vm<GQ::CNT>();
        bar();
        W.pre_w1<0>();
        if (RB) { if (RB == 1) rowst(0); rowst(1); rowst(2); LAS float* p = (LAS float*)(uintptr_t)ta; p[0] = -o.x; p[144] = __builtin_amdgcn_rsqf(o.y * (1.f / D) + EPS); }
        wait_lgkm0();
        if (l == 0) wait_vm<9>(); else wait_vm<4>();
        bar();
        W.pre_w1<1>(); W.pre_w1<2>();
        if (l == 0) {
          wait_vm<12>();
          ld_ok(gs);
          for (int spin = 0;; ++spin) {
            if (__builtin_expect(__all(((gs[1] ^ tag) | (gs[3] ^ tg2)) == 0), 1) || spin_fail(spin, ws)) break;
            __builtin_amdgcn_s_sleep(8);
            ld_again(gs, rs, ts);
          }
          float c0 = __uint_as_float(gs[0]), c1 = __uint_as_float(gs[2]);
          c0 += dppf<0xB1>(c0); c1 += dppf<0xB1>(c1);
          *(LAS float*)(uintptr_t)(L_CS + (lw ? lt - 64 : lt) * 4) = sel(lt & 1, c1, c0);
        }
        if (l == 0 && lw < TBA) { if (fr < 14) touch4(w1 + (size_t)(((bid >> 3) * TBA + lw) * 8 + (bid & 4)) * F + (bid & 3) * 896, fq * (F * 2) + fr * 128, L_SCR + lw * 256); }
        if (l == 0) { wait_lgkm0(); bar(); }
      }
      {
        LOADER_VARS
        GW::Loader L(wo + tp * 128, ws + WS_O + (size_t)tq * 32 * 8192, lw, lane);
        unsigned po = ((bid & 1) * 8 + (slow_res(wo) ^ (j & 1))) * 128 + (j >> 1) * (D * 2);
#pragma unroll
        for (int k = GW::H; k < (NOPO ? 0 : 10); ++k) spf2(wo, po + k * GW::WSTEP);
        asm volatile("" : "+s"(po));
        sched0();
        bar();
#pragma unroll
        for (int k = 10; k < (NOPO ? 0 : 16); ++k) spf2(wo, po + k * GW::WSTEP);
        L.start();
        L.run(16 - GW::H, [&](int) { pf.go(); });
        pf.go();
        L.tail<GW::H - 1>(); bar();
      }
      {
        LOADER_VARS
        if (bid < 224) {
          G1::Loader L(w1 + fp * 128, ws + WS_H + (size_t)fq1 * 64 * 16384, lw, lane);
          L.pre_w();
          const G2A::Loader W((const char*)(w2 + (tq & 23) * 64) + (tq & 8 ? JA * G2::WSTEP : 0), ws, lw, lane);
          const int g0 = (bid & 3) * 14, ga = (g0 + ((slow_res(w1) - g0) & 7)) ^ (j & 1), gb = ga + 8 < g0 + 14 ? ga + 8 : ga;
          const unsigned j2 = j < 32 ? 112 + (j & 15) : j;
          unsigned pa = (j >> 1) * (F * 2) + ga * 128, pb = (j >> 1) * (F * 2) + gb * 128, pd = (j2 >> 1) * (F * 2) + (j & 16 ? gb : ga) * 128;
          asm volatile("" : "+s"(pa), "+s"(pb), "+s"(pd));
          auto pfb = [&](int k) { spf2(w1, pa + k * G1::WSTEP); spf2(w1, pb + k * G1::WSTEP); spf2(w1, pd + k * G1::WSTEP); };
#pragma unroll
          for (int k = G1::H; k < G1::H + PA_1; ++k) pfb(k);
          wait_lgkm0(); pf.go();
          sched0();
          bar();
          pf.go();
          L.start();
          L.run(32 - G1::H, [&](int t) {
            if (pf.pc < 3 + NW1) pf.go();
            else if (t < 32 - G1::H - PA_1) pfb(t + G1::H + PA_1);
          });
          if (EW & 4) {
            wait_vm<2 * G1::CNT>(); bar(); W.pre_w1<1>();
            wait_vm<G1::CNT + 4>(); bar();
            wait_vm<4>(); bar(); W.pre_w1<0>();
          } else L.tail<G1::H - 1>();
          bar();
        } else {
          wait_lgkm0(); pf.go();
          bar();
          if (l == 0) {
            __builtin_amdgcn_s_sleep(24);
            while (pf.pc < 3 + NW1) { wait_lgkm0(); pf.go(); }
            spare_touch(w1, w2, bid, wid, lane);
          }
        }
        pf.pc = 99;
      }
      {
        LOADER_VARS
        constexpr int NB = F / 64 - 2 * JA;
        const char* ub = ws + WS_U + (size_t)tp * 112 * 16384;
        G2A::Loader A((const char*)(w2 + (tq & 23) * 64) + (tq & 8 ? JA * G2::WSTEP : 0), ub + (tq & 8 ? JA * 32768 : 0), lw, lane);
        if (!(EW & 4) || bid >= 224) A.pre_w();
        const GQ::Loader W(wqkv + hp * 64, ws, lw, lane, qkv_rot(bid));
        unsigned pb = ((bid & 4) * 4 + (j & 1) * 8 + slow_res(w2)) * 128 + (j >> 1) * (S * 2) + JA * G2::WSTEP, pa = pb - (bid & 2 ? 0 : JA * G2::WSTEP);
        asm volatile("" : "+s"(pa), "+s"(pb));
        auto side = [&](int n) { n += G2A::H + PA_2; if (n < JA + NB) spf2(w2, (n < JA ? pa : pb) + n * G2::WSTEP); };
#pragma unroll
        for (int k = 0; k < PA_2; ++k) side(k - PA_2);
        G2::Loader L((const char*)(w2 + tq * 64) + 2 * JA * G2::WSTEP, ub + 2 * JA * 32768, lw, lane);
        gu32* xf = (gu32*)(ws + WS_MAIL) + (l * 4 + 2) * MEDGE + (tq * 8 + tp) * 64 + 28 + lw;
        const char* xp = ws + WS_XP + (unsigned)(((bid ^ 2) * 4 + lw) << 14);
        unsigned fl = L_QKV + lw * 256, xv = ph(lane * 16);
        asm volatile("" : "+s"(xf), "+s"(xp), "+s"(fl)); asm volatile("" : "+v"(xv));
        sched0();
        bar();
        A.start();
        A.run(JA - G2A::H, [&](int t) { side(t); });
        auto stage = [&](auto s) { L.issue_w<s.value * G2::SB>(L.w + s.value * G2::WSTEP); L.issue_a<s.value * G2::SB>(L.ab + s.value * 32768); sched0(); };
        wait_vm<G2A::CNT>(); bar(); stage(IC<0>{}); side(JA - 2);
        wait_vm<G2::CNT>(); bar(); stage(IC<1>{}); side(JA - 1);
        wait_vm<G2::CNT>(); bar(); stage(IC<2>{}); stage(IC<3>{}); side(JA);
        L.t = 1; L.islot = 0;
        L.run(NB - G2::H, [&](int t) {
          if (t == NB - G2::H - FLS) asm volatile("s_mov_b32 m0, %2\n\ts_nop 0\n\tglobal_load_lds_dword %0, %1 sc1" :: "v"(0u), "s"(xf), "s"(fl) : "memory");
          side(JA + t);
        });
        auto xdma = [&]() {
#pragma unroll
          for (int i = 0; i < 4; ++i) dma16(xp + i * 2048, xv, L_X + lw * 4096 + i * 1024);
          sched0();
        };
        sched0(); wait_vm<2 * G2::CNT>(); bar();
        if (__builtin_amdgcn_readfirstlane(*(volatile LAS unsigned*)(uintptr_t)fl)) {
          xdma();
          if ((EW & 8) && l == 0) { W.pre_w1<0>(); wait_vm<G2::CNT + 10>(); bar(); W.pre_w1<1>(); wait_vm<16>(); bar(); wait_vm<12>(); }
          else { wait_vm<G2::CNT + 4>(); bar(); wait_vm<4>(); bar(); wait_vm<0>(); }
        } else {
          wait_vm<G2::CNT>(); bar(); wait_vm<0>(); bar();
          for (int spin = 0;; ++spin) {
            if (__builtin_amdgcn_readfirstlane(__hip_atomic_load(xf, RLX_AGENT)) || spin_fail(spin, ws)) break;
            __builtin_amdgcn_s_sleep(2);
          }
          STL(4 + l * 4, 7);
          xdma();
          if ((EW & 8) && l == 0) { W.pre_w(); wait_vm<12>(); } else wait_vm<0>();
        }
        bar();
        xin = (const u16*)(ws + WS_X2);
        if (NL == 1) bar();
      }
    }
    return;
  }

  {
    ST(0, 0);
    PHASE_VARS
    if (cw >= 0) code_warm(cw, lane, L_SCR + wid * 256);
    pfb_q(wqkv, bid, (bid >> 3) * 4 + wid, 0); pfb_q(wqkv, bid, (bid >> 3) * 4 + wid, 1);
    if (wid < 3) {
      if (tid == 0) asm volatile("s_mov_b32 m0, %2\n\ts_nop 0\n\tglobal_load_lds_dword %0, %1 sc1" :: "v"(WS_EPOCH), "s"(ws), "s"(L_TAG) : "memory");
      const rsrc_t rz = mkrsrc(ws + WS_XCH, 3 * MB);
      if (tid < 128) st16(rz, ph((((tid >> 6) * 256 + tq * 8 + tp) * 64 + lane) * 16), (u32x4){0u, SENT, 0u, SENT});
      else {
        const int e = (tid >> 3) & 7, line = e == 0 || e == 7 ? (2 * sb + (e & 1)) * 8 + (hp & 7) : e * 256 + bid;
        st16(rz, WS_MAIL - WS_XCH + line * 256 + (tid & 7) * 16, (u32x4){0u, 0u, 0u, 0u});
      }
      if (wid == 1 && bid < 224) touch4(b1 + fp * 128, lane * 4, L_BIAS);
      if (wid == 2) touch4(b2 + tq * 64, (lane & 31) * 4, L_BIAS + 256);
      if (wid == 0) *(LAS u32x4*)(uintptr_t)(L_TAG + 16) = (u32x4){0u, 0u, 0u, 0u};
    }
    if (CPR) __builtin_amdgcn_s_setprio(CPR);
    ST(0, 4);
    ST(1, 0);
  }
#pragma unroll 1
  for (int l = 0; l < NL; ++l) {
    {
      PHASE_VARS
      if (wid & 2) qkv_attn<1>(ws, l, wp, lane, hp, sb, cw, bid, tp, tq, wqkv);
      else qkv_attn<0>(ws, l, wp, lane, hp, sb, cw, bid, tp, tq, wqkv);
      ST(1 + l * 4, 5);
    }
    {
      PHASE_VARS
      if (WSP == 0) sync_post(ws, l * 4, lane < 16 ? (sb * 16 + lane) * 64 + hp : -1, wid);
      GW::Comp<O42<false, WNE>> g; g.setup(wp, wq, lane);
      f32x4 acc[4][2];
#pragma unroll
      for (int i = 0; i < 4; ++i)
#pragma unroll
        for (int jj = 0; jj < 2; ++jj) acc[i][jj] = (f32x4){0.f, 0.f, 0.f, 0.f};
      unsigned xa = L_X + wid * 4096 + lane * 16;
      { bf16x8 s[2]; sel_frags(true, fr, fq, s); tile_add(acc, s, xa); }
      if (WSP == 1) {
#pragma unroll
        for (int i = 0; i < 4; ++i) asm volatile("" : "+v"(acc[i][0]), "+v"(acc[i][1]));
        sync_post(ws, l * 4, lane < 16 ? (sb * 16 + lane) * 64 + hp : -1, wid);
      }
      const rsrc_t rx = mkrsrc(ws + WS_XCH + l * (MB / 2), MB / 2), rh = mkrsrc(ws + WS_H, S * D * 4);
      unsigned xs = fq != fr >> 2 ? MB : ph((unsigned)(((tq * 8 + tp) * 64 + wp * 32 + wq * 16 + fr) * 16)), xl = ph((unsigned)((tq * 512 + fq * 128 + wq * 16 + fr) * 16));
      const unsigned h0 = ph((unsigned)(((tp * 32 + tq) << 14) + wp * 8192 + (wq * 32 + fr) * 128 + ((pair_chunk(fq) ^ (km_f(fr) << 1)) << 4)));
      u32x4 ho = {h0, h0 ^ 64, h0 + 4096, (h0 ^ 64) + 4096};
      bf16x8 ones = {0x3F80, 0x3F80, 0x3F80, 0x3F80, 0x3F80, 0x3F80, 0x3F80, 0x3F80};
      asm volatile("" : "+v"(xa), "+v"(xs), "+v"(xl), "+v"(ho), "+v"(ones));
      g.prep(acc);
      if (WSP == 2) sync_post(ws, l * 4, lane < 16 ? (sb * 16 + lane) * 64 + hp : -1, wid);
      sync_wait<WSP == 2 ? SW0 : SWW>(ws, l * 4, tq * 8 + tp, lane, 0xffffu, (1u << GW::H) - 1, wid);
      g.run_steps<false>(acc, [&](int it, int i) { if (i == 0 && cw >= 0 && it == -2) code_warm(cw, lane, L_SCR + wid * 256); } CSA);
      MTD;
      MT(0);
      bar();
      sched0();
      MT(1);
      g.pend<0, 4>(acc);
      ST(2 + l * 4, 2);
      u32x4 pk[2][2]; float mp[2];
      {
        f32x4 sm[2], gm[2];
#pragma unroll
        for (int i = 0; i < 2; ++i)
#pragma unroll
          for (int jj = 0; jj < 2; ++jj) {
            pk[jj][i] = cat4(pk4(acc[2 * i][jj]), pk4(acc[2 * i + 1][jj])); asm volatile("" : "+v"(pk[jj][i]));
            const bf16x8 x = __builtin_bit_cast(bf16x8, pk[jj][i]);
            gm[jj] = mfma(x, x, i ? gm[jj] : (f32x4){0.f, 0.f, 0.f, 0.f}); sm[jj] = mfma(ones, x, i ? sm[jj] : (f32x4){0.f, 0.f, 0.f, 0.f});
          }
        u32x4 st;
#pragma unroll
        for (int jj = 0; jj < 2; ++jj) {
          const f32x4 gv = gm[jj];
          st[2 * jj] = __float_as_uint(sm[jj][0]); st[2 * jj + 1] = __float_as_uint(sel(fr & 2, sel(fr & 1, gv[3], gv[2]), sel(fr & 1, gv[1], gv[0])));
          mp[jj] = sm[jj][0] * (1.f / 64.f);
        }
        st16(rx, xs, st);
      }
      sched0();
      MT(2);
#pragma unroll
      for (int p = 0; p < 4; ++p) *(LAS u32x4*)(uintptr_t)(xa + p * 1024) = pk[p >> 1][p & 1];
      asm volatile("" : "+v"(mp[0]), "+v"(mp[1]));
#pragma unroll
      for (int jj = 0; jj < 2; ++jj)
#pragma unroll
        for (int i = 0; i < 2; ++i) {
          const auto rx_ = __builtin_amdgcn_permlane16_swap(pk[jj][i][0], pk[jj][i][2], false, false), ry_ = __builtin_amdgcn_permlane16_swap(pk[jj][i][1], pk[jj][i][3], false, false);
          const unsigned x0 = rx_[0], x1 = rx_[1], y0 = ry_[0], y1 = ry_[1];
          acc[2 * i][jj] = (f32x4){bflo(x0), bfhi(x0), bflo(y0), bfhi(y0)} - mp[jj]; acc[2 * i + 1][jj] = (f32x4){bflo(x1), bfhi(x1), bflo(y1), bfhi(y1)} - mp[jj];
          asm volatile("" : "+v"(acc[2 * i][jj]), "+v"(acc[2 * i + 1][jj]));
        }
      if (XD) __builtin_amdgcn_s_sleep(XD);
      MT(3);
      f32x4 gr[4];
      for (int spin = 0;; ++spin) {
        asm volatile("" ::: "memory");
#pragma unroll
        for (int p = 0; p < 4; ++p) gr[p] = __builtin_bit_cast(f32x4, __builtin_amdgcn_raw_buffer_load_b128(rx, xl + p * 1024, 0, 16));
        const float lo = vmin3(vmin3(gr[0][1], gr[0][3], gr[1][1]), vmin3(gr[1][3], gr[2][1], gr[2][3]), vmin3(gr[3][1], gr[3][3], gr[3][3]));
        if (!__any(lo < 0.f) || spin_fail(spin, ws)) break;
        __builtin_amdgcn_s_sleep(XS);
      }
      ST(2 + l * 4, 5);
      MT(4);
      {
        const f32x4 t = (gr[0] + gr[1]) + (gr[2] + gr[3]);
        float m[2] = {t[0], t[2]}, q[2] = {t[1], t[3]}, u, w, v, y;
        swp32(m[0], m[1]); swp32(q[0], q[1]);
        xch16(m[0] + m[1], u, w); xch16(q[0] + q[1], v, y);
        const float mean = (u + w) * (1.f / D), var = __builtin_fmaxf((v + y) * (1.f / D) - mean * mean, 0.f), r1 = __builtin_amdgcn_rsqf(var + EPS);
        xch32(mean, m[0], m[1]);
        xch32(r1 * __builtin_amdgcn_rsqf(var * r1 * r1 + EPS), q[0], q[1]);
        ST(2 + l * 4, 3);
        MT(5);
#pragma unroll
        for (int jj = 0; jj < 2; ++jj) {
          const float c = (mp[jj] - m[jj]) * q[jj];
#pragma unroll
          for (int i = 0; i < 2; ++i) st16(rh, ho[jj * 2 + i], cat4(pk4(acc[2 * i][jj] * q[jj] + c), pk4(acc[2 * i + 1][jj] * q[jj] + c)));
        }
      }
      MT(6);
      MT(7);
      MTOUT(12, 0);
    }
    {
      PHASE_VARS
      const int post = lane < 28 ? (tp * 28 + lane) * 64 + tq : -1;
      if (bid >= 224) {
        sync_post(ws, l * 4 + 1, post, wid);
        if (l == 0 && bid == 255 && wid == 0) { const unsigned tag = get_tag(); if (lane == 0) __hip_atomic_store((gu32*)(ws + WS_EPOCH), tag, RLX_AGENT); }
        ST(2 + l * 4, 4);
        ST(3 + l * 4, 0);
        if (l == 0) { __builtin_amdgcn_s_sleep(24); spare_touch(w1, w2, bid, wid, lane); }
      } else {
        G1::Comp<O44<false, 32>> g; g.setup(wp, wq, lane);
        f32x4 acc[4][4];
#pragma unroll
        for (int i = 0; i < 4; ++i) { const u32x2 b = *(const LAS u32x2*)(uintptr_t)(L_BIAS + (wp * 4 + i) * 32 + fq * 8);
#pragma unroll
          for (int jj = 0; jj < 4; ++jj) acc[i][jj] = (f32x4){bflo(b.x), bfhi(b.x), bflo(b.y), bfhi(b.y)}; }
        const rsrc_t ro = mkrsrc(ws + WS_U, D * F * 4);
        const unsigned u0 = ph((unsigned)(((fq1 * 56 + fp * 2 + wp) << 14) + (wq * 64 + fr) * 128 + ((pair_chunk(fq) ^ (fr >> 1)) << 4)));
        u32x4 uo = {u0, u0 + 4096, u0 + 8192, u0 + 12288}, uo2 = uo ^ 64;
        asm volatile("" : "+v"(uo), "+v"(uo2));
        g.prep(acc);
        sync_post(ws, l * 4 + 1, post, wid);
        sync_wait(ws, l * 4 + 1, fq1 * 28 + fp, lane, ~0u, (1u << G1::H) - 1, wid);
        g.run_steps<false>(acc, [&](int it, int i) { if (i == 0 && cw >= 0 && it == -2) code_warm(cw, lane, L_SCR + wid * 256); } CSA);
        bar();
        sched0();
        ST(3 + l * 4, 2);
        if (GSH) {
          for_c<0, 2>([&](auto jc) {
            constexpr int jj = jc.value;
            f32x4 a[2];
#pragma unroll
            for (int i = 0; i < 2; ++i) { a[i] = gelu_arg(acc[i][jj]); asm volatile("" : "+v"(a[i])); }
            sched0();
            for_c<0, 4>([&](auto ec) { constexpr int e = ec.value; g.mm<jj * 8 + e>(acc); sched0(); a[0][e] = __builtin_amdgcn_exp2f(a[0][e]); a[1][e] = __builtin_amdgcn_exp2f(a[1][e]); asm volatile("" : "+v"(a[0]), "+v"(a[1])); sched0(); });
            a[0] += 1.f; a[1] += 1.f; asm volatile("" : "+v"(a[0]), "+v"(a[1]));
            sched0();
            for_c<0, 4>([&](auto ec) { constexpr int e = ec.value; g.mm<jj * 8 + 4 + e>(acc); sched0(); a[0][e] = __builtin_amdgcn_rcpf(a[0][e]); a[1][e] = __builtin_amdgcn_rcpf(a[1][e]); asm volatile("" : "+v"(a[0]), "+v"(a[1])); sched0(); });
            st16(ro, uo[jj], pack_pair(acc[0][jj] * a[0], acc[1][jj] * a[1]));
          });
        } else g.pend<0, 8>(acc);
#pragma unroll
        for (int n = GSH ? 2 : 0; n < 8; ++n) { const int i = n >> 2, jj = n & 3; st16(ro, (i ? uo2 : uo)[jj], pack_pair(gelu_tanh(acc[2 * i][jj]), gelu_tanh(acc[2 * i + 1][jj]))); }
        ST(3 + l * 4, 3);
      }
    }
    {
      PHASE_VARS
      asm volatile("" : "+s"(out));
      G2A::Comp<O44<true, ANE>> ga; ga.setup(wp, wq, lane);
#pragma unroll
      for (int jj = 0; jj < 4; ++jj) ga.qrd[0][jj] = 65536 + rd_km(lane, wq * 2 + (jj & 1)) + (((jj >> 1) ^ (tq >> 3)) & 1) * 8192;
      G2::Comp<O42<true, BNE>> g; g.setup(wp, wq, lane);
      g.setwin();
      ga.setwin();
      if (GSP == 1) sync_post(ws, l * 4 + 2, bid < 224 && lane < 32 ? (lane * 8 + fq1) * 64 + fp : -1, wid);
      f32x4 ac4[4][4], acc[4][2];
#pragma unroll
      for (int jj = 0; jj < 2; ++jj) { const float b = bf2f(*(const LAS u16*)(uintptr_t)(L_BIAS + 256 + (wq * 2 + jj) * 32 + fr * 2));
#pragma unroll
        for (int i = 0; i < 4; ++i) { ac4[i][jj] = (f32x4){b, b, b, b}; ac4[i][jj + 2] = (f32x4){0.f, 0.f, 0.f, 0.f}; } }
      unsigned xa = L_X + wid * 4096 + lane * 16, xo = (unsigned)((bid * 4 + wid) << 14) + ph(lane * 16), oo = x_off(tp, tq, wid, fr, fq), xo2 = xo + 4096, oo2 = oo + 32768;
      unsigned fo = lane ? MB : (((tq ^ 8) * 8 + tp) * 64 + 28 + wid) * 4u, one = 1u;
      bf16x8 s[2]; sel_frags(false, fr, fq, s); tile_add(ac4, s, xa);
      const rsrc_t rx = mkrsrc(ws + WS_XP, 16 * MB), rf = mkrsrc(ws + WS_MAIL + (l * 4 + 2) * (MEDGE * 4), MEDGE * 4), ro = mkrsrc(l ? (const void*)out : (const void*)(ws + WS_X2), S * D * 2);
      asm volatile("" : "+v"(xa), "+v"(xo), "+v"(oo), "+v"(xo2), "+v"(oo2), "+v"(fo), "+v"(one), "+v"(s[0]), "+v"(s[1]));
      ga.pin(ac4);
      if (GSP == 2) sync_post(ws, l * 4 + 2, bid < 224 && lane < 32 ? (lane * 8 + fq1) * 64 + fp : -1, wid);
      static_assert(JA % 2 == 0 && G2A::H == 2, "");
      sync_wait<GSP == 2 ? SW0 : SWW>(ws, l * 4 + 2, tq * 8 + tp, lane, 0xfffffffu, tq & 8 ? 1u << JA / 2 : 1u, wid);
      ga.run_steps<false>(ac4, [&](int it, int i) { if (i == 0 && cw >= 0 && it == -2) code_warm(cw, lane, L_SCR + wid * 256); } CSA);
      ga.pend<4, 8>(ac4);
      ST(4 + l * 4, 1);
#pragma unroll
      for (int i = 0; i < 2; ++i)
#pragma unroll
        for (int jj = 0; jj < 2; ++jj) st16(rx, (jj ? xo2 : xo) + i * 2048, cat4(pk4(ac4[2 * i][jj + 2]), pk4(ac4[2 * i + 1][jj + 2])));
#pragma unroll
      for (int i = 0; i < 4; ++i) { acc[i][0] = ac4[i][0]; acc[i][1] = ac4[i][1]; }
#pragma unroll
      for (int k = 0; k < 2; ++k) { g.fp[2][k] = ga.fp[2][k]; g.fp[3][k] = ga.fp[3][k]; g.fq[0][k] = ga.fq[0][k]; g.fq[1][k] = ga.fq[1][k]; }
      sched0();
      g.run_steps<true>(acc, [&](int it, int i) {
        if (i == PS % 4 && it == PS / 4 - (F / 64 - 2 * JA) / 4) {
          wait_vm<0>();
          ST(4 + l * 4, 5);
          __builtin_amdgcn_raw_buffer_store_b32(one, rf, fo, 0, 16);
        }
      } CSA);
      bar();
      sched0();
      g.pend<0, 4>(acc);
      ST(4 + l * 4, 2);
      tile_add(acc, s, xa);
      u32x4 xr[4];
#pragma unroll
      for (int i = 0; i < 2; ++i)
#pragma unroll
        for (int jj = 0; jj < 2; ++jj) { xr[jj * 2 + i] = pack_pair(acc[2 * i][jj], acc[2 * i + 1][jj]); st16(ro, (jj ? oo2 : oo) + i * 64, xr[jj * 2 + i]); }
      ST(4 + l * 4, 3);
      if (l == 0) {
#pragma unroll
        for (int p = 0; p < 4; ++p) *(LAS u32x4*)(uintptr_t)(xa + p * 1024) = xr[p];
        if (NL == 1) sync_post(ws, 3, -1, wid);
      } else ST(8, 4);
    }
  }
  if (wid0 == 0 && blockIdx.x == 45) {
    wait_lgkm0();
    unsigned d = *(volatile LAS unsigned*)(uintptr_t)(19968 + PROBE * 4) - *(volatile LAS unsigned*)(uintptr_t)(19968 + PROBE0 * 4);
    const u64 e = __builtin_amdgcn_s_memrealtime() + PROBEF * (d < 20000 ? d : 0);
    while (__builtin_amdgcn_s_memrealtime() < e) __builtin_amdgcn_s_sleep(1);
  }
  return;
  if (wid0 == 0) {
    const u64 hi = __builtin_amdgcn_s_memrealtime() & ~0xffffffffull;
    wait_lgkm0();
    for (int e = threadIdx.x; e < 128; e += 64) ((gu64*)(ws + (200u << 20)))[((e >> 3) * 1024 + blockIdx.x) * 8 + (e & 7)] = hi | *(volatile LAS unsigned*)(uintptr_t)(19968 + e * 4);
  }
}

extern "C" void kernel_launch(void* const* d_in, const int* in_sizes, int n_in,
                              void* d_out, int out_size, void* d_ws, size_t ws_size,
                              hipStream_t stream) {
  (void)hipFuncSetAttribute((const void*)mega_kernel, hipFuncAttributeMaxDynamicSharedMemorySize, 163840);
  mega_kernel<<<256, 512, 163840, stream>>>((const u16*)d_in[0], (const u16*)d_in[1], (const u16*)d_in[2], (const u16*)d_in[4], (const u16*)d_in[6], (const u16*)d_in[5], (const u16*)d_in[7], (u16*)d_out, (char*)d_ws);
}
```

```cpp
#include <hip/hip_runtime.h>
#include <hip/hip_bf16.h>
#include <stdint.h>

typedef unsigned short u16;
typedef unsigned long long u64;
typedef short bf16x8 __attribute__((ext_vector_type(8)));
typedef short bf16x4 __attribute__((ext_vector_type(4)));
typedef float f32x4 __attribute__((ext_vector_type(4)));
typedef float f32x2 __attribute__((ext_vector_type(2)));
typedef unsigned u32x4 __attribute__((ext_vector_type(4)));
typedef unsigned u32x2 __attribute__((ext_vector_type(2)));
typedef __bf16 bf16v2 __attribute__((ext_vector_type(2)));
#define LAS __attribute__((address_space(3)))
#define GAS __attribute__((address_space(1)))
#define DEVI __device__ __forceinline__
typedef __attribute__((address_space(1))) unsigned gu32;
typedef __attribute__((address_space(1))) unsigned long long gu64;

constexpr int S = 2048, D = 1024, F = 3584;
constexpr float EPS = 1e-5f;

DEVI float bf2f(u16 v) { return __uint_as_float(((unsigned)v) << 16); }
DEVI float bflo(unsigned v) { return __uint_as_float(v << 16); }
DEVI float bfhi(unsigned v) { return __uint_as_float(v & 0xffff0000u); }
DEVI unsigned pk2(float lo, float hi) { bf16v2 p; p.x = (__bf16)lo; p.y = (__bf16)hi; return __builtin_bit_cast(unsigned, p); }

DEVI const void* uni_ptr(const void* p) {
  const u64 v = (u64)p;
  return (const void*)(((u64)(unsigned)__builtin_amdgcn_readfirstlane((unsigned)(v >> 32)) << 32) | (unsigned)__builtin_amdgcn_readfirstlane((unsigned)v));
}
DEVI void dma16(const void* sbase, unsigned voff, unsigned ldsaddr) {
  ldsaddr = __builtin_amdgcn_readfirstlane(ldsaddr);
  asm volatile("s_mov_b32 m0, %2\n\ts_nop 0\n\tglobal_load_lds_dwordx4 %0, %1 sc1" :: "v"(voff), "s"(sbase), "s"(ldsaddr) : "memory");
}
template <int N, int IMM> DEVI void dma_grp(const void* sbase, const unsigned* v, unsigned lb) {
  static_assert(N == 1 || N == 2 || N == 4, "");
  if constexpr (N == 4)
    asm volatile("s_add_u32 m0, %1, %2\n\ts_nop 0\n\tglobal_load_lds_dwordx4 %3, %0 sc1\n\tglobal_load_lds_dwordx4 %4, %0 offset:1024 sc1\n\t"
                 "global_load_lds_dwordx4 %5, %0 offset:2048 sc1\n\tglobal_load_lds_dwordx4 %6, %0 offset:3072 sc1"
                 :: "s"(sbase), "s"(lb), "i"(IMM), "v"(v[0]), "v"(v[1]), "v"(v[2]), "v"(v[3]) : "memory", "scc");
  else if constexpr (N == 2)
    asm volatile("s_add_u32 m0, %1, %2\n\ts_nop 0\n\tglobal_load_lds_dwordx4 %3, %0 sc1\n\tglobal_load_lds_dwordx4 %4, %0 offset:1024 sc1"
                 :: "s"(sbase), "s"(lb), "i"(IMM), "v"(v[0]), "v"(v[1]) : "memory", "scc");
  else
    asm volatile("s_add_u32 m0, %1, %2\n\ts_nop 0\n\tglobal_load_lds_dwordx4 %3, %0 sc1" :: "s"(sbase), "s"(lb), "i"(IMM), "v"(v[0]) : "memory", "scc");
}
template <int N> DEVI void wait_vm() { asm volatile("s_waitcnt vmcnt(%0)" :: "i"(N) : "memory"); }
DEVI void wait_lgkm0() { asm volatile("" ::: "memory"); __builtin_amdgcn_s_waitcnt(0xC07F); asm volatile("" ::: "memory"); }
DEVI void mfma_ip(f32x4& c, const bf16x8& a, const bf16x8& b) { asm volatile("v_mfma_f32_16x16x32_bf16 %0, %1, %2, %0" : "+v"(c) : "v"(a), "v"(b)); }
DEVI void mfma_gap() { __builtin_amdgcn_sched_barrier(0); asm volatile("s_nop 7"); __builtin_amdgcn_sched_barrier(0); }
DEVI void bar() { asm volatile("" ::: "memory"); __builtin_amdgcn_s_barrier(); asm volatile("" ::: "memory"); }
DEVI void sched0() { __builtin_amdgcn_sched_barrier(0); }

DEVI bf16x8 lds_b128(unsigned addr) { return *(const LAS bf16x8*)(uintptr_t)addr; }
DEVI bf16x4 lds_tr4(unsigned addr) { return __builtin_amdgcn_ds_read_tr16_b64_v4i16((LAS bf16x4*)(uintptr_t)addr); }

typedef __amdgpu_buffer_rsrc_t rsrc_t;
DEVI rsrc_t mkrsrc(const void* p, unsigned bytes) { return __builtin_amdgcn_make_buffer_rsrc((void*)p, 0, bytes, 0x00020000); }
DEVI void st16(rsrc_t r, unsigned off, u32x4 v) { __builtin_amdgcn_raw_buffer_store_b128(v, r, off, 0, 16); }
DEVI void ld_raw(u32x4& v, rsrc_t r, unsigned off) { asm volatile("buffer_load_dwordx4 %0, %1, %2, 0 offen sc1 ; raw load" : "=&v"(v) : "v"(off), "s"(r) : "memory"); }
DEVI void ld_again(u32x4& v, rsrc_t r, unsigned off) { asm volatile("buffer_load_dwordx4 %0, %1, %2, 0 offen sc1\n\ts_waitcnt vmcnt(0)" : "=&v"(v) : "v"(off), "s"(r) : "memory"); }
DEVI void ld_ok(u32x4& v) { asm volatile("; raw ok %0" : "+v"(v)); }
#define RLX_AGENT __ATOMIC_RELAXED, __HIP_MEMORY_SCOPE_AGENT

template <int N> DEVI float ror16(float v) { return __int_as_float(__builtin_amdgcn_update_dpp(0, __float_as_int(v), 0x120 + N, 0xf, 0xf, false)); }
DEVI float red16(float v) { v += ror16<8>(v); v += ror16<4>(v); v += ror16<2>(v); v += ror16<1>(v); return v; }
DEVI void xch32(float v, float& a, float& b) { unsigned u = __float_as_uint(v); auto r = __builtin_amdgcn_permlane32_swap(u, u, false, false); unsigned x = r[0], y = r[1]; a = __uint_as_float(x); b = __uint_as_float(y); }
DEVI void xch16(float v, float& a, float& b) { unsigned u = __float_as_uint(v); auto r = __builtin_amdgcn_permlane16_swap(u, u, false, false); unsigned x = r[0], y = r[1]; a = __uint_as_float(x); b = __uint_as_float(y); }
DEVI void swp32(float& a, float& b) { auto r = __builtin_amdgcn_permlane32_swap(__float_as_uint(a), __float_as_uint(b), false, false); unsigned x = r[0], y = r[1]; a = __uint_as_float(x); b = __uint_as_float(y); }
DEVI void swp16(float& a, float& b) { auto r = __builtin_amdgcn_permlane16_swap(__float_as_uint(a), __float_as_uint(b), false, false); unsigned x = r[0], y = r[1]; a = __uint_as_float(x); b = __uint_as_float(y); }
DEVI float colsum4(float v) { float a, b; xch32(v, a, b); v = a + b; xch16(v, a, b); return a + b; }
DEVI float vmax(float a, float b) { float r; asm("v_max_f32 %0, %1, %2" : "=v"(r) : "v"(a), "v"(b)); return r; }
DEVI float vmin3(float a, float b, float c) { float r; asm("v_min3_f32 %0, %1, %2, %3" : "=v"(r) : "v"(a), "v"(b), "v"(c)); return r; }
DEVI float vmax3(float a, float b, float c) { float r; asm("v_max3_f32 %0, %1, %2, %3" : "=v"(r) : "v"(a), "v"(b), "v"(c)); return r; }

DEVI unsigned src_kc(int c, int ld) { int row = c >> 3, ch = (c & 7) ^ ((row >> 1) & 7); return (unsigned)(row * ld * 2 + ch * 16); }
DEVI int km_f(int k) { return ((k >> 1) & 1) | (((k >> 3) & 1) << 1); }
DEVI unsigned ph(unsigned x) { return ((x >> 7) << 8) | (x & 127u); }
DEVI unsigned rd_kc(int lane, int ks) { int r = lane & 15, g = lane >> 4; return (unsigned)(r * 128 + (((ks * 4 + g) ^ (r >> 1)) << 4)); }
DEVI unsigned rd_km(int lane, int ct) {
  int g = lane >> 4, q = (lane & 15) >> 2, p = lane & 3;
  int f = ((q >> 1) & 1) | ((g & 1) << 1);
  return (unsigned)((ct >> 2) * 8192 + (8 * g + q) * 128 + (((ct & 3) ^ f) << 5) + 8 * p);
}


constexpr unsigned CODE_BYTES = 52224;
DEVI void code_warm(int part, int lane, unsigned scratch) {
  unsigned long long pc;
  asm volatile("s_getpc_b64 s[40:41]\n\ts_add_u32 s40, s40, mega_kernel@rel32@lo+4\n\ts_addc_u32 s41, s41, mega_kernel@rel32@hi+12\n\ts_mov_b64 %0, s[40:41]" : "=s"(pc) :: "s40", "s41", "scc");
  const unsigned off = (unsigned)(part * 64 + lane) * 128u;
  scratch = __builtin_amdgcn_readfirstlane(scratch);
  if (off < CODE_BYTES) asm volatile("s_nop 3\n\ts_mov_b32 m0, %2\n\ts_nop 0\n\tglobal_load_lds_dword %0, %1" :: "v"(off), "s"(pc), "s"(scratch) : "memory");
}

DEVI unsigned src_km(int lt, int ld) { const int k = lt >> 3; return (unsigned)(k * ld * 2 + (((lt & 7) ^ (km_f(k) << 1)) << 4)); }
DEVI void touch4(const void* sbase, unsigned voff, unsigned ldsaddr) {
  ldsaddr = __builtin_amdgcn_readfirstlane(ldsaddr); sbase = uni_ptr(sbase);
  asm volatile("s_nop 3\n\ts_mov_b32 m0, %2\n\ts_nop 0\n\tglobal_load_lds_dword %0, %1" :: "v"(voff), "s"(sbase), "s"(ldsaddr) : "memory");
}

template <int IMM> DEVI void spf1(const void* base, unsigned soff) { asm volatile("s_load_dword s101, %0, %1 offset:%2" :: "s"(base), "s"(soff), "i"(IMM) : "s101"); }
template <int IMM = 0> DEVI void spf2(const void* base, unsigned soff) { asm volatile("s_load_dwordx2 s[100:101], %0, %1 offset:%2" :: "s"(base), "s"(soff), "i"(IMM + 60) : "s100", "s101"); }
template <int ST> DEVI void spf2x6(const void* base, unsigned soff) {
  asm volatile("s_load_dwordx2 s[100:101], %0, %1 offset:%2\n\ts_load_dwordx2 s[100:101], %0, %1 offset:%3\n\ts_load_dwordx2 s[100:101], %0, %1 offset:%4\n\t"
               "s_load_dwordx2 s[100:101], %0, %1 offset:%5\n\ts_load_dwordx2 s[100:101], %0, %1 offset:%6\n\ts_load_dwordx2 s[100:101], %0, %1 offset:%7"
               :: "s"(base), "s"(soff), "i"(60), "i"(60 + ST), "i"(60 + 2 * ST), "i"(60 + 3 * ST), "i"(60 + 4 * ST), "i"(60 + 5 * ST) : "s100", "s101");
}
template <int ST> DEVI void spf1x12(const void* base, unsigned soff) {
  spf1<0>(base, soff); spf1<ST>(base, soff); spf1<2 * ST>(base, soff); spf1<3 * ST>(base, soff); spf1<4 * ST>(base, soff); spf1<5 * ST>(base, soff);
  spf1<6 * ST>(base, soff); spf1<7 * ST>(base, soff); spf1<8 * ST>(base, soff); spf1<9 * ST>(base, soff); spf1<10 * ST>(base, soff); spf1<11 * ST>(base, soff);
}
DEVI int lgkm_out() { int n; asm volatile("s_getreg_b32 %0, hwreg(HW_REG_IB_STS, 8, 4)" : "=s"(n)); return n; }
DEVI int slow_res(const void* w) { return (3 - ((unsigned)(uintptr_t)w >> 7)) & 7; }

constexpr int WNE = 16, ANE = 24, BNE = 13, QNE = 46;
struct Plan { int n; signed char q[40], idx[40], k[40], h[40], g[40]; };
template <class O, bool PKM, bool QKM> constexpr Plan make_plan() {
  Plan p{};
  int nr = 0;
  for (int c = 0; c < O::NRO; ++c) nr += ((O::ro[c] >> 4) ? QKM : PKM) ? 4 : 2;
  int prev = 0;
  for (int c = 0; c < O::NRO; ++c) {
    const int q = O::ro[c] >> 4, i = O::ro[c] & 15;
    int av[2] = {0, 0}, k0 = -1;
    for (int m = 0; m < 2 * O::NP; ++m) {
      const int mm = (m + 2 * O::NPEND) % (2 * O::NP);
      if ((q ? O::pj[m >> 1] : O::pi[m >> 1]) == i && m < 2 * O::NPEND) av[((m >> 1) ^ m) & 1] = m;
      if ((q ? O::pj[mm >> 1] : O::pi[mm >> 1]) == i && k0 < 0) k0 = ((mm >> 1) ^ mm) & 1;
    }
    for (int kk = 0; kk < 2; ++kk)
      for (int h = 0; h < ((q ? QKM : PKM) ? 2 : 1); ++h) {
        const int k = k0 ^ kk;
        int g = p.n * O::NE / nr;
        if (g < av[k]) g = av[k];
        if (g < prev + (p.n && O::NE >= nr ? 1 : 0)) g = prev + (p.n && O::NE >= nr ? 1 : 0);
        p.q[p.n] = q; p.idx[p.n] = i; p.k[p.n] = k; p.h[p.n] = h; p.g[p.n] = g; prev = g; ++p.n;
      }
  }
  return p;
}
struct OBase { static constexpr int pidx(int i) { return i; } static constexpr int poff(int) { return 0; } static constexpr int qoff(int j) { return j * 2048; } static constexpr bool swp(int) { return false; } };
template <bool PF, int NE_> struct O44 : OBase {
  static constexpr int TP = 4, TQ = 4, NPA = 4, NP = 16, NPEND = 8, NRO = 8, NE = NE_;
  static constexpr signed char pi[16] = {2, 3, 3, 2, 2, 3, 3, 2, 0, 1, 1, 0, 0, 1, 1, 0}, pj[16] = {0, 0, 1, 1, 2, 2, 3, 3, 0, 0, 1, 1, 2, 2, 3, 3};
  static constexpr signed char ro[8] = {0, PF ? 1 : 16, PF ? 16 : 1, 17, 18, 19, 2, 3};
};
template <bool PF, int NE_> struct O42 : OBase {
  static constexpr int TP = 4, TQ = 2, NPA = 4, NP = 8, NPEND = 4, NRO = 6, NE = NE_;
  static constexpr signed char pi[8] = {2, 3, 3, 2, 0, 1, 1, 0}, pj[8] = {0, 0, 1, 1, 0, 0, 1, 1};
  static constexpr signed char ro[6] = {0, PF ? 1 : 16, PF ? 16 : 1, 17, 3, 2};
};
template <int TH> struct OQ {
  static constexpr int TP = 6, TQ = 5, NPA = 2, NP = 26, NPEND = 13, NRO = 11, NE = QNE;
  static constexpr signed char pi[26] = {3, 4, 5, 5, 4, 3, 3, 4, 5, 5, TH ? 5 : 3, 4, TH ? 3 : 4, 0, 1, 2, 2, 1, 0, 0, 1, 2, 2, TH ? 2 : 0, 1, TH ? 0 : 1};
  static constexpr signed char pj[26] = {0, 0, 0, 1, 1, 1, 2, 2, 2, TH ? 4 : 3, 3, 3, TH ? 3 : 4, 0, 0, 0, 1, 1, 1, 2, 2, 2, TH ? 4 : 3, 3, 3, TH ? 3 : 4};
  static constexpr signed char ro[11] = {0, 1, 16, 2, 17, 18, TH ? 20 : 19, TH ? 19 : 20, 3, 4, 5};
  static constexpr int pidx(int i) { return i / 3; }
  static constexpr int poff(int i) { return i % 3 * 8192; }
  static constexpr int qoff(int j) { return j == 4 ? 0 : (1 + 4 * TH + j) * 2048; }
  static constexpr bool swp(int i) { return i % 3 == 2; }
};
template <bool PKM, bool QKM, int PB, int QB, int XB, int NS, int NT, bool WISP, int BASE, int ROT, int WLD, int WSUB, int ASTEP, int RLX = 0, bool QF = false>
struct Gemm {
  static constexpr int SB = PB + QB + XB, CNT = (PB + QB) / 4096 + (XB ? 1 : 0);
  static constexpr int PO = QF ? QB : 0, QO = QF ? 0 : PB, WB = WISP ? PB : QB, AB = WISP ? QB : PB, WOFF = WISP ? PO : QO, AOFF = WISP ? QO + XB : PO;
  static constexpr int XL = XB / 64, H = NS - 1 - RLX, CA = AB / 4096 + (XB ? 1 : 0), WSTEP = 64 * WLD * 2;
  static_assert(H * CNT <= 63 && BASE + NS * SB <= 163840, "");
  struct Loader {
    static constexpr int NW = WB / 4096, NA = AB / 4096;
    const char* w; const char* ab; unsigned wv[NW], av[NA], xv, lwb, lab, lx; int rot, islot, t; bool xl;
    DEVI Loader(const void* w_, const void* ab_, int lw, int lane, int rot_ = 0)
        : w((const char*)w_ - 3072), ab((const char*)ab_ - 3072), lwb(BASE + WOFF + lw * (NW == 6 ? 2048 : WB / 4)), lab(BASE + AOFF + lw * (AB / 4)), lx(BASE + PB + lw * (XL * 16)),
          rot(rot_), islot((H + ROT) % NS), t(0), xl(lane < XL) {
      const unsigned w0 = (lane >> 3) * (WLD * 2), f0 = (lane & 7) ^ ((lane >> 3) & 2);
#pragma unroll
      for (int i = 0; i < NW; ++i) {
        const unsigned p = NW == 6 ? (i >> 1) * 8 + lw * 2 + (i & 1) : lw * NW + i;
        wv[i] = w0 + ((f0 ^ ((i & 1) << 2)) << 4) + (NW == 6 ? (i >> 1) * WSUB + (lw * 2 + (i & 1)) * (8 * WLD * 2) - (i & 1) * 1024 : (p >> 3) * WSUB + (p & 7) * (8 * WLD * 2) - i * 1024) + 3072;
        asm volatile("" : "+v"(wv[i]));
      }
      asm volatile("" : "+s"(w), "+s"(lwb));
      if (XB) asm volatile("" : "+s"(rot));
      else seta(0, 0, lw, lane);
    }
    DEVI void seta(unsigned a0, unsigned xv_, int lw, int lane) {
      xv = xv_ + 3072;
#pragma unroll
      for (int i = 0; i < NA; ++i) { const unsigned p = lw * NA + i; av[i] = (XB ? a0 + src_kc(p * 64 + lane, D) : p * 2048 + ph(lane * 16)) + 3072 - i * 1024; asm volatile("" : "+v"(av[i])); }
      asm volatile("" : "+s"(ab), "+s"(lab));
      if (XB) { asm volatile("" : "+v"(xv)); asm volatile("" : "+s"(lx)); }
    }
    DEVI int kord(int n) const { if constexpr (XB != 0) return (n + rot) & (NT - 1); else return n; }
    template <int SO> DEVI void issue_w(const char* p, unsigned so = 0) const {
      if constexpr (NW == 6) { dma_grp<2, SO>(p, wv, lwb + so); dma_grp<2, SO + 8192>(p, wv + 2, lwb + so); dma_grp<2, SO + 16384>(p, wv + 4, lwb + so); }
      else dma_grp<NW, SO>(p, wv, lwb + so);
    }
    template <int SO> DEVI void issue_a(const char* p, unsigned so = 0) const {
      if (XB) { if (xl) dma_grp<1, SO>(p, &xv, lx + so); }
      dma_grp<NA, SO>(p, av, lab + so);
    }
    template <int S0, bool W, bool A> DEVI void pre() const {
      if constexpr (S0 < H) {
        if (W) issue_w<((S0 + ROT) % NS) * SB>(w + (kord(0) + S0) * WSTEP);
        if (A) issue_a<((S0 + ROT) % NS) * SB>(ab + (kord(0) + S0) * ASTEP);
        pre<S0 + 1, W, A>();
      }
    }
    template <int T> DEVI void pre_w1() const { issue_w<((T + ROT) % NS) * SB>(w + (kord(0) + T) * WSTEP); sched0(); }
    DEVI void pre_w() const { pre<0, true, false>(); sched0(); }
    template <int T> DEVI void peel() const {
      if constexpr (T < H - 1) {
        wait_vm<(H - 1 - T) * CA + T * CNT>();
        bar();
        issue_w<((T + H + ROT) % NS) * SB>(w + (T + H) * WSTEP); issue_a<((T + H + ROT) % NS) * SB>(ab + (T + H) * ASTEP);
        peel<T + 1>();
      }
    }
    DEVI void start() {
      sched0();
      bar();
      pre<0, false, true>();
      sched0();
      if constexpr (H == 2) wait_vm<CA>();
      else { peel<0>(); islot = (2 * H - 1 + ROT) % NS; t = H - 1; }
    }
    template <class HK> DEVI void run(int tend, HK side) {
#pragma unroll 1
      for (; t < tend; ++t) {
        const char* wk = w + (t + H) * WSTEP; const char* ak = ab + (t + H) * ASTEP;
        unsigned lw_ = lwb + islot * SB, la_ = lab + islot * SB;
        islot = islot + 1 == NS ? 0 : islot + 1;
        asm volatile("" : "+s"(wk), "+s"(ak), "+s"(lw_), "+s"(la_));
        wait_vm<(H - 1) * CNT>();
        bar();
        dma_grp<NW, 0>(wk, wv, lw_);
        side(t);
        dma_grp<NA, 0>(ak, av, la_);
        sched0();
      }
    }
    template <int T, int X, class A, class B, class C> DEVI void step(A pre_, B side, C post) const {
      const int k = kord(T + H);
      const char* wk = w + k * WSTEP; const char* ak = ab + k * ASTEP;
      asm volatile("" : "+s"(wk), "+s"(ak));
      wait_vm<(H - 1) * CNT + X>();
      bar();
      pre_(BASE + ((T + ROT) % NS) * SB);
      issue_w<((T + H + ROT) % NS) * SB>(wk);
      sched0();
      side();
      sched0();
      issue_a<((T + H + ROT) % NS) * SB>(ak);
      post();
      sched0();
    }
    template <int I> DEVI void tail() const { if constexpr (I >= 0) { sched0(); wait_vm<I * CNT>(); bar(); tail<I - 1>(); } }
  };
  static constexpr int NWIN = (NS * SB + 65535) / 65536;
  template <class O> struct Comp {
    static constexpr int TP = O::TP, TQ = O::TQ, NM = 2 * O::NP, NPA = PKM ? O::NPA : 2, NQA = QKM ? TQ : 2;
    static constexpr Plan PL = make_plan<O, PKM, QKM>();
    unsigned prd[NWIN][NPA], qrd[NWIN][NQA];
    bf16x8 fp[TP][2], fq[TQ][2]; bf16x4 lo;
    DEVI void setwin() {
#pragma unroll
      for (int w = 1; w < NWIN; ++w) {
#pragma unroll
        for (int i = 0; i < NPA; ++i) { prd[w][i] = prd[0][i] + w * 65536; asm volatile("" : "+v"(prd[w][i])); }
#pragma unroll
        for (int i = 0; i < NQA; ++i) { qrd[w][i] = qrd[0][i] + w * 65536; asm volatile("" : "+v"(qrd[w][i])); }
      }
#pragma unroll
      for (int i = 0; i < NPA; ++i) asm volatile("" : "+v"(prd[0][i]));
#pragma unroll
      for (int i = 0; i < NQA; ++i) asm volatile("" : "+v"(qrd[0][i]));
    }
    DEVI void setup(int wp, int wq, int lane) {
      const unsigned b = BASE;
      if (PKM) {
#pragma unroll
        for (int i = 0; i < TP; ++i) prd[0][i] = b + rd_km(lane, wp * TP + i);
      } else { prd[0][0] = b + rd_kc(lane, 0) + wp * TP * 2048; prd[0][1] = b + rd_kc(lane, 1) + wp * TP * 2048; }
      if (QKM) {
#pragma unroll
        for (int j = 0; j < TQ; ++j) qrd[0][j] = b + rd_km(lane, wq * TQ + j);
      } else { qrd[0][0] = b + rd_kc(lane, 0) + wq * TQ * 2048; qrd[0][1] = b + rd_kc(lane, 1) + wq * TQ * 2048; }
    }
    template <int C, int N> DEVI static unsigned wa(const unsigned (&b)[NWIN][N], int i) { return b[C >> 16][i] + (C & 0xffff); }
    template <int SLOT, int R> DEVI void rd() {
      constexpr int q = PL.q[R], i = PL.idx[R], k = PL.k[R], h = PL.h[R];
      bf16x8& f = q ? fq[q ? i : 0][k] : fp[q ? 0 : i][k];
      if constexpr (q ? QKM : PKM) {
        unsigned a;
        if constexpr (q) a = wa<SLOT * SB + QO + k * 4096 + h * 512, NQA>(qrd, i); else a = wa<SLOT * SB + PO + O::poff(i) + k * 4096 + h * 512, NPA>(prd, O::pidx(i));
        const bf16x4 x = lds_tr4(a);
        if constexpr (h) f = __builtin_shufflevector(lo, x, 0, 1, 2, 3, 4, 5, 6, 7); else lo = x;
      } else if constexpr (q) f = lds_b128(wa<SLOT * SB + QO + O::qoff(i), 2>(qrd, k));
      else f = lds_b128(wa<SLOT * SB + PO + i * 2048, 2>(prd, k));
    }
    template <int SLOT, int M, int R> DEVI void rds() { if constexpr (R < PL.n) { if constexpr (PL.g[R] == M) rd<SLOT, R>(); if constexpr (PL.g[R] <= M) rds<SLOT, M, R + 1>(); } }
    template <int M, bool IP = false> DEVI void mm(f32x4 (&acc)[TP][TQ]) {
      constexpr int i = O::pi[M >> 1], j = O::pj[M >> 1], k = ((M >> 1) ^ M) & 1;
      if constexpr (IP) { if constexpr (O::swp(i)) mfma_ip(acc[i][j], fq[j][k], fp[i][k]); else mfma_ip(acc[i][j], fp[i][k], fq[j][k]); }
      else if constexpr (O::swp(i)) acc[i][j] = __builtin_amdgcn_mfma_f32_16x16x32_bf16(fq[j][k], fp[i][k], acc[i][j], 0, 0, 0);
      else acc[i][j] = __builtin_amdgcn_mfma_f32_16x16x32_bf16(fp[i][k], fq[j][k], acc[i][j], 0, 0, 0);
    }
    template <int A, int B> DEVI void pend(f32x4 (&acc)[TP][TQ]) { if constexpr (A < B) { mm<2 * A>(acc); mm<2 * A + 1>(acc); pend<A + 1, B>(acc); } }
    template <int SLOT, bool FIRST, int M> DEVI void body(f32x4 (&acc)[TP][TQ]) {
      if constexpr (M < NM) {
        if constexpr (!FIRST || M >= 2 * O::NPEND) { mm<M, true>(acc); sched0(); }
        rds<SLOT, M, 0>();
        sched0();
        body<SLOT, FIRST, M + 1>(acc);
      }
    }
    template <int SLOT, bool FIRST> DEVI void step(f32x4 (&acc)[TP][TQ]) { if (!RLX) wait_lgkm0(); bar(); sched0(); body<SLOT, FIRST, 0>(acc); }
    template <int I, int N, int T0, class MID> DEVI void steps(f32x4 (&acc)[TP][TQ], MID mid) {
      if constexpr (I < N) { mid(I); step<(I + T0 + ROT) % NS, false>(acc); steps<I + 1, N, T0>(acc, mid); }
    }
    DEVI void prep(f32x4 (&acc)[TP][TQ]) { setwin(); pin(acc); }
    DEVI void pin(f32x4 (&acc)[TP][TQ]) {
#pragma unroll
      for (int p = 0; p < O::NP; ++p) asm volatile("" : "+v"(acc[O::pi[p]][O::pj[p]]));
    }
    template <bool CONT, class MID> DEVI void run_steps(f32x4 (&acc)[TP][TQ], MID mid) {
      constexpr int T0 = CONT ? 0 : 1;
      if constexpr (T0) step<ROT % NS, true>(acc);
#pragma unroll 1
      for (int itn = (NT - T0) / NS; itn > 0; --itn) {
        steps<0, NS, T0>(acc, [&](int i) { mid(itn, i); });
      }
      steps<0, (NT - T0) % NS, T0>(acc, [](int) {});
      wait_lgkm0();
      mfma_gap();
    }
  };
};

DEVI f32x4 gelu_arg(f32x4 u) { return u * (-2.302208198f + -0.1029432397f * (u * u)); }
DEVI f32x4 gelu_tanh(f32x4 u) {
  f32x4 a = gelu_arg(u);
#pragma unroll
  for (int e = 0; e < 4; ++e) a[e] = __builtin_amdgcn_exp2f(a[e]);
  a += 1.f;
#pragma unroll
  for (int e = 0; e < 4; ++e) a[e] = __builtin_amdgcn_rcpf(a[e]);
  return u * a;
}

template <int tilesP, int tilesQ, int gp, int gq> DEVI void tile_map(unsigned bid, int& tp, int& tq) {
  const unsigned xcd = bid & 7, idx = bid >> 3;
  constexpr unsigned gP = tilesP / gp, gQ = tilesQ / gq;
  static_assert(gP * gQ == 8, "");
  tp = (xcd % gP) * gp + idx % gp;
  tq = (xcd / gP) * gq + idx / gp;
}

template <int V> struct IC { static constexpr int value = V; };
template <int A, int B, class FN> DEVI void for_c(FN f) { if constexpr (A < B) { f(IC<A>{}); for_c<A + 1, B>(f); } }
DEVI float sel(bool c, float a, float b) { return c ? a : b; }
DEVI u32x2 pk4(const f32x4& v) { return (u32x2){pk2(v[0], v[1]), pk2(v[2], v[3])}; }
DEVI u32x4 cat4(u32x2 a, u32x2 b) { return (u32x4){a.x, a.y, b.x, b.y}; }
DEVI bf16x8 cat(u32x2 a, u32x2 b) { return __builtin_bit_cast(bf16x8, cat4(a, b)); }
DEVI f32x4 mfma(const bf16x8& a, const bf16x8& b, const f32x4& c) { return __builtin_amdgcn_mfma_f32_16x16x32_bf16(a, b, c, 0, 0, 0); }
DEVI u32x4 pack_pair(const f32x4& a, const f32x4& b) {
  const unsigned ax = pk2(a[0], a[1]), ay = pk2(a[2], a[3]), bx = pk2(b[0], b[1]), by = pk2(b[2], b[3]);
  auto rx = __builtin_amdgcn_permlane16_swap(ax, bx, false, false);
  auto ry = __builtin_amdgcn_permlane16_swap(ay, by, false, false);
  const unsigned x0 = rx[0], x1 = rx[1], y0 = ry[0], y1 = ry[1];
  return (u32x4){x0, y0, x1, y1};
}
DEVI int pair_chunk(int fq) { return ((fq & 1) << 1) | (fq >> 1); }
DEVI void sel_frags(bool paired, int fr, int fq, bf16x8 (&s)[2]) {
  const unsigned one = fr & 1 ? 0x3F800000u : 0x3F80u;
#pragma unroll
  for (int i = 0; i < 2; ++i) {
    const bool on = paired ? (fq & 1) == i && (fr >> 3) == (fq >> 1) : (fr >> 2) == fq;
    const int d = paired ? (fr & 7) >> 1 : i * 2 + ((fr >> 1) & 1);
    u32x4 v;
#pragma unroll
    for (int e = 0; e < 4; ++e) v[e] = on && d == e ? one : 0u;
    s[i] = __builtin_bit_cast(bf16x8, v);
  }
}
template <int TQ> DEVI void tile_add(f32x4 (&acc)[4][TQ], const bf16x8 (&s)[2], unsigned a) {
  bf16x8 t[4];
#pragma unroll
  for (int p = 0; p < 4; ++p) t[p] = lds_b128(a + p * 1024);
#pragma unroll
  for (int p = 0; p < 4; ++p)
#pragma unroll
    for (int i = 0; i < 2; ++i) acc[(p & 1) * 2 + i][p >> 1] = mfma(s[i], t[p], acc[(p & 1) * 2 + i][p >> 1]);
}
template <int C> DEVI float dppf(float v) { return __int_as_float(__builtin_amdgcn_update_dpp(0, __float_as_int(v), C, 0xf, 0xf, false)); }
DEVI float red8(float v) { v += dppf<0xB1>(v); v += dppf<0x4E>(v); v += dppf<0x141>(v); return v; }
DEVI float red4(float v) { v += dppf<0xB1>(v); v += dppf<0x4E>(v); return v; }

constexpr unsigned SENT = 0xBF800000u;
constexpr int SPIN_MAX = 1 << 21;

constexpr unsigned MB = 1u << 20;
constexpr unsigned WS_O = 0, WS_H = 8 * MB, WS_U = 16 * MB, WS_X2 = 32 * MB, WS_XP = 40 * MB  ;
constexpr unsigned WS_ST = 120 * MB, ST_CS = 2 * S * 64, WS_XCH = 122 * MB  , WS_EPOCH = 123 * MB, WS_ABORT = 123 * MB + 256  ;
DEVI unsigned tag2(unsigned t) { return t * 0x9E3779B1u ^ 0x5BD1E995u; }
constexpr int MEDGE = 256 * 64;
constexpr unsigned WS_MAIL = 124 * MB;

constexpr int NL = 2, QBR = 3, FLS = 3, NPL = 6, SPON = 1, PC0 = 0, STG = 2, WSP = 1, SWW = 2, GSH = 1, GSP = 1, SSC = 1, RB = 1, SI = 2, LPR = 1, CPR = 0, GLW = 3, GL1 = 3, SW0 = 8, SW1 = 6, XD = 3, XS = 4, JA = 22, PS = 3, TBA = 3, TC1 = 12, NW1 = 9, EW = 4, ET = 8, SPW = 1, PA_1 = 3, PA_2 = 3;
constexpr int L_X = 0, L_TAB = 16384, L_CS = L_TAB + 1152, L_TAG = L_CS + 768, L_BIAS = L_TAG + 128, L_SCR = L_BIAS + 512  , L_QKV = 20480, L_GATE = 32512;
static_assert(L_SCR + 1536 <= L_QKV, "");
typedef Gemm<true, false, 24576, 16384, 2048, 3, D / 64, true, 32768, 0, 3 * D, 2 * D, 128> GQ;
typedef Gemm<true, false, 16384, 8192, 0, 5, D / 64, true, 40960, 2, D, 128, 16384, 1, true> GW;
typedef Gemm<true, true, 16384, 16384, 0, 4, S / 64, true, 32768, 1, F, 128, 32768, 0, true> G1;
typedef Gemm<false, true, 16384, 16384, 0, 3, JA, false, 65536, (3 - JA % 3) % 3, S, 1024, 32768> G2A;
typedef Gemm<false, true, 16384, 8192, 0, 4, F / 64 - 2 * JA, false, 65536, 0, S, 128, 32768> G2;

DEVI unsigned get_tag() { return __builtin_amdgcn_readfirstlane(*(volatile LAS unsigned*)(uintptr_t)L_TAG) + 1u; }
DEVI bool spin_fail(int spin, char* ws) {
  if ((spin & 255) != 255) return false;
  gu32* ab = (gu32*)(ws + WS_ABORT);
  const unsigned tag = get_tag();
  if (spin >= SPIN_MAX) { __hip_atomic_store(ab, tag, RLX_AGENT); return true; }
  return __builtin_amdgcn_readfirstlane(__hip_atomic_load(ab, RLX_AGENT)) == tag;
}
DEVI void sync_post(char* ws, int edge, int post, int wid) {
  unsigned o = post >= 0 && wid == 0 ? post * 4u : MB, one = 1u;
  const rsrc_t r = mkrsrc(ws + WS_MAIL + edge * (MEDGE * 4), MEDGE * 4);
  asm volatile("s_waitcnt vmcnt(0) lgkmcnt(0)" : "+v"(o), "+v"(one) : "s"(r) : "memory");
  sched0();
  bar();
  if (wid == 0) __builtin_amdgcn_raw_buffer_store_b32(one, r, o, 0, 16);
  sched0();
}
struct Gate {
  const char* p; unsigned miss;
  DEVI void ask(int lane) const { asm volatile("s_mov_b32 m0, %2\n\ts_nop 0\n\tglobal_load_lds_dword %0, %1 sc1" :: "v"((lane & 31) * 4), "s"(p), "s"(L_GATE) : "memory"); }
  template <int DELAY> DEVI void wait(char* ws, int edge, int line, int lane, unsigned all, unsigned first, unsigned second, int wid) {
    sched0();
    miss = 0;
    p = ws + WS_MAIL + (edge * MEDGE + line * 64) * 4;
    if (wid == 3) {
      gu32* pw = (gu32*)p + (lane & 31);
      if (DELAY) __builtin_amdgcn_s_sleep(DELAY);
      if (STG < 2) second = all;
      for (int spin = 0;; ++spin) {
        miss = ~(unsigned)__builtin_amdgcn_ballot_w64(__hip_atomic_load(pw, RLX_AGENT) != 0) & all;
        if (spin_fail(spin, ws)) miss = 0;
        if (first && !(miss & first)) { bar(); first = 0; }
        if (!(miss & second)) break;
        __builtin_amdgcn_s_sleep(SW1);
      }
    } else bar();
  }
  int ev;
  DEVI void arm(int ni, int cw) { ev = miss ? ni : cw >= 0 ? 2 : 0; asm volatile("" : "+s"(ev), "+s"(miss)); }
  DEVI void head(int it, int cw, int lane, int wid) {
    if (__builtin_expect(it == ev, 0)) {
      asm volatile("" : "+v"(lane)); asm volatile("" : "+s"(cw), "+s"(wid));
      if (miss) ask(lane);
      if (it == 2) code_warm(cw, lane, L_SCR + wid * 256);
      ev = cw >= 0 && it > 2 ? 2 : 0;
      asm volatile("" : "+s"(ev));
    }
  }
  DEVI void late(char* ws, int lane) {
    if (__builtin_expect(miss != 0, 0)) {
      asm volatile("" : "+v"(lane)); asm volatile("" : "+s"(ws));
      for (int spin = 0;; ++spin) {
        wait_vm<0>();
        miss &= ~(unsigned)__builtin_amdgcn_ballot_w64(*(volatile LAS unsigned*)(uintptr_t)(L_GATE + lane * 4) != 0);
        if (!miss || spin_fail(spin, ws)) break;
        ask(lane);
      }
      miss = 0;
      asm volatile("" : "+s"(miss));
    }
  }
};
DEVI unsigned halo_off(int s0, int lw, int lane) {
  const int c = lw * 32 + (lane & 31), row = c >> 3, ch = (c & 7) ^ ((row >> 1) & 7), tok = s0 - 16 + row;
  return (unsigned)((tok < 0 ? 0 : tok) * D * 2 + ch * 16);
}
DEVI int qkv_rot(int bid) { return (bid & 3) * 4 + (bid & 4) * 2; }
DEVI unsigned x_off(int tp, int tq, int w, int fr, int fq) { return (unsigned)(((tq * 64 + (w >> 1) * 32 + fr) * D + tp * 128 + (w & 1) * 64 + pair_chunk(fq) * 8) * 2); }

DEVI void pfb_q(const u16* w, int bid, int j, int n) {
  const int r = slow_res(w);
  if ((r >> 2) == (bid & 1)) {
    const unsigned o = (unsigned)(((bid & 2) * 4 + r) * 128 + (j >> 1) * (3 * D * 2) + (j & 1) * 64 + ((n + qkv_rot(bid)) & 15) * GQ::WSTEP);
    spf1<0>(w, o); spf1<2048>(w, o); spf1<4096>(w, o);
  }
}
constexpr int PFR = NPL * 128 * 1024, PF_END = TBA * 256 + NW1 * PFR / (F * 2) / 8 * 8;
static_assert(TBA * 256 * F * 2 + NW1 * PFR <= S * F * 2 && 3 * PFR >= D * D * 2, "");
struct Pfc {
  const char* p; const char* q; int pc;
  DEVI Pfc(const u16* wo, const u16* w1, unsigned g) : p((const char*)wo + g * (NPL * 128)), q((const char*)w1 + TBA * 256 * F * 2 + g * (NPL * 128)), pc(PC0) {}
  DEVI void go() {
    unsigned t, u;
    asm volatile("s_getreg_b32 %2, hwreg(HW_REG_IB_STS, 8, 4)\n\ts_cmp_lt_u32 %1, %5\n\ts_cselect_b32 %3, 0, 1\n\ts_or_b32 %2, %2, %3\n\ts_cbranch_scc1 1f\n\t"
                 "s_load_dwordx2 s[100:101], %0, 60\n\ts_load_dwordx2 s[100:101], %0, 188\n\ts_load_dwordx2 s[100:101], %0, 316\n\t"
                 "s_load_dwordx2 s[100:101], %0, 444\n\ts_load_dwordx2 s[100:101], %0, 572\n\ts_load_dwordx2 s[100:101], %0, 700\n\t"
                 ".if %8 == 7\n\ts_load_dwordx2 s[100:101], %0, 828\n\t.endif\n\t"
                 "s_mov_b32 %2, %7\n\ts_cmp_lt_u32 %1, 2\n\ts_cselect_b32 %2, %6, %2\n\ts_mov_b64 s[40:41], %0\n\ts_add_u32 s40, s40, %2\n\ts_addc_u32 s41, s41, 0\n\t"
                 "s_cmp_eq_u32 %1, 2\n\ts_cselect_b64 %0, %4, s[40:41]\n\ts_add_u32 %1, %1, 1\n1:"
                 : "+s"(p), "+s"(pc), "=&s"(t), "=&s"(u) : "s"(q), "i"(3 + NW1), "i"((D * D * 2 - PFR) / 2), "i"(PFR), "i"(NPL) : "s40", "s41", "s100", "s101", "scc");
  }
};
DEVI void spare_touch(const u16* w1, const u16* w2, int bid, int wid, int lane) {
  if (!SPON) return;
  const int j = ((bid >> 3) - 28) * 8 + wid, fr = lane & 15, fq = lane >> 4;
  const unsigned scr = L_QKV + wid * 256;
#pragma unroll 1
  for (int r = PF_END + (j * 2 + ((bid >> 2) & 1)) * 4; r < S; r += 256) { if (fr < 14) touch4(w1 + (size_t)r * F + (bid & 3) * 896, fq * (F * 2) + fr * 128, scr); wait_vm<SPW>(); }
#pragma unroll 1
  for (int r = (j * 2 + (bid & 1)) * 4; r < JA * 64; r += 256) { touch4(w2 + (size_t)(r + (bid & 2) * JA * 32) * S + (bid & 4) * 256, fq * (S * 2) + fr * 128, scr); wait_vm<SPW>(); }
#pragma unroll 1
  for (int r = JA * 128 + ((31 - j) * 2 + (bid & 1)) * 8; r < F; r += 512) { touch4(w2 + (size_t)r * S + ((bid & 7) >> 1) * 512, (lane >> 3) * (S * 2) + (lane & 7) * 128, scr); wait_vm<SPW>(); }
}

template <int TH> DEVI void qkv_attn(char* ws, int l, int h, int lane, int hp, int sb, int cw, int bid, int tp, int tq, const u16* wqkv) {
  asm volatile("; half %1" : "+v"(lane) : "i"(TH));
  const int fr = lane & 15, fq = lane >> 4;
  GQ::Comp<OQ<TH>> g;
#pragma unroll
  for (int c = 0; c < 2; ++c) { g.prd[0][c] = 32768 + rd_km(lane, h * 2 + c); g.qrd[0][c] = 32768 + rd_kc(lane, c); }
  f32x4 acc[6][5];
#pragma unroll
  for (int i = 0; i < 6; ++i)
#pragma unroll
    for (int j = 0; j < 5; ++j) acc[i][j] = (f32x4){0.f, 0.f, 0.f, 0.f};
  const int par = fr & 1, da = fr - 4 * fq - par;
  const bool ca = da >= 0, cb = da >= 2;
  const unsigned tm = L_TAB + (16 + 64 * TH) * 4, tc = L_CS + h * 128;
  unsigned am = tm + fr * 4 - 320, a4 = tm + fq * 16 - 320, aa = tm + 576 + (4 * fq + par - (ca ? 0 : 16)) * 4, ab = tm + 576 + (4 * fq + par + 2 - (cb ? 0 : 16)) * 4;
  unsigned c4 = tc + fq * 16, c1 = tc + 512 + fr * 4, xb = L_QKV + h * 3072 + lane * 16, hm = par ? 0xffff0000u : 0xffffu, m0 = ca ? 0u : hm, m1 = cb ? 0u : hm;
  unsigned oo = ph((unsigned)((((sb * 2 + TH) * 16 + hp) << 13) + fr * 128 + (((h * 4 + pair_chunk(fq)) ^ (fr >> 1)) << 4)));
  asm volatile("" : "+v"(am), "+v"(a4), "+v"(aa), "+v"(ab), "+v"(c4), "+v"(c1), "+v"(xb), "+v"(hm), "+v"(m0), "+v"(m1), "+v"(oo));
  g.prep(acc);
  if (l) {
    const int sbc = (tq >> 1) + fq;
    sync_post(ws, 3, !TH && (lane < 16 || (lane < 32 && (tq & 1) && sbc < 16)) ? (sbc * 16 + fr) * 64 + (tq - 2 * sbc + 1) * 8 + tp : -1, TH * 2 + h);
  }
  Gate gt; gt.miss = 0; gt.p = ws;
  if (l) gt.wait<SW0>(ws, 3, sb * 16 + hp, lane, sb ? 0xffffffu : 0xffff00u, (sb ? 0x10101u : 0x10100u) << ((qkv_rot(bid) & 15) >> 1), 0x30303u << ((qkv_rot(bid) & 15) >> 1), TH * 2 + h);
  else if (!TH || !h) wait_vm<0>();
  else if ((slow_res(wqkv) >> 2) == (bid & 1)) {
    if (ET) __builtin_amdgcn_s_sleep(ET);
    if ((lane & 3) < 3 && lane < 40) touch4(wqkv, (unsigned)(((qkv_rot(bid) * 64 + 128 + (bid >> 3) * 10 + (lane >> 2)) & 1023) * (3 * D * 2) + (lane & 3) * (D * 2) + ((bid & 2) * 4 + slow_res(wqkv)) * 128), L_SCR + 768);
  }
  gt.arm(5, cw);
  g.template run_steps<false>(acc, [&](int it, int i) {
    if (i == 0) gt.head(it, cw, lane, TH * 2 + h);
    if (TH && i == 1) gt.late(ws, lane);
  });
  const rsrc_t ro = mkrsrc(ws + WS_O, S * D * 4);
  if (l == 0) bar();
  float mt[5], cv[2], rq[4], ra[4], rb[4]; f32x4 m4[5], cq[2], ck[2];
#pragma unroll
  for (int c = 0; c < 2; ++c) {
    ck[c] = *(const LAS f32x4*)(uintptr_t)(c4 + 256 + c * 64); cv[c] = *(const LAS float*)(uintptr_t)(c1 + c * 64);
    cq[c] = *(const LAS f32x4*)(uintptr_t)(c4 + c * 64);
  }
#pragma unroll
  for (int j = 4; j >= 0; --j) {
    const int o = 320 + (j == 4 ? -64 - 256 * TH : j * 64);
    if (j < 4 || !TH) mt[j] = *(const LAS float*)(uintptr_t)(am + o);
    if (j < 4 || TH) m4[j] = *(const LAS f32x4*)(uintptr_t)(a4 + o);
  }
#pragma unroll
  for (int j = 0; j < 4; ++j) { rq[j] = *(const LAS float*)(uintptr_t)(am + 896 + j * 64); ra[j] = *(const LAS float*)(uintptr_t)(aa + j * 64); rb[j] = *(const LAS float*)(uintptr_t)(ab + j * 64); }
  u32x2 kp[2][5], qp[2][4], vp[2][5];
  auto fk = [&](int j) { kp[0][(j + 1) % 5] = pk4(acc[1][j]); kp[1][(j + 1) % 5] = pk4(acc[4][j] + ck[1] * mt[j]); };
  auto fv = [&](int j) { vp[0][(j + 1) % 5] = pk4(acc[2][j]); vp[1][(j + 1) % 5] = pk4(acc[5][j] + m4[j] * cv[1]); };
  auto fqq = [&](int j) { qp[0][j] = pk4(acc[0][j]); qp[1][j] = pk4(acc[3][j] + cq[1] * mt[j]); };
  g.template pend<10, 13>(acc);
#pragma unroll
  for (int i = 0; i < 6; ++i) { __builtin_amdgcn_sched_group_barrier(0x100, 3, 0); __builtin_amdgcn_sched_group_barrier(0x8, 1, 0); }
  sched0();
  for_c<0, 20>([&](auto mc) {
    constexpr int m = mc.value, n0 = m * 52 / 20, n1 = (m + 1) * 52 / 20;
    g.template mm<(18 - (m & ~1)) + (m & 1)>(acc);
    sched0();
#pragma unroll
    for (int n = n0; n < n1; ++n) {
      const int t = 12 - (n >> 2), e = n & 3, j = t == 12 ? 4 : t / 3, i = t == 12 ? 1 + TH : t % 3;
      float x = acc[i][j][e];
      asm volatile("v_fmac_f32 %0, %1, %2" : "+v"(x) : "v"(i == 2 ? m4[j][e] : i ? ck[0][e] : cq[0][e]), "v"(i == 2 ? cv[0] : mt[j]));
      acc[i][j][e] = x;
    }
    sched0();
  });
  volatile LAS unsigned* xf = (volatile LAS unsigned*)(uintptr_t)(L_TAG + 16 + (h * 2 + TH) * 4);
  if (TH) { fv(4); *(volatile LAS bf16x8*)(uintptr_t)(xb + 2048) = cat(vp[0][0], vp[1][0]); *xf = l + 1; fk(3); }
  else { fk(3); fv(3); *(volatile LAS bf16x8*)(uintptr_t)xb = cat(kp[0][4], kp[1][4]); *(volatile LAS bf16x8*)(uintptr_t)(xb + 1024) = cat(vp[0][4], vp[1][4]); *xf = l + 1; }
  sched0();
  fqq(3); fk(2); fqq(2); fk(1);
  sched0();
  const f32x4 z = {0.f, 0.f, 0.f, 0.f};
  f32x4 sc[4][2]; float a[4], b[4], m[4];
  auto scores = [&](int j) {
#pragma unroll
    for (int u = 1; u >= 0; --u) sc[j][u] = mfma(cat(kp[0][j + u], kp[1][j + u]), cat(qp[0][j], qp[1][j]), z);
  };
  auto pick = [&](int j) {
    const f32x4 s0 = sc[j][0], s1 = sc[j][1];
    a[j] *= sel(par, sel(ca, s1[1], s0[1]), sel(ca, s1[0], s0[0]));
    b[j] *= sel(par, sel(cb, s1[3], s0[3]), sel(cb, s1[2], s0[2]));
    if (!TH && j == 0) { a[j] = sel(ca || sb, a[j], -1e9f * 1.4426950408889634f); b[j] = sel(cb || sb, b[j], -1e9f * 1.4426950408889634f); }
    m[j] = __builtin_fmaxf(a[j], b[j]);
  };
  auto expo = [&](int j) { a[j] = __builtin_amdgcn_exp2f(a[j] - m[j]); b[j] = __builtin_amdgcn_exp2f(b[j] - m[j]); m[j] = a[j] + b[j]; };
  auto pv = [&](int j) {
    const unsigned pa = pk2(a[j] * ra[j], a[j] * ra[j]), pb = pk2(b[j] * rb[j], b[j] * rb[j]);
    const bf16x8 pf = __builtin_bit_cast(bf16x8, (u32x4){pa & m0, pb & m1, pa & (m0 ^ hm), pb & (m1 ^ hm)});
#pragma unroll
    for (int c = 0; c < 2; ++c) sc[j][c] = mfma(cat(vp[c][j], vp[c][j + 1]), pf, z);
  };
  auto outp = [&](int j) { st16(ro, oo + j * 4096, pack_pair(sc[j][0] * m[j], sc[j][1] * m[j])); };
  unsigned f; u32x4 xv, xk;
  auto xrd = [&]() {
    f = xf[1 - 2 * TH]; xv = *(volatile LAS u32x4*)(uintptr_t)(xb + (TH ? 1024 : 2048));
    if (TH) xk = *(volatile LAS u32x4*)(uintptr_t)xb;
  };
  auto xget = [&]() {
    sched0();
    while (__builtin_expect(__builtin_amdgcn_readfirstlane(f) != l + 1, 0)) xrd();
    vp[0][0] = (u32x2){xv[0], xv[1]}; vp[1][0] = (u32x2){xv[2], xv[3]};
    if (TH) { kp[0][0] = (u32x2){xk[0], xk[1]}; kp[1][0] = (u32x2){xk[2], xk[3]}; }
  };
  {
    fqq(1); fk(0); fqq(0);
    if (!TH) fk(4);
    xrd();
    sched0();
    if (TH) fv(3);
    fv(2); fv(1); fv(0);
#pragma unroll
    for (int c = 0; c < 2; ++c)
#pragma unroll
      for (int j = 0; j < 4; ++j) asm volatile("" : "+v"(qp[c][j]), "+v"(kp[c][j + 1]), "+v"(vp[c][j + 1]));
    if (!TH) asm volatile("" : "+v"(kp[0][0]), "+v"(kp[1][0]));
    xget();
    scores(3); scores(2); scores(1); scores(0);
#pragma unroll
    for (int j = 0; j < 4; ++j) { const float r = rq[j] * (0.17677669529663687f * 1.4426950408889634f); a[j] = r * ra[j]; b[j] = r * rb[j]; }
#pragma unroll
    for (int i = 0; i < 8; ++i) { __builtin_amdgcn_sched_group_barrier(0x8, 1, 0); __builtin_amdgcn_sched_group_barrier(0x2, 2, 0); }
    sched0();
    pick(3); pick(2); pick(1); pick(0);
    float u, w;
    swp32(m[0], m[2]); swp32(m[1], m[3]); u = m[0]; w = m[1];
    asm volatile("v_max_f32 %0, %0, %2\n\tv_max_f32 %1, %1, %3\n\ts_nop 1" : "+v"(u), "+v"(w) : "v"(m[2]), "v"(m[3]));
    swp16(u, w); u = vmax(u, w);
    xch16(u, u, w); xch32(u, m[0], m[2]); xch32(w, m[1], m[3]);
    expo(3); expo(2); expo(1); expo(0);
    pv(3); pv(2); pv(1); pv(0);
    swp32(m[0], m[2]); swp32(m[1], m[3]); u = m[0] + m[2]; w = m[1] + m[3];
    swp16(u, w); u = __builtin_amdgcn_rcpf(u + w);
    xch16(u, u, w); xch32(u, m[0], m[2]); xch32(w, m[1], m[3]);
    outp(3); outp(2); outp(1); outp(0);
  }
}

extern "C" __global__ __launch_bounds__(512, 2) void mega_kernel(const u16* x0, const u16* wqkv, const u16* wo, const u16* w1, const u16* w2, const u16* b1, const u16* b2, u16* out, char* ws) {
  const int wid0 = __builtin_amdgcn_readfirstlane(threadIdx.x >> 6);
  asm volatile("s_getpc_b64 s[40:41]\n\ts_andn2_b32 s40, s40, 63\n\ts_load_dwordx2 s[100:101], s[40:41], %0 offset:124\n\ts_load_dwordx2 s[100:101], s[40:41], %0 offset:252"
               :: "s"(__builtin_amdgcn_readfirstlane(threadIdx.x) << 2) : "s40", "s41", "s100", "s101", "scc");
  sched0();
#define PHASE_VARS \
      int tid = threadIdx.x, bid = blockIdx.x, wid = wid0; \
      asm volatile("" : "+v"(tid)); asm volatile("" : "+s"(bid), "+s"(wid), "+s"(ws)); \
      const int lw = wid - 4, lt = tid - 256, lane = tid & 63, fr = lane & 15, fq = lane >> 4, wp = wid & 1, wq = wid >> 1; \
      const unsigned j = (bid >> 3) * 4 + lw; \
      int hp, sb; tile_map<16, 16, 4, 8>(bid, hp, sb);            \
      int tp, tq; tile_map<8, 32, 4, 8>(bid, tp, tq);             \
      int fp, fq1; tile_map<28, 8, 7, 4>(bid, fp, fq1);           \
      const int cw = bid < 16 ? wid + 4 * (bid >> 3) : -1; \
      (void)cw; (void)j; (void)lt; (void)wp; (void)wq; (void)lw; (void)fr; (void)fq; (void)hp; (void)sb; (void)tp; (void)tq; (void)fp; (void)fq1;
#define LOADER_VARS PHASE_VARS asm volatile("" : "+s"(wqkv), "+s"(wo), "+s"(w1), "+s"(w2));

  if (__builtin_expect(wid0 >= 4, 1)) {
    if (LPR) __builtin_amdgcn_s_setprio(3);
    const u16* xin = x0;
    Pfc pf(wo, w1, blockIdx.x * 4 + wid0 - 4);
#pragma unroll 1
    for (int l = 0; l < NL; ++l) {
      {
        LOADER_VARS
        const int s0 = sb * 128;
        GQ::Loader L(wqkv + hp * 64, xin, lw, lane, qkv_rot(bid));
        if (l == 0) L.pre_w1<0>();
        L.seta((unsigned)(s0 * D * 2), halo_off(s0, lw, lane), lw, lane);
        if (l == 0) { L.issue_a<0>(L.ab + L.kord(0) * 128); if (!SI) L.pre<1, true, true>(); }
        sched0();
        const rsrc_t rs = mkrsrc(ws + WS_ST, MB);
        f32x4 c = {0.f, 0.f, 0.f, 0.f}; f32x2 s1 = {0.f, 0.f}, s2 = {0.f, 0.f}, o = {0.f, 0.f}; float pv = 0.f;
        bf16x8 ones = {0x3F80, 0x3F80, 0x3F80, 0x3F80, 0x3F80, 0x3F80, 0x3F80, 0x3F80};
        const int ct = lw & 1 ? 8 + (sb & 3) : sb & 7; const bool cs = l == 0 && lw >= 2;
        int tpf = (slow_res(wqkv) >> 2) != (bid & 1) ? 0 : lw < 2 ? 10 : 9;
        unsigned ra = GQ::AOFF + ((hp & 3) * 32 + lw * 8) * 128 + lane * 16, rb = rd_km(lane, ct);
        unsigned so = lane & 7 ? MB : ph((unsigned)(((l * S + s0 + (hp & 3) * 32 + lw * 8 + (lane >> 3)) * 4 + (hp >> 2)) * 16));
        unsigned co = fr >= 2 || !cs ? MB : ph((unsigned)(ST_CS + ((hp * 96 + ct * 8 + fq * 2 + fr) * 2 + (sb >> 3)) * 16));
        asm volatile("" : "+v"(ra), "+v"(rb), "+v"(so), "+v"(co), "+v"(c), "+v"(s1), "+v"(s2), "+v"(ones), "+v"(o));
        asm volatile("" : "+s"(tpf));
        if (SI && l == 0) L.pre_w1<1>();
        sched0();
        unsigned to[3], xo = x_off(tp, tq, lw, fr, fq), q2 = ((bid & 2) * 4 + slow_res(wqkv)) * 128 + ((bid >> 3) * 2 + (lw & 1)) * (3 * D * 2);
#pragma unroll
        for (int i = 0; i < 3; ++i) {
          const int r = s0 - 16 + (i == 2 && lw ? 64 : i * 64) + (lt >> 2);
          to[i] = ph((unsigned)(((l * S + (r < 0 ? 0 : r)) * 4 + (lt & 3)) * 16));
        }
        unsigned ta = (lt & 3) < (lw ? 2 : 3) ? L_TAB + ((lt & 3) * 64 + (lt >> 2)) * 4 : L_SCR, ts = ph((unsigned)(ST_CS + (hp * 192 + (lw ? lt - 64 : lt)) * 16));
        asm volatile("" : "+v"(to[0]), "+v"(to[1]), "+v"(to[2]), "+v"(xo), "+v"(ta), "+v"(ts));
        asm volatile("" : "+s"(q2));
        if (SI == 1 && l == 0) L.pre<1, false, true>();
        sched0();
        const GW::Loader W(wo + tp * 128, ws, lw, lane);
        if (SI == 2 && l == 0) L.pre<1, false, true>();
        sched0();
        auto pfq = [&](int n) { const unsigned o = q2 + L.kord(n) * GQ::WSTEP; spf2<0>(wqkv, o); spf2<2048>(wqkv, o); spf2<4096>(wqkv, o); };
        if (l) {
          if (QBR == 3) bar();
          if (!(EW & 8)) L.pre_w();
          if (tpf) { pfq(2 + (lw >> 1)); pfq(4 + (lw >> 1)); }
          if (QBR == 4) bar();
          L.start();
        }
        if (LPR == 1) __builtin_amdgcn_s_setprio(0);
        unsigned tag, tg2; u32x4 gs, a[3];
        auto rowst = [&](int i) {
          const float m0 = __uint_as_float(a[i][0]), m = red4(m0) * 0.25f, q = red4(__uint_as_float(a[i][2]) + 256.f * (m0 - m) * (m0 - m));
          o = (f32x2){sel((lt & 3) == i, m, o.x), sel((lt & 3) == i, q, o.y)};
          asm volatile("" : "+v"(o));
        };
        for_c<0, 16 - GQ::H>([&](auto tc) {
          constexpr int t = tc.value;
          u32x4 v; unsigned tv;
          L.step<t, t == 5 || t == 6 ? 1 : t == 9 || t == 10 ? 4 : 0>([&](unsigned ls) {
            if (t < 4) v = *(const LAS u32x4*)(uintptr_t)(ls + ra);
            if (t == 3) tv = *(volatile LAS unsigned*)(uintptr_t)L_TAG;
          }, [&]() {
            if (t < 8 && cs) {
              constexpr int ls = 32768 + t % 3 * GQ::SB;
              bf16x4 x[4];
#pragma unroll
              for (int e = 0; e < 4; ++e) x[e] = lds_tr4(rb + (ls & ~0x7fff) + (ls & 0x7fff) + (e >> 1) * 4096 + (e & 1) * 512);
#pragma unroll
              for (int ks = 0; ks < 2; ++ks) c = mfma(__builtin_shufflevector(x[2 * ks], x[2 * ks + 1], 0, 1, 2, 3, 4, 5, 6, 7), ones, c);
            } else if (t >= 4) {
              if (t >= 10 || t >= tpf) { if (t < TC1) pf.go(); }
              else if (lw < 2) pfq(t + 4);
              else if (t == 8) { pfq(14); pfq(15); }
            }
            if (t < 4) {
              if (t == 0) { pv = bflo(v[0]); asm volatile("" : "+v"(pv)); }
#pragma unroll
              for (int e = 0; e < 4; ++e) {
                if (SSC) {
                  float x = bflo(v[e]), y = bfhi(v[e]), a0 = s1.x, a1 = s1.y, b0 = s2.x, b1 = s2.y;
                  asm volatile("v_sub_f32 %0, %0, %6\n\tv_sub_f32 %1, %1, %6\n\tv_add_f32 %2, %2, %0\n\tv_add_f32 %3, %3, %1\n\tv_fmac_f32 %4, %0, %0\n\tv_fmac_f32 %5, %1, %1"
                               : "+v"(x), "+v"(y), "+v"(a0), "+v"(a1), "+v"(b0), "+v"(b1) : "v"(pv));
                  s1 = (f32x2){a0, a1}; s2 = (f32x2){b0, b1};
                } else { const f32x2 d = (f32x2){bflo(v[e]), bfhi(v[e])} - pv; s1 += d; s2 += d * d; }
              }
              asm volatile("" : "+v"(s1), "+v"(s2));
            }
            if (t == 3) { tag = __builtin_amdgcn_readfirstlane(tv) + 1u; tg2 = tag2(tag); if (tpf == 10) pfq(7); if (l && tpf == 9) pfq(6); }
            if (t == 4) {
              const float d1 = (s1.x + s1.y) * (1.f / 32.f), ml = pv + d1, m = red8(ml) * 0.125f;
              const float q = red8(vmax((s2.x + s2.y) - 32.f * d1 * d1, 0.f) + 32.f * (ml - m) * (ml - m));
              gs = (u32x4){__float_as_uint(m), tag, __float_as_uint(q), tg2};
              asm volatile("" : "+v"(gs));
            }
            if (t == 8) { gs = (u32x4){__float_as_uint(sel(fr, c[2], c[0])), tag, __float_as_uint(sel(fr, c[3], c[1])), tg2}; asm volatile("" : "+v"(gs)); }
            if (t == (RB ? 12 : 11)) {
#pragma unroll
              for (int i = 0; i < 3; ++i) ld_ok(a[i]);
              for (int spin = 0;; ++spin) {
                unsigned bad = 0;
#pragma unroll
                for (int i = 0; i < 3; ++i) bad |= (a[i][1] ^ tag) | (a[i][3] ^ tg2);
                if (__builtin_expect(__all(bad == 0), 1) || spin_fail(spin, ws)) break;
                __builtin_amdgcn_s_sleep(8);
#pragma unroll
                for (int i = 0; i < 3; ++i) ld_again(a[i], rs, to[i]);
              }
              if (!RB) rowst(0);
            }
            if (t == 12 && !RB) { rowst(1); rowst(2); }
            if (t == 13 && RB == 2) rowst(0);
            if (t == 13 && !RB) { LAS float* p = (LAS float*)(uintptr_t)ta; p[0] = -o.x; p[144] = __builtin_amdgcn_rsqf(o.y * (1.f / D) + EPS); }
          }, [&]() {
            if (t == 4) st16(rs, so, gs);
            if (t == 8) {
              st16(rs, co, gs);
              sched0();
#pragma unroll
              for (int i = 0; i < 3; ++i) ld_raw(a[i], rs, to[i]);
            }
          });
        });
        if (l == 0) {
#pragma unroll
          for (int p = 0; p < 4; ++p) dma16((const char*)xin + (p >> 1) * 32768 + (p & 1) * 64, xo, L_X + lw * 4096 + p * 1024);
          ld_raw(gs, rs, ts);
          wait_vm<GQ::CNT + 5>();
        } else wait_vm<GQ::CNT>();
        bar();
        W.pre_w1<0>();
        if (RB) { if (RB == 1) rowst(0); rowst(1); rowst(2); LAS float* p = (LAS float*)(uintptr_t)ta; p[0] = -o.x; p[144] = __builtin_amdgcn_rsqf(o.y * (1.f / D) + EPS); }
        wait_lgkm0();
        if (l == 0) wait_vm<9>(); else wait_vm<4>();
        bar();
        W.pre_w1<1>(); W.pre_w1<2>();
        if (l == 0) {
          wait_vm<12>();
          ld_ok(gs);
          for (int spin = 0;; ++spin) {
            if (__builtin_expect(__all(((gs[1] ^ tag) | (gs[3] ^ tg2)) == 0), 1) || spin_fail(spin, ws)) break;
            __builtin_amdgcn_s_sleep(8);
            ld_again(gs, rs, ts);
          }
          float c0 = __uint_as_float(gs[0]), c1 = __uint_as_float(gs[2]);
          c0 += dppf<0xB1>(c0); c1 += dppf<0xB1>(c1);
          *(LAS float*)(uintptr_t)(L_CS + (lw ? lt - 64 : lt) * 4) = sel(lt & 1, c1, c0);
        }
        if (l == 0 && lw < TBA) { if (fr < 14) touch4(w1 + (size_t)(((bid >> 3) * TBA + lw) * 8 + (bid & 4)) * F + (bid & 3) * 896, fq * (F * 2) + fr * 128, L_SCR + lw * 256); }
        if (l == 0) { wait_lgkm0(); bar(); }
      }
      {
        LOADER_VARS
        GW::Loader L(wo + tp * 128, ws + WS_O + (size_t)tq * 32 * 8192, lw, lane);
        unsigned po = ((bid & 1) * 8 + (slow_res(wo) ^ (j & 1))) * 128 + (j >> 1) * (D * 2);
#pragma unroll
        for (int k = GW::H; k < 10; ++k) spf2(wo, po + k * GW::WSTEP);
        asm volatile("" : "+s"(po));
        sched0();
        bar();
#pragma unroll
        for (int k = 10; k < 16; ++k) spf2(wo, po + k * GW::WSTEP);
        L.start();
        L.run(16 - GW::H, [&](int) { pf.go(); });
        pf.go();
        L.tail<GW::H - 1>(); bar();
      }
      {
        LOADER_VARS
        if (bid < 224) {
          G1::Loader L(w1 + fp * 128, ws + WS_H + (size_t)fq1 * 64 * 16384, lw, lane);
          L.pre_w();
          const G2A::Loader W((const char*)(w2 + (tq & 23) * 64) + (tq & 8 ? JA * G2::WSTEP : 0), ws, lw, lane);
          const int g0 = (bid & 3) * 14, ga = (g0 + ((slow_res(w1) - g0) & 7)) ^ (j & 1), gb = ga + 8 < g0 + 14 ? ga + 8 : ga;
          const unsigned j2 = j < 32 ? 112 + (j & 15) : j;
          unsigned pa = (j >> 1) * (F * 2) + ga * 128, pb = (j >> 1) * (F * 2) + gb * 128, pd = (j2 >> 1) * (F * 2) + (j & 16 ? gb : ga) * 128;
          asm volatile("" : "+s"(pa), "+s"(pb), "+s"(pd));
          auto pfb = [&](int k) { spf2(w1, pa + k * G1::WSTEP); spf2(w1, pb + k * G1::WSTEP); spf2(w1, pd + k * G1::WSTEP); };
#pragma unroll
          for (int k = G1::H; k < G1::H + PA_1; ++k) pfb(k);
          wait_lgkm0(); pf.go();
          sched0();
          bar();
          pf.go();
          L.start();
          L.run(32 - G1::H, [&](int t) {
            if (pf.pc < 3 + NW1) pf.go();
            else if (t < 32 - G1::H - PA_1) pfb(t + G1::H + PA_1);
          });
          if (EW & 4) {
            wait_vm<2 * G1::CNT>(); bar(); W.pre_w1<1>();
            wait_vm<G1::CNT + 4>(); bar();
            wait_vm<4>(); bar(); W.pre_w1<0>();
          } else L.tail<G1::H - 1>();
          bar();
        } else {
          wait_lgkm0(); pf.go();
          bar();
          if (l == 0) {
            __builtin_amdgcn_s_sleep(24);
            while (pf.pc < 3 + NW1) { wait_lgkm0(); pf.go(); }
            spare_touch(w1, w2, bid, wid, lane);
          }
        }
        pf.pc = 99;
      }
      {
        LOADER_VARS
        constexpr int NB = F / 64 - 2 * JA;
        const char* ub = ws + WS_U + (size_t)tp * 112 * 16384;
        G2A::Loader A((const char*)(w2 + (tq & 23) * 64) + (tq & 8 ? JA * G2::WSTEP : 0), ub + (tq & 8 ? JA * 32768 : 0), lw, lane);
        if (!(EW & 4) || bid >= 224) A.pre_w();
        const GQ::Loader W(wqkv + hp * 64, ws, lw, lane, qkv_rot(bid));
        unsigned pb = ((bid & 4) * 4 + (j & 1) * 8 + slow_res(w2)) * 128 + (j >> 1) * (S * 2) + JA * G2::WSTEP, pa = pb - (bid & 2 ? 0 : JA * G2::WSTEP);
        asm volatile("" : "+s"(pa), "+s"(pb));
        auto side = [&](int n) { n += G2A::H + PA_2; if (n < JA + NB) spf2(w2, (n < JA ? pa : pb) + n * G2::WSTEP); };
#pragma unroll
        for (int k = 0; k < PA_2; ++k) side(k - PA_2);
        G2::Loader L((const char*)(w2 + tq * 64) + 2 * JA * G2::WSTEP, ub + 2 * JA * 32768, lw, lane);
        gu32* xf = (gu32*)(ws + WS_MAIL) + (l * 4 + 2) * MEDGE + (tq * 8 + tp) * 64 + 28 + lw;
        const char* xp = ws + WS_XP + (unsigned)(((bid ^ 2) * 4 + lw) << 14);
        unsigned fl = L_QKV + lw * 256, xv = ph(lane * 16);
        asm volatile("" : "+s"(xf), "+s"(xp), "+s"(fl)); asm volatile("" : "+v"(xv));
        sched0();
        bar();
        A.start();
        A.run(JA - G2A::H, [&](int t) { side(t); });
        auto stage = [&](auto s) { L.issue_w<s.value * G2::SB>(L.w + s.value * G2::WSTEP); L.issue_a<s.value * G2::SB>(L.ab + s.value * 32768); sched0(); };
        wait_vm<G2A::CNT>(); bar(); stage(IC<0>{}); side(JA - 2);
        wait_vm<G2::CNT>(); bar(); stage(IC<1>{}); side(JA - 1);
        wait_vm<G2::CNT>(); bar(); stage(IC<2>{}); stage(IC<3>{}); side(JA);
        L.t = 1; L.islot = 0;
        L.run(NB - G2::H, [&](int t) {
          if (t == NB - G2::H - FLS) asm volatile("s_mov_b32 m0, %2\n\ts_nop 0\n\tglobal_load_lds_dword %0, %1 sc1" :: "v"(0u), "s"(xf), "s"(fl) : "memory");
          side(JA + t);
        });
        auto xdma = [&]() {
#pragma unroll
          for (int i = 0; i < 4; ++i) dma16(xp + i * 2048, xv, L_X + lw * 4096 + i * 1024);
          sched0();
        };
        sched0(); wait_vm<2 * G2::CNT>(); bar();
        if (__builtin_amdgcn_readfirstlane(*(volatile LAS unsigned*)(uintptr_t)fl)) {
          xdma();
          if ((EW & 8) && l == 0) { W.pre_w1<0>(); wait_vm<G2::CNT + 10>(); bar(); W.pre_w1<1>(); wait_vm<16>(); bar(); wait_vm<12>(); }
          else { wait_vm<G2::CNT + 4>(); bar(); wait_vm<4>(); bar(); wait_vm<0>(); }
        } else {
          wait_vm<G2::CNT>(); bar(); wait_vm<0>(); bar();
          for (int spin = 0;; ++spin) {
            if (__builtin_amdgcn_readfirstlane(__hip_atomic_load(xf, RLX_AGENT)) || spin_fail(spin, ws)) break;
            __builtin_amdgcn_s_sleep(2);
          }
          xdma();
          if ((EW & 8) && l == 0) { W.pre_w(); wait_vm<12>(); } else wait_vm<0>();
        }
        bar();
        xin = (const u16*)(ws + WS_X2);
        if (NL == 1) bar();
      }
    }
    return;
  }

  {
    PHASE_VARS
    if (cw >= 0) code_warm(cw, lane, L_SCR + wid * 256);
    pfb_q(wqkv, bid, (bid >> 3) * 4 + wid, 0); pfb_q(wqkv, bid, (bid >> 3) * 4 + wid, 1);
    if (wid < 3) {
      if (tid == 0) asm volatile("s_mov_b32 m0, %2\n\ts_nop 0\n\tglobal_load_lds_dword %0, %1 sc1" :: "v"(WS_EPOCH), "s"(ws), "s"(L_TAG) : "memory");
      const rsrc_t rz = mkrsrc(ws + WS_XCH, 3 * MB);
      if (tid < 128) st16(rz, ph((((tid >> 6) * 256 + tq * 8 + tp) * 64 + lane) * 16), (u32x4){0u, SENT, 0u, SENT});
      else {
        const int e = (tid >> 3) & 7, line = e == 0 || e == 7 ? (2 * sb + (e & 1)) * 8 + (hp & 7) : e * 256 + bid;
        st16(rz, WS_MAIL - WS_XCH + line * 256 + (tid & 7) * 16, (u32x4){0u, 0u, 0u, 0u});
      }
      if (wid == 1 && bid < 224) touch4(b1 + fp * 128, lane * 4, L_BIAS);
      if (wid == 2) touch4(b2 + tq * 64, (lane & 31) * 4, L_BIAS + 256);
      if (wid == 0) *(LAS u32x4*)(uintptr_t)(L_TAG + 16) = (u32x4){0u, 0u, 0u, 0u};
    }
    if (CPR) __builtin_amdgcn_s_setprio(CPR);
  }
#pragma unroll 1
  for (int l = 0; l < NL; ++l) {
    {
      PHASE_VARS
      if (wid & 2) qkv_attn<1>(ws, l, wp, lane, hp, sb, cw, bid, tp, tq, wqkv);
      else qkv_attn<0>(ws, l, wp, lane, hp, sb, cw, bid, tp, tq, wqkv);
    }
    {
      PHASE_VARS
      if (WSP == 0) sync_post(ws, l * 4, lane < 16 ? (sb * 16 + lane) * 64 + hp : -1, wid);
      GW::Comp<O42<false, WNE>> g; g.setup(wp, wq, lane);
      f32x4 acc[4][2];
#pragma unroll
      for (int i = 0; i < 4; ++i)
#pragma unroll
        for (int jj = 0; jj < 2; ++jj) acc[i][jj] = (f32x4){0.f, 0.f, 0.f, 0.f};
      unsigned xa = L_X + wid * 4096 + lane * 16;
      { bf16x8 s[2]; sel_frags(true, fr, fq, s); tile_add(acc, s, xa); }
      if (WSP == 1) {
#pragma unroll
        for (int i = 0; i < 4; ++i) asm volatile("" : "+v"(acc[i][0]), "+v"(acc[i][1]));
        sync_post(ws, l * 4, lane < 16 ? (sb * 16 + lane) * 64 + hp : -1, wid);
      }
      const rsrc_t rx = mkrsrc(ws + WS_XCH + l * (MB / 2), MB / 2), rh = mkrsrc(ws + WS_H, S * D * 4);
      unsigned xs = fq != fr >> 2 ? MB : ph((unsigned)(((tq * 8 + tp) * 64 + wp * 32 + wq * 16 + fr) * 16)), xl = ph((unsigned)((tq * 512 + fq * 128 + wq * 16 + fr) * 16));
      const unsigned h0 = ph((unsigned)(((tp * 32 + tq) << 14) + wp * 8192 + (wq * 32 + fr) * 128 + ((pair_chunk(fq) ^ (km_f(fr) << 1)) << 4)));
      u32x4 ho = {h0, h0 ^ 64, h0 + 4096, (h0 ^ 64) + 4096};
      bf16x8 ones = {0x3F80, 0x3F80, 0x3F80, 0x3F80, 0x3F80, 0x3F80, 0x3F80, 0x3F80};
      asm volatile("" : "+v"(xa), "+v"(xs), "+v"(xl), "+v"(ho), "+v"(ones));
      g.prep(acc);
      if (WSP == 2) sync_post(ws, l * 4, lane < 16 ? (sb * 16 + lane) * 64 + hp : -1, wid);
      Gate gt; gt.wait<WSP == 2 ? SW0 : SWW>(ws, l * 4, tq * 8 + tp, lane, 0xffffu, (1u << GW::H) - 1, (1u << (GW::H + GLW)) - 1, wid);
      gt.arm(3, cw);
      g.run_steps<false>(acc, [&](int it, int i) {
        if (i == 0) gt.head(it, cw, lane, wid);
        if (i == GLW - 1) gt.late(ws, lane);
      });
      bar();
      sched0();
      g.pend<0, 4>(acc);
      u32x4 pk[2][2]; float mp[2];
      {
        f32x4 sm[2], gm[2];
#pragma unroll
        for (int i = 0; i < 2; ++i)
#pragma unroll
          for (int jj = 0; jj < 2; ++jj) {
            pk[jj][i] = cat4(pk4(acc[2 * i][jj]), pk4(acc[2 * i + 1][jj])); asm volatile("" : "+v"(pk[jj][i]));
            const bf16x8 x = __builtin_bit_cast(bf16x8, pk[jj][i]);
            gm[jj] = mfma(x, x, i ? gm[jj] : (f32x4){0.f, 0.f, 0.f, 0.f}); sm[jj] = mfma(ones, x, i ? sm[jj] : (f32x4){0.f, 0.f, 0.f, 0.f});
          }
        u32x4 st;
#pragma unroll
        for (int jj = 0; jj < 2; ++jj) {
          const f32x4 gv = gm[jj];
          st[2 * jj] = __float_as_uint(sm[jj][0]); st[2 * jj + 1] = __float_as_uint(sel(fr & 2, sel(fr & 1, gv[3], gv[2]), sel(fr & 1, gv[1], gv[0])));
          mp[jj] = sm[jj][0] * (1.f / 64.f);
        }
        st16(rx, xs, st);
      }
      sched0();
#pragma unroll
      for (int p = 0; p < 4; ++p) *(LAS u32x4*)(uintptr_t)(xa + p * 1024) = pk[p >> 1][p & 1];
      asm volatile("" : "+v"(mp[0]), "+v"(mp[1]));
#pragma unroll
      for (int jj = 0; jj < 2; ++jj)
#pragma unroll
        for (int i = 0; i < 2; ++i) {
          const auto rx_ = __builtin_amdgcn_permlane16_swap(pk[jj][i][0], pk[jj][i][2], false, false), ry_ = __builtin_amdgcn_permlane16_swap(pk[jj][i][1], pk[jj][i][3], false, false);
          const unsigned x0 = rx_[0], x1 = rx_[1], y0 = ry_[0], y1 = ry_[1];
          acc[2 * i][jj] = (f32x4){bflo(x0), bfhi(x0), bflo(y0), bfhi(y0)} - mp[jj]; acc[2 * i + 1][jj] = (f32x4){bflo(x1), bfhi(x1), bflo(y1), bfhi(y1)} - mp[jj];
          asm volatile("" : "+v"(acc[2 * i][jj]), "+v"(acc[2 * i + 1][jj]));
        }
      if (XD) __builtin_amdgcn_s_sleep(XD);
      f32x4 gr[4];
      for (int spin = 0;; ++spin) {
        asm volatile("" ::: "memory");
#pragma unroll
        for (int p = 0; p < 4; ++p) gr[p] = __builtin_bit_cast(f32x4, __builtin_amdgcn_raw_buffer_load_b128(rx, xl + p * 1024, 0, 16));
        const float lo = vmin3(vmin3(gr[0][1], gr[0][3], gr[1][1]), vmin3(gr[1][3], gr[2][1], gr[2][3]), vmin3(gr[3][1], gr[3][3], gr[3][3]));
        if (!__any(lo < 0.f) || spin_fail(spin, ws)) break;
        __builtin_amdgcn_s_sleep(XS);
      }
      {
        const f32x4 t = (gr[0] + gr[1]) + (gr[2] + gr[3]);
        float m[2] = {t[0], t[2]}, q[2] = {t[1], t[3]}, u, w, v, y;
        swp32(m[0], m[1]); swp32(q[0], q[1]);
        xch16(m[0] + m[1], u, w); xch16(q[0] + q[1], v, y);
        const float mean = (u + w) * (1.f / D), var = __builtin_fmaxf((v + y) * (1.f / D) - mean * mean, 0.f), r1 = __builtin_amdgcn_rsqf(var + EPS);
        xch32(mean, m[0], m[1]);
        xch32(r1 * __builtin_amdgcn_rsqf(var * r1 * r1 + EPS), q[0], q[1]);
#pragma unroll
        for (int jj = 0; jj < 2; ++jj) {
          const float c = (mp[jj] - m[jj]) * q[jj];
#pragma unroll
          for (int i = 0; i < 2; ++i) st16(rh, ho[jj * 2 + i], cat4(pk4(acc[2 * i][jj] * q[jj] + c), pk4(acc[2 * i + 1][jj] * q[jj] + c)));
        }
      }
    }
    {
      PHASE_VARS
      const int post = lane < 28 ? (tp * 28 + lane) * 64 + tq : -1;
      if (bid >= 224) {
        sync_post(ws, l * 4 + 1, post, wid);
        if (l == 0 && bid == 255 && wid == 0) { const unsigned tag = get_tag(); if (lane == 0) __hip_atomic_store((gu32*)(ws + WS_EPOCH), tag, RLX_AGENT); }
        if (l == 0) { __builtin_amdgcn_s_sleep(24); spare_touch(w1, w2, bid, wid, lane); }
      } else {
        G1::Comp<O44<false, 32>> g; g.setup(wp, wq, lane);
        f32x4 acc[4][4];
#pragma unroll
        for (int i = 0; i < 4; ++i) { const u32x2 b = *(const LAS u32x2*)(uintptr_t)(L_BIAS + (wp * 4 + i) * 32 + fq * 8);
#pragma unroll
          for (int jj = 0; jj < 4; ++jj) acc[i][jj] = (f32x4){bflo(b.x), bfhi(b.x), bflo(b.y), bfhi(b.y)}; }
        const rsrc_t ro = mkrsrc(ws + WS_U, D * F * 4);
        const unsigned u0 = ph((unsigned)(((fq1 * 56 + fp * 2 + wp) << 14) + (wq * 64 + fr) * 128 + ((pair_chunk(fq) ^ (fr >> 1)) << 4)));
        u32x4 uo = {u0, u0 + 4096, u0 + 8192, u0 + 12288}, uo2 = uo ^ 64;
        asm volatile("" : "+v"(uo), "+v"(uo2));
        g.prep(acc);
        sync_post(ws, l * 4 + 1, post, wid);
        Gate gt; gt.wait<SW0>(ws, l * 4 + 1, fq1 * 28 + fp, lane, ~0u, (1u << G1::H) - 1, (1u << (G1::H + GL1)) - 1, wid);
        gt.arm(7, cw);
        g.run_steps<false>(acc, [&](int it, int i) {
          if (i == 0) gt.head(it, cw, lane, wid);
          if (i == GL1 - 1) gt.late(ws, lane);
        });
        bar();
        sched0();
        if (GSH) {
          for_c<0, 2>([&](auto jc) {
            constexpr int jj = jc.value;
            f32x4 a[2];
#pragma unroll
            for (int i = 0; i < 2; ++i) { a[i] = gelu_arg(acc[i][jj]); asm volatile("" : "+v"(a[i])); }
            sched0();
            for_c<0, 4>([&](auto ec) { constexpr int e = ec.value; g.mm<jj * 8 + e>(acc); sched0(); a[0][e] = __builtin_amdgcn_exp2f(a[0][e]); a[1][e] = __builtin_amdgcn_exp2f(a[1][e]); asm volatile("" : "+v"(a[0]), "+v"(a[1])); sched0(); });
            a[0] += 1.f; a[1] += 1.f; asm volatile("" : "+v"(a[0]), "+v"(a[1]));
            sched0();
            for_c<0, 4>([&](auto ec) { constexpr int e = ec.value; g.mm<jj * 8 + 4 + e>(acc); sched0(); a[0][e] = __builtin_amdgcn_rcpf(a[0][e]); a[1][e] = __builtin_amdgcn_rcpf(a[1][e]); asm volatile("" : "+v"(a[0]), "+v"(a[1])); sched0(); });
            st16(ro, uo[jj], pack_pair(acc[0][jj] * a[0], acc[1][jj] * a[1]));
          });
        } else g.pend<0, 8>(acc);
#pragma unroll
        for (int n = GSH ? 2 : 0; n < 8; ++n) { const int i = n >> 2, jj = n & 3; st16(ro, (i ? uo2 : uo)[jj], pack_pair(gelu_tanh(acc[2 * i][jj]), gelu_tanh(acc[2 * i + 1][jj]))); }
      }
    }
    {
      PHASE_VARS
      asm volatile("" : "+s"(out));
      G2A::Comp<O44<true, ANE>> ga; ga.setup(wp, wq, lane);
#pragma unroll
      for (int jj = 0; jj < 4; ++jj) ga.qrd[0][jj] = 65536 + rd_km(lane, wq * 2 + (jj & 1)) + (((jj >> 1) ^ (tq >> 3)) & 1) * 8192;
      G2::Comp<O42<true, BNE>> g; g.setup(wp, wq, lane);
      g.setwin();
      ga.setwin();
      if (GSP == 1) sync_post(ws, l * 4 + 2, bid < 224 && lane < 32 ? (lane * 8 + fq1) * 64 + fp : -1, wid);
      f32x4 ac4[4][4], acc[4][2];
#pragma unroll
      for (int jj = 0; jj < 2; ++jj) { const float b = bf2f(*(const LAS u16*)(uintptr_t)(L_BIAS + 256 + (wq * 2 + jj) * 32 + fr * 2));
#pragma unroll
        for (int i = 0; i < 4; ++i) { ac4[i][jj] = (f32x4){b, b, b, b}; ac4[i][jj + 2] = (f32x4){0.f, 0.f, 0.f, 0.f}; } }
      unsigned xa = L_X + wid * 4096 + lane * 16, xo = (unsigned)((bid * 4 + wid) << 14) + ph(lane * 16), oo = x_off(tp, tq, wid, fr, fq), xo2 = xo + 4096, oo2 = oo + 32768;
      unsigned fo = lane ? MB : (((tq ^ 8) * 8 + tp) * 64 + 28 + wid) * 4u, one = 1u;
      bf16x8 s[2]; sel_frags(false, fr, fq, s); tile_add(ac4, s, xa);
      const rsrc_t rx = mkrsrc(ws + WS_XP, 16 * MB), rf = mkrsrc(ws + WS_MAIL + (l * 4 + 2) * (MEDGE * 4), MEDGE * 4), ro = mkrsrc(l ? (const void*)out : (const void*)(ws + WS_X2), S * D * 2);
      asm volatile("" : "+v"(xa), "+v"(xo), "+v"(oo), "+v"(xo2), "+v"(oo2), "+v"(fo), "+v"(one), "+v"(s[0]), "+v"(s[1]));
      ga.pin(ac4);
      if (GSP == 2) sync_post(ws, l * 4 + 2, bid < 224 && lane < 32 ? (lane * 8 + fq1) * 64 + fp : -1, wid);
      static_assert(JA % 2 == 0 && G2A::H == 2, "");
      Gate gt; gt.wait<GSP == 2 ? SW0 : SWW>(ws, l * 4 + 2, tq * 8 + tp, lane, 0xfffffffu, tq & 8 ? 1u << JA / 2 : 1u, tq & 8 ? 7u << JA / 2 : 7u, wid);
      gt.arm((JA - 1) / 3, cw);
      ga.run_steps<false>(ac4, [&](int it, int i) {
        if (i == 0) gt.head(it, cw, lane, wid);
        if (i == 2) gt.late(ws, lane);
      });
      ga.pend<4, 8>(ac4);
#pragma unroll
      for (int i = 0; i < 2; ++i)
#pragma unroll
        for (int jj = 0; jj < 2; ++jj) st16(rx, (jj ? xo2 : xo) + i * 2048, cat4(pk4(ac4[2 * i][jj + 2]), pk4(ac4[2 * i + 1][jj + 2])));
#pragma unroll
      for (int i = 0; i < 4; ++i) { acc[i][0] = ac4[i][0]; acc[i][1] = ac4[i][1]; }
#pragma unroll
      for (int k = 0; k < 2; ++k) { g.fp[2][k] = ga.fp[2][k]; g.fp[3][k] = ga.fp[3][k]; g.fq[0][k] = ga.fq[0][k]; g.fq[1][k] = ga.fq[1][k]; }
      sched0();
      g.run_steps<true>(acc, [&](int it, int i) {
        if (i == PS % 4 && __builtin_expect(it == (F / 64 - 2 * JA) / 4 - PS / 4, 0)) {
          wait_vm<0>();
          __builtin_amdgcn_raw_buffer_store_b32(one, rf, fo, 0, 16);
        }
      });
      bar();
      sched0();
      g.pend<0, 4>(acc);
      tile_add(acc, s, xa);
      u32x4 xr[4];
#pragma unroll
      for (int i = 0; i < 2; ++i)
#pragma unroll
        for (int jj = 0; jj < 2; ++jj) { xr[jj * 2 + i] = pack_pair(acc[2 * i][jj], acc[2 * i + 1][jj]); st16(ro, (jj ? oo2 : oo) + i * 64, xr[jj * 2 + i]); }
      if (l == 0) {
#pragma unroll
        for (int p = 0; p < 4; ++p) *(LAS u32x4*)(uintptr_t)(xa + p * 1024) = xr[p];
        if (NL == 1) sync_post(ws, 3, -1, wid);
      }
    }
  }
}

extern "C" void kernel_launch(void* const* d_in, const int* in_sizes, int n_in,
                              void* d_out, int out_size, void* d_ws, size_t ws_size,
                              hipStream_t stream) {
  (void)hipFuncSetAttribute((const void*)mega_kernel, hipFuncAttributeMaxDynamicSharedMemorySize, 163840);
  mega_kernel<<<256, 512, 163840, stream>>>((const u16*)d_in[0], (const u16*)d_in[1], (const u16*)d_in[2], (const u16*)d_in[4], (const u16*)d_in[6], (const u16*)d_in[5], (const u16*)d_in[7], (u16*)d_out, (char*)d_ws);
}
```

```cpp
#include <hip/hip_runtime.h>
#include <hip/hip_bf16.h>
#include <stdint.h>

typedef unsigned short u16;
typedef unsigned long long u64;
typedef short bf16x8 __attribute__((ext_vector_type(8)));
typedef short bf16x4 __attribute__((ext_vector_type(4)));
typedef float f32x4 __attribute__((ext_vector_type(4)));
typedef float f32x2 __attribute__((ext_vector_type(2)));
typedef unsigned u32x4 __attribute__((ext_vector_type(4)));
typedef unsigned u32x2 __attribute__((ext_vector_type(2)));
typedef __bf16 bf16v2 __attribute__((ext_vector_type(2)));
#define LAS __attribute__((address_space(3)))
#define GAS __attribute__((address_space(1)))
#define DEVI __device__ __forceinline__
typedef __attribute__((address_space(1))) unsigned gu32;
typedef __attribute__((address_space(1))) unsigned long long gu64;

constexpr int S = 2048, D = 1024, F = 3584;
constexpr float EPS = 1e-5f;

DEVI float bf2f(u16 v) { return __uint_as_float(((unsigned)v) << 16); }
DEVI float bflo(unsigned v) { return __uint_as_float(v << 16); }
DEVI float bfhi(unsigned v) { return __uint_as_float(v & 0xffff0000u); }
DEVI unsigned pk2(float lo, float hi) { bf16v2 p; p.x = (__bf16)lo; p.y = (__bf16)hi; return __builtin_bit_cast(unsigned, p); }

DEVI const void* uni_ptr(const void* p) {
  const u64 v = (u64)p;
  return (const void*)(((u64)(unsigned)__builtin_amdgcn_readfirstlane((unsigned)(v >> 32)) << 32) | (unsigned)__builtin_amdgcn_readfirstlane((unsigned)v));
}
DEVI void dma16(const void* sbase, unsigned voff, unsigned ldsaddr) {
  ldsaddr = __builtin_amdgcn_readfirstlane(ldsaddr);
  asm volatile("s_mov_b32 m0, %2\n\ts_nop 0\n\tglobal_load_lds_dwordx4 %0, %1 sc1" :: "v"(voff), "s"(sbase), "s"(ldsaddr) : "memory");
}
template <int N, int IMM> DEVI void dma_grp(const void* sbase, const unsigned* v, unsigned lb) {
  static_assert(N == 1 || N == 2 || N == 4, "");
  if constexpr (N == 4)
    asm volatile("s_add_u32 m0, %1, %2\n\ts_nop 0\n\tglobal_load_lds_dwordx4 %3, %0 sc1\n\tglobal_load_lds_dwordx4 %4, %0 offset:1024 sc1\n\t"
                 "global_load_lds_dwordx4 %5, %0 offset:2048 sc1\n\tglobal_load_lds_dwordx4 %6, %0 offset:3072 sc1"
                 :: "s"(sbase), "s"(lb), "i"(IMM), "v"(v[0]), "v"(v[1]), "v"(v[2]), "v"(v[3]) : "memory", "scc");
  else if constexpr (N == 2)
    asm volatile("s_add_u32 m0, %1, %2\n\ts_nop 0\n\tglobal_load_lds_dwordx4 %3, %0 sc1\n\tglobal_load_lds_dwordx4 %4, %0 offset:1024 sc1"
                 :: "s"(sbase), "s"(lb), "i"(IMM), "v"(v[0]), "v"(v[1]) : "memory", "scc");
  else
    asm volatile("s_add_u32 m0, %1, %2\n\ts_nop 0\n\tglobal_load_lds_dwordx4 %3, %0 sc1" :: "s"(sbase), "s"(lb), "i"(IMM), "v"(v[0]) : "memory", "scc");
}
template <int N> DEVI void wait_vm() { asm volatile("s_waitcnt vmcnt(%0)" :: "i"(N) : "memory"); }
DEVI void wait_lgkm0() { asm volatile("" ::: "memory"); __builtin_amdgcn_s_waitcnt(0xC07F); asm volatile("" ::: "memory"); }
DEVI void mfma_ip(f32x4& c, const bf16x8& a, const bf16x8& b) { asm volatile("v_mfma_f32_16x16x32_bf16 %0, %1, %2, %0" : "+v"(c) : "v"(a), "v"(b)); }
DEVI void mfma_gap() { __builtin_amdgcn_sched_barrier(0); asm volatile("s_nop 7"); __builtin_amdgcn_sched_barrier(0); }
DEVI void bar() { asm volatile("" ::: "memory"); __builtin_amdgcn_s_barrier(); asm volatile("" ::: "memory"); }
DEVI void sched0() { __builtin_amdgcn_sched_barrier(0); }

DEVI bf16x8 lds_b128(unsigned addr) { return *(const LAS bf16x8*)(uintptr_t)addr; }
DEVI bf16x4 lds_tr4(unsigned addr) { return __builtin_amdgcn_ds_read_tr16_b64_v4i16((LAS bf16x4*)(uintptr_t)addr); }

typedef __amdgpu_buffer_rsrc_t rsrc_t;
DEVI rsrc_t mkrsrc(const void* p, unsigned bytes) { return __builtin_amdgcn_make_buffer_rsrc((void*)p, 0, bytes, 0x00020000); }
DEVI void st16(rsrc_t r, unsigned off, u32x4 v) { __builtin_amdgcn_raw_buffer_store_b128(v, r, off, 0, 16); }
DEVI void ld_raw(u32x4& v, rsrc_t r, unsigned off) { asm volatile("buffer_load_dwordx4 %0, %1, %2, 0 offen sc1 ; raw load" : "=&v"(v) : "v"(off), "s"(r) : "memory"); }
DEVI void ld_again(u32x4& v, rsrc_t r, unsigned off) { asm volatile("buffer_load_dwordx4 %0, %1, %2, 0 offen sc1\n\ts_waitcnt vmcnt(0)" : "=&v"(v) : "v"(off), "s"(r) : "memory"); }
DEVI void ld_ok(u32x4& v) { asm volatile("; raw ok %0" : "+v"(v)); }
#define RLX_AGENT __ATOMIC_RELAXED, __HIP_MEMORY_SCOPE_AGENT

template <int N> DEVI float ror16(float v) { return __int_as_float(__builtin_amdgcn_update_dpp(0, __float_as_int(v), 0x120 + N, 0xf, 0xf, false)); }
DEVI float red16(float v) { v += ror16<8>(v); v += ror16<4>(v); v += ror16<2>(v); v += ror16<1>(v); return v; }
DEVI void xch32(float v, float& a, float& b) { unsigned u = __float_as_uint(v); auto r = __builtin_amdgcn_permlane32_swap(u, u, false, false); unsigned x = r[0], y = r[1]; a = __uint_as_float(x); b = __uint_as_float(y); }
DEVI void xch16(float v, float& a, float& b) { unsigned u = __float_as_uint(v); auto r = __builtin_amdgcn_permlane16_swap(u, u, false, false); unsigned x = r[0], y = r[1]; a = __uint_as_float(x); b = __uint_as_float(y); }
DEVI void swp32(float& a, float& b) { auto r = __builtin_amdgcn_permlane32_swap(__float_as_uint(a), __float_as_uint(b), false, false); unsigned x = r[0], y = r[1]; a = __uint_as_float(x); b = __uint_as_float(y); }
DEVI void swp16(float& a, float& b) { auto r = __builtin_amdgcn_permlane16_swap(__float_as_uint(a), __float_as_uint(b), false, false); unsigned x = r[0], y = r[1]; a = __uint_as_float(x); b = __uint_as_float(y); }
DEVI float colsum4(float v) { float a, b; xch32(v, a, b); v = a + b; xch16(v, a, b); return a + b; }
DEVI float vmax(float a, float b) { float r; asm("v_max_f32 %0, %1, %2" : "=v"(r) : "v"(a), "v"(b)); return r; }
DEVI float vmin3(float a, float b, float c) { float r; asm("v_min3_f32 %0, %1, %2, %3" : "=v"(r) : "v"(a), "v"(b), "v"(c)); return r; }
DEVI float vmax3(float a, float b, float c) { float r; asm("v_max3_f32 %0, %1, %2, %3" : "=v"(r) : "v"(a), "v"(b), "v"(c)); return r; }

DEVI unsigned src_kc(int c, int ld) { int row = c >> 3, ch = (c & 7) ^ ((row >> 1) & 7); return (unsigned)(row * ld * 2 + ch * 16); }
DEVI int km_f(int k) { return ((k >> 1) & 1) | (((k >> 3) & 1) << 1); }
DEVI unsigned ph(unsigned x) { return ((x >> 7) << 8) | (x & 127u); }
DEVI unsigned rd_kc(int lane, int ks) { int r = lane & 15, g = lane >> 4; return (unsigned)(r * 128 + (((ks * 4 + g) ^ (r >> 1)) << 4)); }
DEVI unsigned rd_km(int lane, int ct) {
  int g = lane >> 4, q = (lane & 15) >> 2, p = lane & 3;
  int f = ((q >> 1) & 1) | ((g & 1) << 1);
  return (unsigned)((ct >> 2) * 8192 + (8 * g + q) * 128 + (((ct & 3) ^ f) << 5) + 8 * p);
}


constexpr unsigned CODE_BYTES = 53248;
DEVI void code_warm(int part, int lane, unsigned scratch) {
  unsigned long long pc;
  asm volatile("s_getpc_b64 s[40:41]\n\ts_add_u32 s40, s40, mega_kernel@rel32@lo+4\n\ts_addc_u32 s41, s41, mega_kernel@rel32@hi+12\n\ts_mov_b64 %0, s[40:41]" : "=s"(pc) :: "s40", "s41", "scc");
  const unsigned off = (unsigned)(part * 64 + lane) * 128u;
  scratch = __builtin_amdgcn_readfirstlane(scratch);
  if (off < CODE_BYTES) asm volatile("s_nop 3\n\ts_mov_b32 m0, %2\n\ts_nop 0\n\tglobal_load_lds_dword %0, %1" :: "v"(off), "s"(pc), "s"(scratch) : "memory");
}

DEVI unsigned src_km(int lt, int ld) { const int k = lt >> 3; return (unsigned)(k * ld * 2 + (((lt & 7) ^ (km_f(k) << 1)) << 4)); }
DEVI void touch4(const void* sbase, unsigned voff, unsigned ldsaddr) {
  ldsaddr = __builtin_amdgcn_readfirstlane(ldsaddr); sbase = uni_ptr(sbase);
  asm volatile("s_nop 3\n\ts_mov_b32 m0, %2\n\ts_nop 0\n\tglobal_load_lds_dword %0, %1" :: "v"(voff), "s"(sbase), "s"(ldsaddr) : "memory");
}

template <int IMM> DEVI void spf1(const void* base, unsigned soff) { asm volatile("s_load_dword s101, %0, %1 offset:%2" :: "s"(base), "s"(soff), "i"(IMM) : "s101"); }
template <int IMM = 0> DEVI void spf2(const void* base, unsigned soff) { asm volatile("s_load_dwordx2 s[100:101], %0, %1 offset:%2" :: "s"(base), "s"(soff), "i"(IMM + 60) : "s100", "s101"); }
template <int ST> DEVI void spf2x6(const void* base, unsigned soff) {
  asm volatile("s_load_dwordx2 s[100:101], %0, %1 offset:%2\n\ts_load_dwordx2 s[100:101], %0, %1 offset:%3\n\ts_load_dwordx2 s[100:101], %0, %1 offset:%4\n\t"
               "s_load_dwordx2 s[100:101], %0, %1 offset:%5\n\ts_load_dwordx2 s[100:101], %0, %1 offset:%6\n\ts_load_dwordx2 s[100:101], %0, %1 offset:%7"
               :: "s"(base), "s"(soff), "i"(60), "i"(60 + ST), "i"(60 + 2 * ST), "i"(60 + 3 * ST), "i"(60 + 4 * ST), "i"(60 + 5 * ST) : "s100", "s101");
}
template <int ST> DEVI void spf1x12(const void* base, unsigned soff) {
  spf1<0>(base, soff); spf1<ST>(base, soff); spf1<2 * ST>(base, soff); spf1<3 * ST>(base, soff); spf1<4 * ST>(base, soff); spf1<5 * ST>(base, soff);
  spf1<6 * ST>(base, soff); spf1<7 * ST>(base, soff); spf1<8 * ST>(base, soff); spf1<9 * ST>(base, soff); spf1<10 * ST>(base, soff); spf1<11 * ST>(base, soff);
}
DEVI int lgkm_out() { int n; asm volatile("s_getreg_b32 %0, hwreg(HW_REG_IB_STS, 8, 4)" : "=s"(n)); return n; }
DEVI int slow_res(const void* w) { return (3 - ((unsigned)(uintptr_t)w >> 7)) & 7; }

constexpr int WNE = 16, ANE = 24, BNE = 13, QNE = 46;
struct Plan { int n; signed char q[40], idx[40], k[40], h[40], g[40]; };
template <class O, bool PKM, bool QKM> constexpr Plan make_plan() {
  Plan p{};
  int nr = 0;
  for (int c = 0; c < O::NRO; ++c) nr += ((O::ro[c] >> 4) ? QKM : PKM) ? 4 : 2;
  int prev = 0;
  for (int c = 0; c < O::NRO; ++c) {
    const int q = O::ro[c] >> 4, i = O::ro[c] & 15;
    int av[2] = {0, 0}, k0 = -1;
    for (int m = 0; m < 2 * O::NP; ++m) {
      const int mm = (m + 2 * O::NPEND) % (2 * O::NP);
      if ((q ? O::pj[m >> 1] : O::pi[m >> 1]) == i && m < 2 * O::NPEND) av[((m >> 1) ^ m) & 1] = m;
      if ((q ? O::pj[mm >> 1] : O::pi[mm >> 1]) == i && k0 < 0) k0 = ((mm >> 1) ^ mm) & 1;
    }
    for (int kk = 0; kk < 2; ++kk)
      for (int h = 0; h < ((q ? QKM : PKM) ? 2 : 1); ++h) {
        const int k = k0 ^ kk;
        int g = p.n * O::NE / nr;
        if (g < av[k]) g = av[k];
        if (g < prev + (p.n && O::NE >= nr ? 1 : 0)) g = prev + (p.n && O::NE >= nr ? 1 : 0);
        p.q[p.n] = q; p.idx[p.n] = i; p.k[p.n] = k; p.h[p.n] = h; p.g[p.n] = g; prev = g; ++p.n;
      }
  }
  return p;
}
struct OBase { static constexpr int pidx(int i) { return i; } static constexpr int poff(int) { return 0; } static constexpr int qoff(int j) { return j * 2048; } static constexpr bool swp(int) { return false; } };
template <bool PF, int NE_> struct O44 : OBase {
  static constexpr int TP = 4, TQ = 4, NPA = 4, NP = 16, NPEND = 8, NRO = 8, NE = NE_;
  static constexpr signed char pi[16] = {2, 3, 3, 2, 2, 3, 3, 2, 0, 1, 1, 0, 0, 1, 1, 0}, pj[16] = {0, 0, 1, 1, 2, 2, 3, 3, 0, 0, 1, 1, 2, 2, 3, 3};
  static constexpr signed char ro[8] = {0, PF ? 1 : 16, PF ? 16 : 1, 17, 18, 19, 2, 3};
};
template <bool PF, int NE_> struct O42 : OBase {
  static constexpr int TP = 4, TQ = 2, NPA = 4, NP = 8, NPEND = 4, NRO = 6, NE = NE_;
  static constexpr signed char pi[8] = {2, 3, 3, 2, 0, 1, 1, 0}, pj[8] = {0, 0, 1, 1, 0, 0, 1, 1};
  static constexpr signed char ro[6] = {0, PF ? 1 : 16, PF ? 16 : 1, 17, 3, 2};
};
template <int TH> struct OQ {
  static constexpr int TP = 6, TQ = 5, NPA = 2, NP = 26, NPEND = 13, NRO = 11, NE = QNE;
  static constexpr signed char pi[26] = {3, 4, 5, 5, 4, 3, 3, 4, 5, 5, TH ? 5 : 3, 4, TH ? 3 : 4, 0, 1, 2, 2, 1, 0, 0, 1, 2, 2, TH ? 2 : 0, 1, TH ? 0 : 1};
  static constexpr signed char pj[26] = {0, 0, 0, 1, 1, 1, 2, 2, 2, TH ? 4 : 3, 3, 3, TH ? 3 : 4, 0, 0, 0, 1, 1, 1, 2, 2, 2, TH ? 4 : 3, 3, 3, TH ? 3 : 4};
  static constexpr signed char ro[11] = {0, 1, 16, 2, 17, 18, TH ? 20 : 19, TH ? 19 : 20, 3, 4, 5};
  static constexpr int pidx(int i) { return i / 3; }
  static constexpr int poff(int i) { return i % 3 * 8192; }
  static constexpr int qoff(int j) { return j == 4 ? 0 : (1 + 4 * TH + j) * 2048; }
  static constexpr bool swp(int i) { return i % 3 == 2; }
};
template <bool PKM, bool QKM, int PB, int QB, int XB, int NS, int NT, bool WISP, int BASE, int ROT, int WLD, int WSUB, int ASTEP, int RLX = 0, bool QF = false>
struct Gemm {
  static constexpr int SB = PB + QB + XB, CNT = (PB + QB) / 4096 + (XB ? 1 : 0);
  static constexpr int PO = QF ? QB : 0, QO = QF ? 0 : PB, WB = WISP ? PB : QB, AB = WISP ? QB : PB, WOFF = WISP ? PO : QO, AOFF = WISP ? QO + XB : PO;
  static constexpr int XL = XB / 64, H = NS - 1 - RLX, CA = AB / 4096 + (XB ? 1 : 0), WSTEP = 64 * WLD * 2;
  static_assert(H * CNT <= 63 && BASE + NS * SB <= 163840, "");
  struct Loader {
    static constexpr int NW = WB / 4096, NA = AB / 4096;
    const char* w; const char* ab; unsigned wv[NW], av[NA], xv, lwb, lab, lx; int rot, islot, t; bool xl;
    DEVI Loader(const void* w_, const void* ab_, int lw, int lane, int rot_ = 0)
        : w((const char*)w_ - 3072), ab((const char*)ab_ - 3072), lwb(BASE + WOFF + lw * (NW == 6 ? 2048 : WB / 4)), lab(BASE + AOFF + lw * (AB / 4)), lx(BASE + PB + lw * (XL * 16)),
          rot(rot_), islot((H + ROT) % NS), t(0), xl(lane < XL) {
      const unsigned w0 = (lane >> 3) * (WLD * 2), f0 = (lane & 7) ^ ((lane >> 3) & 2);
#pragma unroll
      for (int i = 0; i < NW; ++i) {
        const unsigned p = NW == 6 ? (i >> 1) * 8 + lw * 2 + (i & 1) : lw * NW + i;
        wv[i] = w0 + ((f0 ^ ((i & 1) << 2)) << 4) + (NW == 6 ? (i >> 1) * WSUB + (lw * 2 + (i & 1)) * (8 * WLD * 2) - (i & 1) * 1024 : (p >> 3) * WSUB + (p & 7) * (8 * WLD * 2) - i * 1024) + 3072;
        asm volatile("" : "+v"(wv[i]));
      }
      asm volatile("" : "+s"(w), "+s"(lwb));
      if (XB) asm volatile("" : "+s"(rot));
      else seta(0, 0, lw, lane);
    }
    DEVI void seta(unsigned a0, unsigned xv_, int lw, int lane) {
      xv = xv_ + 3072;
#pragma unroll
      for (int i = 0; i < NA; ++i) { const unsigned p = lw * NA + i; av[i] = (XB ? a0 + src_kc(p * 64 + lane, D) : p * 2048 + ph(lane * 16)) + 3072 - i * 1024; asm volatile("" : "+v"(av[i])); }
      asm volatile("" : "+s"(ab), "+s"(lab));
      if (XB) { asm volatile("" : "+v"(xv)); asm volatile("" : "+s"(lx)); }
    }
    DEVI int kord(int n) const { if constexpr (XB != 0) return (n + rot) & (NT - 1); else return n; }
    template <int SO> DEVI void issue_w(const char* p, unsigned so = 0) const {
      if constexpr (NW == 6) { dma_grp<2, SO>(p, wv, lwb + so); dma_grp<2, SO + 8192>(p, wv + 2, lwb + so); dma_grp<2, SO + 16384>(p, wv + 4, lwb + so); }
      else dma_grp<NW, SO>(p, wv, lwb + so);
    }
    template <int SO> DEVI void issue_a(const char* p, unsigned so = 0) const {
      if (XB) { if (xl) dma_grp<1, SO>(p, &xv, lx + so); }
      dma_grp<NA, SO>(p, av, lab + so);
    }
    template <int S0, bool W, bool A> DEVI void pre() const {
      if constexpr (S0 < H) {
        if (W) issue_w<((S0 + ROT) % NS) * SB>(w + (kord(0) + S0) * WSTEP);
        if (A) issue_a<((S0 + ROT) % NS) * SB>(ab + (kord(0) + S0) * ASTEP);
        pre<S0 + 1, W, A>();
      }
    }
    template <int T> DEVI void pre_w1() const { issue_w<((T + ROT) % NS) * SB>(w + (kord(0) + T) * WSTEP); sched0(); }
    DEVI void pre_w() const { pre<0, true, false>(); sched0(); }
    template <int T> DEVI void peel() const {
      if constexpr (T < H - 1) {
        wait_vm<(H - 1 - T) * CA + T * CNT>();
        bar();
        issue_w<((T + H + ROT) % NS) * SB>(w + (T + H) * WSTEP); issue_a<((T + H + ROT) % NS) * SB>(ab + (T + H) * ASTEP);
        peel<T + 1>();
      }
    }
    DEVI void start() {
      sched0();
      bar();
      pre<0, false, true>();
      sched0();
      if constexpr (H == 2) wait_vm<CA>();
      else { peel<0>(); islot = (2 * H - 1 + ROT) % NS; t = H - 1; }
    }
    template <class HK> DEVI void run(int tend, HK side) {
#pragma unroll 1
      for (; t < tend; ++t) {
        const char* wk = w + (t + H) * WSTEP; const char* ak = ab + (t + H) * ASTEP;
        unsigned lw_ = lwb + islot * SB, la_ = lab + islot * SB;
        islot = islot + 1 == NS ? 0 : islot + 1;
        asm volatile("" : "+s"(wk), "+s"(ak), "+s"(lw_), "+s"(la_));
        wait_vm<(H - 1) * CNT>();
        bar();
        dma_grp<NW, 0>(wk, wv, lw_);
        side(t);
        dma_grp<NA, 0>(ak, av, la_);
        sched0();
      }
    }
    template <int T, int X, class A, class B, class C> DEVI void step(A pre_, B side, C post) const {
      const int k = kord(T + H);
      const char* wk = w + k * WSTEP; const char* ak = ab + k * ASTEP;
      asm volatile("" : "+s"(wk), "+s"(ak));
      wait_vm<(H - 1) * CNT + X>();
      bar();
      pre_(BASE + ((T + ROT) % NS) * SB);
      issue_w<((T + H + ROT) % NS) * SB>(wk);
      sched0();
      side();
      sched0();
      issue_a<((T + H + ROT) % NS) * SB>(ak);
      post();
      sched0();
    }
    template <int I> DEVI void tail() const { if constexpr (I >= 0) { sched0(); wait_vm<I * CNT>(); bar(); tail<I - 1>(); } }
  };
  static constexpr int NWIN = (NS * SB + 65535) / 65536;
  template <class O> struct Comp {
    static constexpr int TP = O::TP, TQ = O::TQ, NM = 2 * O::NP, NPA = PKM ? O::NPA : 2, NQA = QKM ? TQ : 2;
    static constexpr Plan PL = make_plan<O, PKM, QKM>();
    unsigned prd[NWIN][NPA], qrd[NWIN][NQA];
    bf16x8 fp[TP][2], fq[TQ][2]; bf16x4 lo;
    DEVI void setwin() {
#pragma unroll
      for (int w = 1; w < NWIN; ++w) {
#pragma unroll
        for (int i = 0; i < NPA; ++i) { prd[w][i] = prd[0][i] + w * 65536; asm volatile("" : "+v"(prd[w][i])); }
#pragma unroll
        for (int i = 0; i < NQA; ++i) { qrd[w][i] = qrd[0][i] + w * 65536; asm volatile("" : "+v"(qrd[w][i])); }
      }
#pragma unroll
      for (int i = 0; i < NPA; ++i) asm volatile("" : "+v"(prd[0][i]));
#pragma unroll
      for (int i = 0; i < NQA; ++i) asm volatile("" : "+v"(qrd[0][i]));
    }
    DEVI void setup(int wp, int wq, int lane) {
      const unsigned b = BASE;
      if (PKM) {
#pragma unroll
        for (int i = 0; i < TP; ++i) prd[0][i] = b + rd_km(lane, wp * TP + i);
      } else { prd[0][0] = b + rd_kc(lane, 0) + wp * TP * 2048; prd[0][1] = b + rd_kc(lane, 1) + wp * TP * 2048; }
      if (QKM) {
#pragma unroll
        for (int j = 0; j < TQ; ++j) qrd[0][j] = b + rd_km(lane, wq * TQ + j);
      } else { qrd[0][0] = b + rd_kc(lane, 0) + wq * TQ * 2048; qrd[0][1] = b + rd_kc(lane, 1) + wq * TQ * 2048; }
    }
    template <int C, int N> DEVI static unsigned wa(const unsigned (&b)[NWIN][N], int i) { return b[C >> 16][i] + (C & 0xffff); }
    template <int SLOT, int R> DEVI void rd() {
      constexpr int q = PL.q[R], i = PL.idx[R], k = PL.k[R], h = PL.h[R];
      bf16x8& f = q ? fq[q ? i : 0][k] : fp[q ? 0 : i][k];
      if constexpr (q ? QKM : PKM) {
        unsigned a;
        if constexpr (q) a = wa<SLOT * SB + QO + k * 4096 + h * 512, NQA>(qrd, i); else a = wa<SLOT * SB + PO + O::poff(i) + k * 4096 + h * 512, NPA>(prd, O::pidx(i));
        const bf16x4 x = lds_tr4(a);
        if constexpr (h) f = __builtin_shufflevector(lo, x, 0, 1, 2, 3, 4, 5, 6, 7); else lo = x;
      } else if constexpr (q) f = lds_b128(wa<SLOT * SB + QO + O::qoff(i), 2>(qrd, k));
      else f = lds_b128(wa<SLOT * SB + PO + i * 2048, 2>(prd, k));
    }
    template <int SLOT, int M, int R> DEVI void rds() { if constexpr (R < PL.n) { if constexpr (PL.g[R] == M) rd<SLOT, R>(); if constexpr (PL.g[R] <= M) rds<SLOT, M, R + 1>(); } }
    template <int M, bool IP = false> DEVI void mm(f32x4 (&acc)[TP][TQ]) {
      constexpr int i = O::pi[M >> 1], j = O::pj[M >> 1], k = ((M >> 1) ^ M) & 1;
      if constexpr (IP) { if constexpr (O::swp(i)) mfma_ip(acc[i][j], fq[j][k], fp[i][k]); else mfma_ip(acc[i][j], fp[i][k], fq[j][k]); }
      else if constexpr (O::swp(i)) acc[i][j] = __builtin_amdgcn_mfma_f32_16x16x32_bf16(fq[j][k], fp[i][k], acc[i][j], 0, 0, 0);
      else acc[i][j] = __builtin_amdgcn_mfma_f32_16x16x32_bf16(fp[i][k], fq[j][k], acc[i][j], 0, 0, 0);
    }
    template <int A, int B> DEVI void pend(f32x4 (&acc)[TP][TQ]) { if constexpr (A < B) { mm<2 * A>(acc); mm<2 * A + 1>(acc); pend<A + 1, B>(acc); } }
    template <int SLOT, bool FIRST, int M> DEVI void body(f32x4 (&acc)[TP][TQ]) {
      if constexpr (M < NM) {
        if constexpr (!FIRST || M >= 2 * O::NPEND) { mm<M, true>(acc); sched0(); }
        rds<SLOT, M, 0>();
        sched0();
        body<SLOT, FIRST, M + 1>(acc);
      }
    }
    template <int SLOT, bool FIRST> DEVI void step(f32x4 (&acc)[TP][TQ]) { if (!RLX) wait_lgkm0(); bar(); sched0(); body<SLOT, FIRST, 0>(acc); }
    template <int I, int N, int T0, class MID> DEVI void steps(f32x4 (&acc)[TP][TQ], MID mid) {
      if constexpr (I < N) { mid(I); step<(I + T0 + ROT) % NS, false>(acc); steps<I + 1, N, T0>(acc, mid); }
    }
    DEVI void prep(f32x4 (&acc)[TP][TQ]) { setwin(); pin(acc); }
    DEVI void pin(f32x4 (&acc)[TP][TQ]) {
#pragma unroll
      for (int p = 0; p < O::NP; ++p) asm volatile("" : "+v"(acc[O::pi[p]][O::pj[p]]));
    }
    template <bool CONT, class MID> DEVI void run_steps(f32x4 (&acc)[TP][TQ], MID mid) {
      constexpr int T0 = CONT ? 0 : 1;
      if constexpr (T0) { mid((NT - T0) / NS, -1); step<ROT % NS, true>(acc); }
#pragma unroll 1
      for (int itn = (NT - T0) / NS; itn > 0; --itn) {
        steps<0, NS, T0>(acc, [&](int i) { mid(itn, i); });
      }
      steps<0, (NT - T0) % NS, T0>(acc, [](int) {});
      wait_lgkm0();
      mfma_gap();
    }
  };
};

DEVI f32x4 gelu_arg(f32x4 u) { return u * (-2.302208198f + -0.1029432397f * (u * u)); }
DEVI f32x4 gelu_tanh(f32x4 u) {
  f32x4 a = gelu_arg(u);
#pragma unroll
  for (int e = 0; e < 4; ++e) a[e] = __builtin_amdgcn_exp2f(a[e]);
  a += 1.f;
#pragma unroll
  for (int e = 0; e < 4; ++e) a[e] = __builtin_amdgcn_rcpf(a[e]);
  return u * a;
}

template <int tilesP, int tilesQ, int gp, int gq> DEVI void tile_map(unsigned bid, int& tp, int& tq) {
  const unsigned xcd = bid & 7, idx = bid >> 3;
  constexpr unsigned gP = tilesP / gp, gQ = tilesQ / gq;
  static_assert(gP * gQ == 8, "");
  tp = (xcd % gP) * gp + idx % gp;
  tq = (xcd / gP) * gq + idx / gp;
}

template <int V> struct IC { static constexpr int value = V; };
template <int A, int B, class FN> DEVI void for_c(FN f) { if constexpr (A < B) { f(IC<A>{}); for_c<A + 1, B>(f); } }
DEVI float sel(bool c, float a, float b) { return c ? a : b; }
DEVI u32x2 pk4(const f32x4& v) { return (u32x2){pk2(v[0], v[1]), pk2(v[2], v[3])}; }
DEVI u32x4 cat4(u32x2 a, u32x2 b) { return (u32x4){a.x, a.y, b.x, b.y}; }
DEVI bf16x8 cat(u32x2 a, u32x2 b) { return __builtin_bit_cast(bf16x8, cat4(a, b)); }
DEVI f32x4 mfma(const bf16x8& a, const bf16x8& b, const f32x4& c) { return __builtin_amdgcn_mfma_f32_16x16x32_bf16(a, b, c, 0, 0, 0); }
DEVI u32x4 pack_pair(const f32x4& a, const f32x4& b) {
  const unsigned ax = pk2(a[0], a[1]), ay = pk2(a[2], a[3]), bx = pk2(b[0], b[1]), by = pk2(b[2], b[3]);
  auto rx = __builtin_amdgcn_permlane16_swap(ax, bx, false, false);
  auto ry = __builtin_amdgcn_permlane16_swap(ay, by, false, false);
  const unsigned x0 = rx[0], x1 = rx[1], y0 = ry[0], y1 = ry[1];
  return (u32x4){x0, y0, x1, y1};
}
DEVI int pair_chunk(int fq) { return ((fq & 1) << 1) | (fq >> 1); }
DEVI void sel_frags(bool paired, int fr, int fq, bf16x8 (&s)[2]) {
  const unsigned one = fr & 1 ? 0x3F800000u : 0x3F80u;
#pragma unroll
  for (int i = 0; i < 2; ++i) {
    const bool on = paired ? (fq & 1) == i && (fr >> 3) == (fq >> 1) : (fr >> 2) == fq;
    const int d = paired ? (fr & 7) >> 1 : i * 2 + ((fr >> 1) & 1);
    u32x4 v;
#pragma unroll
    for (int e = 0; e < 4; ++e) v[e] = on && d == e ? one : 0u;
    s[i] = __builtin_bit_cast(bf16x8, v);
  }
}
template <int TQ> DEVI void tile_add(f32x4 (&acc)[4][TQ], const bf16x8 (&s)[2], unsigned a) {
  bf16x8 t[4];
#pragma unroll
  for (int p = 0; p < 4; ++p) t[p] = lds_b128(a + p * 1024);
#pragma unroll
  for (int p = 0; p < 4; ++p)
#pragma unroll
    for (int i = 0; i < 2; ++i) acc[(p & 1) * 2 + i][p >> 1] = mfma(s[i], t[p], acc[(p & 1) * 2 + i][p >> 1]);
}
template <int C> DEVI float dppf(float v) { return __int_as_float(__builtin_amdgcn_update_dpp(0, __float_as_int(v), C, 0xf, 0xf, false)); }
DEVI float red8(float v) { v += dppf<0xB1>(v); v += dppf<0x4E>(v); v += dppf<0x141>(v); return v; }
DEVI float red4(float v) { v += dppf<0xB1>(v); v += dppf<0x4E>(v); return v; }

constexpr unsigned SENT = 0xBF800000u;
constexpr int SPIN_MAX = 1 << 21;

constexpr unsigned MB = 1u << 20;
constexpr unsigned WS_O = 0, WS_H = 8 * MB, WS_U = 16 * MB, WS_X2 = 32 * MB, WS_XP = 40 * MB  ;
constexpr unsigned WS_ST = 120 * MB, ST_CS = 2 * S * 64, WS_XCH = 122 * MB  , WS_EPOCH = 123 * MB, WS_ABORT = 123 * MB + 256  ;
DEVI unsigned tag2(unsigned t) { return t * 0x9E3779B1u ^ 0x5BD1E995u; }
constexpr int MEDGE = 256 * 64;
constexpr unsigned WS_MAIL = 124 * MB;

constexpr int NL = 2, QBR = 3, FLS = 3, NPL = 6, SPON = 1, PC0 = 0, PGAP = 3, WSP = 2, SWW = 0, GSH = 1, GSP = 2, SSC = 1, RB = 1, SI = 2, LPR = 1, CPR = 0, GLW = 4, GL1 = 4, SW0 = 0, SW1 = 6, XD = 3, XS = 4, JA = 22, PS = 3, TBA = 3, TC1 = 12, NW1 = 9, EW = 4, ET = 8, SPW = 1, PA_1 = 3, PA_2 = 3;
constexpr int L_X = 0, L_TAB = 16384, L_CS = L_TAB + 1152, L_TAG = L_CS + 768, L_BIAS = L_TAG + 128, L_SCR = L_BIAS + 512  , L_QKV = 20480, L_GATE = 32512;
static_assert(L_SCR + 1536 <= L_QKV, "");
typedef Gemm<true, false, 24576, 16384, 2048, 3, D / 64, true, 32768, 0, 3 * D, 2 * D, 128> GQ;
typedef Gemm<true, false, 16384, 8192, 0, 5, D / 64, true, 40960, 2, D, 128, 16384, 1, true> GW;
typedef Gemm<true, true, 16384, 16384, 0, 4, S / 64, true, 32768, 1, F, 128, 32768, 0, true> G1;
typedef Gemm<false, true, 16384, 16384, 0, 3, JA, false, 65536, (3 - JA % 3) % 3, S, 1024, 32768> G2A;
typedef Gemm<false, true, 16384, 8192, 0, 4, F / 64 - 2 * JA, false, 65536, 0, S, 128, 32768> G2;

DEVI unsigned get_tag() { return __builtin_amdgcn_readfirstlane(*(volatile LAS unsigned*)(uintptr_t)L_TAG) + 1u; }
DEVI bool spin_fail(int spin, char* ws) {
  if ((spin & 255) != 255) return false;
  gu32* ab = (gu32*)(ws + WS_ABORT);
  const unsigned tag = get_tag();
  if (spin >= SPIN_MAX) { __hip_atomic_store(ab, tag, RLX_AGENT); return true; }
  return __builtin_amdgcn_readfirstlane(__hip_atomic_load(ab, RLX_AGENT)) == tag;
}
DEVI void sync_post(char* ws, int edge, int post, int wid) {
  unsigned o = post >= 0 && wid == 0 ? (post >> 6) * 256u : MB, bit = 1u << (post & 31);
  const rsrc_t r = mkrsrc(ws + WS_MAIL + edge * (MEDGE * 4), MEDGE * 4);
  asm volatile("s_waitcnt vmcnt(0) lgkmcnt(0)" : "+v"(o), "+v"(bit) : "s"(r) : "memory");
  sched0();
  bar();
  if (wid == 0) asm volatile("buffer_atomic_or %0, %1, %2, 0 offen sc1" :: "v"(bit), "v"(o), "s"(r) : "memory");
  sched0();
}
#define POLL1(r) "s_waitcnt vmcnt(2)\n\tv_readfirstlane_b32 %[seen], " r "\n\tglobal_atomic_or " r ", %[z], %[z], %[p] sc0 sc1\n\t" \
  "s_andn2_b32 %[t], %[first], %[seen]\n\ts_cbranch_scc1 3f\n\ts_cmp_eq_u32 %[first], 0\n\ts_cbranch_scc1 4f\n\ts_barrier\n\ts_mov_b32 %[first], 0\n" \
  "4:\n\ts_andn2_b32 %[t], %[second], %[seen]\n\ts_cbranch_scc0 2f\n3:\n\t"
struct Gate {
  const char* p; unsigned miss, due, a, b, c;
  DEVI Gate() : p(nullptr), miss(0), due(0) { asm volatile("v_mov_b32 %0, 0\n\tv_mov_b32 %1, 0\n\tv_mov_b32 %2, 0" : "=v"(a), "=v"(b), "=v"(c)); }
  DEVI void ask() const { asm volatile("s_mov_b32 m0, %2\n\ts_nop 0\n\tglobal_load_lds_dword %0, %1 sc1" :: "v"(0u), "s"(p), "s"(L_GATE) : "memory"); }
  template <int DELAY> DEVI void wait(char* ws, int edge, int line, unsigned all, unsigned first, unsigned second, int wid) {
    sched0();
    p = ws + WS_MAIL + (edge * MEDGE + line * 64) * 4;
    second &= all;
    if (wid == 3) {
      due = 1;
      if (DELAY) __builtin_amdgcn_s_sleep(DELAY);
      int spin = 255;
#pragma unroll 1
      for (;; spin += 256) {
        asm volatile("" : "+s"(spin));
        unsigned seen, t, n;
        asm volatile("s_mov_b64 exec, 1\n\ts_waitcnt vmcnt(0)\n\tglobal_atomic_or %[a], %[z], %[z], %[p] sc0 sc1\n\ts_sleep %[gap]\n\t"
                     "global_atomic_or %[b], %[z], %[z], %[p] sc0 sc1\n\ts_sleep %[gap]\n\tglobal_atomic_or %[c], %[z], %[z], %[p] sc0 sc1\n\ts_movk_i32 %[n], 128\n"
                     "1:\n\t" POLL1("%[a]") POLL1("%[b]") POLL1("%[c]") "s_sub_u32 %[n], %[n], 1\n\ts_cbranch_scc0 1b\n"
                     "2:\n\ts_mov_b64 exec, -1 ; raw polls %[a] %[b] %[c]"
                     : [seen] "=&s"(seen), [t] "=&s"(t), [n] "=&s"(n), [first] "+&s"(first)
                     : [p] "s"(p), [second] "s"(second), [gap] "i"(PGAP), [a] "v"(a), [b] "v"(b), [c] "v"(c), [z] "v"(0u) : "memory", "scc");
        miss = ~seen & all;
        if (__builtin_expect(!(miss & second) && !first, 1)) break;
        if (spin_fail(spin, ws)) { if (first) bar(); miss = 0; break; }
      }
    } else bar();
  }
  int ev;
  DEVI void arm(int ni, int cw) { ev = miss ? ni : cw >= 0 ? 2 : 0; asm volatile("" : "+s"(ev), "+s"(miss), "+s"(due)); }
  DEVI void head(int it, int cw, int lane, int wid) {
    if (__builtin_expect(it == ev, 0)) {
      asm volatile("" : "+v"(lane)); asm volatile("" : "+s"(cw), "+s"(wid));
      if (miss) ask();
      if (it == 2) code_warm(cw, lane, L_SCR + wid * 256);
      ev = cw >= 0 && it > 2 ? 2 : 0;
      asm volatile("" : "+s"(ev));
    }
  }
  DEVI void late(char* ws) {
    if (__builtin_expect(due != 0, 0)) {
      asm volatile("" : "+s"(ws));
      asm volatile("s_waitcnt vmcnt(0) ; raw settled %0 %1 %2" :: "v"(a), "v"(b), "v"(c) : "memory");
      for (int spin = 0; miss; ++spin) {
        miss &= ~__builtin_amdgcn_readfirstlane(*(volatile LAS unsigned*)(uintptr_t)L_GATE);
        if (!miss || spin_fail(spin, ws)) break;
        ask();
        wait_vm<0>();
      }
      miss = due = 0;
      asm volatile("" : "+s"(miss), "+s"(due));
    }
  }
};
DEVI unsigned halo_off(int s0, int lw, int lane) {
  const int c = lw * 32 + (lane & 31), row = c >> 3, ch = (c & 7) ^ ((row >> 1) & 7), tok = s0 - 16 + row;
  return (unsigned)((tok < 0 ? 0 : tok) * D * 2 + ch * 16);
}
DEVI int qkv_rot(int bid) { return (bid & 3) * 4 + (bid & 4) * 2; }
DEVI unsigned x_off(int tp, int tq, int w, int fr, int fq) { return (unsigned)(((tq * 64 + (w >> 1) * 32 + fr) * D + tp * 128 + (w & 1) * 64 + pair_chunk(fq) * 8) * 2); }

DEVI void pfb_q(const u16* w, int bid, int j, int n) {
  const int r = slow_res(w);
  if ((r >> 2) == (bid & 1)) {
    const unsigned o = (unsigned)(((bid & 2) * 4 + r) * 128 + (j >> 1) * (3 * D * 2) + (j & 1) * 64 + ((n + qkv_rot(bid)) & 15) * GQ::WSTEP);
    spf1<0>(w, o); spf1<2048>(w, o); spf1<4096>(w, o);
  }
}
constexpr int PFR = NPL * 128 * 1024, PF_END = TBA * 256 + NW1 * PFR / (F * 2) / 8 * 8;
static_assert(TBA * 256 * F * 2 + NW1 * PFR <= S * F * 2 && 3 * PFR >= D * D * 2, "");
struct Pfc {
  const char* p; const char* q; int pc;
  DEVI Pfc(const u16* wo, const u16* w1, unsigned g) : p((const char*)wo + g * (NPL * 128)), q((const char*)w1 + TBA * 256 * F * 2 + g * (NPL * 128)), pc(PC0) {}
  DEVI void go() {
    unsigned t, u;
    asm volatile("s_getreg_b32 %2, hwreg(HW_REG_IB_STS, 8, 4)\n\ts_cmp_lt_u32 %1, %5\n\ts_cselect_b32 %3, 0, 1\n\ts_or_b32 %2, %2, %3\n\ts_cbranch_scc1 1f\n\t"
                 "s_load_dwordx2 s[100:101], %0, 60\n\ts_load_dwordx2 s[100:101], %0, 188\n\ts_load_dwordx2 s[100:101], %0, 316\n\t"
                 "s_load_dwordx2 s[100:101], %0, 444\n\ts_load_dwordx2 s[100:101], %0, 572\n\ts_load_dwordx2 s[100:101], %0, 700\n\t"
                 ".if %8 == 7\n\ts_load_dwordx2 s[100:101], %0, 828\n\t.endif\n\t"
                 "s_mov_b32 %2, %7\n\ts_cmp_lt_u32 %1, 2\n\ts_cselect_b32 %2, %6, %2\n\ts_mov_b64 s[40:41], %0\n\ts_add_u32 s40, s40, %2\n\ts_addc_u32 s41, s41, 0\n\t"
                 "s_cmp_eq_u32 %1, 2\n\ts_cselect_b64 %0, %4, s[40:41]\n\ts_add_u32 %1, %1, 1\n1:"
                 : "+s"(p), "+s"(pc), "=&s"(t), "=&s"(u) : "s"(q), "i"(3 + NW1), "i"((D * D * 2 - PFR) / 2), "i"(PFR), "i"(NPL) : "s40", "s41", "s100", "s101", "scc");
  }
};
DEVI void spare_touch(const u16* w1, const u16* w2, int bid, int wid, int lane) {
  if (!SPON) return;
  const int j = ((bid >> 3) - 28) * 8 + wid, fr = lane & 15, fq = lane >> 4;
  const unsigned scr = L_QKV + wid * 256;
#pragma unroll 1
  for (int r = PF_END + (j * 2 + ((bid >> 2) & 1)) * 4; r < S; r += 256) { if (fr < 14) touch4(w1 + (size_t)r * F + (bid & 3) * 896, fq * (F * 2) + fr * 128, scr); wait_vm<SPW>(); }
#pragma unroll 1
  for (int r = (j * 2 + (bid & 1)) * 4; r < JA * 64; r += 256) { touch4(w2 + (size_t)(r + (bid & 2) * JA * 32) * S + (bid & 4) * 256, fq * (S * 2) + fr * 128, scr); wait_vm<SPW>(); }
#pragma unroll 1
  for (int r = JA * 128 + ((31 - j) * 2 + (bid & 1)) * 8; r < F; r += 512) { touch4(w2 + (size_t)r * S + ((bid & 7) >> 1) * 512, (lane >> 3) * (S * 2) + (lane & 7) * 128, scr); wait_vm<SPW>(); }
}

template <int TH> DEVI void qkv_attn(char* ws, int l, int h, int lane, int hp, int sb, int cw, int bid, int tp, int tq, const u16* wqkv) {
  asm volatile("; half %1" : "+v"(lane) : "i"(TH));
  const int fr = lane & 15, fq = lane >> 4;
  GQ::Comp<OQ<TH>> g;
#pragma unroll
  for (int c = 0; c < 2; ++c) { g.prd[0][c] = 32768 + rd_km(lane, h * 2 + c); g.qrd[0][c] = 32768 + rd_kc(lane, c); }
  f32x4 acc[6][5];
#pragma unroll
  for (int i = 0; i < 6; ++i)
#pragma unroll
    for (int j = 0; j < 5; ++j) acc[i][j] = (f32x4){0.f, 0.f, 0.f, 0.f};
  const int par = fr & 1, da = fr - 4 * fq - par;
  const bool ca = da >= 0, cb = da >= 2;
  const unsigned tm = L_TAB + (16 + 64 * TH) * 4, tc = L_CS + h * 128;
  unsigned am = tm + fr * 4 - 320, a4 = tm + fq * 16 - 320, aa = tm + 576 + (4 * fq + par - (ca ? 0 : 16)) * 4, ab = tm + 576 + (4 * fq + par + 2 - (cb ? 0 : 16)) * 4;
  unsigned c4 = tc + fq * 16, c1 = tc + 512 + fr * 4, xb = L_QKV + h * 3072 + lane * 16, hm = par ? 0xffff0000u : 0xffffu, m0 = ca ? 0u : hm, m1 = cb ? 0u : hm;
  unsigned oo = ph((unsigned)((((sb * 2 + TH) * 16 + hp) << 13) + fr * 128 + (((h * 4 + pair_chunk(fq)) ^ (fr >> 1)) << 4)));
  asm volatile("" : "+v"(am), "+v"(a4), "+v"(aa), "+v"(ab), "+v"(c4), "+v"(c1), "+v"(xb), "+v"(hm), "+v"(m0), "+v"(m1), "+v"(oo));
  g.prep(acc);
  if (l) {
    const int sbc = (tq >> 1) + fq;
    sync_post(ws, 3, !TH && (lane < 16 || (lane < 32 && (tq & 1) && sbc < 16)) ? (sbc * 16 + fr) * 64 + (tq - 2 * sbc + 1) * 8 + tp : -1, TH * 2 + h);
  }
  Gate gt;
  if (l) gt.wait<SW0>(ws, 3, sb * 16 + hp, sb ? 0xffffffu : 0xffff00u, (sb ? 0x10101u : 0x10100u) << ((qkv_rot(bid) & 15) >> 1), 0x30303u << ((qkv_rot(bid) & 15) >> 1), TH * 2 + h);
  else if (!TH || !h) wait_vm<0>();
  else if ((slow_res(wqkv) >> 2) == (bid & 1)) {
    if (ET) __builtin_amdgcn_s_sleep(ET);
    if ((lane & 3) < 3 && lane < 40) touch4(wqkv, (unsigned)(((qkv_rot(bid) * 64 + 128 + (bid >> 3) * 10 + (lane >> 2)) & 1023) * (3 * D * 2) + (lane & 3) * (D * 2) + ((bid & 2) * 4 + slow_res(wqkv)) * 128), L_SCR + 768);
  }
  g.template run_steps<false>(acc, [&](int it, int i) {
    if (i < 0) gt.arm(it, cw);
        if (i == 0) gt.head(it, cw, lane, TH * 2 + h);
    if (TH && i == 1) gt.late(ws);
  });
  const rsrc_t ro = mkrsrc(ws + WS_O, S * D * 4);
  if (l == 0) bar();
  float mt[5], cv[2], rq[4], ra[4], rb[4]; f32x4 m4[5], cq[2], ck[2];
#pragma unroll
  for (int c = 0; c < 2; ++c) {
    ck[c] = *(const LAS f32x4*)(uintptr_t)(c4 + 256 + c * 64); cv[c] = *(const LAS float*)(uintptr_t)(c1 + c * 64);
    cq[c] = *(const LAS f32x4*)(uintptr_t)(c4 + c * 64);
  }
#pragma unroll
  for (int j = 4; j >= 0; --j) {
    const int o = 320 + (j == 4 ? -64 - 256 * TH : j * 64);
    if (j < 4 || !TH) mt[j] = *(const LAS float*)(uintptr_t)(am + o);
    if (j < 4 || TH) m4[j] = *(const LAS f32x4*)(uintptr_t)(a4 + o);
  }
#pragma unroll
  for (int j = 0; j < 4; ++j) { rq[j] = *(const LAS float*)(uintptr_t)(am + 896 + j * 64); ra[j] = *(const LAS float*)(uintptr_t)(aa + j * 64); rb[j] = *(const LAS float*)(uintptr_t)(ab + j * 64); }
  u32x2 kp[2][5], qp[2][4], vp[2][5];
  auto fk = [&](int j) { kp[0][(j + 1) % 5] = pk4(acc[1][j]); kp[1][(j + 1) % 5] = pk4(acc[4][j] + ck[1] * mt[j]); };
  auto fv = [&](int j) { vp[0][(j + 1) % 5] = pk4(acc[2][j]); vp[1][(j + 1) % 5] = pk4(acc[5][j] + m4[j] * cv[1]); };
  auto fqq = [&](int j) { qp[0][j] = pk4(acc[0][j]); qp[1][j] = pk4(acc[3][j] + cq[1] * mt[j]); };
  g.template pend<10, 13>(acc);
#pragma unroll
  for (int i = 0; i < 6; ++i) { __builtin_amdgcn_sched_group_barrier(0x100, 3, 0); __builtin_amdgcn_sched_group_barrier(0x8, 1, 0); }
  sched0();
  for_c<0, 20>([&](auto mc) {
    constexpr int m = mc.value, n0 = m * 52 / 20, n1 = (m + 1) * 52 / 20;
    g.template mm<(18 - (m & ~1)) + (m & 1)>(acc);
    sched0();
#pragma unroll
    for (int n = n0; n < n1; ++n) {
      const int t = 12 - (n >> 2), e = n & 3, j = t == 12 ? 4 : t / 3, i = t == 12 ? 1 + TH : t % 3;
      float x = acc[i][j][e];
      asm volatile("v_fmac_f32 %0, %1, %2" : "+v"(x) : "v"(i == 2 ? m4[j][e] : i ? ck[0][e] : cq[0][e]), "v"(i == 2 ? cv[0] : mt[j]));
      acc[i][j][e] = x;
    }
    sched0();
  });
  volatile LAS unsigned* xf = (volatile LAS unsigned*)(uintptr_t)(L_TAG + 16 + (h * 2 + TH) * 4);
  if (TH) { fv(4); *(volatile LAS bf16x8*)(uintptr_t)(xb + 2048) = cat(vp[0][0], vp[1][0]); *xf = l + 1; fk(3); }
  else { fk(3); fv(3); *(volatile LAS bf16x8*)(uintptr_t)xb = cat(kp[0][4], kp[1][4]); *(volatile LAS bf16x8*)(uintptr_t)(xb + 1024) = cat(vp[0][4], vp[1][4]); *xf = l + 1; }
  sched0();
  fqq(3); fk(2); fqq(2); fk(1);
  sched0();
  const f32x4 z = {0.f, 0.f, 0.f, 0.f};
  f32x4 sc[4][2]; float a[4], b[4], m[4];
  auto scores = [&](int j) {
#pragma unroll
    for (int u = 1; u >= 0; --u) sc[j][u] = mfma(cat(kp[0][j + u], kp[1][j + u]), cat(qp[0][j], qp[1][j]), z);
  };
  auto pick = [&](int j) {
    const f32x4 s0 = sc[j][0], s1 = sc[j][1];
    a[j] *= sel(par, sel(ca, s1[1], s0[1]), sel(ca, s1[0], s0[0]));
    b[j] *= sel(par, sel(cb, s1[3], s0[3]), sel(cb, s1[2], s0[2]));
    if (!TH && j == 0) { a[j] = sel(ca || sb, a[j], -1e9f * 1.4426950408889634f); b[j] = sel(cb || sb, b[j], -1e9f * 1.4426950408889634f); }
    m[j] = __builtin_fmaxf(a[j], b[j]);
  };
  auto expo = [&](int j) { a[j] = __builtin_amdgcn_exp2f(a[j] - m[j]); b[j] = __builtin_amdgcn_exp2f(b[j] - m[j]); m[j] = a[j] + b[j]; };
  auto pv = [&](int j) {
    const unsigned pa = pk2(a[j] * ra[j], a[j] * ra[j]), pb = pk2(b[j] * rb[j], b[j] * rb[j]);
    const bf16x8 pf = __builtin_bit_cast(bf16x8, (u32x4){pa & m0, pb & m1, pa & (m0 ^ hm), pb & (m1 ^ hm)});
#pragma unroll
    for (int c = 0; c < 2; ++c) sc[j][c] = mfma(cat(vp[c][j], vp[c][j + 1]), pf, z);
  };
  auto outp = [&](int j) { st16(ro, oo + j * 4096, pack_pair(sc[j][0] * m[j], sc[j][1] * m[j])); };
  unsigned f; u32x4 xv, xk;
  auto xrd = [&]() {
    f = xf[1 - 2 * TH]; xv = *(volatile LAS u32x4*)(uintptr_t)(xb + (TH ? 1024 : 2048));
    if (TH) xk = *(volatile LAS u32x4*)(uintptr_t)xb;
  };
  auto xget = [&]() {
    sched0();
    while (__builtin_expect(__builtin_amdgcn_readfirstlane(f) != l + 1, 0)) xrd();
    vp[0][0] = (u32x2){xv[0], xv[1]}; vp[1][0] = (u32x2){xv[2], xv[3]};
    if (TH) { kp[0][0] = (u32x2){xk[0], xk[1]}; kp[1][0] = (u32x2){xk[2], xk[3]}; }
  };
  {
    fqq(1); fk(0); fqq(0);
    if (!TH) fk(4);
    xrd();
    sched0();
    if (TH) fv(3);
    fv(2); fv(1); fv(0);
#pragma unroll
    for (int c = 0; c < 2; ++c)
#pragma unroll
      for (int j = 0; j < 4; ++j) asm volatile("" : "+v"(qp[c][j]), "+v"(kp[c][j + 1]), "+v"(vp[c][j + 1]));
    if (!TH) asm volatile("" : "+v"(kp[0][0]), "+v"(kp[1][0]));
    xget();
    scores(3); scores(2); scores(1); scores(0);
#pragma unroll
    for (int j = 0; j < 4; ++j) { const float r = rq[j] * (0.17677669529663687f * 1.4426950408889634f); a[j] = r * ra[j]; b[j] = r * rb[j]; }
#pragma unroll
    for (int i = 0; i < 8; ++i) { __builtin_amdgcn_sched_group_barrier(0x8, 1, 0); __builtin_amdgcn_sched_group_barrier(0x2, 2, 0); }
    sched0();
    pick(3); pick(2); pick(1); pick(0);
    float u, w;
    swp32(m[0], m[2]); swp32(m[1], m[3]); u = m[0]; w = m[1];
    asm volatile("v_max_f32 %0, %0, %2\n\tv_max_f32 %1, %1, %3\n\ts_nop 1" : "+v"(u), "+v"(w) : "v"(m[2]), "v"(m[3]));
    swp16(u, w); u = vmax(u, w);
    xch16(u, u, w); xch32(u, m[0], m[2]); xch32(w, m[1], m[3]);
    expo(3); expo(2); expo(1); expo(0);
    pv(3); pv(2); pv(1); pv(0);
    swp32(m[0], m[2]); swp32(m[1], m[3]); u = m[0] + m[2]; w = m[1] + m[3];
    swp16(u, w); u = __builtin_amdgcn_rcpf(u + w);
    xch16(u, u, w); xch32(u, m[0], m[2]); xch32(w, m[1], m[3]);
    outp(3); outp(2); outp(1); outp(0);
  }
}

extern "C" __global__ __launch_bounds__(512, 2) void mega_kernel(const u16* x0, const u16* wqkv, const u16* wo, const u16* w1, const u16* w2, const u16* b1, const u16* b2, u16* out, char* ws) {
  const int wid0 = __builtin_amdgcn_readfirstlane(threadIdx.x >> 6);
  asm volatile("s_getpc_b64 s[40:41]\n\ts_andn2_b32 s40, s40, 63\n\ts_load_dwordx2 s[100:101], s[40:41], %0 offset:124\n\ts_load_dwordx2 s[100:101], s[40:41], %0 offset:252"
               :: "s"(__builtin_amdgcn_readfirstlane(threadIdx.x) << 2) : "s40", "s41", "s100", "s101", "scc");
  sched0();
#define PHASE_VARS \
      int tid = threadIdx.x, bid = blockIdx.x, wid = wid0; \
      asm volatile("" : "+v"(tid)); asm volatile("" : "+s"(bid), "+s"(wid), "+s"(ws)); \
      const int lw = wid - 4, lt = tid - 256, lane = tid & 63, fr = lane & 15, fq = lane >> 4, wp = wid & 1, wq = wid >> 1; \
      const unsigned j = (bid >> 3) * 4 + lw; \
      int hp, sb; tile_map<16, 16, 4, 8>(bid, hp, sb);            \
      int tp, tq; tile_map<8, 32, 4, 8>(bid, tp, tq);             \
      int fp, fq1; tile_map<28, 8, 7, 4>(bid, fp, fq1);           \
      const int cw = bid < 16 ? wid + 4 * (bid >> 3) : -1; \
      (void)cw; (void)j; (void)lt; (void)wp; (void)wq; (void)lw; (void)fr; (void)fq; (void)hp; (void)sb; (void)tp; (void)tq; (void)fp; (void)fq1;
#define LOADER_VARS PHASE_VARS asm volatile("" : "+s"(wqkv), "+s"(wo), "+s"(w1), "+s"(w2));

  if (__builtin_expect(wid0 >= 4, 1)) {
    if (LPR) __builtin_amdgcn_s_setprio(3);
    const u16* xin = x0;
    Pfc pf(wo, w1, blockIdx.x * 4 + wid0 - 4);
#pragma unroll 1
    for (int l = 0; l < NL; ++l) {
      {
        LOADER_VARS
        const int s0 = sb * 128;
        GQ::Loader L(wqkv + hp * 64, xin, lw, lane, qkv_rot(bid));
        if (l == 0) L.pre_w1<0>();
        L.seta((unsigned)(s0 * D * 2), halo_off(s0, lw, lane), lw, lane);
        if (l == 0) { L.issue_a<0>(L.ab + L.kord(0) * 128); if (!SI) L.pre<1, true, true>(); }
        sched0();
        const rsrc_t rs = mkrsrc(ws + WS_ST, MB);
        f32x4 c = {0.f, 0.f, 0.f, 0.f}; f32x2 s1 = {0.f, 0.f}, s2 = {0.f, 0.f}, o = {0.f, 0.f}; float pv = 0.f;
        bf16x8 ones = {0x3F80, 0x3F80, 0x3F80, 0x3F80, 0x3F80, 0x3F80, 0x3F80, 0x3F80};
        const int ct = lw & 1 ? 8 + (sb & 3) : sb & 7; const bool cs = l == 0 && lw >= 2;
        int tpf = (slow_res(wqkv) >> 2) != (bid & 1) ? 0 : lw < 2 ? 10 : 9;
        unsigned ra = GQ::AOFF + ((hp & 3) * 32 + lw * 8) * 128 + lane * 16, rb = rd_km(lane, ct);
        unsigned so = lane & 7 ? MB : ph((unsigned)(((l * S + s0 + (hp & 3) * 32 + lw * 8 + (lane >> 3)) * 4 + (hp >> 2)) * 16));
        unsigned co = fr >= 2 || !cs ? MB : ph((unsigned)(ST_CS + ((hp * 96 + ct * 8 + fq * 2 + fr) * 2 + (sb >> 3)) * 16));
        asm volatile("" : "+v"(ra), "+v"(rb), "+v"(so), "+v"(co), "+v"(c), "+v"(s1), "+v"(s2), "+v"(ones), "+v"(o));
        asm volatile("" : "+s"(tpf));
        if (SI && l == 0) L.pre_w1<1>();
        sched0();
        unsigned to[3], xo = x_off(tp, tq, lw, fr, fq), q2 = ((bid & 2) * 4 + slow_res(wqkv)) * 128 + ((bid >> 3) * 2 + (lw & 1)) * (3 * D * 2);
#pragma unroll
        for (int i = 0; i < 3; ++i) {
          const int r = s0 - 16 + (i == 2 && lw ? 64 : i * 64) + (lt >> 2);
          to[i] = ph((unsigned)(((l * S + (r < 0 ? 0 : r)) * 4 + (lt & 3)) * 16));
        }
        unsigned ta = (lt & 3) < (lw ? 2 : 3) ? L_TAB + ((lt & 3) * 64 + (lt >> 2)) * 4 : L_SCR, ts = ph((unsigned)(ST_CS + (hp * 192 + (lw ? lt - 64 : lt)) * 16));
        asm volatile("" : "+v"(to[0]), "+v"(to[1]), "+v"(to[2]), "+v"(xo), "+v"(ta), "+v"(ts));
        asm volatile("" : "+s"(q2));
        if (SI == 1 && l == 0) L.pre<1, false, true>();
        sched0();
        const GW::Loader W(wo + tp * 128, ws, lw, lane);
        if (SI == 2 && l == 0) L.pre<1, false, true>();
        sched0();
        auto pfq = [&](int n) { const unsigned o = q2 + L.kord(n) * GQ::WSTEP; spf2<0>(wqkv, o); spf2<2048>(wqkv, o); spf2<4096>(wqkv, o); };
        if (l) {
          if (QBR == 3) bar();
          if (!(EW & 8)) L.pre_w();
          if (tpf) { pfq(2 + (lw >> 1)); pfq(4 + (lw >> 1)); }
          if (QBR == 4) bar();
          L.start();
        }
        if (LPR == 1) __builtin_amdgcn_s_setprio(0);
        unsigned tag, tg2; u32x4 gs, a[3];
        auto rowst = [&](int i) {
          const float m0 = __uint_as_float(a[i][0]), m = red4(m0) * 0.25f, q = red4(__uint_as_float(a[i][2]) + 256.f * (m0 - m) * (m0 - m));
          o = (f32x2){sel((lt & 3) == i, m, o.x), sel((lt & 3) == i, q, o.y)};
          asm volatile("" : "+v"(o));
        };
        for_c<0, 16 - GQ::H>([&](auto tc) {
          constexpr int t = tc.value;
          u32x4 v; unsigned tv;
          L.step<t, t == 5 || t == 6 ? 1 : t == 9 || t == 10 ? 4 : 0>([&](unsigned ls) {
            if (t < 4) v = *(const LAS u32x4*)(uintptr_t)(ls + ra);
            if (t == 3) tv = *(volatile LAS unsigned*)(uintptr_t)L_TAG;
          }, [&]() {
            if (t < 8 && cs) {
              constexpr int ls = 32768 + t % 3 * GQ::SB;
              bf16x4 x[4];
#pragma unroll
              for (int e = 0; e < 4; ++e) x[e] = lds_tr4(rb + (ls & ~0x7fff) + (ls & 0x7fff) + (e >> 1) * 4096 + (e & 1) * 512);
#pragma unroll
              for (int ks = 0; ks < 2; ++ks) c = mfma(__builtin_shufflevector(x[2 * ks], x[2 * ks + 1], 0, 1, 2, 3, 4, 5, 6, 7), ones, c);
            } else if (t >= 4) {
              if (t >= 10 || t >= tpf) { if (t < TC1) pf.go(); }
              else if (lw < 2) pfq(t + 4);
              else if (t == 8) { pfq(14); pfq(15); }
            }
            if (t < 4) {
              if (t == 0) { pv = bflo(v[0]); asm volatile("" : "+v"(pv)); }
#pragma unroll
              for (int e = 0; e < 4; ++e) {
                if (SSC) {
                  float x = bflo(v[e]), y = bfhi(v[e]), a0 = s1.x, a1 = s1.y, b0 = s2.x, b1 = s2.y;
                  asm volatile("v_sub_f32 %0, %0, %6\n\tv_sub_f32 %1, %1, %6\n\tv_add_f32 %2, %2, %0\n\tv_add_f32 %3, %3, %1\n\tv_fmac_f32 %4, %0, %0\n\tv_fmac_f32 %5, %1, %1"
                               : "+v"(x), "+v"(y), "+v"(a0), "+v"(a1), "+v"(b0), "+v"(b1) : "v"(pv));
                  s1 = (f32x2){a0, a1}; s2 = (f32x2){b0, b1};
                } else { const f32x2 d = (f32x2){bflo(v[e]), bfhi(v[e])} - pv; s1 += d; s2 += d * d; }
              }
              asm volatile("" : "+v"(s1), "+v"(s2));
            }
            if (t == 3) { tag = __builtin_amdgcn_readfirstlane(tv) + 1u; tg2 = tag2(tag); if (tpf == 10) pfq(7); if (l && tpf == 9) pfq(6); }
            if (t == 4) {
              const float d1 = (s1.x + s1.y) * (1.f / 32.f), ml = pv + d1, m = red8(ml) * 0.125f;
              const float q = red8(vmax((s2.x + s2.y) - 32.f * d1 * d1, 0.f) + 32.f * (ml - m) * (ml - m));
              gs = (u32x4){__float_as_uint(m), tag, __float_as_uint(q), tg2};
              asm volatile("" : "+v"(gs));
            }
            if (t == 8) { gs = (u32x4){__float_as_uint(sel(fr, c[2], c[0])), tag, __float_as_uint(sel(fr, c[3], c[1])), tg2}; asm volatile("" : "+v"(gs)); }
            if (t == (RB ? 12 : 11)) {
#pragma unroll
              for (int i = 0; i < 3; ++i) ld_ok(a[i]);
              for (int spin = 0;; ++spin) {
                unsigned bad = 0;
#pragma unroll
                for (int i = 0; i < 3; ++i) bad |= (a[i][1] ^ tag) | (a[i][3] ^ tg2);
                if (__builtin_expect(__all(bad == 0), 1) || spin_fail(spin, ws)) break;
                __builtin_amdgcn_s_sleep(8);
#pragma unroll
                for (int i = 0; i < 3; ++i) ld_again(a[i], rs, to[i]);
              }
              if (!RB) rowst(0);
            }
            if (t == 12 && !RB) { rowst(1); rowst(2); }
            if (t == 13 && RB == 2) rowst(0);
            if (t == 13 && !RB) { LAS float* p = (LAS float*)(uintptr_t)ta; p[0] = -o.x; p[144] = __builtin_amdgcn_rsqf(o.y * (1.f / D) + EPS); }
          }, [&]() {
            if (t == 4) st16(rs, so, gs);
            if (t == 8) {
              st16(rs, co, gs);
              sched0();
#pragma unroll
              for (int i = 0; i < 3; ++i) ld_raw(a[i], rs, to[i]);
            }
          });
        });
        if (l == 0) {
#pragma unroll
          for (int p = 0; p < 4; ++p) dma16((const char*)xin + (p >> 1) * 32768 + (p & 1) * 64, xo, L_X + lw * 4096 + p * 1024);
          ld_raw(gs, rs, ts);
          wait_vm<GQ::CNT + 5>();
        } else wait_vm<GQ::CNT>();
        bar();
        W.pre_w1<0>();
        if (RB) { if (RB == 1) rowst(0); rowst(1); rowst(2); LAS float* p = (LAS float*)(uintptr_t)ta; p[0] = -o.x; p[144] = __builtin_amdgcn_rsqf(o.y * (1.f / D) + EPS); }
        wait_lgkm0();
        if (l == 0) wait_vm<9>(); else wait_vm<4>();
        bar();
        W.pre_w1<1>(); W.pre_w1<2>();
        if (l == 0) {
          wait_vm<12>();
          ld_ok(gs);
          for (int spin = 0;; ++spin) {
            if (__builtin_expect(__all(((gs[1] ^ tag) | (gs[3] ^ tg2)) == 0), 1) || spin_fail(spin, ws)) break;
            __builtin_amdgcn_s_sleep(8);
            ld_again(gs, rs, ts);
          }
          float c0 = __uint_as_float(gs[0]), c1 = __uint_as_float(gs[2]);
          c0 += dppf<0xB1>(c0); c1 += dppf<0xB1>(c1);
          *(LAS float*)(uintptr_t)(L_CS + (lw ? lt - 64 : lt) * 4) = sel(lt & 1, c1, c0);
        }
        if (l == 0 && lw < TBA) { if (fr < 14) touch4(w1 + (size_t)(((bid >> 3) * TBA + lw) * 8 + (bid & 4)) * F + (bid & 3) * 896, fq * (F * 2) + fr * 128, L_SCR + lw * 256); }
        if (l == 0) { wait_lgkm0(); bar(); }
      }
      {
        LOADER_VARS
        GW::Loader L(wo + tp * 128, ws + WS_O + (size_t)tq * 32 * 8192, lw, lane);
        unsigned po = ((bid & 1) * 8 + (slow_res(wo) ^ (j & 1))) * 128 + (j >> 1) * (D * 2);
#pragma unroll
        for (int k = GW::H; k < 10; ++k) spf2(wo, po + k * GW::WSTEP);
        asm volatile("" : "+s"(po));
        sched0();
        bar();
#pragma unroll
        for (int k = 10; k < 16; ++k) spf2(wo, po + k * GW::WSTEP);
        L.start();
        L.run(16 - GW::H, [&](int) { pf.go(); });
        pf.go();
        L.tail<GW::H - 1>(); bar();
      }
      {
        LOADER_VARS
        if (bid < 224) {
          G1::Loader L(w1 + fp * 128, ws + WS_H + (size_t)fq1 * 64 * 16384, lw, lane);
          L.pre_w();
          const G2A::Loader W((const char*)(w2 + (tq & 23) * 64) + (tq & 8 ? JA * G2::WSTEP : 0), ws, lw, lane);
          const int g0 = (bid & 3) * 14, ga = (g0 + ((slow_res(w1) - g0) & 7)) ^ (j & 1), gb = ga + 8 < g0 + 14 ? ga + 8 : ga;
          const unsigned j2 = j < 32 ? 112 + (j & 15) : j;
          unsigned pa = (j >> 1) * (F * 2) + ga * 128, pb = (j >> 1) * (F * 2) + gb * 128, pd = (j2 >> 1) * (F * 2) + (j & 16 ? gb : ga) * 128;
          asm volatile("" : "+s"(pa), "+s"(pb), "+s"(pd));
          auto pfb = [&](int k) { spf2(w1, pa + k * G1::WSTEP); spf2(w1, pb + k * G1::WSTEP); spf2(w1, pd + k * G1::WSTEP); };
#pragma unroll
          for (int k = G1::H; k < G1::H + PA_1; ++k) pfb(k);
          wait_lgkm0(); pf.go();
          sched0();
          bar();
          pf.go();
          L.start();
          L.run(32 - G1::H, [&](int t) {
            if (pf.pc < 3 + NW1) pf.go();
            else if (t < 32 - G1::H - PA_1) pfb(t + G1::H + PA_1);
          });
          if (EW & 4) {
            wait_vm<2 * G1::CNT>(); bar(); W.pre_w1<1>();
            wait_vm<G1::CNT + 4>(); bar();
            wait_vm<4>(); bar(); W.pre_w1<0>();
          } else L.tail<G1::H - 1>();
          bar();
        } else {
          wait_lgkm0(); pf.go();
          bar();
          if (l == 0) {
            __builtin_amdgcn_s_sleep(24);
            while (pf.pc < 3 + NW1) { wait_lgkm0(); pf.go(); }
            spare_touch(w1, w2, bid, wid, lane);
          }
        }
        pf.pc = 99;
      }
      {
        LOADER_VARS
        constexpr int NB = F / 64 - 2 * JA;
        const char* ub = ws + WS_U + (size_t)tp * 112 * 16384;
        G2A::Loader A((const char*)(w2 + (tq & 23) * 64) + (tq & 8 ? JA * G2::WSTEP : 0), ub + (tq & 8 ? JA * 32768 : 0), lw, lane);
        if (!(EW & 4) || bid >= 224) A.pre_w();
        const GQ::Loader W(wqkv + hp * 64, ws, lw, lane, qkv_rot(bid));
        unsigned pb = ((bid & 4) * 4 + (j & 1) * 8 + slow_res(w2)) * 128 + (j >> 1) * (S * 2) + JA * G2::WSTEP, pa = pb - (bid & 2 ? 0 : JA * G2::WSTEP);
        asm volatile("" : "+s"(pa), "+s"(pb));
        auto side = [&](int n) { n += G2A::H + PA_2; if (n < JA + NB) spf2(w2, (n < JA ? pa : pb) + n * G2::WSTEP); };
#pragma unroll
        for (int k = 0; k < PA_2; ++k) side(k - PA_2);
        G2::Loader L((const char*)(w2 + tq * 64) + 2 * JA * G2::WSTEP, ub + 2 * JA * 32768, lw, lane);
        gu32* xf = (gu32*)(ws + WS_MAIL) + (l * 4 + 2) * MEDGE + (tq * 8 + tp) * 64 + 28 + lw;
        const char* xp = ws + WS_XP + (unsigned)(((bid ^ 2) * 4 + lw) << 14);
        unsigned fl = L_QKV + lw * 256, xv = ph(lane * 16);
        asm volatile("" : "+s"(xf), "+s"(xp), "+s"(fl)); asm volatile("" : "+v"(xv));
        sched0();
        bar();
        A.start();
        A.run(JA - G2A::H, [&](int t) { side(t); });
        auto stage = [&](auto s) { L.issue_w<s.value * G2::SB>(L.w + s.value * G2::WSTEP); L.issue_a<s.value * G2::SB>(L.ab + s.value * 32768); sched0(); };
        wait_vm<G2A::CNT>(); bar(); stage(IC<0>{}); side(JA - 2);
        wait_vm<G2::CNT>(); bar(); stage(IC<1>{}); side(JA - 1);
        wait_vm<G2::CNT>(); bar(); stage(IC<2>{}); stage(IC<3>{}); side(JA);
        L.t = 1; L.islot = 0;
        L.run(NB - G2::H, [&](int t) {
          if (t == NB - G2::H - FLS) asm volatile("s_mov_b32 m0, %2\n\ts_nop 0\n\tglobal_load_lds_dword %0, %1 sc1" :: "v"(0u), "s"(xf), "s"(fl) : "memory");
          side(JA + t);
        });
        auto xdma = [&]() {
#pragma unroll
          for (int i = 0; i < 4; ++i) dma16(xp + i * 2048, xv, L_X + lw * 4096 + i * 1024);
          sched0();
        };
        sched0(); wait_vm<2 * G2::CNT>(); bar();
        if (__builtin_amdgcn_readfirstlane(*(volatile LAS unsigned*)(uintptr_t)fl)) {
          xdma();
          if ((EW & 8) && l == 0) { W.pre_w1<0>(); wait_vm<G2::CNT + 10>(); bar(); W.pre_w1<1>(); wait_vm<16>(); bar(); wait_vm<12>(); }
          else { wait_vm<G2::CNT + 4>(); bar(); wait_vm<4>(); bar(); wait_vm<0>(); }
        } else {
          wait_vm<G2::CNT>(); bar(); wait_vm<0>(); bar();
          for (int spin = 0;; ++spin) {
            if (__builtin_amdgcn_readfirstlane(__hip_atomic_load(xf, RLX_AGENT)) || spin_fail(spin, ws)) break;
            __builtin_amdgcn_s_sleep(2);
          }
          xdma();
          if ((EW & 8) && l == 0) { W.pre_w(); wait_vm<12>(); } else wait_vm<0>();
        }
        bar();
        xin = (const u16*)(ws + WS_X2);
        if (NL == 1) bar();
      }
    }
    return;
  }

  {
    PHASE_VARS
    if (cw >= 0) code_warm(cw, lane, L_SCR + wid * 256);
    pfb_q(wqkv, bid, (bid >> 3) * 4 + wid, 0); pfb_q(wqkv, bid, (bid >> 3) * 4 + wid, 1);
    if (wid < 3) {
      if (tid == 0) asm volatile("s_mov_b32 m0, %2\n\ts_nop 0\n\tglobal_load_lds_dword %0, %1 sc1" :: "v"(WS_EPOCH), "s"(ws), "s"(L_TAG) : "memory");
      const rsrc_t rz = mkrsrc(ws + WS_XCH, 3 * MB);
      if (tid < 128) st16(rz, ph((((tid >> 6) * 256 + tq * 8 + tp) * 64 + lane) * 16), (u32x4){0u, SENT, 0u, SENT});
      else {
        const int e = (tid >> 3) & 7, line = e == 0 || e == 7 ? (2 * sb + (e & 1)) * 8 + (hp & 7) : e * 256 + bid;
        st16(rz, WS_MAIL - WS_XCH + line * 256 + (tid & 7) * 16, (u32x4){0u, 0u, 0u, 0u});
      }
      if (wid == 1 && bid < 224) touch4(b1 + fp * 128, lane * 4, L_BIAS);
      if (wid == 2) touch4(b2 + tq * 64, (lane & 31) * 4, L_BIAS + 256);
      if (wid == 0) *(LAS u32x4*)(uintptr_t)(L_TAG + 16) = (u32x4){0u, 0u, 0u, 0u};
    }
    if (CPR) __builtin_amdgcn_s_setprio(CPR);
  }
#pragma unroll 1
  for (int l = 0; l < NL; ++l) {
    {
      PHASE_VARS
      if (wid & 2) qkv_attn<1>(ws, l, wp, lane, hp, sb, cw, bid, tp, tq, wqkv);
      else qkv_attn<0>(ws, l, wp, lane, hp, sb, cw, bid, tp, tq, wqkv);
    }
    {
      PHASE_VARS
      if (WSP == 0) sync_post(ws, l * 4, lane < 16 ? (sb * 16 + lane) * 64 + hp : -1, wid);
      GW::Comp<O42<false, WNE>> g; g.setup(wp, wq, lane);
      f32x4 acc[4][2];
#pragma unroll
      for (int i = 0; i < 4; ++i)
#pragma unroll
        for (int jj = 0; jj < 2; ++jj) acc[i][jj] = (f32x4){0.f, 0.f, 0.f, 0.f};
      unsigned xa = L_X + wid * 4096 + lane * 16;
      { bf16x8 s[2]; sel_frags(true, fr, fq, s); tile_add(acc, s, xa); }
      if (WSP == 1) {
#pragma unroll
        for (int i = 0; i < 4; ++i) asm volatile("" : "+v"(acc[i][0]), "+v"(acc[i][1]));
        sync_post(ws, l * 4, lane < 16 ? (sb * 16 + lane) * 64 + hp : -1, wid);
      }
      const rsrc_t rx = mkrsrc(ws + WS_XCH + l * (MB / 2), MB / 2), rh = mkrsrc(ws + WS_H, S * D * 4);
      unsigned xs = fq != fr >> 2 ? MB : ph((unsigned)(((tq * 8 + tp) * 64 + wp * 32 + wq * 16 + fr) * 16)), xl = ph((unsigned)((tq * 512 + fq * 128 + wq * 16 + fr) * 16));
      const unsigned h0 = ph((unsigned)(((tp * 32 + tq) << 14) + wp * 8192 + (wq * 32 + fr) * 128 + ((pair_chunk(fq) ^ (km_f(fr) << 1)) << 4)));
      u32x4 ho = {h0, h0 ^ 64, h0 + 4096, (h0 ^ 64) + 4096};
      bf16x8 ones = {0x3F80, 0x3F80, 0x3F80, 0x3F80, 0x3F80, 0x3F80, 0x3F80, 0x3F80};
      asm volatile("" : "+v"(xa), "+v"(xs), "+v"(xl), "+v"(ho), "+v"(ones));
      g.prep(acc);
      if (WSP == 2) sync_post(ws, l * 4, lane < 16 ? (sb * 16 + lane) * 64 + hp : -1, wid);
      Gate gt; gt.wait<WSP == 2 ? SW0 : SWW>(ws, l * 4, tq * 8 + tp, 0xffffu, (1u << GW::H) - 1, (1u << (GW::H + GLW)) - 1, wid);
      g.run_steps<false>(acc, [&](int it, int i) {
        if (i < 0) gt.arm(it, cw);
        if (i == 0) gt.head(it, cw, lane, wid);
        if (i == GLW - 1) gt.late(ws);
      });
      bar();
      sched0();
      g.pend<0, 4>(acc);
      u32x4 pk[2][2]; float mp[2];
      {
        f32x4 sm[2], gm[2];
#pragma unroll
        for (int i = 0; i < 2; ++i)
#pragma unroll
          for (int jj = 0; jj < 2; ++jj) {
            pk[jj][i] = cat4(pk4(acc[2 * i][jj]), pk4(acc[2 * i + 1][jj])); asm volatile("" : "+v"(pk[jj][i]));
            const bf16x8 x = __builtin_bit_cast(bf16x8, pk[jj][i]);
            gm[jj] = mfma(x, x, i ? gm[jj] : (f32x4){0.f, 0.f, 0.f, 0.f}); sm[jj] = mfma(ones, x, i ? sm[jj] : (f32x4){0.f, 0.f, 0.f, 0.f});
          }
        u32x4 st;
#pragma unroll
        for (int jj = 0; jj < 2; ++jj) {
          const f32x4 gv = gm[jj];
          st[2 * jj] = __float_as_uint(sm[jj][0]); st[2 * jj + 1] = __float_as_uint(sel(fr & 2, sel(fr & 1, gv[3], gv[2]), sel(fr & 1, gv[1], gv[0])));
          mp[jj] = sm[jj][0] * (1.f / 64.f);
        }
        st16(rx, xs, st);
      }
      sched0();
#pragma unroll
      for (int p = 0; p < 4; ++p) *(LAS u32x4*)(uintptr_t)(xa + p * 1024) = pk[p >> 1][p & 1];
      asm volatile("" : "+v"(mp[0]), "+v"(mp[1]));
#pragma unroll
      for (int jj = 0; jj < 2; ++jj)
#pragma unroll
        for (int i = 0; i < 2; ++i) {
          const auto rx_ = __builtin_amdgcn_permlane16_swap(pk[jj][i][0], pk[jj][i][2], false, false), ry_ = __builtin_amdgcn_permlane16_swap(pk[jj][i][1], pk[jj][i][3], false, false);
          const unsigned x0 = rx_[0], x1 = rx_[1], y0 = ry_[0], y1 = ry_[1];
          acc[2 * i][jj] = (f32x4){bflo(x0), bfhi(x0), bflo(y0), bfhi(y0)} - mp[jj]; acc[2 * i + 1][jj] = (f32x4){bflo(x1), bfhi(x1), bflo(y1), bfhi(y1)} - mp[jj];
          asm volatile("" : "+v"(acc[2 * i][jj]), "+v"(acc[2 * i + 1][jj]));
        }
      if (XD) __builtin_amdgcn_s_sleep(XD);
      f32x4 gr[4];
      for (int spin = 0;; ++spin) {
        asm volatile("" ::: "memory");
#pragma unroll
        for (int p = 0; p < 4; ++p) gr[p] = __builtin_bit_cast(f32x4, __builtin_amdgcn_raw_buffer_load_b128(rx, xl + p * 1024, 0, 16));
        const float lo = vmin3(vmin3(gr[0][1], gr[0][3], gr[1][1]), vmin3(gr[1][3], gr[2][1], gr[2][3]), vmin3(gr[3][1], gr[3][3], gr[3][3]));
        if (!__any(lo < 0.f) || spin_fail(spin, ws)) break;
        __builtin_amdgcn_s_sleep(XS);
      }
      {
        const f32x4 t = (gr[0] + gr[1]) + (gr[2] + gr[3]);
        float m[2] = {t[0], t[2]}, q[2] = {t[1], t[3]}, u, w, v, y;
        swp32(m[0], m[1]); swp32(q[0], q[1]);
        xch16(m[0] + m[1], u, w); xch16(q[0] + q[1], v, y);
        const float mean = (u + w) * (1.f / D), var = __builtin_fmaxf((v + y) * (1.f / D) - mean * mean, 0.f), r1 = __builtin_amdgcn_rsqf(var + EPS);
        xch32(mean, m[0], m[1]);
        xch32(r1 * __builtin_amdgcn_rsqf(var * r1 * r1 + EPS), q[0], q[1]);
#pragma unroll
        for (int jj = 0; jj < 2; ++jj) {
          const float c = (mp[jj] - m[jj]) * q[jj];
#pragma unroll
          for (int i = 0; i < 2; ++i) st16(rh, ho[jj * 2 + i], cat4(pk4(acc[2 * i][jj] * q[jj] + c), pk4(acc[2 * i + 1][jj] * q[jj] + c)));
        }
      }
    }
    {
      PHASE_VARS
      const int post = lane < 28 ? (tp * 28 + lane) * 64 + tq : -1;
      if (bid >= 224) {
        sync_post(ws, l * 4 + 1, post, wid);
        if (l == 0 && bid == 255 && wid == 0) { const unsigned tag = get_tag(); if (lane == 0) __hip_atomic_store((gu32*)(ws + WS_EPOCH), tag, RLX_AGENT); }
        if (l == 0) { __builtin_amdgcn_s_sleep(24); spare_touch(w1, w2, bid, wid, lane); }
      } else {
        G1::Comp<O44<false, 32>> g; g.setup(wp, wq, lane);
        f32x4 acc[4][4];
#pragma unroll
        for (int i = 0; i < 4; ++i) { const u32x2 b = *(const LAS u32x2*)(uintptr_t)(L_BIAS + (wp * 4 + i) * 32 + fq * 8);
#pragma unroll
          for (int jj = 0; jj < 4; ++jj) acc[i][jj] = (f32x4){bflo(b.x), bfhi(b.x), bflo(b.y), bfhi(b.y)}; }
        const rsrc_t ro = mkrsrc(ws + WS_U, D * F * 4);
        const unsigned u0 = ph((unsigned)(((fq1 * 56 + fp * 2 + wp) << 14) + (wq * 64 + fr) * 128 + ((pair_chunk(fq) ^ (fr >> 1)) << 4)));
        u32x4 uo = {u0, u0 + 4096, u0 + 8192, u0 + 12288}, uo2 = uo ^ 64;
        asm volatile("" : "+v"(uo), "+v"(uo2));
        g.prep(acc);
        sync_post(ws, l * 4 + 1, post, wid);
        Gate gt; gt.wait<SW0>(ws, l * 4 + 1, fq1 * 28 + fp, ~0u, (1u << G1::H) - 1, (1u << (G1::H + GL1)) - 1, wid);
        g.run_steps<false>(acc, [&](int it, int i) {
          if (i < 0) gt.arm(it, cw);
        if (i == 0) gt.head(it, cw, lane, wid);
          if (i == GL1 - 1) gt.late(ws);
        });
        bar();
        sched0();
        if (GSH) {
          for_c<0, 2>([&](auto jc) {
            constexpr int jj = jc.value;
            f32x4 a[2];
#pragma unroll
            for (int i = 0; i < 2; ++i) { a[i] = gelu_arg(acc[i][jj]); asm volatile("" : "+v"(a[i])); }
            sched0();
            for_c<0, 4>([&](auto ec) { constexpr int e = ec.value; g.mm<jj * 8 + e>(acc); sched0(); a[0][e] = __builtin_amdgcn_exp2f(a[0][e]); a[1][e] = __builtin_amdgcn_exp2f(a[1][e]); asm volatile("" : "+v"(a[0]), "+v"(a[1])); sched0(); });
            a[0] += 1.f; a[1] += 1.f; asm volatile("" : "+v"(a[0]), "+v"(a[1]));
            sched0();
            for_c<0, 4>([&](auto ec) { constexpr int e = ec.value; g.mm<jj * 8 + 4 + e>(acc); sched0(); a[0][e] = __builtin_amdgcn_rcpf(a[0][e]); a[1][e] = __builtin_amdgcn_rcpf(a[1][e]); asm volatile("" : "+v"(a[0]), "+v"(a[1])); sched0(); });
            st16(ro, uo[jj], pack_pair(acc[0][jj] * a[0], acc[1][jj] * a[1]));
          });
        } else g.pend<0, 8>(acc);
#pragma unroll
        for (int n = GSH ? 2 : 0; n < 8; ++n) { const int i = n >> 2, jj = n & 3; st16(ro, (i ? uo2 : uo)[jj], pack_pair(gelu_tanh(acc[2 * i][jj]), gelu_tanh(acc[2 * i + 1][jj]))); }
      }
    }
    {
      PHASE_VARS
      asm volatile("" : "+s"(out));
      G2A::Comp<O44<true, ANE>> ga; ga.setup(wp, wq, lane);
#pragma unroll
      for (int jj = 0; jj < 4; ++jj) ga.qrd[0][jj] = 65536 + rd_km(lane, wq * 2 + (jj & 1)) + (((jj >> 1) ^ (tq >> 3)) & 1) * 8192;
      G2::Comp<O42<true, BNE>> g; g.setup(wp, wq, lane);
      g.setwin();
      ga.setwin();
      if (GSP == 1) sync_post(ws, l * 4 + 2, bid < 224 && lane < 32 ? (lane * 8 + fq1) * 64 + fp : -1, wid);
      f32x4 ac4[4][4], acc[4][2];
#pragma unroll
      for (int jj = 0; jj < 2; ++jj) { const float b = bf2f(*(const LAS u16*)(uintptr_t)(L_BIAS + 256 + (wq * 2 + jj) * 32 + fr * 2));
#pragma unroll
        for (int i = 0; i < 4; ++i) { ac4[i][jj] = (f32x4){b, b, b, b}; ac4[i][jj + 2] = (f32x4){0.f, 0.f, 0.f, 0.f}; } }
      unsigned xa = L_X + wid * 4096 + lane * 16, xo = (unsigned)((bid * 4 + wid) << 14) + ph(lane * 16), oo = x_off(tp, tq, wid, fr, fq), xo2 = xo + 4096, oo2 = oo + 32768;
      unsigned fo = lane ? MB : (((tq ^ 8) * 8 + tp) * 64 + 28 + wid) * 4u, one = 1u;
      bf16x8 s[2]; sel_frags(false, fr, fq, s); tile_add(ac4, s, xa);
      const rsrc_t rx = mkrsrc(ws + WS_XP, 16 * MB), rf = mkrsrc(ws + WS_MAIL + (l * 4 + 2) * (MEDGE * 4), MEDGE * 4), ro = mkrsrc(l ? (const void*)out : (const void*)(ws + WS_X2), S * D * 2);
      asm volatile("" : "+v"(xa), "+v"(xo), "+v"(oo), "+v"(xo2), "+v"(oo2), "+v"(fo), "+v"(one), "+v"(s[0]), "+v"(s[1]));
      ga.pin(ac4);
      if (GSP == 2) sync_post(ws, l * 4 + 2, bid < 224 && lane < 32 ? (lane * 8 + fq1) * 64 + fp : -1, wid);
      static_assert(JA % 2 == 0 && G2A::H == 2, "");
      Gate gt; gt.wait<GSP == 2 ? SW0 : SWW>(ws, l * 4 + 2, tq * 8 + tp, 0xfffffffu, tq & 8 ? 1u << JA / 2 : 1u, tq & 8 ? 7u << JA / 2 : 7u, wid);
      ga.run_steps<false>(ac4, [&](int it, int i) {
        if (i < 0) gt.arm(it, cw);
        if (i == 0) gt.head(it, cw, lane, wid);
        if (i == 2) gt.late(ws);
      });
      ga.pend<4, 8>(ac4);
#pragma unroll
      for (int i = 0; i < 2; ++i)
#pragma unroll
        for (int jj = 0; jj < 2; ++jj) st16(rx, (jj ? xo2 : xo) + i * 2048, cat4(pk4(ac4[2 * i][jj + 2]), pk4(ac4[2 * i + 1][jj + 2])));
#pragma unroll
      for (int i = 0; i < 4; ++i) { acc[i][0] = ac4[i][0]; acc[i][1] = ac4[i][1]; }
#pragma unroll
      for (int k = 0; k < 2; ++k) { g.fp[2][k] = ga.fp[2][k]; g.fp[3][k] = ga.fp[3][k]; g.fq[0][k] = ga.fq[0][k]; g.fq[1][k] = ga.fq[1][k]; }
      sched0();
      g.run_steps<true>(acc, [&](int it, int i) {
        if (i == PS % 4 && __builtin_expect(it == (F / 64 - 2 * JA) / 4 - PS / 4, 0)) {
          wait_vm<0>();
          __builtin_amdgcn_raw_buffer_store_b32(one, rf, fo, 0, 16);
        }
      });
      bar();
      sched0();
      g.pend<0, 4>(acc);
      tile_add(acc, s, xa);
      u32x4 xr[4];
#pragma unroll
      for (int i = 0; i < 2; ++i)
#pragma unroll
        for (int jj = 0; jj < 2; ++jj) { xr[jj * 2 + i] = pack_pair(acc[2 * i][jj], acc[2 * i + 1][jj]); st16(ro, (jj ? oo2 : oo) + i * 64, xr[jj * 2 + i]); }
      if (l == 0) {
#pragma unroll
        for (int p = 0; p < 4; ++p) *(LAS u32x4*)(uintptr_t)(xa + p * 1024) = xr[p];
        if (NL == 1) sync_post(ws, 3, -1, wid);
      }
    }
  }
}

extern "C" void kernel_launch(void* const* d_in, const int* in_sizes, int n_in,
                              void* d_out, int out_size, void* d_ws, size_t ws_size,
                              hipStream_t stream) {
  (void)hipFuncSetAttribute((const void*)mega_kernel, hipFuncAttributeMaxDynamicSharedMemorySize, 163840);
  mega_kernel<<<256, 512, 163840, stream>>>((const u16*)d_in[0], (const u16*)d_in[1], (const u16*)d_in[2], (const u16*)d_in[4], (const u16*)d_in[6], (const u16*)d_in[5], (const u16*)d_in[7], (u16*)d_out, (char*)d_ws);
}
```

```cpp
#include <hip/hip_runtime.h>
#include <hip/hip_bf16.h>
#include <stdint.h>

typedef unsigned short u16;
typedef unsigned long long u64;
typedef short bf16x8 __attribute__((ext_vector_type(8)));
typedef short bf16x4 __attribute__((ext_vector_type(4)));
typedef float f32x4 __attribute__((ext_vector_type(4)));
typedef float f32x2 __attribute__((ext_vector_type(2)));
typedef unsigned u32x4 __attribute__((ext_vector_type(4)));
typedef unsigned u32x2 __attribute__((ext_vector_type(2)));
typedef __bf16 bf16v2 __attribute__((ext_vector_type(2)));
#define LAS __attribute__((address_space(3)))
#define GAS __attribute__((address_space(1)))
#define DEVI __device__ __forceinline__
typedef __attribute__((address_space(1))) unsigned gu32;
typedef __attribute__((address_space(1))) unsigned long long gu64;

constexpr int S = 2048, D = 1024, F = 3584;
constexpr float EPS = 1e-5f;

DEVI float bf2f(u16 v) { return __uint_as_float(((unsigned)v) << 16); }
DEVI float bflo(unsigned v) { return __uint_as_float(v << 16); }
DEVI float bfhi(unsigned v) { return __uint_as_float(v & 0xffff0000u); }
DEVI unsigned pk2(float lo, float hi) { bf16v2 p; p.x = (__bf16)lo; p.y = (__bf16)hi; return __builtin_bit_cast(unsigned, p); }

DEVI const void* uni_ptr(const void* p) {
  const u64 v = (u64)p;
  return (const void*)(((u64)(unsigned)__builtin_amdgcn_readfirstlane((unsigned)(v >> 32)) << 32) | (unsigned)__builtin_amdgcn_readfirstlane((unsigned)v));
}
DEVI void dma16(const void* sbase, unsigned voff, unsigned ldsaddr) {
  ldsaddr = __builtin_amdgcn_readfirstlane(ldsaddr);
  asm volatile("s_mov_b32 m0, %2\n\ts_nop 0\n\tglobal_load_lds_dwordx4 %0, %1 sc1" :: "v"(voff), "s"(sbase), "s"(ldsaddr) : "memory");
}
template <int N, int IMM> DEVI void dma_grp(const void* sbase, const unsigned* v, unsigned lb) {
  static_assert(N == 1 || N == 2 || N == 4, "");
  if constexpr (N == 4)
    asm volatile("s_add_u32 m0, %1, %2\n\ts_nop 0\n\tglobal_load_lds_dwordx4 %3, %0 sc1\n\tglobal_load_lds_dwordx4 %4, %0 offset:1024 sc1\n\t"
                 "global_load_lds_dwordx4 %5, %0 offset:2048 sc1\n\tglobal_load_lds_dwordx4 %6, %0 offset:3072 sc1"
                 :: "s"(sbase), "s"(lb), "i"(IMM), "v"(v[0]), "v"(v[1]), "v"(v[2]), "v"(v[3]) : "memory", "scc");
  else if constexpr (N == 2)
    asm volatile("s_add_u32 m0, %1, %2\n\ts_nop 0\n\tglobal_load_lds_dwordx4 %3, %0 sc1\n\tglobal_load_lds_dwordx4 %4, %0 offset:1024 sc1"
                 :: "s"(sbase), "s"(lb), "i"(IMM), "v"(v[0]), "v"(v[1]) : "memory", "scc");
  else
    asm volatile("s_add_u32 m0, %1, %2\n\ts_nop 0\n\tglobal_load_lds_dwordx4 %3, %0 sc1" :: "s"(sbase), "s"(lb), "i"(IMM), "v"(v[0]) : "memory", "scc");
}
template <int N> DEVI void wait_vm() { asm volatile("s_waitcnt vmcnt(%0)" :: "i"(N) : "memory"); }
DEVI void wait_lgkm0() { asm volatile("" ::: "memory"); __builtin_amdgcn_s_waitcnt(0xC07F); asm volatile("" ::: "memory"); }
DEVI void mfma_ip(f32x4& c, const bf16x8& a, const bf16x8& b) { asm volatile("v_mfma_f32_16x16x32_bf16 %0, %1, %2, %0" : "+v"(c) : "v"(a), "v"(b)); }
DEVI void mfma_gap() { __builtin_amdgcn_sched_barrier(0); asm volatile("s_nop 7"); __builtin_amdgcn_sched_barrier(0); }
DEVI void bar() { asm volatile("" ::: "memory"); __builtin_amdgcn_s_barrier(); asm volatile("" ::: "memory"); }
DEVI void sched0() { __builtin_amdgcn_sched_barrier(0); }

DEVI bf16x8 lds_b128(unsigned addr) { return *(const LAS bf16x8*)(uintptr_t)addr; }
DEVI bf16x4 lds_tr4(unsigned addr) { return __builtin_amdgcn_ds_read_tr16_b64_v4i16((LAS bf16x4*)(uintptr_t)addr); }

typedef __amdgpu_buffer_rsrc_t rsrc_t;
DEVI rsrc_t mkrsrc(const void* p, unsigned bytes) { return __builtin_amdgcn_make_buffer_rsrc((void*)p, 0, bytes, 0x00020000); }
DEVI void st16(rsrc_t r, unsigned off, u32x4 v) { __builtin_amdgcn_raw_buffer_store_b128(v, r, off, 0, 16); }
DEVI void ld_raw(u32x4& v, rsrc_t r, unsigned off) { asm volatile("buffer_load_dwordx4 %0, %1, %2, 0 offen sc1 ; raw load" : "=&v"(v) : "v"(off), "s"(r) : "memory"); }
DEVI void ld_again(u32x4& v, rsrc_t r, unsigned off) { asm volatile("buffer_load_dwordx4 %0, %1, %2, 0 offen sc1\n\ts_waitcnt vmcnt(0)" : "=&v"(v) : "v"(off), "s"(r) : "memory"); }
DEVI void ld_ok(u32x4& v) { asm volatile("; raw ok %0" : "+v"(v)); }
#define RLX_AGENT __ATOMIC_RELAXED, __HIP_MEMORY_SCOPE_AGENT

template <int N> DEVI float ror16(float v) { return __int_as_float(__builtin_amdgcn_update_dpp(0, __float_as_int(v), 0x120 + N, 0xf, 0xf, false)); }
DEVI float red16(float v) { v += ror16<8>(v); v += ror16<4>(v); v += ror16<2>(v); v += ror16<1>(v); return v; }
DEVI void xch32(float v, float& a, float& b) { unsigned u = __float_as_uint(v); auto r = __builtin_amdgcn_permlane32_swap(u, u, false, false); unsigned x = r[0], y = r[1]; a = __uint_as_float(x); b = __uint_as_float(y); }
DEVI void xch16(float v, float& a, float& b) { unsigned u = __float_as_uint(v); auto r = __builtin_amdgcn_permlane16_swap(u, u, false, false); unsigned x = r[0], y = r[1]; a = __uint_as_float(x); b = __uint_as_float(y); }
DEVI void swp32(float& a, float& b) { auto r = __builtin_amdgcn_permlane32_swap(__float_as_uint(a), __float_as_uint(b), false, false); unsigned x = r[0], y = r[1]; a = __uint_as_float(x); b = __uint_as_float(y); }
DEVI void swp16(float& a, float& b) { auto r = __builtin_amdgcn_permlane16_swap(__float_as_uint(a), __float_as_uint(b), false, false); unsigned x = r[0], y = r[1]; a = __uint_as_float(x); b = __uint_as_float(y); }
DEVI float colsum4(float v) { float a, b; xch32(v, a, b); v = a + b; xch16(v, a, b); return a + b; }
DEVI float vmax(float a, float b) { float r; asm("v_max_f32 %0, %1, %2" : "=v"(r) : "v"(a), "v"(b)); return r; }
DEVI float vmin3(float a, float b, float c) { float r; asm("v_min3_f32 %0, %1, %2, %3" : "=v"(r) : "v"(a), "v"(b), "v"(c)); return r; }
DEVI float vmax3(float a, float b, float c) { float r; asm("v_max3_f32 %0, %1, %2, %3" : "=v"(r) : "v"(a), "v"(b), "v"(c)); return r; }

DEVI unsigned src_kc(int c, int ld) { int row = c >> 3, ch = (c & 7) ^ ((row >> 1) & 7); return (unsigned)(row * ld * 2 + ch * 16); }
DEVI int km_f(int k) { return ((k >> 1) & 1) | (((k >> 3) & 1) << 1); }
DEVI unsigned ph(unsigned x) { return ((x >> 7) << 8) | (x & 127u); }
DEVI unsigned rd_kc(int lane, int ks) { int r = lane & 15, g = lane >> 4; return (unsigned)(r * 128 + (((ks * 4 + g) ^ (r >> 1)) << 4)); }
DEVI unsigned rd_km(int lane, int ct) {
  int g = lane >> 4, q = (lane & 15) >> 2, p = lane & 3;
  int f = ((q >> 1) & 1) | ((g & 1) << 1);
  return (unsigned)((ct >> 2) * 8192 + (8 * g + q) * 128 + (((ct & 3) ^ f) << 5) + 8 * p);
}


constexpr unsigned CODE_BYTES = 53248;
DEVI void code_warm(int part, int lane, unsigned scratch) {
  unsigned long long pc;
  asm volatile("s_getpc_b64 s[40:41]\n\ts_add_u32 s40, s40, mega_kernel@rel32@lo+4\n\ts_addc_u32 s41, s41, mega_kernel@rel32@hi+12\n\ts_mov_b64 %0, s[40:41]" : "=s"(pc) :: "s40", "s41", "scc");
  const unsigned off = (unsigned)(part * 64 + lane) * 128u;
  scratch = __builtin_amdgcn_readfirstlane(scratch);
  if (off < CODE_BYTES) asm volatile("s_nop 3\n\ts_mov_b32 m0, %2\n\ts_nop 0\n\tglobal_load_lds_dword %0, %1" :: "v"(off), "s"(pc), "s"(scratch) : "memory");
}

DEVI unsigned src_km(int lt, int ld) { const int k = lt >> 3; return (unsigned)(k * ld * 2 + (((lt & 7) ^ (km_f(k) << 1)) << 4)); }
DEVI void touch4(const void* sbase, unsigned voff, unsigned ldsaddr) {
  ldsaddr = __builtin_amdgcn_readfirstlane(ldsaddr); sbase = uni_ptr(sbase);
  asm volatile("s_nop 3\n\ts_mov_b32 m0, %2\n\ts_nop 0\n\tglobal_load_lds_dword %0, %1" :: "v"(voff), "s"(sbase), "s"(ldsaddr) : "memory");
}

template <int IMM> DEVI void spf1(const void* base, unsigned soff) { asm volatile("s_load_dword s101, %0, %1 offset:%2" :: "s"(base), "s"(soff), "i"(IMM) : "s101"); }
template <int IMM = 0> DEVI void spf2(const void* base, unsigned soff) { asm volatile("s_load_dwordx2 s[100:101], %0, %1 offset:%2" :: "s"(base), "s"(soff), "i"(IMM + 60) : "s100", "s101"); }
template <int ST> DEVI void spf2x6(const void* base, unsigned soff) {
  asm volatile("s_load_dwordx2 s[100:101], %0, %1 offset:%2\n\ts_load_dwordx2 s[100:101], %0, %1 offset:%3\n\ts_load_dwordx2 s[100:101], %0, %1 offset:%4\n\t"
               "s_load_dwordx2 s[100:101], %0, %1 offset:%5\n\ts_load_dwordx2 s[100:101], %0, %1 offset:%6\n\ts_load_dwordx2 s[100:101], %0, %1 offset:%7"
               :: "s"(base), "s"(soff), "i"(60), "i"(60 + ST), "i"(60 + 2 * ST), "i"(60 + 3 * ST), "i"(60 + 4 * ST), "i"(60 + 5 * ST) : "s100", "s101");
}
template <int ST> DEVI void spf1x12(const void* base, unsigned soff) {
  spf1<0>(base, soff); spf1<ST>(base, soff); spf1<2 * ST>(base, soff); spf1<3 * ST>(base, soff); spf1<4 * ST>(base, soff); spf1<5 * ST>(base, soff);
  spf1<6 * ST>(base, soff); spf1<7 * ST>(base, soff); spf1<8 * ST>(base, soff); spf1<9 * ST>(base, soff); spf1<10 * ST>(base, soff); spf1<11 * ST>(base, soff);
}
DEVI int lgkm_out() { int n; asm volatile("s_getreg_b32 %0, hwreg(HW_REG_IB_STS, 8, 4)" : "=s"(n)); return n; }
DEVI int slow_res(const void* w) { return (3 - ((unsigned)(uintptr_t)w >> 7)) & 7; }

constexpr int WNE = 16, ANE = 24, BNE = 13, QNE = 46;
struct Plan { int n; signed char q[40], idx[40], k[40], h[40], g[40]; };
template <class O, bool PKM, bool QKM> constexpr Plan make_plan() {
  Plan p{};
  int nr = 0;
  for (int c = 0; c < O::NRO; ++c) nr += ((O::ro[c] >> 4) ? QKM : PKM) ? 4 : 2;
  int prev = 0;
  for (int c = 0; c < O::NRO; ++c) {
    const int q = O::ro[c] >> 4, i = O::ro[c] & 15;
    int av[2] = {0, 0}, k0 = -1;
    for (int m = 0; m < 2 * O::NP; ++m) {
      const int mm = (m + 2 * O::NPEND) % (2 * O::NP);
      if ((q ? O::pj[m >> 1] : O::pi[m >> 1]) == i && m < 2 * O::NPEND) av[((m >> 1) ^ m) & 1] = m;
      if ((q ? O::pj[mm >> 1] : O::pi[mm >> 1]) == i && k0 < 0) k0 = ((mm >> 1) ^ mm) & 1;
    }
    for (int kk = 0; kk < 2; ++kk)
      for (int h = 0; h < ((q ? QKM : PKM) ? 2 : 1); ++h) {
        const int k = k0 ^ kk;
        int g = p.n * O::NE / nr;
        if (g < av[k]) g = av[k];
        if (g < prev + (p.n && O::NE >= nr ? 1 : 0)) g = prev + (p.n && O::NE >= nr ? 1 : 0);
        p.q[p.n] = q; p.idx[p.n] = i; p.k[p.n] = k; p.h[p.n] = h; p.g[p.n] = g; prev = g; ++p.n;
      }
  }
  return p;
}
struct OBase { static constexpr int pidx(int i) { return i; } static constexpr int poff(int) { return 0; } static constexpr int qoff(int j) { return j * 2048; } static constexpr bool swp(int) { return false; } };
template <bool PF, int NE_> struct O44 : OBase {
  static constexpr int TP = 4, TQ = 4, NPA = 4, NP = 16, NPEND = 8, NRO = 8, NE = NE_;
  static constexpr signed char pi[16] = {2, 3, 3, 2, 2, 3, 3, 2, 0, 1, 1, 0, 0, 1, 1, 0}, pj[16] = {0, 0, 1, 1, 2, 2, 3, 3, 0, 0, 1, 1, 2, 2, 3, 3};
  static constexpr signed char ro[8] = {0, PF ? 1 : 16, PF ? 16 : 1, 17, 18, 19, 2, 3};
};
template <bool PF, int NE_> struct O42 : OBase {
  static constexpr int TP = 4, TQ = 2, NPA = 4, NP = 8, NPEND = 4, NRO = 6, NE = NE_;
  static constexpr signed char pi[8] = {2, 3, 3, 2, 0, 1, 1, 0}, pj[8] = {0, 0, 1, 1, 0, 0, 1, 1};
  static constexpr signed char ro[6] = {0, PF ? 1 : 16, PF ? 16 : 1, 17, 3, 2};
};
template <int TH> struct OQ {
  static constexpr int TP = 6, TQ = 5, NPA = 2, NP = 26, NPEND = 13, NRO = 11, NE = QNE;
  static constexpr signed char pi[26] = {3, 4, 5, 5, 4, 3, 3, 4, 5, 5, TH ? 5 : 3, 4, TH ? 3 : 4, 0, 1, 2, 2, 1, 0, 0, 1, 2, 2, TH ? 2 : 0, 1, TH ? 0 : 1};
  static constexpr signed char pj[26] = {0, 0, 0, 1, 1, 1, 2, 2, 2, TH ? 4 : 3, 3, 3, TH ? 3 : 4, 0, 0, 0, 1, 1, 1, 2, 2, 2, TH ? 4 : 3, 3, 3, TH ? 3 : 4};
  static constexpr signed char ro[11] = {0, 1, 16, 2, 17, 18, TH ? 20 : 19, TH ? 19 : 20, 3, 4, 5};
  static constexpr int pidx(int i) { return i / 3; }
  static constexpr int poff(int i) { return i % 3 * 8192; }
  static constexpr int qoff(int j) { return j == 4 ? 0 : (1 + 4 * TH + j) * 2048; }
  static constexpr bool swp(int i) { return i % 3 == 2; }
};
template <bool PKM, bool QKM, int PB, int QB, int XB, int NS, int NT, bool WISP, int BASE, int ROT, int WLD, int WSUB, int ASTEP, int RLX = 0, bool QF = false>
struct Gemm {
  static constexpr int SB = PB + QB + XB, CNT = (PB + QB) / 4096 + (XB ? 1 : 0);
  static constexpr int PO = QF ? QB : 0, QO = QF ? 0 : PB, WB = WISP ? PB : QB, AB = WISP ? QB : PB, WOFF = WISP ? PO : QO, AOFF = WISP ? QO + XB : PO;
  static constexpr int XL = XB / 64, H = NS - 1 - RLX, CA = AB / 4096 + (XB ? 1 : 0), WSTEP = 64 * WLD * 2;
  static_assert(H * CNT <= 63 && BASE + NS * SB <= 163840, "");
  struct Loader {
    static constexpr int NW = WB / 4096, NA = AB / 4096;
    const char* w; const char* ab; unsigned wv[NW], av[NA], xv, lwb, lab, lx; int rot, islot, t; bool xl;
    DEVI Loader(const void* w_, const void* ab_, int lw, int lane, int rot_ = 0)
        : w((const char*)w_ - 3072), ab((const char*)ab_ - 3072), lwb(BASE + WOFF + lw * (NW == 6 ? 2048 : WB / 4)), lab(BASE + AOFF + lw * (AB / 4)), lx(BASE + PB + lw * (XL * 16)),
          rot(rot_), islot((H + ROT) % NS), t(0), xl(lane < XL) {
      const unsigned w0 = (lane >> 3) * (WLD * 2), f0 = (lane & 7) ^ ((lane >> 3) & 2);
#pragma unroll
      for (int i = 0; i < NW; ++i) {
        const unsigned p = NW == 6 ? (i >> 1) * 8 + lw * 2 + (i & 1) : lw * NW + i;
        wv[i] = w0 + ((f0 ^ ((i & 1) << 2)) << 4) + (NW == 6 ? (i >> 1) * WSUB + (lw * 2 + (i & 1)) * (8 * WLD * 2) - (i & 1) * 1024 : (p >> 3) * WSUB + (p & 7) * (8 * WLD * 2) - i * 1024) + 3072;
        asm volatile("" : "+v"(wv[i]));
      }
      asm volatile("" : "+s"(w), "+s"(lwb));
      if (XB) asm volatile("" : "+s"(rot));
      else seta(0, 0, lw, lane);
    }
    DEVI void seta(unsigned a0, unsigned xv_, int lw, int lane) {
      xv = xv_ + 3072;
#pragma unroll
      for (int i = 0; i < NA; ++i) { const unsigned p = lw * NA + i; av[i] = (XB ? a0 + src_kc(p * 64 + lane, D) : p * 2048 + ph(lane * 16)) + 3072 - i * 1024; asm volatile("" : "+v"(av[i])); }
      asm volatile("" : "+s"(ab), "+s"(lab));
      if (XB) { asm volatile("" : "+v"(xv)); asm volatile("" : "+s"(lx)); }
    }
    DEVI int kord(int n) const { if constexpr (XB != 0) return (n + rot) & (NT - 1); else return n; }
    template <int SO> DEVI void issue_w(const char* p, unsigned so = 0) const {
      if constexpr (NW == 6) { dma_grp<2, SO>(p, wv, lwb + so); dma_grp<2, SO + 8192>(p, wv + 2, lwb + so); dma_grp<2, SO + 16384>(p, wv + 4, lwb + so); }
      else dma_grp<NW, SO>(p, wv, lwb + so);
    }
    template <int SO> DEVI void issue_a(const char* p, unsigned so = 0) const {
      if (XB) { if (xl) dma_grp<1, SO>(p, &xv, lx + so); }
      dma_grp<NA, SO>(p, av, lab + so);
    }
    template <int S0, bool W, bool A> DEVI void pre() const {
      if constexpr (S0 < H) {
        if (W) issue_w<((S0 + ROT) % NS) * SB>(w + (kord(0) + S0) * WSTEP);
        if (A) issue_a<((S0 + ROT) % NS) * SB>(ab + (kord(0) + S0) * ASTEP);
        pre<S0 + 1, W, A>();
      }
    }
    template <int T> DEVI void pre_w1() const { issue_w<((T + ROT) % NS) * SB>(w + (kord(0) + T) * WSTEP); sched0(); }
    DEVI void pre_w() const { pre<0, true, false>(); sched0(); }
    template <int T> DEVI void peel() const {
      if constexpr (T < H - 1) {
        wait_vm<(H - 1 - T) * CA + T * CNT>();
        bar();
        issue_w<((T + H + ROT) % NS) * SB>(w + (T + H) * WSTEP); issue_a<((T + H + ROT) % NS) * SB>(ab + (T + H) * ASTEP);
        peel<T + 1>();
      }
    }
    DEVI void start() {
      sched0();
      bar();
      pre<0, false, true>();
      sched0();
      if constexpr (H == 2) wait_vm<CA>();
      else { peel<0>(); islot = (2 * H - 1 + ROT) % NS; t = H - 1; }
    }
    template <class HK> DEVI void run(int tend, HK side) {
#pragma unroll 1
      for (; t < tend; ++t) {
        const char* wk = w + (t + H) * WSTEP; const char* ak = ab + (t + H) * ASTEP;
        unsigned lw_ = lwb + islot * SB, la_ = lab + islot * SB;
        islot = islot + 1 == NS ? 0 : islot + 1;
        asm volatile("" : "+s"(wk), "+s"(ak), "+s"(lw_), "+s"(la_));
        wait_vm<(H - 1) * CNT>();
        bar();
        dma_grp<NW, 0>(wk, wv, lw_);
        side(t);
        dma_grp<NA, 0>(ak, av, la_);
        sched0();
      }
    }
    template <int T, int X, class A, class B, class C> DEVI void step(A pre_, B side, C post) const {
      const int k = kord(T + H);
      const char* wk = w + k * WSTEP; const char* ak = ab + k * ASTEP;
      asm volatile("" : "+s"(wk), "+s"(ak));
      wait_vm<(H - 1) * CNT + X>();
      bar();
      pre_(BASE + ((T + ROT) % NS) * SB);
      issue_w<((T + H + ROT) % NS) * SB>(wk);
      sched0();
      side();
      sched0();
      issue_a<((T + H + ROT) % NS) * SB>(ak);
      post();
      sched0();
    }
    template <int I> DEVI void tail() const { if constexpr (I >= 0) { sched0(); wait_vm<I * CNT>(); bar(); tail<I - 1>(); } }
  };
  static constexpr int NWIN = (NS * SB + 65535) / 65536;
  template <class O> struct Comp {
    static constexpr int TP = O::TP, TQ = O::TQ, NM = 2 * O::NP, NPA = PKM ? O::NPA : 2, NQA = QKM ? TQ : 2;
    static constexpr Plan PL = make_plan<O, PKM, QKM>();
    unsigned prd[NWIN][NPA], qrd[NWIN][NQA];
    bf16x8 fp[TP][2], fq[TQ][2]; bf16x4 lo;
    DEVI void setwin() {
#pragma unroll
      for (int w = 1; w < NWIN; ++w) {
#pragma unroll
        for (int i = 0; i < NPA; ++i) { prd[w][i] = prd[0][i] + w * 65536; asm volatile("" : "+v"(prd[w][i])); }
#pragma unroll
        for (int i = 0; i < NQA; ++i) { qrd[w][i] = qrd[0][i] + w * 65536; asm volatile("" : "+v"(qrd[w][i])); }
      }
#pragma unroll
      for (int i = 0; i < NPA; ++i) asm volatile("" : "+v"(prd[0][i]));
#pragma unroll
      for (int i = 0; i < NQA; ++i) asm volatile("" : "+v"(qrd[0][i]));
    }
    DEVI void setup(int wp, int wq, int lane) {
      const unsigned b = BASE;
      if (PKM) {
#pragma unroll
        for (int i = 0; i < TP; ++i) prd[0][i] = b + rd_km(lane, wp * TP + i);
      } else { prd[0][0] = b + rd_kc(lane, 0) + wp * TP * 2048; prd[0][1] = b + rd_kc(lane, 1) + wp * TP * 2048; }
      if (QKM) {
#pragma unroll
        for (int j = 0; j < TQ; ++j) qrd[0][j] = b + rd_km(lane, wq * TQ + j);
      } else { qrd[0][0] = b + rd_kc(lane, 0) + wq * TQ * 2048; qrd[0][1] = b + rd_kc(lane, 1) + wq * TQ * 2048; }
    }
    template <int C, int N> DEVI static unsigned wa(const unsigned (&b)[NWIN][N], int i) { return b[C >> 16][i] + (C & 0xffff); }
    template <int SLOT, int R> DEVI void rd() {
      constexpr int q = PL.q[R], i = PL.idx[R], k = PL.k[R], h = PL.h[R];
      bf16x8& f = q ? fq[q ? i : 0][k] : fp[q ? 0 : i][k];
      if constexpr (q ? QKM : PKM) {
        unsigned a;
        if constexpr (q) a = wa<SLOT * SB + QO + k * 4096 + h * 512, NQA>(qrd, i); else a = wa<SLOT * SB + PO + O::poff(i) + k * 4096 + h * 512, NPA>(prd, O::pidx(i));
        const bf16x4 x = lds_tr4(a);
        if constexpr (h) f = __builtin_shufflevector(lo, x, 0, 1, 2, 3, 4, 5, 6, 7); else lo = x;
      } else if constexpr (q) f = lds_b128(wa<SLOT * SB + QO + O::qoff(i), 2>(qrd, k));
      else f = lds_b128(wa<SLOT * SB + PO + i * 2048, 2>(prd, k));
    }
    template <int SLOT, int M, int R> DEVI void rds() { if constexpr (R < PL.n) { if constexpr (PL.g[R] == M) rd<SLOT, R>(); if constexpr (PL.g[R] <= M) rds<SLOT, M, R + 1>(); } }
    template <int M, bool IP = false> DEVI void mm(f32x4 (&acc)[TP][TQ]) {
      constexpr int i = O::pi[M >> 1], j = O::pj[M >> 1], k = ((M >> 1) ^ M) & 1;
      if constexpr (IP) { if constexpr (O::swp(i)) mfma_ip(acc[i][j], fq[j][k], fp[i][k]); else mfma_ip(acc[i][j], fp[i][k], fq[j][k]); }
      else if constexpr (O::swp(i)) acc[i][j] = __builtin_amdgcn_mfma_f32_16x16x32_bf16(fq[j][k], fp[i][k], acc[i][j], 0, 0, 0);
      else acc[i][j] = __builtin_amdgcn_mfma_f32_16x16x32_bf16(fp[i][k], fq[j][k], acc[i][j], 0, 0, 0);
    }
    template <int A, int B> DEVI void pend(f32x4 (&acc)[TP][TQ]) { if constexpr (A < B) { mm<2 * A>(acc); mm<2 * A + 1>(acc); pend<A + 1, B>(acc); } }
    template <int SLOT, bool FIRST, int M> DEVI void body(f32x4 (&acc)[TP][TQ]) {
      if constexpr (M < NM) {
        if constexpr (!FIRST || M >= 2 * O::NPEND) { mm<M, true>(acc); sched0(); }
        rds<SLOT, M, 0>();
        sched0();
        body<SLOT, FIRST, M + 1>(acc);
      }
    }
    template <int SLOT, bool FIRST> DEVI void step(f32x4 (&acc)[TP][TQ]) { if (!RLX) wait_lgkm0(); bar(); sched0(); body<SLOT, FIRST, 0>(acc); }
    template <int I, int N, int T0, class MID> DEVI void steps(f32x4 (&acc)[TP][TQ], MID mid) {
      if constexpr (I < N) { mid(I); step<(I + T0 + ROT) % NS, false>(acc); steps<I + 1, N, T0>(acc, mid); }
    }
    DEVI void prep(f32x4 (&acc)[TP][TQ]) { setwin(); pin(acc); }
    DEVI void pin(f32x4 (&acc)[TP][TQ]) {
#pragma unroll
      for (int p = 0; p < O::NP; ++p) asm volatile("" : "+v"(acc[O::pi[p]][O::pj[p]]));
    }
    template <bool CONT, class MID> DEVI void run_steps(f32x4 (&acc)[TP][TQ], MID mid) {
      constexpr int T0 = CONT ? 0 : 1;
      if constexpr (T0) { mid((NT - T0) / NS, -1); step<ROT % NS, true>(acc); }
#pragma unroll 1
      for (int itn = (NT - T0) / NS; itn > 0; --itn) {
        steps<0, NS, T0>(acc, [&](int i) { mid(itn, i); });
      }
      steps<0, (NT - T0) % NS, T0>(acc, [](int) {});
      wait_lgkm0();
      mfma_gap();
    }
  };
};

DEVI f32x4 gelu_arg(f32x4 u) { return u * (-2.302208198f + -0.1029432397f * (u * u)); }
DEVI f32x4 gelu_tanh(f32x4 u) {
  f32x4 a = gelu_arg(u);
#pragma unroll
  for (int e = 0; e < 4; ++e) a[e] = __builtin_amdgcn_exp2f(a[e]);
  a += 1.f;
#pragma unroll
  for (int e = 0; e < 4; ++e) a[e] = __builtin_amdgcn_rcpf(a[e]);
  return u * a;
}

template <int tilesP, int tilesQ, int gp, int gq> DEVI void tile_map(unsigned bid, int& tp, int& tq) {
  const unsigned xcd = bid & 7, idx = bid >> 3;
  constexpr unsigned gP = tilesP / gp, gQ = tilesQ / gq;
  static_assert(gP * gQ == 8, "");
  tp = (xcd % gP) * gp + idx % gp;
  tq = (xcd / gP) * gq + idx / gp;
}

template <int V> struct IC { static constexpr int value = V; };
template <int A, int B, class FN> DEVI void for_c(FN f) { if constexpr (A < B) { f(IC<A>{}); for_c<A + 1, B>(f); } }
DEVI float sel(bool c, float a, float b) { return c ? a : b; }
DEVI u32x2 pk4(const f32x4& v) { return (u32x2){pk2(v[0], v[1]), pk2(v[2], v[3])}; }
DEVI u32x4 cat4(u32x2 a, u32x2 b) { return (u32x4){a.x, a.y, b.x, b.y}; }
DEVI bf16x8 cat(u32x2 a, u32x2 b) { return __builtin_bit_cast(bf16x8, cat4(a, b)); }
DEVI f32x4 mfma(const bf16x8& a, const bf16x8& b, const f32x4& c) { return __builtin_amdgcn_mfma_f32_16x16x32_bf16(a, b, c, 0, 0, 0); }
DEVI u32x4 pack_pair(const f32x4& a, const f32x4& b) {
  const unsigned ax = pk2(a[0], a[1]), ay = pk2(a[2], a[3]), bx = pk2(b[0], b[1]), by = pk2(b[2], b[3]);
  auto rx = __builtin_amdgcn_permlane16_swap(ax, bx, false, false);
  auto ry = __builtin_amdgcn_permlane16_swap(ay, by, false, false);
  const unsigned x0 = rx[0], x1 = rx[1], y0 = ry[0], y1 = ry[1];
  return (u32x4){x0, y0, x1, y1};
}
DEVI int pair_chunk(int fq) { return ((fq & 1) << 1) | (fq >> 1); }
DEVI void sel_frags(bool paired, int fr, int fq, bf16x8 (&s)[2]) {
  const unsigned one = fr & 1 ? 0x3F800000u : 0x3F80u;
#pragma unroll
  for (int i = 0; i < 2; ++i) {
    const bool on = paired ? (fq & 1) == i && (fr >> 3) == (fq >> 1) : (fr >> 2) == fq;
    const int d = paired ? (fr & 7) >> 1 : i * 2 + ((fr >> 1) & 1);
    u32x4 v;
#pragma unroll
    for (int e = 0; e < 4; ++e) v[e] = on && d == e ? one : 0u;
    s[i] = __builtin_bit_cast(bf16x8, v);
  }
}
template <int TQ> DEVI void tile_add(f32x4 (&acc)[4][TQ], const bf16x8 (&s)[2], unsigned a) {
  bf16x8 t[4];
#pragma unroll
  for (int p = 0; p < 4; ++p) t[p] = lds_b128(a + p * 1024);
#pragma unroll
  for (int p = 0; p < 4; ++p)
#pragma unroll
    for (int i = 0; i < 2; ++i) acc[(p & 1) * 2 + i][p >> 1] = mfma(s[i], t[p], acc[(p & 1) * 2 + i][p >> 1]);
}
template <int C> DEVI float dppf(float v) { return __int_as_float(__builtin_amdgcn_update_dpp(0, __float_as_int(v), C, 0xf, 0xf, false)); }
DEVI float red8(float v) { v += dppf<0xB1>(v); v += dppf<0x4E>(v); v += dppf<0x141>(v); return v; }
DEVI float red4(float v) { v += dppf<0xB1>(v); v += dppf<0x4E>(v); return v; }

constexpr unsigned SENT = 0xBF800000u;
constexpr int SPIN_MAX = 1 << 21;

constexpr unsigned MB = 1u << 20;
constexpr unsigned WS_O = 0, WS_H = 8 * MB, WS_U = 16 * MB, WS_X2 = 32 * MB, WS_XP = 40 * MB  ;
constexpr unsigned WS_ST = 120 * MB, ST_CS = 2 * S * 64, WS_XCH = 122 * MB  , WS_EPOCH = 123 * MB, WS_ABORT = 123 * MB + 256  ;
DEVI unsigned tag2(unsigned t) { return t * 0x9E3779B1u ^ 0x5BD1E995u; }
constexpr int MEDGE = 256 * 64;
constexpr unsigned WS_MAIL = 124 * MB;

constexpr int NL = 2, QBR = 4, FLS = 3, NPL = 6, SPON = 1, PC0 = 0, PGAP = 3, WSP = 3, SWW = 0, GSH = 1, GSP = 3, SSC = 1, RB = 1, SI = 2, LPR = 1, CPR = 0, GLW = 4, GL1 = 4, SW0 = 0, SW1 = 6, XE = 0, E2 = 1, LB = 3, XD = 1, XS = 4, JA = 22, PS = 3, TBA = 3, TC1 = 12, NW1 = 9, EW = 12, ET = 8, SPW = 1, PA_1 = 3, PA_2 = 3;
constexpr int L_X = 0, L_TAB = 16384, L_CS = L_TAB + 1152, L_TAG = L_CS + 768, L_BIAS = L_TAG + 128, L_SCR = L_BIAS + 512  , L_QKV = 20480;
static_assert(L_SCR + 1536 <= L_QKV, "");
typedef Gemm<true, false, 24576, 16384, 2048, 3, D / 64, true, 32768, 0, 3 * D, 2 * D, 128> GQ;
typedef Gemm<true, false, 16384, 8192, 0, 5, D / 64, true, 40960, 2, D, 128, 16384, 1, true> GW;
typedef Gemm<true, true, 16384, 16384, 0, 4, S / 64, true, 32768, 1, F, 128, 32768, 0, true> G1;
typedef Gemm<false, true, 16384, 16384, 0, 3, JA, false, 65536, (3 - JA % 3) % 3, S, 1024, 32768> G2A;
typedef Gemm<false, true, 16384, 8192, 0, 4, F / 64 - 2 * JA, false, 65536, 0, S, 128, 32768> G2;

DEVI unsigned get_tag() { return __builtin_amdgcn_readfirstlane(*(volatile LAS unsigned*)(uintptr_t)L_TAG) + 1u; }
DEVI bool spin_fail(int spin, char* ws) {
  if ((spin & 255) != 255) return false;
  gu32* ab = (gu32*)(ws + WS_ABORT);
  const unsigned tag = get_tag();
  if (spin >= SPIN_MAX) { __hip_atomic_store(ab, tag, RLX_AGENT); return true; }
  return __builtin_amdgcn_readfirstlane(__hip_atomic_load(ab, RLX_AGENT)) == tag;
}
DEVI void sync_post(char* ws, int edge, int post, int wid) {
  unsigned o = post >= 0 && wid == 0 ? (post >> 6) * 256u : MB, bit = 1u << (post & 31);
  const rsrc_t r = mkrsrc(ws + WS_MAIL + edge * (MEDGE * 4), MEDGE * 4);
  asm volatile("s_waitcnt vmcnt(0) lgkmcnt(0)" : "+v"(o), "+v"(bit) : "s"(r) : "memory");
  sched0();
  bar();
  if (wid == 0) asm volatile("buffer_atomic_or %0, %1, %2, 0 offen sc1" :: "v"(bit), "v"(o), "s"(r) : "memory");
  sched0();
}
#define POLL1(r) "s_waitcnt vmcnt(2)\n\tv_readfirstlane_b32 %[seen], " r "\n\tglobal_atomic_or " r ", %[z], %[z], %[p] sc0 sc1\n\t" \
  "s_andn2_b32 %[t], %[first], %[seen]\n\ts_cbranch_scc1 3f\n\ts_cmp_eq_u32 %[first], 0\n\ts_cbranch_scc1 4f\n\ts_barrier\n\ts_mov_b32 %[first], 0\n" \
  "4:\n\ts_andn2_b32 %[t], %[second], %[seen]\n\ts_cbranch_scc0 2f\n3:\n\t"
struct Gate {
  const char* p; unsigned miss, due, a, b, c;
  DEVI Gate() : p(nullptr), miss(0), due(0) { asm volatile("v_mov_b32 %0, 0\n\tv_mov_b32 %1, 0\n\tv_mov_b32 %2, 0" : "=v"(a), "=v"(b), "=v"(c)); }
  DEVI void ask() const { asm volatile("s_mov_b64 exec, 1\n\tglobal_atomic_or %0, %1, %1, %2 sc0 sc1\n\ts_mov_b64 exec, -1" :: "v"(a), "v"(0u), "s"(p) : "memory"); }
  template <int DELAY> DEVI void wait(char* ws, int edge, int line, unsigned all, unsigned first, unsigned second, int wid) {
    sched0();
    p = ws + WS_MAIL + (edge * MEDGE + line * 64) * 4;
    second &= all;
    if (wid == 3) {
      due = 1;
      if (DELAY) __builtin_amdgcn_s_sleep(DELAY);
      int spin = 255;
#pragma unroll 1
      for (;; spin += 256) {
        asm volatile("" : "+s"(spin));
        unsigned seen, t, n;
        asm volatile("s_mov_b64 exec, 1\n\ts_waitcnt vmcnt(0)\n\tglobal_atomic_or %[a], %[z], %[z], %[p] sc0 sc1\n\ts_sleep %[gap]\n\t"
                     "global_atomic_or %[b], %[z], %[z], %[p] sc0 sc1\n\ts_sleep %[gap]\n\tglobal_atomic_or %[c], %[z], %[z], %[p] sc0 sc1\n\ts_movk_i32 %[n], 128\n"
                     "1:\n\t" POLL1("%[a]") POLL1("%[b]") POLL1("%[c]") "s_sub_u32 %[n], %[n], 1\n\ts_cbranch_scc0 1b\n"
                     "2:\n\ts_mov_b64 exec, -1 ; raw polls %[a] %[b] %[c]"
                     : [seen] "=&s"(seen), [t] "=&s"(t), [n] "=&s"(n), [first] "+&s"(first)
                     : [p] "s"(p), [second] "s"(second), [gap] "i"(PGAP), [a] "v"(a), [b] "v"(b), [c] "v"(c), [z] "v"(0u) : "memory", "scc");
        miss = ~seen & all;
        if (__builtin_expect(!(miss & second) && !first, 1)) break;
        if (spin_fail(spin, ws)) { if (first) bar(); miss = 0; break; }
      }
    } else bar();
  }
  int ev;
  DEVI void arm(int ni, int cw) { ev = miss ? ni : cw >= 0 ? 2 : 0; asm volatile("" : "+s"(ev), "+s"(miss), "+s"(due)); }
  DEVI void head(int it, int cw, int lane, int wid) {
    if (__builtin_expect(it == ev, 0)) {
      asm volatile("" : "+v"(lane)); asm volatile("" : "+s"(cw), "+s"(wid));
      if (miss) ask();
      if (it == 2) code_warm(cw, lane, L_SCR + wid * 256);
      ev = cw >= 0 && it > 2 ? 2 : 0;
      asm volatile("" : "+s"(ev));
    }
  }
  DEVI void late(char* ws) {
    if (__builtin_expect(due != 0, 0)) {
      asm volatile("" : "+s"(ws));
      asm volatile("s_waitcnt vmcnt(0) ; raw settled %0 %1 %2" :: "v"(a), "v"(b), "v"(c) : "memory");
      for (int spin = 0; miss; ++spin) {
        unsigned seen;
        asm volatile("v_readfirstlane_b32 %0, %1" : "=s"(seen) : "v"(a));
        miss &= ~seen;
        if (!miss || spin_fail(spin, ws)) break;
        ask();
        wait_vm<0>();
      }
      miss = due = 0;
      asm volatile("" : "+s"(miss), "+s"(due));
    }
  }
};
DEVI unsigned halo_off(int s0, int lw, int lane) {
  const int c = lw * 32 + (lane & 31), row = c >> 3, ch = (c & 7) ^ ((row >> 1) & 7), tok = s0 - 16 + row;
  return (unsigned)((tok < 0 ? 0 : tok) * D * 2 + ch * 16);
}
DEVI int qkv_rot(int bid) { return (bid & 3) * 4 + (bid & 4) * 2; }
DEVI unsigned x_off(int tp, int tq, int w, int fr, int fq) { return (unsigned)(((tq * 64 + (w >> 1) * 32 + fr) * D + tp * 128 + (w & 1) * 64 + pair_chunk(fq) * 8) * 2); }

DEVI void pfb_q(const u16* w, int bid, int j, int n) {
  const int r = slow_res(w);
  if ((r >> 2) == (bid & 1)) {
    const unsigned o = (unsigned)(((bid & 2) * 4 + r) * 128 + (j >> 1) * (3 * D * 2) + (j & 1) * 64 + ((n + qkv_rot(bid)) & 15) * GQ::WSTEP);
    spf1<0>(w, o); spf1<2048>(w, o); spf1<4096>(w, o);
  }
}
constexpr int PFR = NPL * 128 * 1024, PF_END = TBA * 256 + NW1 * PFR / (F * 2) / 8 * 8;
static_assert(TBA * 256 * F * 2 + NW1 * PFR <= S * F * 2 && 3 * PFR >= D * D * 2, "");
struct Pfc {
  const char* p; const char* q; int pc;
  DEVI Pfc(const u16* wo, const u16* w1, unsigned g) : p((const char*)wo + g * (NPL * 128)), q((const char*)w1 + TBA * 256 * F * 2 + g * (NPL * 128)), pc(PC0) {}
  DEVI void go() {
    unsigned t, u;
    asm volatile("s_getreg_b32 %2, hwreg(HW_REG_IB_STS, 8, 4)\n\ts_cmp_lt_u32 %1, %5\n\ts_cselect_b32 %3, 0, 1\n\ts_or_b32 %2, %2, %3\n\ts_cbranch_scc1 1f\n\t"
                 "s_load_dwordx2 s[100:101], %0, 60\n\ts_load_dwordx2 s[100:101], %0, 188\n\ts_load_dwordx2 s[100:101], %0, 316\n\t"
                 "s_load_dwordx2 s[100:101], %0, 444\n\ts_load_dwordx2 s[100:101], %0, 572\n\ts_load_dwordx2 s[100:101], %0, 700\n\t"
                 ".if %8 == 7\n\ts_load_dwordx2 s[100:101], %0, 828\n\t.endif\n\t"
                 "s_mov_b32 %2, %7\n\ts_cmp_lt_u32 %1, 2\n\ts_cselect_b32 %2, %6, %2\n\ts_mov_b64 s[40:41], %0\n\ts_add_u32 s40, s40, %2\n\ts_addc_u32 s41, s41, 0\n\t"
                 "s_cmp_eq_u32 %1, 2\n\ts_cselect_b64 %0, %4, s[40:41]\n\ts_add_u32 %1, %1, 1\n1:"
                 : "+s"(p), "+s"(pc), "=&s"(t), "=&s"(u) : "s"(q), "i"(3 + NW1), "i"((D * D * 2 - PFR) / 2), "i"(PFR), "i"(NPL) : "s40", "s41", "s100", "s101", "scc");
  }
};
DEVI void spare_touch(const u16* w1, const u16* w2, int bid, int wid, int lane) {
  if (!SPON) return;
  const int j = ((bid >> 3) - 28) * 8 + wid, fr = lane & 15, fq = lane >> 4;
  const unsigned scr = L_QKV + wid * 256;
#pragma unroll 1
  for (int r = PF_END + (j * 2 + ((bid >> 2) & 1)) * 4; r < S; r += 256) { if (fr < 14) touch4(w1 + (size_t)r * F + (bid & 3) * 896, fq * (F * 2) + fr * 128, scr); wait_vm<SPW>(); }
#pragma unroll 1
  for (int r = (j * 2 + (bid & 1)) * 4; r < JA * 64; r += 256) { touch4(w2 + (size_t)(r + (bid & 2) * JA * 32) * S + (bid & 4) * 256, fq * (S * 2) + fr * 128, scr); wait_vm<SPW>(); }
#pragma unroll 1
  for (int r = JA * 128 + ((31 - j) * 2 + (bid & 1)) * 8; r < F; r += 512) { touch4(w2 + (size_t)r * S + ((bid & 7) >> 1) * 512, (lane >> 3) * (S * 2) + (lane & 7) * 128, scr); wait_vm<SPW>(); }
}

template <int TH> DEVI void qkv_attn(char* ws, int l, int h, int lane, int hp, int sb, int cw, int bid, int tp, int tq, const u16* wqkv) {
  asm volatile("; half %1" : "+v"(lane) : "i"(TH));
  const int fr = lane & 15, fq = lane >> 4;
  GQ::Comp<OQ<TH>> g;
#pragma unroll
  for (int c = 0; c < 2; ++c) { g.prd[0][c] = 32768 + rd_km(lane, h * 2 + c); g.qrd[0][c] = 32768 + rd_kc(lane, c); }
  f32x4 acc[6][5];
#pragma unroll
  for (int i = 0; i < 6; ++i)
#pragma unroll
    for (int j = 0; j < 5; ++j) acc[i][j] = (f32x4){0.f, 0.f, 0.f, 0.f};
  const int par = fr & 1, da = fr - 4 * fq - par;
  const bool ca = da >= 0, cb = da >= 2;
  const unsigned tm = L_TAB + (16 + 64 * TH) * 4, tc = L_CS + h * 128;
  unsigned am = tm + fr * 4 - 320, a4 = tm + fq * 16 - 320, aa = tm + 576 + (4 * fq + par - (ca ? 0 : 16)) * 4, ab = tm + 576 + (4 * fq + par + 2 - (cb ? 0 : 16)) * 4;
  unsigned c4 = tc + fq * 16, c1 = tc + 512 + fr * 4, xb = L_QKV + h * 3072 + lane * 16, hm = par ? 0xffff0000u : 0xffffu, m0 = ca ? 0u : hm, m1 = cb ? 0u : hm;
  unsigned oo = ph((unsigned)((((sb * 2 + TH) * 16 + hp) << 13) + fr * 128 + (((h * 4 + pair_chunk(fq)) ^ (fr >> 1)) << 4)));
  asm volatile("" : "+v"(am), "+v"(a4), "+v"(aa), "+v"(ab), "+v"(c4), "+v"(c1), "+v"(xb), "+v"(hm), "+v"(m0), "+v"(m1), "+v"(oo));
  g.prep(acc);
  if (l) {
    const int sbc = (tq >> 1) + fq;
    sync_post(ws, 3, !TH && (lane < 16 || (lane < 32 && (tq & 1) && sbc < 16)) ? (sbc * 16 + fr) * 64 + (tq - 2 * sbc + 1) * 8 + tp : -1, TH * 2 + h);
  }
  Gate gt;
  if (l) gt.wait<SW0>(ws, 3, sb * 16 + hp, sb ? 0xffffffu : 0xffff00u, (sb ? 0x10101u : 0x10100u) << ((qkv_rot(bid) & 15) >> 1), 0x30303u << ((qkv_rot(bid) & 15) >> 1), TH * 2 + h);
  else if (!TH || !h) wait_vm<0>();
  else if ((slow_res(wqkv) >> 2) == (bid & 1)) {
    if (ET) __builtin_amdgcn_s_sleep(ET);
    if ((lane & 3) < 3 && lane < 40) touch4(wqkv, (unsigned)(((qkv_rot(bid) * 64 + 128 + (bid >> 3) * 10 + (lane >> 2)) & 1023) * (3 * D * 2) + (lane & 3) * (D * 2) + ((bid & 2) * 4 + slow_res(wqkv)) * 128), L_SCR + 768);
  }
  g.template run_steps<false>(acc, [&](int it, int i) {
    if (i < 0) gt.arm(it, cw);
        if (i == 0) gt.head(it, cw, lane, TH * 2 + h);
    if (TH && i == 1) gt.late(ws);
  });
  const rsrc_t ro = mkrsrc(ws + WS_O, S * D * 4);
  if (l == 0) bar();
  float mt[5], cv[2], rq[4], ra[4], rb[4]; f32x4 m4[5], cq[2], ck[2];
#pragma unroll
  for (int c = 0; c < 2; ++c) {
    ck[c] = *(const LAS f32x4*)(uintptr_t)(c4 + 256 + c * 64); cv[c] = *(const LAS float*)(uintptr_t)(c1 + c * 64);
    cq[c] = *(const LAS f32x4*)(uintptr_t)(c4 + c * 64);
  }
#pragma unroll
  for (int j = 4; j >= 0; --j) {
    const int o = 320 + (j == 4 ? -64 - 256 * TH : j * 64);
    if (j < 4 || !TH) mt[j] = *(const LAS float*)(uintptr_t)(am + o);
    if (j < 4 || TH) m4[j] = *(const LAS f32x4*)(uintptr_t)(a4 + o);
  }
#pragma unroll
  for (int j = 0; j < 4; ++j) { rq[j] = *(const LAS float*)(uintptr_t)(am + 896 + j * 64); ra[j] = *(const LAS float*)(uintptr_t)(aa + j * 64); rb[j] = *(const LAS float*)(uintptr_t)(ab + j * 64); }
  u32x2 kp[2][5], qp[2][4], vp[2][5];
  auto fk = [&](int j) { kp[0][(j + 1) % 5] = pk4(acc[1][j]); kp[1][(j + 1) % 5] = pk4(acc[4][j] + ck[1] * mt[j]); };
  auto fv = [&](int j) { vp[0][(j + 1) % 5] = pk4(acc[2][j]); vp[1][(j + 1) % 5] = pk4(acc[5][j] + m4[j] * cv[1]); };
  auto fqq = [&](int j) { qp[0][j] = pk4(acc[0][j]); qp[1][j] = pk4(acc[3][j] + cq[1] * mt[j]); };
  g.template pend<10, 13>(acc);
#pragma unroll
  for (int i = 0; i < 6; ++i) { __builtin_amdgcn_sched_group_barrier(0x100, 3, 0); __builtin_amdgcn_sched_group_barrier(0x8, 1, 0); }
  sched0();
  for_c<0, 20>([&](auto mc) {
    constexpr int m = mc.value, n0 = m * 52 / 20, n1 = (m + 1) * 52 / 20;
    g.template mm<(18 - (m & ~1)) + (m & 1)>(acc);
    sched0();
#pragma unroll
    for (int n = n0; n < n1; ++n) {
      const int t = 12 - (n >> 2), e = n & 3, j = t == 12 ? 4 : t / 3, i = t == 12 ? 1 + TH : t % 3;
      float x = acc[i][j][e];
      asm volatile("v_fmac_f32 %0, %1, %2" : "+v"(x) : "v"(i == 2 ? m4[j][e] : i ? ck[0][e] : cq[0][e]), "v"(i == 2 ? cv[0] : mt[j]));
      acc[i][j][e] = x;
    }
    sched0();
  });
  volatile LAS unsigned* xf = (volatile LAS unsigned*)(uintptr_t)(L_TAG + 16 + (h * 2 + TH) * 4);
  if (TH) { fv(4); *(volatile LAS bf16x8*)(uintptr_t)(xb + 2048) = cat(vp[0][0], vp[1][0]); *xf = l + 1; fk(3); }
  else { fk(3); fv(3); *(volatile LAS bf16x8*)(uintptr_t)xb = cat(kp[0][4], kp[1][4]); *(volatile LAS bf16x8*)(uintptr_t)(xb + 1024) = cat(vp[0][4], vp[1][4]); *xf = l + 1; }
  sched0();
  fqq(3); fk(2); fqq(2); fk(1);
  sched0();
  const f32x4 z = {0.f, 0.f, 0.f, 0.f};
  f32x4 sc[4][2]; float a[4], b[4], m[4];
  auto scores = [&](int j) {
#pragma unroll
    for (int u = 1; u >= 0; --u) sc[j][u] = mfma(cat(kp[0][j + u], kp[1][j + u]), cat(qp[0][j], qp[1][j]), z);
  };
  auto pick = [&](int j) {
    const f32x4 s0 = sc[j][0], s1 = sc[j][1];
    a[j] *= sel(par, sel(ca, s1[1], s0[1]), sel(ca, s1[0], s0[0]));
    b[j] *= sel(par, sel(cb, s1[3], s0[3]), sel(cb, s1[2], s0[2]));
    if (!TH && j == 0) { a[j] = sel(ca || sb, a[j], -1e9f * 1.4426950408889634f); b[j] = sel(cb || sb, b[j], -1e9f * 1.4426950408889634f); }
    m[j] = __builtin_fmaxf(a[j], b[j]);
  };
  auto expo = [&](int j) { a[j] = __builtin_amdgcn_exp2f(a[j] - m[j]); b[j] = __builtin_amdgcn_exp2f(b[j] - m[j]); m[j] = a[j] + b[j]; };
  auto pv = [&](int j) {
    const unsigned pa = pk2(a[j] * ra[j], a[j] * ra[j]), pb = pk2(b[j] * rb[j], b[j] * rb[j]);
    const bf16x8 pf = __builtin_bit_cast(bf16x8, (u32x4){pa & m0, pb & m1, pa & (m0 ^ hm), pb & (m1 ^ hm)});
#pragma unroll
    for (int c = 0; c < 2; ++c) sc[j][c] = mfma(cat(vp[c][j], vp[c][j + 1]), pf, z);
  };
  auto outp = [&](int j) { st16(ro, oo + j * 4096, pack_pair(sc[j][0] * m[j], sc[j][1] * m[j])); };
  unsigned f; u32x4 xv, xk;
  auto xrd = [&]() {
    f = xf[1 - 2 * TH]; xv = *(volatile LAS u32x4*)(uintptr_t)(xb + (TH ? 1024 : 2048));
    if (TH) xk = *(volatile LAS u32x4*)(uintptr_t)xb;
  };
  auto xget = [&]() {
    sched0();
    while (__builtin_expect(__builtin_amdgcn_readfirstlane(f) != l + 1, 0)) xrd();
    vp[0][0] = (u32x2){xv[0], xv[1]}; vp[1][0] = (u32x2){xv[2], xv[3]};
    if (TH) { kp[0][0] = (u32x2){xk[0], xk[1]}; kp[1][0] = (u32x2){xk[2], xk[3]}; }
  };
  {
    fqq(1); fk(0); fqq(0);
    if (!TH) fk(4);
    xrd();
    sched0();
    if (TH) fv(3);
    fv(2); fv(1); fv(0);
#pragma unroll
    for (int c = 0; c < 2; ++c)
#pragma unroll
      for (int j = 0; j < 4; ++j) asm volatile("" : "+v"(qp[c][j]), "+v"(kp[c][j + 1]), "+v"(vp[c][j + 1]));
    if (!TH) asm volatile("" : "+v"(kp[0][0]), "+v"(kp[1][0]));
    xget();
    scores(3); scores(2); scores(1); scores(0);
#pragma unroll
    for (int j = 0; j < 4; ++j) { const float r = rq[j] * (0.17677669529663687f * 1.4426950408889634f); a[j] = r * ra[j]; b[j] = r * rb[j]; }
#pragma unroll
    for (int i = 0; i < 8; ++i) { __builtin_amdgcn_sched_group_barrier(0x8, 1, 0); __builtin_amdgcn_sched_group_barrier(0x2, 2, 0); }
    sched0();
    pick(3); pick(2); pick(1); pick(0);
    float u, w;
    swp32(m[0], m[2]); swp32(m[1], m[3]); u = m[0]; w = m[1];
    asm volatile("v_max_f32 %0, %0, %2\n\tv_max_f32 %1, %1, %3\n\ts_nop 1" : "+v"(u), "+v"(w) : "v"(m[2]), "v"(m[3]));
    swp16(u, w); u = vmax(u, w);
    xch16(u, u, w); xch32(u, m[0], m[2]); xch32(w, m[1], m[3]);
    expo(3); expo(2); expo(1); expo(0);
    pv(3); pv(2); pv(1); pv(0);
    swp32(m[0], m[2]); swp32(m[1], m[3]); u = m[0] + m[2]; w = m[1] + m[3];
    swp16(u, w); u = __builtin_amdgcn_rcpf(u + w);
    xch16(u, u, w); xch32(u, m[0], m[2]); xch32(w, m[1], m[3]);
    outp(3); outp(2); outp(1); outp(0);
  }
}

extern "C" __global__ __launch_bounds__(512, 2) void mega_kernel(const u16* x0, const u16* wqkv, const u16* wo, const u16* w1, const u16* w2, const u16* b1, const u16* b2, u16* out, char* ws) {
  const int wid0 = __builtin_amdgcn_readfirstlane(threadIdx.x >> 6);
  asm volatile("s_getpc_b64 s[40:41]\n\ts_andn2_b32 s40, s40, 63\n\ts_load_dwordx2 s[100:101], s[40:41], %0 offset:124\n\ts_load_dwordx2 s[100:101], s[40:41], %0 offset:252"
               :: "s"(__builtin_amdgcn_readfirstlane(threadIdx.x) << 2) : "s40", "s41", "s100", "s101", "scc");
  sched0();
#define PHASE_VARS \
      int tid = threadIdx.x, bid = blockIdx.x, wid = wid0; \
      asm volatile("" : "+v"(tid)); asm volatile("" : "+s"(bid), "+s"(wid), "+s"(ws)); \
      const int lw = wid - 4, lt = tid - 256, lane = tid & 63, fr = lane & 15, fq = lane >> 4, wp = wid & 1, wq = wid >> 1; \
      const unsigned j = (bid >> 3) * 4 + lw; \
      int hp, sb; tile_map<16, 16, 4, 8>(bid, hp, sb);            \
      int tp, tq; tile_map<8, 32, 4, 8>(bid, tp, tq);             \
      int fp, fq1; tile_map<28, 8, 7, 4>(bid, fp, fq1);           \
      const int cw = bid < 16 ? wid + 4 * (bid >> 3) : -1; \
      (void)cw; (void)j; (void)lt; (void)wp; (void)wq; (void)lw; (void)fr; (void)fq; (void)hp; (void)sb; (void)tp; (void)tq; (void)fp; (void)fq1;
#define LOADER_VARS PHASE_VARS asm volatile("" : "+s"(wqkv), "+s"(wo), "+s"(w1), "+s"(w2));

  if (__builtin_expect(wid0 >= 4, 1)) {
    if (LPR) __builtin_amdgcn_s_setprio(3);
    const u16* xin = x0;
    Pfc pf(wo, w1, blockIdx.x * 4 + wid0 - 4);
#pragma unroll 1
    for (int l = 0; l < NL; ++l) {
      {
        LOADER_VARS
        const int s0 = sb * 128;
        GQ::Loader L(wqkv + hp * 64, xin, lw, lane, qkv_rot(bid));
        if (l == 0) L.pre_w1<0>();
        L.seta((unsigned)(s0 * D * 2), halo_off(s0, lw, lane), lw, lane);
        if (l == 0) { L.issue_a<0>(L.ab + L.kord(0) * 128); if (!SI) L.pre<1, true, true>(); }
        sched0();
        const rsrc_t rs = mkrsrc(ws + WS_ST, MB);
        f32x4 c = {0.f, 0.f, 0.f, 0.f}; f32x2 s1 = {0.f, 0.f}, s2 = {0.f, 0.f}, o = {0.f, 0.f}; float pv = 0.f;
        bf16x8 ones = {0x3F80, 0x3F80, 0x3F80, 0x3F80, 0x3F80, 0x3F80, 0x3F80, 0x3F80};
        const int ct = lw & 1 ? 8 + (sb & 3) : sb & 7; const bool cs = l == 0 && lw >= 2;
        int tpf = (slow_res(wqkv) >> 2) != (bid & 1) ? 0 : lw < 2 ? 10 : 9;
        unsigned ra = GQ::AOFF + ((hp & 3) * 32 + lw * 8) * 128 + lane * 16, rb = rd_km(lane, ct);
        unsigned so = lane & 7 ? MB : ph((unsigned)(((l * S + s0 + (hp & 3) * 32 + lw * 8 + (lane >> 3)) * 4 + (hp >> 2)) * 16));
        unsigned co = fr >= 2 || !cs ? MB : ph((unsigned)(ST_CS + ((hp * 96 + ct * 8 + fq * 2 + fr) * 2 + (sb >> 3)) * 16));
        asm volatile("" : "+v"(ra), "+v"(rb), "+v"(so), "+v"(co), "+v"(c), "+v"(s1), "+v"(s2), "+v"(ones), "+v"(o));
        asm volatile("" : "+s"(tpf));
        if (SI && l == 0) L.pre_w1<1>();
        sched0();
        unsigned to[3], xo = x_off(tp, tq, lw, fr, fq), q2 = ((bid & 2) * 4 + slow_res(wqkv)) * 128 + ((bid >> 3) * 2 + (lw & 1)) * (3 * D * 2);
#pragma unroll
        for (int i = 0; i < 3; ++i) {
          const int r = s0 - 16 + (i == 2 && lw ? 64 : i * 64) + (lt >> 2);
          to[i] = ph((unsigned)(((l * S + (r < 0 ? 0 : r)) * 4 + (lt & 3)) * 16));
        }
        unsigned ta = (lt & 3) < (lw ? 2 : 3) ? L_TAB + ((lt & 3) * 64 + (lt >> 2)) * 4 : L_SCR, ts = ph((unsigned)(ST_CS + (hp * 192 + (lw ? lt - 64 : lt)) * 16));
        asm volatile("" : "+v"(to[0]), "+v"(to[1]), "+v"(to[2]), "+v"(xo), "+v"(ta), "+v"(ts));
        asm volatile("" : "+s"(q2));
        if (SI == 1 && l == 0) L.pre<1, false, true>();
        sched0();
        const GW::Loader W(wo + tp * 128, ws, lw, lane);
        if (SI == 2 && l == 0) L.pre<1, false, true>();
        sched0();
        auto pfq = [&](int n) { const unsigned o = q2 + L.kord(n) * GQ::WSTEP; spf2<0>(wqkv, o); spf2<2048>(wqkv, o); spf2<4096>(wqkv, o); };
        if (l) {
          if (QBR == 3) bar();
          if (!(EW & 8)) L.pre_w();
          if (tpf) { pfq(2 + (lw >> 1)); pfq(4 + (lw >> 1)); }
          if (QBR == 4) bar();
          L.start();
        }
        if (LPR == 1) __builtin_amdgcn_s_setprio(0);
        unsigned tag, tg2; u32x4 gs, a[3];
        auto rowst = [&](int i) {
          const float m0 = __uint_as_float(a[i][0]), m = red4(m0) * 0.25f, q = red4(__uint_as_float(a[i][2]) + 256.f * (m0 - m) * (m0 - m));
          o = (f32x2){sel((lt & 3) == i, m, o.x), sel((lt & 3) == i, q, o.y)};
          asm volatile("" : "+v"(o));
        };
        for_c<0, 16 - GQ::H>([&](auto tc) {
          constexpr int t = tc.value;
          u32x4 v; unsigned tv;
          L.step<t, t == 5 || t == 6 ? 1 : t == 9 || t == 10 ? 4 : 0>([&](unsigned ls) {
            if (t < 4) v = *(const LAS u32x4*)(uintptr_t)(ls + ra);
            if (t == 3) tv = *(volatile LAS unsigned*)(uintptr_t)L_TAG;
          }, [&]() {
            if (t < 8 && cs) {
              constexpr int ls = 32768 + t % 3 * GQ::SB;
              bf16x4 x[4];
#pragma unroll
              for (int e = 0; e < 4; ++e) x[e] = lds_tr4(rb + (ls & ~0x7fff) + (ls & 0x7fff) + (e >> 1) * 4096 + (e & 1) * 512);
#pragma unroll
              for (int ks = 0; ks < 2; ++ks) c = mfma(__builtin_shufflevector(x[2 * ks], x[2 * ks + 1], 0, 1, 2, 3, 4, 5, 6, 7), ones, c);
            } else if (t >= 4) {
              if (t >= 10 || t >= tpf) { if (t < TC1) pf.go(); }
              else if (lw < 2) pfq(t + 4);
              else if (t == 8) { pfq(14); pfq(15); }
            }
            if (t < 4) {
              if (t == 0) { pv = bflo(v[0]); asm volatile("" : "+v"(pv)); }
#pragma unroll
              for (int e = 0; e < 4; ++e) {
                if (SSC) {
                  float x = bflo(v[e]), y = bfhi(v[e]), a0 = s1.x, a1 = s1.y, b0 = s2.x, b1 = s2.y;
                  asm volatile("v_sub_f32 %0, %0, %6\n\tv_sub_f32 %1, %1, %6\n\tv_add_f32 %2, %2, %0\n\tv_add_f32 %3, %3, %1\n\tv_fmac_f32 %4, %0, %0\n\tv_fmac_f32 %5, %1, %1"
                               : "+v"(x), "+v"(y), "+v"(a0), "+v"(a1), "+v"(b0), "+v"(b1) : "v"(pv));
                  s1 = (f32x2){a0, a1}; s2 = (f32x2){b0, b1};
                } else { const f32x2 d = (f32x2){bflo(v[e]), bfhi(v[e])} - pv; s1 += d; s2 += d * d; }
              }
              asm volatile("" : "+v"(s1), "+v"(s2));
            }
            if (t == 3) { tag = __builtin_amdgcn_readfirstlane(tv) + 1u; tg2 = tag2(tag); if (tpf == 10) pfq(7); if (l && tpf == 9) pfq(6); }
            if (t == 4) {
              const float d1 = (s1.x + s1.y) * (1.f / 32.f), ml = pv + d1, m = red8(ml) * 0.125f;
              const float q = red8(vmax((s2.x + s2.y) - 32.f * d1 * d1, 0.f) + 32.f * (ml - m) * (ml - m));
              gs = (u32x4){__float_as_uint(m), tag, __float_as_uint(q), tg2};
              asm volatile("" : "+v"(gs));
            }
            if (t == 8) { gs = (u32x4){__float_as_uint(sel(fr, c[2], c[0])), tag, __float_as_uint(sel(fr, c[3], c[1])), tg2}; asm volatile("" : "+v"(gs)); }
            if (t == (RB ? 12 : 11)) {
#pragma unroll
              for (int i = 0; i < 3; ++i) ld_ok(a[i]);
              for (int spin = 0;; ++spin) {
                unsigned bad = 0;
#pragma unroll
                for (int i = 0; i < 3; ++i) bad |= (a[i][1] ^ tag) | (a[i][3] ^ tg2);
                if (__builtin_expect(__all(bad == 0), 1) || spin_fail(spin, ws)) break;
                __builtin_amdgcn_s_sleep(8);
#pragma unroll
                for (int i = 0; i < 3; ++i) ld_again(a[i], rs, to[i]);
              }
              if (!RB) rowst(0);
            }
            if (t == 12 && !RB) { rowst(1); rowst(2); }
            if (t == 13 && RB == 2) rowst(0);
            if (t == 13 && !RB) { LAS float* p = (LAS float*)(uintptr_t)ta; p[0] = -o.x; p[144] = __builtin_amdgcn_rsqf(o.y * (1.f / D) + EPS); }
          }, [&]() {
            if (t == 4) st16(rs, so, gs);
            if (t == 8) {
              st16(rs, co, gs);
              sched0();
#pragma unroll
              for (int i = 0; i < 3; ++i) ld_raw(a[i], rs, to[i]);
            }
          });
        });
        if (l == 0) {
#pragma unroll
          for (int p = 0; p < 4; ++p) dma16((const char*)xin + (p >> 1) * 32768 + (p & 1) * 64, xo, L_X + lw * 4096 + p * 1024);
          ld_raw(gs, rs, ts);
          wait_vm<GQ::CNT + 5>();
        } else wait_vm<GQ::CNT>();
        bar();
        W.pre_w1<0>();
        if (RB) { if (RB == 1) rowst(0); rowst(1); rowst(2); LAS float* p = (LAS float*)(uintptr_t)ta; p[0] = -o.x; p[144] = __builtin_amdgcn_rsqf(o.y * (1.f / D) + EPS); }
        wait_lgkm0();
        if (l == 0) wait_vm<9>(); else wait_vm<4>();
        bar();
        W.pre_w1<1>(); W.pre_w1<2>();
        if (l == 0) {
          wait_vm<12>();
          ld_ok(gs);
          for (int spin = 0;; ++spin) {
            if (__builtin_expect(__all(((gs[1] ^ tag) | (gs[3] ^ tg2)) == 0), 1) || spin_fail(spin, ws)) break;
            __builtin_amdgcn_s_sleep(8);
            ld_again(gs, rs, ts);
          }
          float c0 = __uint_as_float(gs[0]), c1 = __uint_as_float(gs[2]);
          c0 += dppf<0xB1>(c0); c1 += dppf<0xB1>(c1);
          *(LAS float*)(uintptr_t)(L_CS + (lw ? lt - 64 : lt) * 4) = sel(lt & 1, c1, c0);
        }
        if (l == 0 && lw < TBA) { if (fr < 14) touch4(w1 + (size_t)(((bid >> 3) * TBA + lw) * 8 + (bid & 4)) * F + (bid & 3) * 896, fq * (F * 2) + fr * 128, L_SCR + lw * 256); }
        if (l == 0) { wait_lgkm0(); bar(); }
      }
      {
        LOADER_VARS
        GW::Loader L(wo + tp * 128, ws + WS_O + (size_t)tq * 32 * 8192, lw, lane);
        const G1::Loader W(w1 + fp * 128, ws, lw, lane);
        unsigned po = ((bid & 1) * 8 + (slow_res(wo) ^ (j & 1))) * 128 + (j >> 1) * (D * 2);
#pragma unroll
        for (int k = GW::H; k < 10; ++k) spf2(wo, po + k * GW::WSTEP);
        asm volatile("" : "+s"(po));
        if (!(LB & 1)) { sched0(); bar(); } else wait_lgkm0();
#pragma unroll
        for (int k = 10; k < 16; ++k) spf2(wo, po + k * GW::WSTEP);
        if (LB & 1) { sched0(); bar(); }
        L.start();
        L.run(16 - GW::H, [&](int) { pf.go(); });
        pf.go();
        if (E2 && bid < 224) {
          wait_vm<2 * GW::CNT>(); bar(); W.pre_w1<1>();
          wait_vm<GW::CNT + 4>(); bar(); W.pre_w1<2>();
          wait_vm<8>(); bar();
          bar(); W.pre_w1<0>();
        } else { L.tail<GW::H - 1>(); bar(); }
      }
      {
        LOADER_VARS
        if (bid < 224) {
          G1::Loader L(w1 + fp * 128, ws + WS_H + (size_t)fq1 * 64 * 16384, lw, lane);
          if (!E2) L.pre_w();
          const G2A::Loader W((const char*)(w2 + (tq & 23) * 64) + (tq & 8 ? JA * G2::WSTEP : 0), ws, lw, lane);
          const int g0 = (bid & 3) * 14, ga = (g0 + ((slow_res(w1) - g0) & 7)) ^ (j & 1), gb = ga + 8 < g0 + 14 ? ga + 8 : ga;
          const unsigned j2 = j < 32 ? 112 + (j & 15) : j;
          unsigned pa = (j >> 1) * (F * 2) + ga * 128, pb = (j >> 1) * (F * 2) + gb * 128, pd = (j2 >> 1) * (F * 2) + (j & 16 ? gb : ga) * 128;
          asm volatile("" : "+s"(pa), "+s"(pb), "+s"(pd));
          auto pfb = [&](int k) { spf2(w1, pa + k * G1::WSTEP); spf2(w1, pb + k * G1::WSTEP); spf2(w1, pd + k * G1::WSTEP); };
#pragma unroll
          for (int k = G1::H; k < G1::H + PA_1; ++k) pfb(k);
          wait_lgkm0(); pf.go();
          sched0();
          bar();
          if (!(LB & 2)) pf.go();
          L.start();
          L.run(32 - G1::H, [&](int t) {
            if (pf.pc < 3 + NW1) pf.go();
            else if (t < 32 - G1::H - PA_1) pfb(t + G1::H + PA_1);
          });
          if (EW & 4) {
            wait_vm<2 * G1::CNT>(); bar(); W.pre_w1<1>();
            wait_vm<G1::CNT + 4>(); bar();
            wait_vm<4>(); bar(); W.pre_w1<0>();
          } else L.tail<G1::H - 1>();
          bar();
        } else {
          wait_lgkm0(); pf.go();
          bar();
          if (l == 0) {
            __builtin_amdgcn_s_sleep(24);
            while (pf.pc < 3 + NW1) { wait_lgkm0(); pf.go(); }
            spare_touch(w1, w2, bid, wid, lane);
          }
        }
        pf.pc = 99;
      }
      {
        LOADER_VARS
        constexpr int NB = F / 64 - 2 * JA;
        const char* ub = ws + WS_U + (size_t)tp * 112 * 16384;
        G2A::Loader A((const char*)(w2 + (tq & 23) * 64) + (tq & 8 ? JA * G2::WSTEP : 0), ub + (tq & 8 ? JA * 32768 : 0), lw, lane);
        if (!(EW & 4) || bid >= 224) A.pre_w();
        const GQ::Loader W(wqkv + hp * 64, ws, lw, lane, qkv_rot(bid));
        unsigned pb = ((bid & 4) * 4 + (j & 1) * 8 + slow_res(w2)) * 128 + (j >> 1) * (S * 2) + JA * G2::WSTEP, pa = pb - (bid & 2 ? 0 : JA * G2::WSTEP);
        asm volatile("" : "+s"(pa), "+s"(pb));
        auto side = [&](int n) { n += G2A::H + PA_2; if (n < JA + NB) spf2(w2, (n < JA ? pa : pb) + n * G2::WSTEP); };
#pragma unroll
        for (int k = 0; k < PA_2; ++k) side(k - PA_2);
        G2::Loader L((const char*)(w2 + tq * 64) + 2 * JA * G2::WSTEP, ub + 2 * JA * 32768, lw, lane);
        gu32* xf = (gu32*)(ws + WS_MAIL) + (l * 4 + 2) * MEDGE + (tq * 8 + tp) * 64 + 28 + lw;
        const char* xp = ws + WS_XP + (unsigned)(((bid ^ 2) * 4 + lw) << 14);
        unsigned fl = L_QKV + lw * 256, xv = ph(lane * 16);
        asm volatile("" : "+s"(xf), "+s"(xp), "+s"(fl)); asm volatile("" : "+v"(xv));
        sched0();
        bar();
        A.start();
        A.run(JA - G2A::H, [&](int t) { side(t); });
        auto stage = [&](auto s) { L.issue_w<s.value * G2::SB>(L.w + s.value * G2::WSTEP); L.issue_a<s.value * G2::SB>(L.ab + s.value * 32768); sched0(); };
        wait_vm<G2A::CNT>(); bar(); stage(IC<0>{}); side(JA - 2);
        wait_vm<G2::CNT>(); bar(); stage(IC<1>{}); side(JA - 1);
        wait_vm<G2::CNT>(); bar(); stage(IC<2>{}); stage(IC<3>{}); side(JA);
        L.t = 1; L.islot = 0;
        L.run(NB - G2::H, [&](int t) {
          if (t == NB - G2::H - FLS) asm volatile("s_mov_b32 m0, %2\n\ts_nop 0\n\tglobal_load_lds_dword %0, %1 sc1" :: "v"(0u), "s"(xf), "s"(fl) : "memory");
          side(JA + t);
        });
        auto xdma = [&]() {
#pragma unroll
          for (int i = 0; i < 4; ++i) dma16(xp + i * 2048, xv, L_X + lw * 4096 + i * 1024);
          sched0();
        };
        sched0(); wait_vm<2 * G2::CNT>(); bar();
        if (__builtin_amdgcn_readfirstlane(*(volatile LAS unsigned*)(uintptr_t)fl)) {
          xdma();
          if ((EW & 8) && l == 0) { W.pre_w1<0>(); wait_vm<G2::CNT + 10>(); bar(); W.pre_w1<1>(); wait_vm<16>(); bar(); wait_vm<12>(); }
          else { wait_vm<G2::CNT + 4>(); bar(); wait_vm<4>(); bar(); wait_vm<0>(); }
        } else {
          wait_vm<G2::CNT>(); bar(); wait_vm<0>(); bar();
          for (int spin = 0;; ++spin) {
            if (__builtin_amdgcn_readfirstlane(__hip_atomic_load(xf, RLX_AGENT)) || spin_fail(spin, ws)) break;
            __builtin_amdgcn_s_sleep(2);
          }
          xdma();
          if ((EW & 8) && l == 0) { W.pre_w(); wait_vm<12>(); } else wait_vm<0>();
        }
        bar();
        xin = (const u16*)(ws + WS_X2);
        if (NL == 1) bar();
      }
    }
    return;
  }

  {
    PHASE_VARS
    if (cw >= 0) code_warm(cw, lane, L_SCR + wid * 256);
    pfb_q(wqkv, bid, (bid >> 3) * 4 + wid, 0); pfb_q(wqkv, bid, (bid >> 3) * 4 + wid, 1);
    if (wid < 3) {
      if (tid == 0) asm volatile("s_mov_b32 m0, %2\n\ts_nop 0\n\tglobal_load_lds_dword %0, %1 sc1" :: "v"(WS_EPOCH), "s"(ws), "s"(L_TAG) : "memory");
      const rsrc_t rz = mkrsrc(ws + WS_XCH, 3 * MB);
      if (tid < 128) st16(rz, ph((((tid >> 6) * 256 + tq * 8 + tp) * 64 + lane) * 16), (u32x4){0u, SENT, 0u, SENT});
      else {
        const int e = (tid >> 3) & 7, line = e == 0 || e == 7 ? (2 * sb + (e & 1)) * 8 + (hp & 7) : e * 256 + bid;
        st16(rz, WS_MAIL - WS_XCH + line * 256 + (tid & 7) * 16, (u32x4){0u, 0u, 0u, 0u});
      }
      if (wid == 1 && bid < 224) touch4(b1 + fp * 128, lane * 4, L_BIAS);
      if (wid == 2) touch4(b2 + tq * 64, (lane & 31) * 4, L_BIAS + 256);
      if (wid == 0) *(LAS u32x4*)(uintptr_t)(L_TAG + 16) = (u32x4){0u, 0u, 0u, 0u};
    }
    if (CPR) __builtin_amdgcn_s_setprio(CPR);
  }
#pragma unroll 1
  for (int l = 0; l < NL; ++l) {
    {
      PHASE_VARS
      if (wid & 2) qkv_attn<1>(ws, l, wp, lane, hp, sb, cw, bid, tp, tq, wqkv);
      else qkv_attn<0>(ws, l, wp, lane, hp, sb, cw, bid, tp, tq, wqkv);
    }
    {
      PHASE_VARS
      GW::Comp<O42<false, WNE>> g; g.setup(wp, wq, lane);
      g.setwin();
      const rsrc_t rx = mkrsrc(ws + WS_XCH + l * (MB / 2), MB / 2), rh = mkrsrc(ws + WS_H, S * D * 4);
      unsigned xa = L_X + wid * 4096 + lane * 16;
      unsigned xs = fq != fr >> 2 ? MB : ph((unsigned)(((tq * 8 + tp) * 64 + wp * 32 + wq * 16 + fr) * 16)), xl = ph((unsigned)((tq * 512 + fq * 128 + wq * 16 + fr) * 16));
      const unsigned h0 = ph((unsigned)(((tp * 32 + tq) << 14) + wp * 8192 + (wq * 32 + fr) * 128 + ((pair_chunk(fq) ^ (km_f(fr) << 1)) << 4)));
      u32x4 ho = {h0, h0 ^ 64, h0 + 4096, (h0 ^ 64) + 4096};
      bf16x8 ones = {0x3F80, 0x3F80, 0x3F80, 0x3F80, 0x3F80, 0x3F80, 0x3F80, 0x3F80};
      asm volatile("" : "+v"(xa), "+v"(xs), "+v"(xl), "+v"(ho), "+v"(ones));
      Gate gt;
      auto edge = [&]() {
        sync_post(ws, l * 4, lane < 16 ? (sb * 16 + lane) * 64 + hp : -1, wid);
        gt.wait<0>(ws, l * 4, tq * 8 + tp, 0xffffu, (1u << GW::H) - 1, (1u << (GW::H + GLW)) - 1, wid);
        sched0();
      };
      if (WSP == 3) edge();
      f32x4 acc[4][2];
#pragma unroll
      for (int i = 0; i < 4; ++i)
#pragma unroll
        for (int jj = 0; jj < 2; ++jj) acc[i][jj] = (f32x4){0.f, 0.f, 0.f, 0.f};
      { bf16x8 s[2]; sel_frags(true, fr, fq, s); tile_add(acc, s, xa); }
      g.pin(acc);
      if (WSP == 2) edge(); else mfma_gap();
      g.run_steps<false>(acc, [&](int it, int i) {
        if (i < 0) gt.arm(it, cw);
        if (i == 0) gt.head(it, cw, lane, wid);
        if (i == GLW - 1) gt.late(ws);
      });
      bar();
      sched0();
      g.pend<0, 4>(acc);
      u32x4 pk[2][2]; float mp[2];
      {
        f32x4 sm[2], gm[2];
#pragma unroll
        for (int i = 0; i < 2; ++i)
#pragma unroll
          for (int jj = 0; jj < 2; ++jj) {
            pk[jj][i] = cat4(pk4(acc[2 * i][jj]), pk4(acc[2 * i + 1][jj])); asm volatile("" : "+v"(pk[jj][i]));
            const bf16x8 x = __builtin_bit_cast(bf16x8, pk[jj][i]);
            gm[jj] = mfma(x, x, i ? gm[jj] : (f32x4){0.f, 0.f, 0.f, 0.f}); sm[jj] = mfma(ones, x, i ? sm[jj] : (f32x4){0.f, 0.f, 0.f, 0.f});
          }
        u32x4 st;
#pragma unroll
        for (int jj = 0; jj < 2; ++jj) {
          const f32x4 gv = gm[jj];
          st[2 * jj] = __float_as_uint(sm[jj][0]); st[2 * jj + 1] = __float_as_uint(sel(fr & 2, sel(fr & 1, gv[3], gv[2]), sel(fr & 1, gv[1], gv[0])));
          mp[jj] = sm[jj][0] * (1.f / 64.f);
        }
        st16(rx, xs, st);
      }
      sched0();
      f32x4 gr[4];
      auto gld = [&]() {
        asm volatile("" ::: "memory");
#pragma unroll
        for (int p = 0; p < 4; ++p) gr[p] = __builtin_bit_cast(f32x4, __builtin_amdgcn_raw_buffer_load_b128(rx, xl + p * 1024, 0, 16));
        sched0();
      };
      if (XE == 1) gld();
#pragma unroll
      for (int p = 0; p < 4; ++p) *(LAS u32x4*)(uintptr_t)(xa + p * 1024) = pk[p >> 1][p & 1];
      asm volatile("" : "+v"(mp[0]), "+v"(mp[1]));
#pragma unroll
      for (int jj = 0; jj < 2; ++jj)
#pragma unroll
        for (int i = 0; i < 2; ++i) {
          const auto rx_ = __builtin_amdgcn_permlane16_swap(pk[jj][i][0], pk[jj][i][2], false, false), ry_ = __builtin_amdgcn_permlane16_swap(pk[jj][i][1], pk[jj][i][3], false, false);
          const unsigned x0 = rx_[0], x1 = rx_[1], y0 = ry_[0], y1 = ry_[1];
          acc[2 * i][jj] = (f32x4){bflo(x0), bfhi(x0), bflo(y0), bfhi(y0)} - mp[jj]; acc[2 * i + 1][jj] = (f32x4){bflo(x1), bfhi(x1), bflo(y1), bfhi(y1)} - mp[jj];
          asm volatile("" : "+v"(acc[2 * i][jj]), "+v"(acc[2 * i + 1][jj]));
          if (XE == 2 + jj * 2 + i) { sched0(); gld(); }
        }
      if (!XE) { if (XD) __builtin_amdgcn_s_sleep(XD); gld(); }
      for (int spin = 0;; ++spin) {
        const float lo = vmin3(vmin3(gr[0][1], gr[0][3], gr[1][1]), vmin3(gr[1][3], gr[2][1], gr[2][3]), vmin3(gr[3][1], gr[3][3], gr[3][3]));
        if (!__any(lo < 0.f) || spin_fail(spin, ws)) break;
        if (XS) __builtin_amdgcn_s_sleep(XS);
        gld();
      }
      {
        const f32x4 t = (gr[0] + gr[1]) + (gr[2] + gr[3]);
        float m[2] = {t[0], t[2]}, q[2] = {t[1], t[3]}, u, w, v, y;
        swp32(m[0], m[1]); swp32(q[0], q[1]);
        xch16(m[0] + m[1], u, w); xch16(q[0] + q[1], v, y);
        const float mean = (u + w) * (1.f / D), var = __builtin_fmaxf((v + y) * (1.f / D) - mean * mean, 0.f), r1 = __builtin_amdgcn_rsqf(var + EPS);
        xch32(mean, m[0], m[1]);
        xch32(r1 * __builtin_amdgcn_rsqf(var * r1 * r1 + EPS), q[0], q[1]);
#pragma unroll
        for (int jj = 0; jj < 2; ++jj) {
          const float c = (mp[jj] - m[jj]) * q[jj];
#pragma unroll
          for (int i = 0; i < 2; ++i) st16(rh, ho[jj * 2 + i], cat4(pk4(acc[2 * i][jj] * q[jj] + c), pk4(acc[2 * i + 1][jj] * q[jj] + c)));
        }
      }
    }
    {
      PHASE_VARS
      const int post = lane < 28 ? (tp * 28 + lane) * 64 + tq : -1;
      if (bid >= 224) {
        sync_post(ws, l * 4 + 1, post, wid);
        if (l == 0 && bid == 255 && wid == 0) { const unsigned tag = get_tag(); if (lane == 0) __hip_atomic_store((gu32*)(ws + WS_EPOCH), tag, RLX_AGENT); }
        if (l == 0) { __builtin_amdgcn_s_sleep(24); spare_touch(w1, w2, bid, wid, lane); }
      } else {
        G1::Comp<O44<false, 32>> g; g.setup(wp, wq, lane);
        f32x4 acc[4][4];
#pragma unroll
        for (int i = 0; i < 4; ++i) { const u32x2 b = *(const LAS u32x2*)(uintptr_t)(L_BIAS + (wp * 4 + i) * 32 + fq * 8);
#pragma unroll
          for (int jj = 0; jj < 4; ++jj) acc[i][jj] = (f32x4){bflo(b.x), bfhi(b.x), bflo(b.y), bfhi(b.y)}; }
        const rsrc_t ro = mkrsrc(ws + WS_U, D * F * 4);
        const unsigned u0 = ph((unsigned)(((fq1 * 56 + fp * 2 + wp) << 14) + (wq * 64 + fr) * 128 + ((pair_chunk(fq) ^ (fr >> 1)) << 4)));
        u32x4 uo = {u0, u0 + 4096, u0 + 8192, u0 + 12288}, uo2 = uo ^ 64;
        asm volatile("" : "+v"(uo), "+v"(uo2));
        g.prep(acc);
        sync_post(ws, l * 4 + 1, post, wid);
        Gate gt; gt.wait<SW0>(ws, l * 4 + 1, fq1 * 28 + fp, ~0u, (1u << G1::H) - 1, (1u << (G1::H + GL1)) - 1, wid);
        g.run_steps<false>(acc, [&](int it, int i) {
          if (i < 0) gt.arm(it, cw);
        if (i == 0) gt.head(it, cw, lane, wid);
          if (i == GL1 - 1) gt.late(ws);
        });
        bar();
        sched0();
        if (GSH) {
          for_c<0, 2>([&](auto jc) {
            constexpr int jj = jc.value;
            f32x4 a[2];
#pragma unroll
            for (int i = 0; i < 2; ++i) { a[i] = gelu_arg(acc[i][jj]); asm volatile("" : "+v"(a[i])); }
            sched0();
            for_c<0, 4>([&](auto ec) { constexpr int e = ec.value; g.mm<jj * 8 + e>(acc); sched0(); a[0][e] = __builtin_amdgcn_exp2f(a[0][e]); a[1][e] = __builtin_amdgcn_exp2f(a[1][e]); asm volatile("" : "+v"(a[0]), "+v"(a[1])); sched0(); });
            a[0] += 1.f; a[1] += 1.f; asm volatile("" : "+v"(a[0]), "+v"(a[1]));
            sched0();
            for_c<0, 4>([&](auto ec) { constexpr int e = ec.value; g.mm<jj * 8 + 4 + e>(acc); sched0(); a[0][e] = __builtin_amdgcn_rcpf(a[0][e]); a[1][e] = __builtin_amdgcn_rcpf(a[1][e]); asm volatile("" : "+v"(a[0]), "+v"(a[1])); sched0(); });
            st16(ro, uo[jj], pack_pair(acc[0][jj] * a[0], acc[1][jj] * a[1]));
          });
        } else g.pend<0, 8>(acc);
#pragma unroll
        for (int n = GSH ? 2 : 0; n < 8; ++n) { const int i = n >> 2, jj = n & 3; st16(ro, (i ? uo2 : uo)[jj], pack_pair(gelu_tanh(acc[2 * i][jj]), gelu_tanh(acc[2 * i + 1][jj]))); }
      }
    }
    {
      PHASE_VARS
      asm volatile("" : "+s"(out));
      G2A::Comp<O44<true, ANE>> ga; ga.setup(wp, wq, lane);
#pragma unroll
      for (int jj = 0; jj < 4; ++jj) ga.qrd[0][jj] = 65536 + rd_km(lane, wq * 2 + (jj & 1)) + (((jj >> 1) ^ (tq >> 3)) & 1) * 8192;
      G2::Comp<O42<true, BNE>> g; g.setup(wp, wq, lane);
      g.setwin();
      ga.setwin();
      Gate gt;
      auto edge = [&]() {
        sync_post(ws, l * 4 + 2, bid < 224 && lane < 32 ? (lane * 8 + fq1) * 64 + fp : -1, wid);
        gt.wait<0>(ws, l * 4 + 2, tq * 8 + tp, 0xfffffffu, tq & 8 ? 1u << JA / 2 : 1u, tq & 8 ? 7u << JA / 2 : 7u, wid);
        sched0();
      };
      if (GSP == 3) edge();
      f32x4 ac4[4][4], acc[4][2];
#pragma unroll
      for (int jj = 0; jj < 2; ++jj) { const float b = bf2f(*(const LAS u16*)(uintptr_t)(L_BIAS + 256 + (wq * 2 + jj) * 32 + fr * 2));
#pragma unroll
        for (int i = 0; i < 4; ++i) { ac4[i][jj] = (f32x4){b, b, b, b}; ac4[i][jj + 2] = (f32x4){0.f, 0.f, 0.f, 0.f}; } }
      unsigned xa = L_X + wid * 4096 + lane * 16, xo = (unsigned)((bid * 4 + wid) << 14) + ph(lane * 16), oo = x_off(tp, tq, wid, fr, fq), xo2 = xo + 4096, oo2 = oo + 32768;
      unsigned fo = lane ? MB : (((tq ^ 8) * 8 + tp) * 64 + 28 + wid) * 4u, one = 1u;
      bf16x8 s[2]; sel_frags(false, fr, fq, s);
      if (GSP == 4) { asm volatile("" : "+v"(xa), "+v"(xo), "+v"(oo), "+v"(fo), "+v"(s[0]), "+v"(s[1])); edge(); }
      tile_add(ac4, s, xa);
      const rsrc_t rx = mkrsrc(ws + WS_XP, 16 * MB), rf = mkrsrc(ws + WS_MAIL + (l * 4 + 2) * (MEDGE * 4), MEDGE * 4), ro = mkrsrc(l ? (const void*)out : (const void*)(ws + WS_X2), S * D * 2);
      asm volatile("" : "+v"(xa), "+v"(xo), "+v"(oo), "+v"(xo2), "+v"(oo2), "+v"(fo), "+v"(one), "+v"(s[0]), "+v"(s[1]));
      ga.pin(ac4);
      static_assert(JA % 2 == 0 && G2A::H == 2, "");
      if (GSP == 2) edge(); else mfma_gap();
      ga.run_steps<false>(ac4, [&](int it, int i) {
        if (i < 0) gt.arm(it, cw);
        if (i == 0) gt.head(it, cw, lane, wid);
        if (i == 2) gt.late(ws);
      });
      ga.pend<4, 8>(ac4);
#pragma unroll
      for (int i = 0; i < 2; ++i)
#pragma unroll
        for (int jj = 0; jj < 2; ++jj) st16(rx, (jj ? xo2 : xo) + i * 2048, cat4(pk4(ac4[2 * i][jj + 2]), pk4(ac4[2 * i + 1][jj + 2])));
#pragma unroll
      for (int i = 0; i < 4; ++i) { acc[i][0] = ac4[i][0]; acc[i][1] = ac4[i][1]; }
#pragma unroll
      for (int k = 0; k < 2; ++k) { g.fp[2][k] = ga.fp[2][k]; g.fp[3][k] = ga.fp[3][k]; g.fq[0][k] = ga.fq[0][k]; g.fq[1][k] = ga.fq[1][k]; }
      sched0();
      g.run_steps<true>(acc, [&](int it, int i) {
        if (i == PS % 4 && __builtin_expect(it == (F / 64 - 2 * JA) / 4 - PS / 4, 0)) {
          wait_vm<0>();
          __builtin_amdgcn_raw_buffer_store_b32(one, rf, fo, 0, 16);
        }
      });
      bar();
      sched0();
      g.pend<0, 4>(acc);
      tile_add(acc, s, xa);
      u32x4 xr[4];
#pragma unroll
      for (int i = 0; i < 2; ++i)
#pragma unroll
        for (int jj = 0; jj < 2; ++jj) { xr[jj * 2 + i] = pack_pair(acc[2 * i][jj], acc[2 * i + 1][jj]); st16(ro, (jj ? oo2 : oo) + i * 64, xr[jj * 2 + i]); }
      if (l == 0) {
#pragma unroll
        for (int p = 0; p < 4; ++p) *(LAS u32x4*)(uintptr_t)(xa + p * 1024) = xr[p];
        if (NL == 1) sync_post(ws, 3, -1, wid);
      }
    }
  }
}

extern "C" void kernel_launch(void* const* d_in, const int* in_sizes, int n_in,
                              void* d_out, int out_size, void* d_ws, size_t ws_size,
                              hipStream_t stream) {
  (void)hipFuncSetAttribute((const void*)mega_kernel, hipFuncAttributeMaxDynamicSharedMemorySize, 163840);
  mega_kernel<<<256, 512, 163840, stream>>>((const u16*)d_in[0], (const u16*)d_in[1], (const u16*)d_in[2], (const u16*)d_in[4], (const u16*)d_in[6], (const u16*)d_in[5], (const u16*)d_in[7], (u16*)d_out, (char*)d_ws);
}
```

```cpp
#define LEAN
#define STAMP
#define PROBE 46
#define PROBE0 38
#define PROBEF 4
#include <hip/hip_runtime.h>
#include <hip/hip_bf16.h>
#include <stdint.h>

typedef unsigned short u16;
typedef unsigned long long u64;
typedef short bf16x8 __attribute__((ext_vector_type(8)));
typedef short bf16x4 __attribute__((ext_vector_type(4)));
typedef float f32x4 __attribute__((ext_vector_type(4)));
typedef float f32x2 __attribute__((ext_vector_type(2)));
typedef unsigned u32x4 __attribute__((ext_vector_type(4)));
typedef unsigned u32x2 __attribute__((ext_vector_type(2)));
typedef __bf16 bf16v2 __attribute__((ext_vector_type(2)));
#define LAS __attribute__((address_space(3)))
#define GAS __attribute__((address_space(1)))
#define DEVI __device__ __forceinline__
typedef __attribute__((address_space(1))) unsigned gu32;
typedef __attribute__((address_space(1))) unsigned long long gu64;

constexpr int S = 2048, D = 1024, F = 3584;
constexpr float EPS = 1e-5f;

DEVI float bf2f(u16 v) { return __uint_as_float(((unsigned)v) << 16); }
DEVI float bflo(unsigned v) { return __uint_as_float(v << 16); }
DEVI float bfhi(unsigned v) { return __uint_as_float(v & 0xffff0000u); }
DEVI unsigned pk2(float lo, float hi) { bf16v2 p; p.x = (__bf16)lo; p.y = (__bf16)hi; return __builtin_bit_cast(unsigned, p); }

DEVI const void* uni_ptr(const void* p) {
  const u64 v = (u64)p;
  return (const void*)(((u64)(unsigned)__builtin_amdgcn_readfirstlane((unsigned)(v >> 32)) << 32) | (unsigned)__builtin_amdgcn_readfirstlane((unsigned)v));
}
DEVI void dma16(const void* sbase, unsigned voff, unsigned ldsaddr) {
  ldsaddr = __builtin_amdgcn_readfirstlane(ldsaddr);
  asm volatile("s_mov_b32 m0, %2\n\ts_nop 0\n\tglobal_load_lds_dwordx4 %0, %1 sc1" :: "v"(voff), "s"(sbase), "s"(ldsaddr) : "memory");
}
template <int N, int IMM> DEVI void dma_grp(const void* sbase, const unsigned* v, unsigned lb) {
  static_assert(N == 1 || N == 2 || N == 4, "");
  if constexpr (N == 4)
    asm volatile("s_add_u32 m0, %1, %2\n\ts_nop 0\n\tglobal_load_lds_dwordx4 %3, %0 sc1\n\tglobal_load_lds_dwordx4 %4, %0 offset:1024 sc1\n\t"
                 "global_load_lds_dwordx4 %5, %0 offset:2048 sc1\n\tglobal_load_lds_dwordx4 %6, %0 offset:3072 sc1"
                 :: "s"(sbase), "s"(lb), "i"(IMM), "v"(v[0]), "v"(v[1]), "v"(v[2]), "v"(v[3]) : "memory", "scc");
  else if constexpr (N == 2)
    asm volatile("s_add_u32 m0, %1, %2\n\ts_nop 0\n\tglobal_load_lds_dwordx4 %3, %0 sc1\n\tglobal_load_lds_dwordx4 %4, %0 offset:1024 sc1"
                 :: "s"(sbase), "s"(lb), "i"(IMM), "v"(v[0]), "v"(v[1]) : "memory", "scc");
  else
    asm volatile("s_add_u32 m0, %1, %2\n\ts_nop 0\n\tglobal_load_lds_dwordx4 %3, %0 sc1" :: "s"(sbase), "s"(lb), "i"(IMM), "v"(v[0]) : "memory", "scc");
}
template <int N> DEVI void wait_vm() { asm volatile("s_waitcnt vmcnt(%0)" :: "i"(N) : "memory"); }
DEVI void wait_lgkm0() { asm volatile("" ::: "memory"); __builtin_amdgcn_s_waitcnt(0xC07F); asm volatile("" ::: "memory"); }
DEVI void mfma_ip(f32x4& c, const bf16x8& a, const bf16x8& b) { asm volatile("v_mfma_f32_16x16x32_bf16 %0, %1, %2, %0" : "+v"(c) : "v"(a), "v"(b)); }
DEVI void mfma_gap() { __builtin_amdgcn_sched_barrier(0); asm volatile("s_nop 7"); __builtin_amdgcn_sched_barrier(0); }
DEVI void bar() { asm volatile("" ::: "memory"); __builtin_amdgcn_s_barrier(); asm volatile("" ::: "memory"); }
DEVI void sched0() { __builtin_amdgcn_sched_barrier(0); }

DEVI bf16x8 lds_b128(unsigned addr) { return *(const LAS bf16x8*)(uintptr_t)addr; }
DEVI bf16x4 lds_tr4(unsigned addr) { return __builtin_amdgcn_ds_read_tr16_b64_v4i16((LAS bf16x4*)(uintptr_t)addr); }

typedef __amdgpu_buffer_rsrc_t rsrc_t;
DEVI rsrc_t mkrsrc(const void* p, unsigned bytes) { return __builtin_amdgcn_make_buffer_rsrc((void*)p, 0, bytes, 0x00020000); }
DEVI void st16(rsrc_t r, unsigned off, u32x4 v) { __builtin_amdgcn_raw_buffer_store_b128(v, r, off, 0, 16); }
DEVI void ld_raw(u32x4& v, rsrc_t r, unsigned off) { asm volatile("buffer_load_dwordx4 %0, %1, %2, 0 offen sc1 ; raw load" : "=&v"(v) : "v"(off), "s"(r) : "memory"); }
DEVI void ld_again(u32x4& v, rsrc_t r, unsigned off) { asm volatile("buffer_load_dwordx4 %0, %1, %2, 0 offen sc1\n\ts_waitcnt vmcnt(0)" : "=&v"(v) : "v"(off), "s"(r) : "memory"); }
DEVI void ld_ok(u32x4& v) { asm volatile("; raw ok %0" : "+v"(v)); }
#define RLX_AGENT __ATOMIC_RELAXED, __HIP_MEMORY_SCOPE_AGENT

template <int N> DEVI float ror16(float v) { return __int_as_float(__builtin_amdgcn_update_dpp(0, __float_as_int(v), 0x120 + N, 0xf, 0xf, false)); }
DEVI float red16(float v) { v += ror16<8>(v); v += ror16<4>(v); v += ror16<2>(v); v += ror16<1>(v); return v; }
DEVI void xch32(float v, float& a, float& b) { unsigned u = __float_as_uint(v); auto r = __builtin_amdgcn_permlane32_swap(u, u, false, false); unsigned x = r[0], y = r[1]; a = __uint_as_float(x); b = __uint_as_float(y); }
DEVI void xch16(float v, float& a, float& b) { unsigned u = __float_as_uint(v); auto r = __builtin_amdgcn_permlane16_swap(u, u, false, false); unsigned x = r[0], y = r[1]; a = __uint_as_float(x); b = __uint_as_float(y); }
DEVI void swp32(float& a, float& b) { auto r = __builtin_amdgcn_permlane32_swap(__float_as_uint(a), __float_as_uint(b), false, false); unsigned x = r[0], y = r[1]; a = __uint_as_float(x); b = __uint_as_float(y); }
DEVI void swp16(float& a, float& b) { auto r = __builtin_amdgcn_permlane16_swap(__float_as_uint(a), __float_as_uint(b), false, false); unsigned x = r[0], y = r[1]; a = __uint_as_float(x); b = __uint_as_float(y); }
DEVI float colsum4(float v) { float a, b; xch32(v, a, b); v = a + b; xch16(v, a, b); return a + b; }
DEVI float vmax(float a, float b) { float r; asm("v_max_f32 %0, %1, %2" : "=v"(r) : "v"(a), "v"(b)); return r; }
DEVI float vmin3(float a, float b, float c) { float r; asm("v_min3_f32 %0, %1, %2, %3" : "=v"(r) : "v"(a), "v"(b), "v"(c)); return r; }
DEVI float vmax3(float a, float b, float c) { float r; asm("v_max3_f32 %0, %1, %2, %3" : "=v"(r) : "v"(a), "v"(b), "v"(c)); return r; }

DEVI unsigned src_kc(int c, int ld) { int row = c >> 3, ch = (c & 7) ^ ((row >> 1) & 7); return (unsigned)(row * ld * 2 + ch * 16); }
DEVI int km_f(int k) { return ((k >> 1) & 1) | (((k >> 3) & 1) << 1); }
DEVI unsigned ph(unsigned x) { return ((x >> 7) << 8) | (x & 127u); }
DEVI unsigned rd_kc(int lane, int ks) { int r = lane & 15, g = lane >> 4; return (unsigned)(r * 128 + (((ks * 4 + g) ^ (r >> 1)) << 4)); }
DEVI unsigned rd_km(int lane, int ct) {
  int g = lane >> 4, q = (lane & 15) >> 2, p = lane & 3;
  int f = ((q >> 1) & 1) | ((g & 1) << 1);
  return (unsigned)((ct >> 2) * 8192 + (8 * g + q) * 128 + (((ct & 3) ^ f) << 5) + 8 * p);
}

DEVI void stamp(unsigned a) { unsigned x, y;
  asm volatile("s_memrealtime s[98:99]\n\tv_mov_b32 %1, %2\n\ts_waitcnt lgkmcnt(0)\n\tv_mov_b32 %0, s98\n\tds_write_b32 %1, %0" : "=&v"(x), "=&v"(y) : "s"(a) : "s98", "s99"); }
#define ST(k, i) do { if (__builtin_amdgcn_readfirstlane(threadIdx.x) == 0) stamp(19968 + ((k) * 8 + (i)) * 4); } while (0)
#define STL(k, i) do { if (__builtin_amdgcn_readfirstlane(threadIdx.x) == 256) stamp(19968 + ((k) * 8 + (i)) * 4); } while (0)

constexpr unsigned CODE_BYTES = 53248;
DEVI void code_warm(int part, int lane, unsigned scratch) {
  unsigned long long pc;
  asm volatile("s_getpc_b64 s[40:41]\n\ts_add_u32 s40, s40, mega_kernel@rel32@lo+4\n\ts_addc_u32 s41, s41, mega_kernel@rel32@hi+12\n\ts_mov_b64 %0, s[40:41]" : "=s"(pc) :: "s40", "s41", "scc");
  const unsigned off = (unsigned)(part * 64 + lane) * 128u;
  scratch = __builtin_amdgcn_readfirstlane(scratch);
  if (off < CODE_BYTES) asm volatile("s_nop 3\n\ts_mov_b32 m0, %2\n\ts_nop 0\n\tglobal_load_lds_dword %0, %1" :: "v"(off), "s"(pc), "s"(scratch) : "memory");
}

DEVI unsigned src_km(int lt, int ld) { const int k = lt >> 3; return (unsigned)(k * ld * 2 + (((lt & 7) ^ (km_f(k) << 1)) << 4)); }
DEVI void touch4(const void* sbase, unsigned voff, unsigned ldsaddr) {
  ldsaddr = __builtin_amdgcn_readfirstlane(ldsaddr); sbase = uni_ptr(sbase);
  asm volatile("s_nop 3\n\ts_mov_b32 m0, %2\n\ts_nop 0\n\tglobal_load_lds_dword %0, %1" :: "v"(voff), "s"(sbase), "s"(ldsaddr) : "memory");
}

template <int IMM> DEVI void spf1(const void* base, unsigned soff) { asm volatile("s_load_dword s101, %0, %1 offset:%2" :: "s"(base), "s"(soff), "i"(IMM) : "s101"); }
template <int IMM = 0> DEVI void spf2(const void* base, unsigned soff) { asm volatile("s_load_dwordx2 s[100:101], %0, %1 offset:%2" :: "s"(base), "s"(soff), "i"(IMM + 60) : "s100", "s101"); }
template <int ST> DEVI void spf2x6(const void* base, unsigned soff) {
  asm volatile("s_load_dwordx2 s[100:101], %0, %1 offset:%2\n\ts_load_dwordx2 s[100:101], %0, %1 offset:%3\n\ts_load_dwordx2 s[100:101], %0, %1 offset:%4\n\t"
               "s_load_dwordx2 s[100:101], %0, %1 offset:%5\n\ts_load_dwordx2 s[100:101], %0, %1 offset:%6\n\ts_load_dwordx2 s[100:101], %0, %1 offset:%7"
               :: "s"(base), "s"(soff), "i"(60), "i"(60 + ST), "i"(60 + 2 * ST), "i"(60 + 3 * ST), "i"(60 + 4 * ST), "i"(60 + 5 * ST) : "s100", "s101");
}
template <int ST> DEVI void spf1x12(const void* base, unsigned soff) {
  spf1<0>(base, soff); spf1<ST>(base, soff); spf1<2 * ST>(base, soff); spf1<3 * ST>(base, soff); spf1<4 * ST>(base, soff); spf1<5 * ST>(base, soff);
  spf1<6 * ST>(base, soff); spf1<7 * ST>(base, soff); spf1<8 * ST>(base, soff); spf1<9 * ST>(base, soff); spf1<10 * ST>(base, soff); spf1<11 * ST>(base, soff);
}
DEVI int lgkm_out() { int n; asm volatile("s_getreg_b32 %0, hwreg(HW_REG_IB_STS, 8, 4)" : "=s"(n)); return n; }
DEVI int slow_res(const void* w) { return (3 - ((unsigned)(uintptr_t)w >> 7)) & 7; }

#define MTD
#define MT(i)
#define MTOUT(k, w)
#define CSP
#define CSA
#define WNE 16
#define ANE 24
#define BNE 13
#define QNE 46
struct Plan { int n; signed char q[40], idx[40], k[40], h[40], g[40]; };
template <class O, bool PKM, bool QKM> constexpr Plan make_plan() {
  Plan p{};
  int nr = 0;
  for (int c = 0; c < O::NRO; ++c) nr += ((O::ro[c] >> 4) ? QKM : PKM) ? 4 : 2;
  int prev = 0;
  for (int c = 0; c < O::NRO; ++c) {
    const int q = O::ro[c] >> 4, i = O::ro[c] & 15;
    int av[2] = {0, 0}, k0 = -1;
    for (int m = 0; m < 2 * O::NP; ++m) {
      const int mm = (m + 2 * O::NPEND) % (2 * O::NP);
      if ((q ? O::pj[m >> 1] : O::pi[m >> 1]) == i && m < 2 * O::NPEND) av[((m >> 1) ^ m) & 1] = m;
      if ((q ? O::pj[mm >> 1] : O::pi[mm >> 1]) == i && k0 < 0) k0 = ((mm >> 1) ^ mm) & 1;
    }
    for (int kk = 0; kk < 2; ++kk)
      for (int h = 0; h < ((q ? QKM : PKM) ? 2 : 1); ++h) {
        const int k = k0 ^ kk;
        int g = p.n * O::NE / nr;
        if (g < av[k]) g = av[k];
        if (g < prev + (p.n && O::NE >= nr ? 1 : 0)) g = prev + (p.n && O::NE >= nr ? 1 : 0);
        p.q[p.n] = q; p.idx[p.n] = i; p.k[p.n] = k; p.h[p.n] = h; p.g[p.n] = g; prev = g; ++p.n;
      }
  }
  return p;
}
struct OBase { static constexpr int pidx(int i) { return i; } static constexpr int poff(int) { return 0; } static constexpr int qoff(int j) { return j * 2048; } static constexpr bool swp(int) { return false; } };
template <bool PF, int NE_> struct O44 : OBase {
  static constexpr int TP = 4, TQ = 4, NPA = 4, NP = 16, NPEND = 8, NRO = 8, NE = NE_;
  static constexpr signed char pi[16] = {2, 3, 3, 2, 2, 3, 3, 2, 0, 1, 1, 0, 0, 1, 1, 0}, pj[16] = {0, 0, 1, 1, 2, 2, 3, 3, 0, 0, 1, 1, 2, 2, 3, 3};
  static constexpr signed char ro[8] = {0, PF ? 1 : 16, PF ? 16 : 1, 17, 18, 19, 2, 3};
};
template <bool PF, int NE_> struct O42 : OBase {
  static constexpr int TP = 4, TQ = 2, NPA = 4, NP = 8, NPEND = 4, NRO = 6, NE = NE_;
  static constexpr signed char pi[8] = {2, 3, 3, 2, 0, 1, 1, 0}, pj[8] = {0, 0, 1, 1, 0, 0, 1, 1};
  static constexpr signed char ro[6] = {0, PF ? 1 : 16, PF ? 16 : 1, 17, 3, 2};
};
template <int TH> struct OQ {
  static constexpr int TP = 6, TQ = 5, NPA = 2, NP = 26, NPEND = 13, NRO = 11, NE = QNE;
  static constexpr signed char pi[26] = {3, 4, 5, 5, 4, 3, 3, 4, 5, 5, TH ? 5 : 3, 4, TH ? 3 : 4, 0, 1, 2, 2, 1, 0, 0, 1, 2, 2, TH ? 2 : 0, 1, TH ? 0 : 1};
  static constexpr signed char pj[26] = {0, 0, 0, 1, 1, 1, 2, 2, 2, TH ? 4 : 3, 3, 3, TH ? 3 : 4, 0, 0, 0, 1, 1, 1, 2, 2, 2, TH ? 4 : 3, 3, 3, TH ? 3 : 4};
  static constexpr signed char ro[11] = {0, 1, 16, 2, 17, 18, TH ? 20 : 19, TH ? 19 : 20, 3, 4, 5};
  static constexpr int pidx(int i) { return i / 3; }
  static constexpr int poff(int i) { return i % 3 * 8192; }
  static constexpr int qoff(int j) { return j == 4 ? 0 : (1 + 4 * TH + j) * 2048; }
  static constexpr bool swp(int i) { return i % 3 == 2; }
};
template <bool PKM, bool QKM, int PB, int QB, int XB, int NS, int NT, bool WISP, int BASE, int ROT, int WLD, int WSUB, int ASTEP, int RLX = 0, bool QF = false>
struct Gemm {
  static constexpr int SB = PB + QB + XB, CNT = (PB + QB) / 4096 + (XB ? 1 : 0);
  static constexpr int PO = QF ? QB : 0, QO = QF ? 0 : PB, WB = WISP ? PB : QB, AB = WISP ? QB : PB, WOFF = WISP ? PO : QO, AOFF = WISP ? QO + XB : PO;
  static constexpr int XL = XB / 64, H = NS - 1 - RLX, CA = AB / 4096 + (XB ? 1 : 0), WSTEP = 64 * WLD * 2;
  static_assert(H * CNT <= 63 && BASE + NS * SB <= 163840, "");
  struct Loader {
    static constexpr int NW = WB / 4096, NA = AB / 4096;
    const char* w; const char* ab; unsigned wv[NW], av[NA], xv, lwb, lab, lx; int rot, islot, t; bool xl;
    DEVI Loader(const void* w_, const void* ab_, int lw, int lane, int rot_ = 0)
        : w((const char*)w_ - 3072), ab((const char*)ab_ - 3072), lwb(BASE + WOFF + lw * (NW == 6 ? 2048 : WB / 4)), lab(BASE + AOFF + lw * (AB / 4)), lx(BASE + PB + lw * (XL * 16)),
          rot(rot_), islot((H + ROT) % NS), t(0), xl(lane < XL) {
      const unsigned w0 = (lane >> 3) * (WLD * 2), f0 = (lane & 7) ^ ((lane >> 3) & 2);
#pragma unroll
      for (int i = 0; i < NW; ++i) {
        const unsigned p = NW == 6 ? (i >> 1) * 8 + lw * 2 + (i & 1) : lw * NW + i;
        wv[i] = w0 + ((f0 ^ ((i & 1) << 2)) << 4) + (NW == 6 ? (i >> 1) * WSUB + (lw * 2 + (i & 1)) * (8 * WLD * 2) - (i & 1) * 1024 : (p >> 3) * WSUB + (p & 7) * (8 * WLD * 2) - i * 1024) + 3072;
        asm volatile("" : "+v"(wv[i]));
      }
      asm volatile("" : "+s"(w), "+s"(lwb));
      if (XB) asm volatile("" : "+s"(rot));
      else seta(0, 0, lw, lane);
    }
    DEVI void seta(unsigned a0, unsigned xv_, int lw, int lane) {
      xv = xv_ + 3072;
#pragma unroll
      for (int i = 0; i < NA; ++i) { const unsigned p = lw * NA + i; av[i] = (XB ? a0 + src_kc(p * 64 + lane, D) : p * 2048 + ph(lane * 16)) + 3072 - i * 1024; asm volatile("" : "+v"(av[i])); }
      asm volatile("" : "+s"(ab), "+s"(lab));
      if (XB) { asm volatile("" : "+v"(xv)); asm volatile("" : "+s"(lx)); }
    }
    DEVI int kord(int n) const { if constexpr (XB != 0) return (n + rot) & (NT - 1); else return n; }
    template <int SO> DEVI void issue_w(const char* p, unsigned so = 0) const {
      if constexpr (NW == 6) { dma_grp<2, SO>(p, wv, lwb + so); dma_grp<2, SO + 8192>(p, wv + 2, lwb + so); dma_grp<2, SO + 16384>(p, wv + 4, lwb + so); }
      else dma_grp<NW, SO>(p, wv, lwb + so);
    }
    template <int SO> DEVI void issue_a(const char* p, unsigned so = 0) const {
      if (XB) { if (xl) dma_grp<1, SO>(p, &xv, lx + so); }
      dma_grp<NA, SO>(p, av, lab + so);
    }
    template <int S0, bool W, bool A> DEVI void pre() const {
      if constexpr (S0 < H) {
        if (W) issue_w<((S0 + ROT) % NS) * SB>(w + (kord(0) + S0) * WSTEP);
        if (A) issue_a<((S0 + ROT) % NS) * SB>(ab + (kord(0) + S0) * ASTEP);
        pre<S0 + 1, W, A>();
      }
    }
    template <int T> DEVI void pre_w1() const { issue_w<((T + ROT) % NS) * SB>(w + (kord(0) + T) * WSTEP); sched0(); }
    DEVI void pre_w() const { pre<0, true, false>(); sched0(); }
    template <int T> DEVI void peel() const {
      if constexpr (T < H - 1) {
        wait_vm<(H - 1 - T) * CA + T * CNT>();
        bar();
        issue_w<((T + H + ROT) % NS) * SB>(w + (T + H) * WSTEP); issue_a<((T + H + ROT) % NS) * SB>(ab + (T + H) * ASTEP);
        peel<T + 1>();
      }
    }
    DEVI void start() {
      sched0();
      bar();
      pre<0, false, true>();
      sched0();
      if constexpr (H == 2) wait_vm<CA>();
      else { peel<0>(); islot = (2 * H - 1 + ROT) % NS; t = H - 1; }
    }
    template <class HK> DEVI void run(int tend, HK side) {
#pragma unroll 1
      for (; t < tend; ++t) {
        const char* wk = w + (t + H) * WSTEP; const char* ak = ab + (t + H) * ASTEP;
        unsigned lw_ = lwb + islot * SB, la_ = lab + islot * SB;
        islot = islot + 1 == NS ? 0 : islot + 1;
        asm volatile("" : "+s"(wk), "+s"(ak), "+s"(lw_), "+s"(la_));
        wait_vm<(H - 1) * CNT>();
        bar();
        dma_grp<NW, 0>(wk, wv, lw_);
        side(t);
        dma_grp<NA, 0>(ak, av, la_);
        sched0();
      }
    }
    template <int T, int X, class A, class B, class C> DEVI void step(A pre_, B side, C post) const {
      const int k = kord(T + H);
      const char* wk = w + k * WSTEP; const char* ak = ab + k * ASTEP;
      asm volatile("" : "+s"(wk), "+s"(ak));
      wait_vm<(H - 1) * CNT + X>();
      bar();
      pre_(BASE + ((T + ROT) % NS) * SB);
      issue_w<((T + H + ROT) % NS) * SB>(wk);
      sched0();
      side();
      sched0();
      issue_a<((T + H + ROT) % NS) * SB>(ak);
      post();
      sched0();
    }
    template <int I> DEVI void tail() const { if constexpr (I >= 0) { sched0(); wait_vm<I * CNT>(); bar(); tail<I - 1>(); } }
  };
  static constexpr int NWIN = (NS * SB + 65535) / 65536;
  template <class O> struct Comp {
    static constexpr int TP = O::TP, TQ = O::TQ, NM = 2 * O::NP, NPA = PKM ? O::NPA : 2, NQA = QKM ? TQ : 2;
    static constexpr Plan PL = make_plan<O, PKM, QKM>();
    unsigned prd[NWIN][NPA], qrd[NWIN][NQA];
    bf16x8 fp[TP][2], fq[TQ][2]; bf16x4 lo;
    DEVI void setwin() {
#pragma unroll
      for (int w = 1; w < NWIN; ++w) {
#pragma unroll
        for (int i = 0; i < NPA; ++i) { prd[w][i] = prd[0][i] + w * 65536; asm volatile("" : "+v"(prd[w][i])); }
#pragma unroll
        for (int i = 0; i < NQA; ++i) { qrd[w][i] = qrd[0][i] + w * 65536; asm volatile("" : "+v"(qrd[w][i])); }
      }
#pragma unroll
      for (int i = 0; i < NPA; ++i) asm volatile("" : "+v"(prd[0][i]));
#pragma unroll
      for (int i = 0; i < NQA; ++i) asm volatile("" : "+v"(qrd[0][i]));
    }
    DEVI void setup(int wp, int wq, int lane) {
      const unsigned b = BASE;
      if (PKM) {
#pragma unroll
        for (int i = 0; i < TP; ++i) prd[0][i] = b + rd_km(lane, wp * TP + i);
      } else { prd[0][0] = b + rd_kc(lane, 0) + wp * TP * 2048; prd[0][1] = b + rd_kc(lane, 1) + wp * TP * 2048; }
      if (QKM) {
#pragma unroll
        for (int j = 0; j < TQ; ++j) qrd[0][j] = b + rd_km(lane, wq * TQ + j);
      } else { qrd[0][0] = b + rd_kc(lane, 0) + wq * TQ * 2048; qrd[0][1] = b + rd_kc(lane, 1) + wq * TQ * 2048; }
    }
    template <int C, int N> DEVI static unsigned wa(const unsigned (&b)[NWIN][N], int i) { return b[C >> 16][i] + (C & 0xffff); }
    template <int SLOT, int R> DEVI void rd() {
      constexpr int q = PL.q[R], i = PL.idx[R], k = PL.k[R], h = PL.h[R];
      bf16x8& f = q ? fq[q ? i : 0][k] : fp[q ? 0 : i][k];
      if constexpr (q ? QKM : PKM) {
        unsigned a;
        if constexpr (q) a = wa<SLOT * SB + QO + k * 4096 + h * 512, NQA>(qrd, i); else a = wa<SLOT * SB + PO + O::poff(i) + k * 4096 + h * 512, NPA>(prd, O::pidx(i));
        const bf16x4 x = lds_tr4(a);
        if constexpr (h) f = __builtin_shufflevector(lo, x, 0, 1, 2, 3, 4, 5, 6, 7); else lo = x;
      } else if constexpr (q) f = lds_b128(wa<SLOT * SB + QO + O::qoff(i), 2>(qrd, k));
      else f = lds_b128(wa<SLOT * SB + PO + i * 2048, 2>(prd, k));
    }
    template <int SLOT, int M, int R> DEVI void rds() { if constexpr (R < PL.n) { if constexpr (PL.g[R] == M) rd<SLOT, R>(); if constexpr (PL.g[R] <= M) rds<SLOT, M, R + 1>(); } }
    template <int M, bool IP = false> DEVI void mm(f32x4 (&acc)[TP][TQ]) {
      constexpr int i = O::pi[M >> 1], j = O::pj[M >> 1], k = ((M >> 1) ^ M) & 1;
      if constexpr (IP) { if constexpr (O::swp(i)) mfma_ip(acc[i][j], fq[j][k], fp[i][k]); else mfma_ip(acc[i][j], fp[i][k], fq[j][k]); }
      else if constexpr (O::swp(i)) acc[i][j] = __builtin_amdgcn_mfma_f32_16x16x32_bf16(fq[j][k], fp[i][k], acc[i][j], 0, 0, 0);
      else acc[i][j] = __builtin_amdgcn_mfma_f32_16x16x32_bf16(fp[i][k], fq[j][k], acc[i][j], 0, 0, 0);
    }
    template <int A, int B> DEVI void pend(f32x4 (&acc)[TP][TQ]) { if constexpr (A < B) { mm<2 * A>(acc); mm<2 * A + 1>(acc); pend<A + 1, B>(acc); } }
    template <int SLOT, bool FIRST, int M> DEVI void body(f32x4 (&acc)[TP][TQ]) {
      if constexpr (M < NM) {
        if constexpr (!FIRST || M >= 2 * O::NPEND) { mm<M, true>(acc); sched0(); }
        rds<SLOT, M, 0>();
        sched0();
        body<SLOT, FIRST, M + 1>(acc);
      }
    }
    template <int SLOT, bool FIRST> DEVI void step(f32x4 (&acc)[TP][TQ]) { if (!RLX) wait_lgkm0(); bar(); sched0(); body<SLOT, FIRST, 0>(acc); }
    template <int I, int N, int T0, class MID> DEVI void steps(f32x4 (&acc)[TP][TQ], MID mid) {
      if constexpr (I < N) { mid(I); step<(I + T0 + ROT) % NS, false>(acc); steps<I + 1, N, T0>(acc, mid); }
    }
    DEVI void prep(f32x4 (&acc)[TP][TQ]) { setwin(); pin(acc); }
    DEVI void pin(f32x4 (&acc)[TP][TQ]) {
#pragma unroll
      for (int p = 0; p < O::NP; ++p) asm volatile("" : "+v"(acc[O::pi[p]][O::pj[p]]));
    }
    template <bool CONT, class MID> DEVI void run_steps(f32x4 (&acc)[TP][TQ], MID mid CSP) {
      constexpr int T0 = CONT ? 0 : 1;
      if constexpr (T0) { mid((NT - T0) / NS, -1); step<ROT % NS, true>(acc); }
#pragma unroll 1
      for (int itn = (NT - T0) / NS; itn > 0; --itn) {
        steps<0, NS, T0>(acc, [&](int i) { mid(itn, i); });
      }
      steps<0, (NT - T0) % NS, T0>(acc, [](int) {});
      wait_lgkm0();
      mfma_gap();
    }
  };
};

DEVI f32x4 gelu_arg(f32x4 u) { return u * (-2.302208198f + -0.1029432397f * (u * u)); }
DEVI f32x4 gelu_tanh(f32x4 u) {
  f32x4 a = gelu_arg(u);
#pragma unroll
  for (int e = 0; e < 4; ++e) a[e] = __builtin_amdgcn_exp2f(a[e]);
  a += 1.f;
#pragma unroll
  for (int e = 0; e < 4; ++e) a[e] = __builtin_amdgcn_rcpf(a[e]);
  return u * a;
}

template <int tilesP, int tilesQ, int gp, int gq> DEVI void tile_map(unsigned bid, int& tp, int& tq) {
  const unsigned xcd = bid & 7, idx = bid >> 3;
  constexpr unsigned gP = tilesP / gp, gQ = tilesQ / gq;
  static_assert(gP * gQ == 8, "");
  tp = (xcd % gP) * gp + idx % gp;
  tq = (xcd / gP) * gq + idx / gp;
}

template <int V> struct IC { static constexpr int value = V; };
template <int A, int B, class FN> DEVI void for_c(FN f) { if constexpr (A < B) { f(IC<A>{}); for_c<A + 1, B>(f); } }
DEVI float sel(bool c, float a, float b) { return c ? a : b; }
DEVI u32x2 pk4(const f32x4& v) { return (u32x2){pk2(v[0], v[1]), pk2(v[2], v[3])}; }
DEVI u32x4 cat4(u32x2 a, u32x2 b) { return (u32x4){a.x, a.y, b.x, b.y}; }
DEVI bf16x8 cat(u32x2 a, u32x2 b) { return __builtin_bit_cast(bf16x8, cat4(a, b)); }
DEVI f32x4 mfma(const bf16x8& a, const bf16x8& b, const f32x4& c) { return __builtin_amdgcn_mfma_f32_16x16x32_bf16(a, b, c, 0, 0, 0); }
DEVI u32x4 pack_pair(const f32x4& a, const f32x4& b) {
  const unsigned ax = pk2(a[0], a[1]), ay = pk2(a[2], a[3]), bx = pk2(b[0], b[1]), by = pk2(b[2], b[3]);
  auto rx = __builtin_amdgcn_permlane16_swap(ax, bx, false, false);
  auto ry = __builtin_amdgcn_permlane16_swap(ay, by, false, false);
  const unsigned x0 = rx[0], x1 = rx[1], y0 = ry[0], y1 = ry[1];
  return (u32x4){x0, y0, x1, y1};
}
DEVI int pair_chunk(int fq) { return ((fq & 1) << 1) | (fq >> 1); }
DEVI void sel_frags(bool paired, int fr, int fq, bf16x8 (&s)[2]) {
  const unsigned one = fr & 1 ? 0x3F800000u : 0x3F80u;
#pragma unroll
  for (int i = 0; i < 2; ++i) {
    const bool on = paired ? (fq & 1) == i && (fr >> 3) == (fq >> 1) : (fr >> 2) == fq;
    const int d = paired ? (fr & 7) >> 1 : i * 2 + ((fr >> 1) & 1);
    u32x4 v;
#pragma unroll
    for (int e = 0; e < 4; ++e) v[e] = on && d == e ? one : 0u;
    s[i] = __builtin_bit_cast(bf16x8, v);
  }
}
template <int TQ> DEVI void tile_add(f32x4 (&acc)[4][TQ], const bf16x8 (&s)[2], unsigned a) {
  bf16x8 t[4];
#pragma unroll
  for (int p = 0; p < 4; ++p) t[p] = lds_b128(a + p * 1024);
#pragma unroll
  for (int p = 0; p < 4; ++p)
#pragma unroll
    for (int i = 0; i < 2; ++i) acc[(p & 1) * 2 + i][p >> 1] = mfma(s[i], t[p], acc[(p & 1) * 2 + i][p >> 1]);
}
template <int C> DEVI float dppf(float v) { return __int_as_float(__builtin_amdgcn_update_dpp(0, __float_as_int(v), C, 0xf, 0xf, false)); }
DEVI float red8(float v) { v += dppf<0xB1>(v); v += dppf<0x4E>(v); v += dppf<0x141>(v); return v; }
DEVI float red4(float v) { v += dppf<0xB1>(v); v += dppf<0x4E>(v); return v; }

constexpr unsigned SENT = 0xBF800000u;
constexpr int SPIN_MAX = 1 << 21;

constexpr unsigned MB = 1u << 20;
constexpr unsigned WS_O = 0, WS_H = 8 * MB, WS_U = 16 * MB, WS_X2 = 32 * MB, WS_XP = 40 * MB  ;
constexpr unsigned WS_ST = 120 * MB, ST_CS = 2 * S * 64, WS_XCH = 122 * MB  , WS_EPOCH = 123 * MB, WS_ABORT = 123 * MB + 256  ;
DEVI unsigned tag2(unsigned t) { return t * 0x9E3779B1u ^ 0x5BD1E995u; }
constexpr int MEDGE = 256 * 64;
constexpr unsigned WS_MAIL = 124 * MB;

#define NL 2
#define QBR 4
#define FLS 3
#define NPL 6
#define SPON 1
#define PC0 0
#define PGAP 3
#define WSP 3
#define SWW 0
#define GSH 1
#define GSP 3
#define SSC 1
#define RB 1
#define SI 2
#define LPR 1
#define CPR 0
#define GLW 4
#define GL1 4
#define SW0 0
#define SW1 6
#define XE 0
#define E2 1
#define LB 3
#define XD 1
#define XS 4
#define JA 22
#define PS 3
#define TBA 3
#define TC1 12
#define NW1 9
#define EW 12
#define ET 8
#define SPW 1
#define PA_1 3
#define PA_2 3
constexpr int L_X = 0, L_TAB = 16384, L_CS = L_TAB + 1152, L_TAG = L_CS + 768, L_BIAS = L_TAG + 128, L_SCR = L_BIAS + 512  , L_QKV = 20480;
static_assert(L_SCR + 1536 <= L_QKV, "");
typedef Gemm<true, false, 24576, 16384, 2048, 3, D / 64, true, 32768, 0, 3 * D, 2 * D, 128> GQ;
typedef Gemm<true, false, 16384, 8192, 0, 5, D / 64, true, 40960, 2, D, 128, 16384, 1, true> GW;
typedef Gemm<true, true, 16384, 16384, 0, 4, S / 64, true, 32768, 1, F, 128, 32768, 0, true> G1;
typedef Gemm<false, true, 16384, 16384, 0, 3, JA, false, 65536, (3 - JA % 3) % 3, S, 1024, 32768> G2A;
typedef Gemm<false, true, 16384, 8192, 0, 4, F / 64 - 2 * JA, false, 65536, 0, S, 128, 32768> G2;

DEVI unsigned get_tag() { return __builtin_amdgcn_readfirstlane(*(volatile LAS unsigned*)(uintptr_t)L_TAG) + 1u; }
DEVI bool spin_fail(int spin, char* ws) {
  if ((spin & 255) != 255) return false;
  gu32* ab = (gu32*)(ws + WS_ABORT);
  const unsigned tag = get_tag();
  if (spin >= SPIN_MAX) { __hip_atomic_store(ab, tag, RLX_AGENT); return true; }
  return __builtin_amdgcn_readfirstlane(__hip_atomic_load(ab, RLX_AGENT)) == tag;
}
DEVI void sync_post(char* ws, int edge, int post, int wid) {
  unsigned o = post >= 0 && wid == 0 ? (post >> 6) * 256u : MB, bit = 1u << (post & 31);
  const rsrc_t r = mkrsrc(ws + WS_MAIL + edge * (MEDGE * 4), MEDGE * 4);
  asm volatile("s_waitcnt vmcnt(0) lgkmcnt(0)" : "+v"(o), "+v"(bit) : "s"(r) : "memory");
  sched0();
  bar();
  ST(edge + 1, 6);
  if (wid == 0) asm volatile("buffer_atomic_or %0, %1, %2, 0 offen sc1" :: "v"(bit), "v"(o), "s"(r) : "memory");
  sched0();
}
#define POLL1(r) "s_waitcnt vmcnt(2)\n\tv_readfirstlane_b32 %[seen], " r "\n\tglobal_atomic_or " r ", %[z], %[z], %[p] sc0 sc1\n\t" \
  "s_andn2_b32 %[t], %[first], %[seen]\n\ts_cbranch_scc1 3f\n\ts_cmp_eq_u32 %[first], 0\n\ts_cbranch_scc1 4f\n\ts_barrier\n\ts_mov_b32 %[first], 0\n" \
  "4:\n\ts_andn2_b32 %[t], %[second], %[seen]\n\ts_cbranch_scc0 2f\n3:\n\t"
struct Gate {
  const char* p; unsigned miss, due, a, b, c;
  DEVI Gate() : p(nullptr), miss(0), due(0) { asm volatile("v_mov_b32 %0, 0\n\tv_mov_b32 %1, 0\n\tv_mov_b32 %2, 0" : "=v"(a), "=v"(b), "=v"(c)); }
  DEVI void ask() const { asm volatile("s_mov_b64 exec, 1\n\tglobal_atomic_or %0, %1, %1, %2 sc0 sc1\n\ts_mov_b64 exec, -1" :: "v"(a), "v"(0u), "s"(p) : "memory"); }
  template <int DELAY> DEVI void wait(char* ws, int edge, int line, unsigned all, unsigned first, unsigned second, int wid) {
    sched0();
    p = ws + WS_MAIL + (edge * MEDGE + line * 64) * 4;
    second &= all;
    if (wid == 3) {
      due = 1;
      if (DELAY) __builtin_amdgcn_s_sleep(DELAY);
      int spin = 255;
#pragma unroll 1
      for (;; spin += 256) {
        asm volatile("" : "+s"(spin));
        unsigned seen, t, n;
        asm volatile("s_mov_b64 exec, 1\n\ts_waitcnt vmcnt(0)\n\tglobal_atomic_or %[a], %[z], %[z], %[p] sc0 sc1\n\ts_sleep %[gap]\n\t"
                     "global_atomic_or %[b], %[z], %[z], %[p] sc0 sc1\n\ts_sleep %[gap]\n\tglobal_atomic_or %[c], %[z], %[z], %[p] sc0 sc1\n\ts_movk_i32 %[n], 128\n"
                     "1:\n\t" POLL1("%[a]") POLL1("%[b]") POLL1("%[c]") "s_sub_u32 %[n], %[n], 1\n\ts_cbranch_scc0 1b\n"
                     "2:\n\ts_mov_b64 exec, -1 ; raw polls %[a] %[b] %[c]"
                     : [seen] "=&s"(seen), [t] "=&s"(t), [n] "=&s"(n), [first] "+&s"(first)
                     : [p] "s"(p), [second] "s"(second), [gap] "i"(PGAP), [a] "v"(a), [b] "v"(b), [c] "v"(c), [z] "v"(0u) : "memory", "scc");
        miss = ~seen & all;
        if (__builtin_expect(!(miss & second) && !first, 1)) break;
        if (spin_fail(spin, ws)) { if (first) bar(); miss = 0; break; }
      }
    } else bar();
    ST(edge + 1, 4);
    ST(edge + 2, 0);
  }
  int ev;
  DEVI void arm(int ni, int cw) { ev = miss ? ni : cw >= 0 ? 2 : 0; asm volatile("" : "+s"(ev), "+s"(miss), "+s"(due)); }
  DEVI void head(int it, int cw, int lane, int wid) {
    if (__builtin_expect(it == ev, 0)) {
      asm volatile("" : "+v"(lane)); asm volatile("" : "+s"(cw), "+s"(wid));
      if (miss) ask();
      if (it == 2) code_warm(cw, lane, L_SCR + wid * 256);
      ev = cw >= 0 && it > 2 ? 2 : 0;
      asm volatile("" : "+s"(ev));
    }
  }
  DEVI void late(char* ws) {
    if (__builtin_expect(due != 0, 0)) {
      asm volatile("" : "+s"(ws));
      asm volatile("s_waitcnt vmcnt(0) ; raw settled %0 %1 %2" :: "v"(a), "v"(b), "v"(c) : "memory");
      for (int spin = 0; miss; ++spin) {
        unsigned seen;
        asm volatile("v_readfirstlane_b32 %0, %1" : "=s"(seen) : "v"(a));
        miss &= ~seen;
        if (!miss || spin_fail(spin, ws)) break;
        ask();
        wait_vm<0>();
      }
      miss = due = 0;
      asm volatile("" : "+s"(miss), "+s"(due));
    }
  }
};
DEVI unsigned halo_off(int s0, int lw, int lane) {
  const int c = lw * 32 + (lane & 31), row = c >> 3, ch = (c & 7) ^ ((row >> 1) & 7), tok = s0 - 16 + row;
  return (unsigned)((tok < 0 ? 0 : tok) * D * 2 + ch * 16);
}
DEVI int qkv_rot(int bid) { return (bid & 3) * 4 + (bid & 4) * 2; }
DEVI unsigned x_off(int tp, int tq, int w, int fr, int fq) { return (unsigned)(((tq * 64 + (w >> 1) * 32 + fr) * D + tp * 128 + (w & 1) * 64 + pair_chunk(fq) * 8) * 2); }

DEVI void pfb_q(const u16* w, int bid, int j, int n) {
  const int r = slow_res(w);
  if ((r >> 2) == (bid & 1)) {
    const unsigned o = (unsigned)(((bid & 2) * 4 + r) * 128 + (j >> 1) * (3 * D * 2) + (j & 1) * 64 + ((n + qkv_rot(bid)) & 15) * GQ::WSTEP);
    spf1<0>(w, o); spf1<2048>(w, o); spf1<4096>(w, o);
  }
}
constexpr int PFR = NPL * 128 * 1024, PF_END = TBA * 256 + NW1 * PFR / (F * 2) / 8 * 8;
static_assert(TBA * 256 * F * 2 + NW1 * PFR <= S * F * 2 && 3 * PFR >= D * D * 2, "");
struct Pfc {
  const char* p; const char* q; int pc;
  DEVI Pfc(const u16* wo, const u16* w1, unsigned g) : p((const char*)wo + g * (NPL * 128)), q((const char*)w1 + TBA * 256 * F * 2 + g * (NPL * 128)), pc(PC0) {}
  DEVI void go() {
    unsigned t, u;
    asm volatile("s_getreg_b32 %2, hwreg(HW_REG_IB_STS, 8, 4)\n\ts_cmp_lt_u32 %1, %5\n\ts_cselect_b32 %3, 0, 1\n\ts_or_b32 %2, %2, %3\n\ts_cbranch_scc1 1f\n\t"
                 "s_load_dwordx2 s[100:101], %0, 60\n\ts_load_dwordx2 s[100:101], %0, 188\n\ts_load_dwordx2 s[100:101], %0, 316\n\t"
                 "s_load_dwordx2 s[100:101], %0, 444\n\ts_load_dwordx2 s[100:101], %0, 572\n\ts_load_dwordx2 s[100:101], %0, 700\n\t"
                 ".if %8 == 7\n\ts_load_dwordx2 s[100:101], %0, 828\n\t.endif\n\t"
                 "s_mov_b32 %2, %7\n\ts_cmp_lt_u32 %1, 2\n\ts_cselect_b32 %2, %6, %2\n\ts_mov_b64 s[40:41], %0\n\ts_add_u32 s40, s40, %2\n\ts_addc_u32 s41, s41, 0\n\t"
                 "s_cmp_eq_u32 %1, 2\n\ts_cselect_b64 %0, %4, s[40:41]\n\ts_add_u32 %1, %1, 1\n1:"
                 : "+s"(p), "+s"(pc), "=&s"(t), "=&s"(u) : "s"(q), "i"(3 + NW1), "i"((D * D * 2 - PFR) / 2), "i"(PFR), "i"(NPL) : "s40", "s41", "s100", "s101", "scc");
  }
};
DEVI void spare_touch(const u16* w1, const u16* w2, int bid, int wid, int lane) {
  if (!SPON) return;
  const int j = ((bid >> 3) - 28) * 8 + wid, fr = lane & 15, fq = lane >> 4;
  const unsigned scr = L_QKV + wid * 256;
#pragma unroll 1
  for (int r = PF_END + (j * 2 + ((bid >> 2) & 1)) * 4; r < S; r += 256) { if (fr < 14) touch4(w1 + (size_t)r * F + (bid & 3) * 896, fq * (F * 2) + fr * 128, scr); wait_vm<SPW>(); }
#pragma unroll 1
  for (int r = (j * 2 + (bid & 1)) * 4; r < JA * 64; r += 256) { touch4(w2 + (size_t)(r + (bid & 2) * JA * 32) * S + (bid & 4) * 256, fq * (S * 2) + fr * 128, scr); wait_vm<SPW>(); }
#pragma unroll 1
  for (int r = JA * 128 + ((31 - j) * 2 + (bid & 1)) * 8; r < F; r += 512) { touch4(w2 + (size_t)r * S + ((bid & 7) >> 1) * 512, (lane >> 3) * (S * 2) + (lane & 7) * 128, scr); wait_vm<SPW>(); }
}

template <int TH> DEVI void qkv_attn(char* ws, int l, int h, int lane, int hp, int sb, int cw, int bid, int tp, int tq, const u16* wqkv) {
  asm volatile("; half %1" : "+v"(lane) : "i"(TH));
  const int fr = lane & 15, fq = lane >> 4;
  GQ::Comp<OQ<TH>> g;
#pragma unroll
  for (int c = 0; c < 2; ++c) { g.prd[0][c] = 32768 + rd_km(lane, h * 2 + c); g.qrd[0][c] = 32768 + rd_kc(lane, c); }
  f32x4 acc[6][5];
#pragma unroll
  for (int i = 0; i < 6; ++i)
#pragma unroll
    for (int j = 0; j < 5; ++j) acc[i][j] = (f32x4){0.f, 0.f, 0.f, 0.f};
  const int par = fr & 1, da = fr - 4 * fq - par;
  const bool ca = da >= 0, cb = da >= 2;
  const unsigned tm = L_TAB + (16 + 64 * TH) * 4, tc = L_CS + h * 128;
  unsigned am = tm + fr * 4 - 320, a4 = tm + fq * 16 - 320, aa = tm + 576 + (4 * fq + par - (ca ? 0 : 16)) * 4, ab = tm + 576 + (4 * fq + par + 2 - (cb ? 0 : 16)) * 4;
  unsigned c4 = tc + fq * 16, c1 = tc + 512 + fr * 4, xb = L_QKV + h * 3072 + lane * 16, hm = par ? 0xffff0000u : 0xffffu, m0 = ca ? 0u : hm, m1 = cb ? 0u : hm;
  unsigned oo = ph((unsigned)((((sb * 2 + TH) * 16 + hp) << 13) + fr * 128 + (((h * 4 + pair_chunk(fq)) ^ (fr >> 1)) << 4)));
  asm volatile("" : "+v"(am), "+v"(a4), "+v"(aa), "+v"(ab), "+v"(c4), "+v"(c1), "+v"(xb), "+v"(hm), "+v"(m0), "+v"(m1), "+v"(oo));
  g.prep(acc);
  if (l) {
    const int sbc = (tq >> 1) + fq;
    sync_post(ws, 3, !TH && (lane < 16 || (lane < 32 && (tq & 1) && sbc < 16)) ? (sbc * 16 + fr) * 64 + (tq - 2 * sbc + 1) * 8 + tp : -1, TH * 2 + h);
  }
  Gate gt;
  if (l) gt.wait<SW0>(ws, 3, sb * 16 + hp, sb ? 0xffffffu : 0xffff00u, (sb ? 0x10101u : 0x10100u) << ((qkv_rot(bid) & 15) >> 1), 0x30303u << ((qkv_rot(bid) & 15) >> 1), TH * 2 + h);
  else if (!TH || !h) wait_vm<0>();
  else if ((slow_res(wqkv) >> 2) == (bid & 1)) {
    if (ET) __builtin_amdgcn_s_sleep(ET);
    if ((lane & 3) < 3 && lane < 40) touch4(wqkv, (unsigned)(((qkv_rot(bid) * 64 + 128 + (bid >> 3) * 10 + (lane >> 2)) & 1023) * (3 * D * 2) + (lane & 3) * (D * 2) + ((bid & 2) * 4 + slow_res(wqkv)) * 128), L_SCR + 768);
  }
  g.template run_steps<false>(acc, [&](int it, int i) {
    if (i < 0) gt.arm(it, cw);
        if (i == 0) gt.head(it, cw, lane, TH * 2 + h);
    if (TH && i == 1) gt.late(ws);
  } CSA);
  const rsrc_t ro = mkrsrc(ws + WS_O, S * D * 4);
  MTD;
  MT(0);
  if (l == 0) bar();
  ST(1 + l * 4, 2);
  float mt[5], cv[2], rq[4], ra[4], rb[4]; f32x4 m4[5], cq[2], ck[2];
#pragma unroll
  for (int c = 0; c < 2; ++c) {
    ck[c] = *(const LAS f32x4*)(uintptr_t)(c4 + 256 + c * 64); cv[c] = *(const LAS float*)(uintptr_t)(c1 + c * 64);
    cq[c] = *(const LAS f32x4*)(uintptr_t)(c4 + c * 64);
  }
#pragma unroll
  for (int j = 4; j >= 0; --j) {
    const int o = 320 + (j == 4 ? -64 - 256 * TH : j * 64);
    if (j < 4 || !TH) mt[j] = *(const LAS float*)(uintptr_t)(am + o);
    if (j < 4 || TH) m4[j] = *(const LAS f32x4*)(uintptr_t)(a4 + o);
  }
#pragma unroll
  for (int j = 0; j < 4; ++j) { rq[j] = *(const LAS float*)(uintptr_t)(am + 896 + j * 64); ra[j] = *(const LAS float*)(uintptr_t)(aa + j * 64); rb[j] = *(const LAS float*)(uintptr_t)(ab + j * 64); }
  u32x2 kp[2][5], qp[2][4], vp[2][5];
  auto fk = [&](int j) { kp[0][(j + 1) % 5] = pk4(acc[1][j]); kp[1][(j + 1) % 5] = pk4(acc[4][j] + ck[1] * mt[j]); };
  auto fv = [&](int j) { vp[0][(j + 1) % 5] = pk4(acc[2][j]); vp[1][(j + 1) % 5] = pk4(acc[5][j] + m4[j] * cv[1]); };
  auto fqq = [&](int j) { qp[0][j] = pk4(acc[0][j]); qp[1][j] = pk4(acc[3][j] + cq[1] * mt[j]); };
  g.template pend<10, 13>(acc);
#pragma unroll
  for (int i = 0; i < 6; ++i) { __builtin_amdgcn_sched_group_barrier(0x100, 3, 0); __builtin_amdgcn_sched_group_barrier(0x8, 1, 0); }
  sched0();
  MT(2);
  for_c<0, 20>([&](auto mc) {
    constexpr int m = mc.value, n0 = m * 52 / 20, n1 = (m + 1) * 52 / 20;
    g.template mm<(18 - (m & ~1)) + (m & 1)>(acc);
    sched0();
#pragma unroll
    for (int n = n0; n < n1; ++n) {
      const int t = 12 - (n >> 2), e = n & 3, j = t == 12 ? 4 : t / 3, i = t == 12 ? 1 + TH : t % 3;
      float x = acc[i][j][e];
      asm volatile("v_fmac_f32 %0, %1, %2" : "+v"(x) : "v"(i == 2 ? m4[j][e] : i ? ck[0][e] : cq[0][e]), "v"(i == 2 ? cv[0] : mt[j]));
      acc[i][j][e] = x;
    }
    sched0();
  });
  volatile LAS unsigned* xf = (volatile LAS unsigned*)(uintptr_t)(L_TAG + 16 + (h * 2 + TH) * 4);
  if (TH) { fv(4); *(volatile LAS bf16x8*)(uintptr_t)(xb + 2048) = cat(vp[0][0], vp[1][0]); *xf = l + 1; fk(3); }
  else { fk(3); fv(3); *(volatile LAS bf16x8*)(uintptr_t)xb = cat(kp[0][4], kp[1][4]); *(volatile LAS bf16x8*)(uintptr_t)(xb + 1024) = cat(vp[0][4], vp[1][4]); *xf = l + 1; }
  sched0();
  fqq(3); fk(2); fqq(2); fk(1);
  sched0();
  const f32x4 z = {0.f, 0.f, 0.f, 0.f};
  f32x4 sc[4][2]; float a[4], b[4], m[4];
  auto scores = [&](int j) {
#pragma unroll
    for (int u = 1; u >= 0; --u) sc[j][u] = mfma(cat(kp[0][j + u], kp[1][j + u]), cat(qp[0][j], qp[1][j]), z);
  };
  auto pick = [&](int j) {
    const f32x4 s0 = sc[j][0], s1 = sc[j][1];
    a[j] *= sel(par, sel(ca, s1[1], s0[1]), sel(ca, s1[0], s0[0]));
    b[j] *= sel(par, sel(cb, s1[3], s0[3]), sel(cb, s1[2], s0[2]));
    if (!TH && j == 0) { a[j] = sel(ca || sb, a[j], -1e9f * 1.4426950408889634f); b[j] = sel(cb || sb, b[j], -1e9f * 1.4426950408889634f); }
    m[j] = __builtin_fmaxf(a[j], b[j]);
  };
  auto expo = [&](int j) { a[j] = __builtin_amdgcn_exp2f(a[j] - m[j]); b[j] = __builtin_amdgcn_exp2f(b[j] - m[j]); m[j] = a[j] + b[j]; };
  auto pv = [&](int j) {
    const unsigned pa = pk2(a[j] * ra[j], a[j] * ra[j]), pb = pk2(b[j] * rb[j], b[j] * rb[j]);
    const bf16x8 pf = __builtin_bit_cast(bf16x8, (u32x4){pa & m0, pb & m1, pa & (m0 ^ hm), pb & (m1 ^ hm)});
#pragma unroll
    for (int c = 0; c < 2; ++c) sc[j][c] = mfma(cat(vp[c][j], vp[c][j + 1]), pf, z);
  };
  auto outp = [&](int j) { st16(ro, oo + j * 4096, pack_pair(sc[j][0] * m[j], sc[j][1] * m[j])); };
  unsigned f; u32x4 xv, xk;
  auto xrd = [&]() {
    f = xf[1 - 2 * TH]; xv = *(volatile LAS u32x4*)(uintptr_t)(xb + (TH ? 1024 : 2048));
    if (TH) xk = *(volatile LAS u32x4*)(uintptr_t)xb;
  };
  auto xget = [&]() {
    sched0();
    while (__builtin_expect(__builtin_amdgcn_readfirstlane(f) != l + 1, 0)) xrd();
    vp[0][0] = (u32x2){xv[0], xv[1]}; vp[1][0] = (u32x2){xv[2], xv[3]};
    if (TH) { kp[0][0] = (u32x2){xk[0], xk[1]}; kp[1][0] = (u32x2){xk[2], xk[3]}; }
  };
  MT(3);
  {
    fqq(1); fk(0); fqq(0);
    if (!TH) fk(4);
    xrd();
    sched0();
    if (TH) fv(3);
    fv(2); fv(1); fv(0);
#pragma unroll
    for (int c = 0; c < 2; ++c)
#pragma unroll
      for (int j = 0; j < 4; ++j) asm volatile("" : "+v"(qp[c][j]), "+v"(kp[c][j + 1]), "+v"(vp[c][j + 1]));
    if (!TH) asm volatile("" : "+v"(kp[0][0]), "+v"(kp[1][0]));
    MT(4);
    xget();
    scores(3); scores(2); scores(1); scores(0);
#pragma unroll
    for (int j = 0; j < 4; ++j) { const float r = rq[j] * (0.17677669529663687f * 1.4426950408889634f); a[j] = r * ra[j]; b[j] = r * rb[j]; }
#pragma unroll
    for (int i = 0; i < 8; ++i) { __builtin_amdgcn_sched_group_barrier(0x8, 1, 0); __builtin_amdgcn_sched_group_barrier(0x2, 2, 0); }
    sched0();
    pick(3); pick(2); pick(1); pick(0);
    float u, w;
    swp32(m[0], m[2]); swp32(m[1], m[3]); u = m[0]; w = m[1];
    asm volatile("v_max_f32 %0, %0, %2\n\tv_max_f32 %1, %1, %3\n\ts_nop 1" : "+v"(u), "+v"(w) : "v"(m[2]), "v"(m[3]));
    swp16(u, w); u = vmax(u, w);
    xch16(u, u, w); xch32(u, m[0], m[2]); xch32(w, m[1], m[3]);
    MT(5);
    expo(3); expo(2); expo(1); expo(0);
    pv(3); pv(2); pv(1); pv(0);
    swp32(m[0], m[2]); swp32(m[1], m[3]); u = m[0] + m[2]; w = m[1] + m[3];
    swp16(u, w); u = __builtin_amdgcn_rcpf(u + w);
    xch16(u, u, w); xch32(u, m[0], m[2]); xch32(w, m[1], m[3]);
    MT(6);
    outp(3); outp(2); outp(1); outp(0);
  }
  MT(7);
  MTOUT(10 + TH, TH * 2);
}

extern "C" __global__ __launch_bounds__(512, 2) void mega_kernel(const u16* x0, const u16* wqkv, const u16* wo, const u16* w1, const u16* w2, const u16* b1, const u16* b2, u16* out, char* ws) {
  const int wid0 = __builtin_amdgcn_readfirstlane(threadIdx.x >> 6);
  ST(0, 7);
  STL(0, 6);
  asm volatile("s_getpc_b64 s[40:41]\n\ts_andn2_b32 s40, s40, 63\n\ts_load_dwordx2 s[100:101], s[40:41], %0 offset:124\n\ts_load_dwordx2 s[100:101], s[40:41], %0 offset:252"
               :: "s"(__builtin_amdgcn_readfirstlane(threadIdx.x) << 2) : "s40", "s41", "s100", "s101", "scc");
  sched0();
#define PHASE_VARS \
      int tid = threadIdx.x, bid = blockIdx.x, wid = wid0; \
      asm volatile("" : "+v"(tid)); asm volatile("" : "+s"(bid), "+s"(wid), "+s"(ws)); \
      const int lw = wid - 4, lt = tid - 256, lane = tid & 63, fr = lane & 15, fq = lane >> 4, wp = wid & 1, wq = wid >> 1; \
      const unsigned j = (bid >> 3) * 4 + lw; \
      int hp, sb; tile_map<16, 16, 4, 8>(bid, hp, sb);            \
      int tp, tq; tile_map<8, 32, 4, 8>(bid, tp, tq);             \
      int fp, fq1; tile_map<28, 8, 7, 4>(bid, fp, fq1);           \
      const int cw = bid < 16 ? wid + 4 * (bid >> 3) : -1; \
      (void)cw; (void)j; (void)lt; (void)wp; (void)wq; (void)lw; (void)fr; (void)fq; (void)hp; (void)sb; (void)tp; (void)tq; (void)fp; (void)fq1;
#define LOADER_VARS PHASE_VARS asm volatile("" : "+s"(wqkv), "+s"(wo), "+s"(w1), "+s"(w2));

  if (__builtin_expect(wid0 >= 4, 1)) {
    if (LPR) __builtin_amdgcn_s_setprio(3);
    const u16* xin = x0;
    Pfc pf(wo, w1, blockIdx.x * 4 + wid0 - 4);
#pragma unroll 1
    for (int l = 0; l < NL; ++l) {
      {
        LOADER_VARS
        const int s0 = sb * 128;
        GQ::Loader L(wqkv + hp * 64, xin, lw, lane, qkv_rot(bid));
        if (l == 0) L.pre_w1<0>();
        L.seta((unsigned)(s0 * D * 2), halo_off(s0, lw, lane), lw, lane);
        if (l == 0) { L.issue_a<0>(L.ab + L.kord(0) * 128); if (!SI) L.pre<1, true, true>(); }
        sched0();
        const rsrc_t rs = mkrsrc(ws + WS_ST, MB);
        f32x4 c = {0.f, 0.f, 0.f, 0.f}; f32x2 s1 = {0.f, 0.f}, s2 = {0.f, 0.f}, o = {0.f, 0.f}; float pv = 0.f;
        bf16x8 ones = {0x3F80, 0x3F80, 0x3F80, 0x3F80, 0x3F80, 0x3F80, 0x3F80, 0x3F80};
        const int ct = lw & 1 ? 8 + (sb & 3) : sb & 7; const bool cs = l == 0 && lw >= 2;
        int tpf = (slow_res(wqkv) >> 2) != (bid & 1) ? 0 : lw < 2 ? 10 : 9;
        unsigned ra = GQ::AOFF + ((hp & 3) * 32 + lw * 8) * 128 + lane * 16, rb = rd_km(lane, ct);
        unsigned so = lane & 7 ? MB : ph((unsigned)(((l * S + s0 + (hp & 3) * 32 + lw * 8 + (lane >> 3)) * 4 + (hp >> 2)) * 16));
        unsigned co = fr >= 2 || !cs ? MB : ph((unsigned)(ST_CS + ((hp * 96 + ct * 8 + fq * 2 + fr) * 2 + (sb >> 3)) * 16));
        asm volatile("" : "+v"(ra), "+v"(rb), "+v"(so), "+v"(co), "+v"(c), "+v"(s1), "+v"(s2), "+v"(ones), "+v"(o));
        asm volatile("" : "+s"(tpf));
        if (SI && l == 0) L.pre_w1<1>();
        sched0();
        unsigned to[3], xo = x_off(tp, tq, lw, fr, fq), q2 = ((bid & 2) * 4 + slow_res(wqkv)) * 128 + ((bid >> 3) * 2 + (lw & 1)) * (3 * D * 2);
#pragma unroll
        for (int i = 0; i < 3; ++i) {
          const int r = s0 - 16 + (i == 2 && lw ? 64 : i * 64) + (lt >> 2);
          to[i] = ph((unsigned)(((l * S + (r < 0 ? 0 : r)) * 4 + (lt & 3)) * 16));
        }
        unsigned ta = (lt & 3) < (lw ? 2 : 3) ? L_TAB + ((lt & 3) * 64 + (lt >> 2)) * 4 : L_SCR, ts = ph((unsigned)(ST_CS + (hp * 192 + (lw ? lt - 64 : lt)) * 16));
        asm volatile("" : "+v"(to[0]), "+v"(to[1]), "+v"(to[2]), "+v"(xo), "+v"(ta), "+v"(ts));
        asm volatile("" : "+s"(q2));
        if (SI == 1 && l == 0) L.pre<1, false, true>();
        sched0();
        const GW::Loader W(wo + tp * 128, ws, lw, lane);
        if (SI == 2 && l == 0) L.pre<1, false, true>();
        sched0();
        auto pfq = [&](int n) { const unsigned o = q2 + L.kord(n) * GQ::WSTEP; spf2<0>(wqkv, o); spf2<2048>(wqkv, o); spf2<4096>(wqkv, o); };
        if (l) {
          if (QBR == 3) bar();
          if (!(EW & 8)) L.pre_w();
          if (tpf) { pfq(2 + (lw >> 1)); pfq(4 + (lw >> 1)); }
          if (QBR == 4) bar();
          L.start();
        }
        else STL(0, 2);
        if (LPR == 1) __builtin_amdgcn_s_setprio(0);
        unsigned tag, tg2; u32x4 gs, a[3];
        auto rowst = [&](int i) {
          const float m0 = __uint_as_float(a[i][0]), m = red4(m0) * 0.25f, q = red4(__uint_as_float(a[i][2]) + 256.f * (m0 - m) * (m0 - m));
          o = (f32x2){sel((lt & 3) == i, m, o.x), sel((lt & 3) == i, q, o.y)};
          asm volatile("" : "+v"(o));
        };
        for_c<0, 16 - GQ::H>([&](auto tc) {
          constexpr int t = tc.value;
          u32x4 v; unsigned tv;
          L.step<t, t == 5 || t == 6 ? 1 : t == 9 || t == 10 ? 4 : 0>([&](unsigned ls) {
            if (t < 4) v = *(const LAS u32x4*)(uintptr_t)(ls + ra);
            if (t == 3) tv = *(volatile LAS unsigned*)(uintptr_t)L_TAG;
          }, [&]() {
            if (t < 8 && cs) {
              constexpr int ls = 32768 + t % 3 * GQ::SB;
              bf16x4 x[4];
#pragma unroll
              for (int e = 0; e < 4; ++e) x[e] = lds_tr4(rb + (ls & ~0x7fff) + (ls & 0x7fff) + (e >> 1) * 4096 + (e & 1) * 512);
#pragma unroll
              for (int ks = 0; ks < 2; ++ks) c = mfma(__builtin_shufflevector(x[2 * ks], x[2 * ks + 1], 0, 1, 2, 3, 4, 5, 6, 7), ones, c);
            } else if (t >= 4) {
              if (t >= 10 || t >= tpf) { if (t < TC1) pf.go(); }
              else if (lw < 2) pfq(t + 4);
              else if (t == 8) { pfq(14); pfq(15); }
            }
            if (t < 4) {
              if (t == 0) { pv = bflo(v[0]); asm volatile("" : "+v"(pv)); }
#pragma unroll
              for (int e = 0; e < 4; ++e) {
                if (SSC) {
                  float x = bflo(v[e]), y = bfhi(v[e]), a0 = s1.x, a1 = s1.y, b0 = s2.x, b1 = s2.y;
                  asm volatile("v_sub_f32 %0, %0, %6\n\tv_sub_f32 %1, %1, %6\n\tv_add_f32 %2, %2, %0\n\tv_add_f32 %3, %3, %1\n\tv_fmac_f32 %4, %0, %0\n\tv_fmac_f32 %5, %1, %1"
                               : "+v"(x), "+v"(y), "+v"(a0), "+v"(a1), "+v"(b0), "+v"(b1) : "v"(pv));
                  s1 = (f32x2){a0, a1}; s2 = (f32x2){b0, b1};
                } else { const f32x2 d = (f32x2){bflo(v[e]), bfhi(v[e])} - pv; s1 += d; s2 += d * d; }
              }
              asm volatile("" : "+v"(s1), "+v"(s2));
            }
            if (t == 3) { tag = __builtin_amdgcn_readfirstlane(tv) + 1u; tg2 = tag2(tag); if (tpf == 10) pfq(7); if (l && tpf == 9) pfq(6); }
            if (t == 4) {
              const float d1 = (s1.x + s1.y) * (1.f / 32.f), ml = pv + d1, m = red8(ml) * 0.125f;
              const float q = red8(vmax((s2.x + s2.y) - 32.f * d1 * d1, 0.f) + 32.f * (ml - m) * (ml - m));
              gs = (u32x4){__float_as_uint(m), tag, __float_as_uint(q), tg2};
              asm volatile("" : "+v"(gs));
            }
            if (t == 8) { gs = (u32x4){__float_as_uint(sel(fr, c[2], c[0])), tag, __float_as_uint(sel(fr, c[3], c[1])), tg2}; asm volatile("" : "+v"(gs)); }
            if (t == (RB ? 12 : 11)) {
#pragma unroll
              for (int i = 0; i < 3; ++i) ld_ok(a[i]);
              for (int spin = 0;; ++spin) {
                unsigned bad = 0;
#pragma unroll
                for (int i = 0; i < 3; ++i) bad |= (a[i][1] ^ tag) | (a[i][3] ^ tg2);
                if (__builtin_expect(__all(bad == 0), 1) || spin_fail(spin, ws)) break;
                __builtin_amdgcn_s_sleep(8);
#pragma unroll
                for (int i = 0; i < 3; ++i) ld_again(a[i], rs, to[i]);
              }
              if (!RB) rowst(0);
            }
            if (t == 12 && !RB) { rowst(1); rowst(2); }
            if (t == 13 && RB == 2) rowst(0);
            if (t == 13 && !RB) { LAS float* p = (LAS float*)(uintptr_t)ta; p[0] = -o.x; p[144] = __builtin_amdgcn_rsqf(o.y * (1.f / D) + EPS); }
          }, [&]() {
            if (t == 4) st16(rs, so, gs);
            if (t == 8) {
              st16(rs, co, gs);
              sched0();
#pragma unroll
              for (int i = 0; i < 3; ++i) ld_raw(a[i], rs, to[i]);
            }
          });
        });
        if (l == 0) {
#pragma unroll
          for (int p = 0; p < 4; ++p) dma16((const char*)xin + (p >> 1) * 32768 + (p & 1) * 64, xo, L_X + lw * 4096 + p * 1024);
          ld_raw(gs, rs, ts);
          wait_vm<GQ::CNT + 5>();
        } else wait_vm<GQ::CNT>();
        bar();
        W.pre_w1<0>();
        if (RB) { if (RB == 1) rowst(0); rowst(1); rowst(2); LAS float* p = (LAS float*)(uintptr_t)ta; p[0] = -o.x; p[144] = __builtin_amdgcn_rsqf(o.y * (1.f / D) + EPS); }
        wait_lgkm0();
        if (l == 0) wait_vm<9>(); else wait_vm<4>();
        bar();
        W.pre_w1<1>(); W.pre_w1<2>();
        if (l == 0) {
          wait_vm<12>();
          ld_ok(gs);
          for (int spin = 0;; ++spin) {
            if (__builtin_expect(__all(((gs[1] ^ tag) | (gs[3] ^ tg2)) == 0), 1) || spin_fail(spin, ws)) break;
            __builtin_amdgcn_s_sleep(8);
            ld_again(gs, rs, ts);
          }
          float c0 = __uint_as_float(gs[0]), c1 = __uint_as_float(gs[2]);
          c0 += dppf<0xB1>(c0); c1 += dppf<0xB1>(c1);
          *(LAS float*)(uintptr_t)(L_CS + (lw ? lt - 64 : lt) * 4) = sel(lt & 1, c1, c0);
        }
        if (l == 0 && lw < TBA) { if (fr < 14) touch4(w1 + (size_t)(((bid >> 3) * TBA + lw) * 8 + (bid & 4)) * F + (bid & 3) * 896, fq * (F * 2) + fr * 128, L_SCR + lw * 256); }
        if (l == 0) { wait_lgkm0(); bar(); }
      }
      {
        LOADER_VARS
        GW::Loader L(wo + tp * 128, ws + WS_O + (size_t)tq * 32 * 8192, lw, lane);
        const G1::Loader W(w1 + fp * 128, ws, lw, lane);
        unsigned po = ((bid & 1) * 8 + (slow_res(wo) ^ (j & 1))) * 128 + (j >> 1) * (D * 2);
#pragma unroll
        for (int k = GW::H; k < 10; ++k) spf2(wo, po + k * GW::WSTEP);
        asm volatile("" : "+s"(po));
        if (!(LB & 1)) { sched0(); bar(); } else wait_lgkm0();
#pragma unroll
        for (int k = 10; k < 16; ++k) spf2(wo, po + k * GW::WSTEP);
        if (LB & 1) { sched0(); bar(); }
        L.start();
        L.run(16 - GW::H, [&](int) { pf.go(); });
        pf.go();
        if (E2 && bid < 224) {
          wait_vm<2 * GW::CNT>(); bar(); W.pre_w1<1>();
          wait_vm<GW::CNT + 4>(); bar(); W.pre_w1<2>();
          wait_vm<8>(); bar();
          bar(); W.pre_w1<0>();
        } else { L.tail<GW::H - 1>(); bar(); }
      }
      {
        LOADER_VARS
        if (bid < 224) {
          G1::Loader L(w1 + fp * 128, ws + WS_H + (size_t)fq1 * 64 * 16384, lw, lane);
          if (!E2) L.pre_w();
          const G2A::Loader W((const char*)(w2 + (tq & 23) * 64) + (tq & 8 ? JA * G2::WSTEP : 0), ws, lw, lane);
          const int g0 = (bid & 3) * 14, ga = (g0 + ((slow_res(w1) - g0) & 7)) ^ (j & 1), gb = ga + 8 < g0 + 14 ? ga + 8 : ga;
          const unsigned j2 = j < 32 ? 112 + (j & 15) : j;
          unsigned pa = (j >> 1) * (F * 2) + ga * 128, pb = (j >> 1) * (F * 2) + gb * 128, pd = (j2 >> 1) * (F * 2) + (j & 16 ? gb : ga) * 128;
          asm volatile("" : "+s"(pa), "+s"(pb), "+s"(pd));
          auto pfb = [&](int k) { spf2(w1, pa + k * G1::WSTEP); spf2(w1, pb + k * G1::WSTEP); spf2(w1, pd + k * G1::WSTEP); };
#pragma unroll
          for (int k = G1::H; k < G1::H + PA_1; ++k) pfb(k);
          wait_lgkm0(); pf.go();
          sched0();
          bar();
          if (!(LB & 2)) pf.go();
          L.start();
          L.run(32 - G1::H, [&](int t) {
            if (pf.pc < 3 + NW1) pf.go();
            else if (t < 32 - G1::H - PA_1) pfb(t + G1::H + PA_1);
          });
          if (EW & 4) {
            wait_vm<2 * G1::CNT>(); bar(); W.pre_w1<1>();
            wait_vm<G1::CNT + 4>(); bar();
            wait_vm<4>(); bar(); W.pre_w1<0>();
          } else L.tail<G1::H - 1>();
          bar();
        } else {
          wait_lgkm0(); pf.go();
          bar();
          if (l == 0) {
            __builtin_amdgcn_s_sleep(24);
            while (pf.pc < 3 + NW1) { wait_lgkm0(); pf.go(); }
            spare_touch(w1, w2, bid, wid, lane);
          }
        }
        pf.pc = 99;
      }
      {
        LOADER_VARS
        constexpr int NB = F / 64 - 2 * JA;
        const char* ub = ws + WS_U + (size_t)tp * 112 * 16384;
        G2A::Loader A((const char*)(w2 + (tq & 23) * 64) + (tq & 8 ? JA * G2::WSTEP : 0), ub + (tq & 8 ? JA * 32768 : 0), lw, lane);
        if (!(EW & 4) || bid >= 224) A.pre_w();
        const GQ::Loader W(wqkv + hp * 64, ws, lw, lane, qkv_rot(bid));
        unsigned pb = ((bid & 4) * 4 + (j & 1) * 8 + slow_res(w2)) * 128 + (j >> 1) * (S * 2) + JA * G2::WSTEP, pa = pb - (bid & 2 ? 0 : JA * G2::WSTEP);
        asm volatile("" : "+s"(pa), "+s"(pb));
        auto side = [&](int n) { n += G2A::H + PA_2; if (n < JA + NB) spf2(w2, (n < JA ? pa : pb) + n * G2::WSTEP); };
#pragma unroll
        for (int k = 0; k < PA_2; ++k) side(k - PA_2);
        G2::Loader L((const char*)(w2 + tq * 64) + 2 * JA * G2::WSTEP, ub + 2 * JA * 32768, lw, lane);
        gu32* xf = (gu32*)(ws + WS_MAIL) + (l * 4 + 2) * MEDGE + (tq * 8 + tp) * 64 + 28 + lw;
        const char* xp = ws + WS_XP + (unsigned)(((bid ^ 2) * 4 + lw) << 14);
        unsigned fl = L_QKV + lw * 256, xv = ph(lane * 16);
        asm volatile("" : "+s"(xf), "+s"(xp), "+s"(fl)); asm volatile("" : "+v"(xv));
        sched0();
        bar();
        A.start();
        A.run(JA - G2A::H, [&](int t) { side(t); });
        auto stage = [&](auto s) { L.issue_w<s.value * G2::SB>(L.w + s.value * G2::WSTEP); L.issue_a<s.value * G2::SB>(L.ab + s.value * 32768); sched0(); };
        wait_vm<G2A::CNT>(); bar(); stage(IC<0>{}); side(JA - 2);
        wait_vm<G2::CNT>(); bar(); stage(IC<1>{}); side(JA - 1);
        wait_vm<G2::CNT>(); bar(); stage(IC<2>{}); stage(IC<3>{}); side(JA);
        L.t = 1; L.islot = 0;
        L.run(NB - G2::H, [&](int t) {
          if (t == NB - G2::H - FLS) asm volatile("s_mov_b32 m0, %2\n\ts_nop 0\n\tglobal_load_lds_dword %0, %1 sc1" :: "v"(0u), "s"(xf), "s"(fl) : "memory");
          side(JA + t);
        });
        auto xdma = [&]() {
#pragma unroll
          for (int i = 0; i < 4; ++i) dma16(xp + i * 2048, xv, L_X + lw * 4096 + i * 1024);
          sched0();
        };
        sched0(); wait_vm<2 * G2::CNT>(); bar();
        if (__builtin_amdgcn_readfirstlane(*(volatile LAS unsigned*)(uintptr_t)fl)) {
          xdma();
          if ((EW & 8) && l == 0) { W.pre_w1<0>(); wait_vm<G2::CNT + 10>(); bar(); W.pre_w1<1>(); wait_vm<16>(); bar(); wait_vm<12>(); }
          else { wait_vm<G2::CNT + 4>(); bar(); wait_vm<4>(); bar(); wait_vm<0>(); }
        } else {
          wait_vm<G2::CNT>(); bar(); wait_vm<0>(); bar();
          for (int spin = 0;; ++spin) {
            if (__builtin_amdgcn_readfirstlane(__hip_atomic_load(xf, RLX_AGENT)) || spin_fail(spin, ws)) break;
            __builtin_amdgcn_s_sleep(2);
          }
          STL(4 + l * 4, 7);
          xdma();
          if ((EW & 8) && l == 0) { W.pre_w(); wait_vm<12>(); } else wait_vm<0>();
        }
        bar();
        xin = (const u16*)(ws + WS_X2);
        if (NL == 1) bar();
      }
    }
    return;
  }

  {
    ST(0, 0);
    PHASE_VARS
    if (cw >= 0) code_warm(cw, lane, L_SCR + wid * 256);
    pfb_q(wqkv, bid, (bid >> 3) * 4 + wid, 0); pfb_q(wqkv, bid, (bid >> 3) * 4 + wid, 1);
    if (wid < 3) {
      if (tid == 0) asm volatile("s_mov_b32 m0, %2\n\ts_nop 0\n\tglobal_load_lds_dword %0, %1 sc1" :: "v"(WS_EPOCH), "s"(ws), "s"(L_TAG) : "memory");
      const rsrc_t rz = mkrsrc(ws + WS_XCH, 3 * MB);
      if (tid < 128) st16(rz, ph((((tid >> 6) * 256 + tq * 8 + tp) * 64 + lane) * 16), (u32x4){0u, SENT, 0u, SENT});
      else {
        const int e = (tid >> 3) & 7, line = e == 0 || e == 7 ? (2 * sb + (e & 1)) * 8 + (hp & 7) : e * 256 + bid;
        st16(rz, WS_MAIL - WS_XCH + line * 256 + (tid & 7) * 16, (u32x4){0u, 0u, 0u, 0u});
      }
      if (wid == 1 && bid < 224) touch4(b1 + fp * 128, lane * 4, L_BIAS);
      if (wid == 2) touch4(b2 + tq * 64, (lane & 31) * 4, L_BIAS + 256);
      if (wid == 0) *(LAS u32x4*)(uintptr_t)(L_TAG + 16) = (u32x4){0u, 0u, 0u, 0u};
    }
    if (CPR) __builtin_amdgcn_s_setprio(CPR);
    ST(0, 4);
    ST(1, 0);
  }
#pragma unroll 1
  for (int l = 0; l < NL; ++l) {
    {
      PHASE_VARS
      if (wid & 2) qkv_attn<1>(ws, l, wp, lane, hp, sb, cw, bid, tp, tq, wqkv);
      else qkv_attn<0>(ws, l, wp, lane, hp, sb, cw, bid, tp, tq, wqkv);
      ST(1 + l * 4, 5);
    }
    {
      PHASE_VARS
      GW::Comp<O42<false, WNE>> g; g.setup(wp, wq, lane);
      g.setwin();
      const rsrc_t rx = mkrsrc(ws + WS_XCH + l * (MB / 2), MB / 2), rh = mkrsrc(ws + WS_H, S * D * 4);
      unsigned xa = L_X + wid * 4096 + lane * 16;
      unsigned xs = fq != fr >> 2 ? MB : ph((unsigned)(((tq * 8 + tp) * 64 + wp * 32 + wq * 16 + fr) * 16)), xl = ph((unsigned)((tq * 512 + fq * 128 + wq * 16 + fr) * 16));
      const unsigned h0 = ph((unsigned)(((tp * 32 + tq) << 14) + wp * 8192 + (wq * 32 + fr) * 128 + ((pair_chunk(fq) ^ (km_f(fr) << 1)) << 4)));
      u32x4 ho = {h0, h0 ^ 64, h0 + 4096, (h0 ^ 64) + 4096};
      bf16x8 ones = {0x3F80, 0x3F80, 0x3F80, 0x3F80, 0x3F80, 0x3F80, 0x3F80, 0x3F80};
      asm volatile("" : "+v"(xa), "+v"(xs), "+v"(xl), "+v"(ho), "+v"(ones));
      Gate gt;
      auto edge = [&]() {
        sync_post(ws, l * 4, lane < 16 ? (sb * 16 + lane) * 64 + hp : -1, wid);
        gt.wait<0>(ws, l * 4, tq * 8 + tp, 0xffffu, (1u << GW::H) - 1, (1u << (GW::H + GLW)) - 1, wid);
        sched0();
      };
      if (WSP == 3) edge();
      f32x4 acc[4][2];
#pragma unroll
      for (int i = 0; i < 4; ++i)
#pragma unroll
        for (int jj = 0; jj < 2; ++jj) acc[i][jj] = (f32x4){0.f, 0.f, 0.f, 0.f};
      { bf16x8 s[2]; sel_frags(true, fr, fq, s); tile_add(acc, s, xa); }
      g.pin(acc);
      if (WSP == 2) edge(); else mfma_gap();
      g.run_steps<false>(acc, [&](int it, int i) {
        if (i < 0) gt.arm(it, cw);
        if (i == 0) gt.head(it, cw, lane, wid);
        if (i == GLW - 1) gt.late(ws);
      } CSA);
      MTD;
      MT(0);
      bar();
      sched0();
      MT(1);
      g.pend<0, 4>(acc);
      ST(2 + l * 4, 2);
      u32x4 pk[2][2]; float mp[2];
      {
        f32x4 sm[2], gm[2];
#pragma unroll
        for (int i = 0; i < 2; ++i)
#pragma unroll
          for (int jj = 0; jj < 2; ++jj) {
            pk[jj][i] = cat4(pk4(acc[2 * i][jj]), pk4(acc[2 * i + 1][jj])); asm volatile("" : "+v"(pk[jj][i]));
            const bf16x8 x = __builtin_bit_cast(bf16x8, pk[jj][i]);
            gm[jj] = mfma(x, x, i ? gm[jj] : (f32x4){0.f, 0.f, 0.f, 0.f}); sm[jj] = mfma(ones, x, i ? sm[jj] : (f32x4){0.f, 0.f, 0.f, 0.f});
          }
        u32x4 st;
#pragma unroll
        for (int jj = 0; jj < 2; ++jj) {
          const f32x4 gv = gm[jj];
          st[2 * jj] = __float_as_uint(sm[jj][0]); st[2 * jj + 1] = __float_as_uint(sel(fr & 2, sel(fr & 1, gv[3], gv[2]), sel(fr & 1, gv[1], gv[0])));
          mp[jj] = sm[jj][0] * (1.f / 64.f);
        }
        st16(rx, xs, st);
      }
      sched0();
      MT(2);
      f32x4 gr[4];
      auto gld = [&]() {
        asm volatile("" ::: "memory");
#pragma unroll
        for (int p = 0; p < 4; ++p) gr[p] = __builtin_bit_cast(f32x4, __builtin_amdgcn_raw_buffer_load_b128(rx, xl + p * 1024, 0, 16));
        sched0();
      };
      if (XE == 1) gld();
#pragma unroll
      for (int p = 0; p < 4; ++p) *(LAS u32x4*)(uintptr_t)(xa + p * 1024) = pk[p >> 1][p & 1];
      asm volatile("" : "+v"(mp[0]), "+v"(mp[1]));
#pragma unroll
      for (int jj = 0; jj < 2; ++jj)
#pragma unroll
        for (int i = 0; i < 2; ++i) {
          const auto rx_ = __builtin_amdgcn_permlane16_swap(pk[jj][i][0], pk[jj][i][2], false, false), ry_ = __builtin_amdgcn_permlane16_swap(pk[jj][i][1], pk[jj][i][3], false, false);
          const unsigned x0 = rx_[0], x1 = rx_[1], y0 = ry_[0], y1 = ry_[1];
          acc[2 * i][jj] = (f32x4){bflo(x0), bfhi(x0), bflo(y0), bfhi(y0)} - mp[jj]; acc[2 * i + 1][jj] = (f32x4){bflo(x1), bfhi(x1), bflo(y1), bfhi(y1)} - mp[jj];
          asm volatile("" : "+v"(acc[2 * i][jj]), "+v"(acc[2 * i + 1][jj]));
          if (XE == 2 + jj * 2 + i) { sched0(); gld(); }
        }
      if (!XE) { if (XD) __builtin_amdgcn_s_sleep(XD); gld(); }
      MT(3);
      for (int spin = 0;; ++spin) {
        const float lo = vmin3(vmin3(gr[0][1], gr[0][3], gr[1][1]), vmin3(gr[1][3], gr[2][1], gr[2][3]), vmin3(gr[3][1], gr[3][3], gr[3][3]));
        if (!__any(lo < 0.f) || spin_fail(spin, ws)) break;
        if (XS) __builtin_amdgcn_s_sleep(XS);
        gld();
      }
      ST(2 + l * 4, 5);
      MT(4);
      {
        const f32x4 t = (gr[0] + gr[1]) + (gr[2] + gr[3]);
        float m[2] = {t[0], t[2]}, q[2] = {t[1], t[3]}, u, w, v, y;
        swp32(m[0], m[1]); swp32(q[0], q[1]);
        xch16(m[0] + m[1], u, w); xch16(q[0] + q[1], v, y);
        const float mean = (u + w) * (1.f / D), var = __builtin_fmaxf((v + y) * (1.f / D) - mean * mean, 0.f), r1 = __builtin_amdgcn_rsqf(var + EPS);
        xch32(mean, m[0], m[1]);
        xch32(r1 * __builtin_amdgcn_rsqf(var * r1 * r1 + EPS), q[0], q[1]);
        ST(2 + l * 4, 3);
        MT(5);
#pragma unroll
        for (int jj = 0; jj < 2; ++jj) {
          const float c = (mp[jj] - m[jj]) * q[jj];
#pragma unroll
          for (int i = 0; i < 2; ++i) st16(rh, ho[jj * 2 + i], cat4(pk4(acc[2 * i][jj] * q[jj] + c), pk4(acc[2 * i + 1][jj] * q[jj] + c)));
        }
      }
      MT(6);
      MT(7);
      MTOUT(12, 0);
    }
    {
      PHASE_VARS
      const int post = lane < 28 ? (tp * 28 + lane) * 64 + tq : -1;
      if (bid >= 224) {
        sync_post(ws, l * 4 + 1, post, wid);
        if (l == 0 && bid == 255 && wid == 0) { const unsigned tag = get_tag(); if (lane == 0) __hip_atomic_store((gu32*)(ws + WS_EPOCH), tag, RLX_AGENT); }
        ST(2 + l * 4, 4);
        ST(3 + l * 4, 0);
        if (l == 0) { __builtin_amdgcn_s_sleep(24); spare_touch(w1, w2, bid, wid, lane); }
      } else {
        G1::Comp<O44<false, 32>> g; g.setup(wp, wq, lane);
        f32x4 acc[4][4];
#pragma unroll
        for (int i = 0; i < 4; ++i) { const u32x2 b = *(const LAS u32x2*)(uintptr_t)(L_BIAS + (wp * 4 + i) * 32 + fq * 8);
#pragma unroll
          for (int jj = 0; jj < 4; ++jj) acc[i][jj] = (f32x4){bflo(b.x), bfhi(b.x), bflo(b.y), bfhi(b.y)}; }
        const rsrc_t ro = mkrsrc(ws + WS_U, D * F * 4);
        const unsigned u0 = ph((unsigned)(((fq1 * 56 + fp * 2 + wp) << 14) + (wq * 64 + fr) * 128 + ((pair_chunk(fq) ^ (fr >> 1)) << 4)));
        u32x4 uo = {u0, u0 + 4096, u0 + 8192, u0 + 12288}, uo2 = uo ^ 64;
        asm volatile("" : "+v"(uo), "+v"(uo2));
        g.prep(acc);
        sync_post(ws, l * 4 + 1, post, wid);
        Gate gt; gt.wait<SW0>(ws, l * 4 + 1, fq1 * 28 + fp, ~0u, (1u << G1::H) - 1, (1u << (G1::H + GL1)) - 1, wid);
        g.run_steps<false>(acc, [&](int it, int i) {
          if (i < 0) gt.arm(it, cw);
        if (i == 0) gt.head(it, cw, lane, wid);
          if (i == GL1 - 1) gt.late(ws);
        } CSA);
        bar();
        sched0();
        ST(3 + l * 4, 2);
        if (GSH) {
          for_c<0, 2>([&](auto jc) {
            constexpr int jj = jc.value;
            f32x4 a[2];
#pragma unroll
            for (int i = 0; i < 2; ++i) { a[i] = gelu_arg(acc[i][jj]); asm volatile("" : "+v"(a[i])); }
            sched0();
            for_c<0, 4>([&](auto ec) { constexpr int e = ec.value; g.mm<jj * 8 + e>(acc); sched0(); a[0][e] = __builtin_amdgcn_exp2f(a[0][e]); a[1][e] = __builtin_amdgcn_exp2f(a[1][e]); asm volatile("" : "+v"(a[0]), "+v"(a[1])); sched0(); });
            a[0] += 1.f; a[1] += 1.f; asm volatile("" : "+v"(a[0]), "+v"(a[1]));
            sched0();
            for_c<0, 4>([&](auto ec) { constexpr int e = ec.value; g.mm<jj * 8 + 4 + e>(acc); sched0(); a[0][e] = __builtin_amdgcn_rcpf(a[0][e]); a[1][e] = __builtin_amdgcn_rcpf(a[1][e]); asm volatile("" : "+v"(a[0]), "+v"(a[1])); sched0(); });
            st16(ro, uo[jj], pack_pair(acc[0][jj] * a[0], acc[1][jj] * a[1]));
          });
        } else g.pend<0, 8>(acc);
#pragma unroll
        for (int n = GSH ? 2 : 0; n < 8; ++n) { const int i = n >> 2, jj = n & 3; st16(ro, (i ? uo2 : uo)[jj], pack_pair(gelu_tanh(acc[2 * i][jj]), gelu_tanh(acc[2 * i + 1][jj]))); }
        ST(3 + l * 4, 3);
      }
    }
    {
      PHASE_VARS
      asm volatile("" : "+s"(out));
      G2A::Comp<O44<true, ANE>> ga; ga.setup(wp, wq, lane);
#pragma unroll
      for (int jj = 0; jj < 4; ++jj) ga.qrd[0][jj] = 65536 + rd_km(lane, wq * 2 + (jj & 1)) + (((jj >> 1) ^ (tq >> 3)) & 1) * 8192;
      G2::Comp<O42<true, BNE>> g; g.setup(wp, wq, lane);
      g.setwin();
      ga.setwin();
      Gate gt;
      auto edge = [&]() {
        sync_post(ws, l * 4 + 2, bid < 224 && lane < 32 ? (lane * 8 + fq1) * 64 + fp : -1, wid);
        gt.wait<0>(ws, l * 4 + 2, tq * 8 + tp, 0xfffffffu, tq & 8 ? 1u << JA / 2 : 1u, tq & 8 ? 7u << JA / 2 : 7u, wid);
        sched0();
      };
      if (GSP == 3) edge();
      f32x4 ac4[4][4], acc[4][2];
#pragma unroll
      for (int jj = 0; jj < 2; ++jj) { const float b = bf2f(*(const LAS u16*)(uintptr_t)(L_BIAS + 256 + (wq * 2 + jj) * 32 + fr * 2));
#pragma unroll
        for (int i = 0; i < 4; ++i) { ac4[i][jj] = (f32x4){b, b, b, b}; ac4[i][jj + 2] = (f32x4){0.f, 0.f, 0.f, 0.f}; } }
      unsigned xa = L_X + wid * 4096 + lane * 16, xo = (unsigned)((bid * 4 + wid) << 14) + ph(lane * 16), oo = x_off(tp, tq, wid, fr, fq), xo2 = xo + 4096, oo2 = oo + 32768;
      unsigned fo = lane ? MB : (((tq ^ 8) * 8 + tp) * 64 + 28 + wid) * 4u, one = 1u;
      bf16x8 s[2]; sel_frags(false, fr, fq, s);
      if (GSP == 4) { asm volatile("" : "+v"(xa), "+v"(xo), "+v"(oo), "+v"(fo), "+v"(s[0]), "+v"(s[1])); edge(); }
      tile_add(ac4, s, xa);
      const rsrc_t rx = mkrsrc(ws + WS_XP, 16 * MB), rf = mkrsrc(ws + WS_MAIL + (l * 4 + 2) * (MEDGE * 4), MEDGE * 4), ro = mkrsrc(l ? (const void*)out : (const void*)(ws + WS_X2), S * D * 2);
      asm volatile("" : "+v"(xa), "+v"(xo), "+v"(oo), "+v"(xo2), "+v"(oo2), "+v"(fo), "+v"(one), "+v"(s[0]), "+v"(s[1]));
      ga.pin(ac4);
      static_assert(JA % 2 == 0 && G2A::H == 2, "");
      if (GSP == 2) edge(); else mfma_gap();
      ga.run_steps<false>(ac4, [&](int it, int i) {
        if (i < 0) gt.arm(it, cw);
        if (i == 0) gt.head(it, cw, lane, wid);
        if (i == 2) gt.late(ws);
      } CSA);
      ga.pend<4, 8>(ac4);
      ST(4 + l * 4, 1);
#pragma unroll
      for (int i = 0; i < 2; ++i)
#pragma unroll
        for (int jj = 0; jj < 2; ++jj) st16(rx, (jj ? xo2 : xo) + i * 2048, cat4(pk4(ac4[2 * i][jj + 2]), pk4(ac4[2 * i + 1][jj + 2])));
#pragma unroll
      for (int i = 0; i < 4; ++i) { acc[i][0] = ac4[i][0]; acc[i][1] = ac4[i][1]; }
#pragma unroll
      for (int k = 0; k < 2; ++k) { g.fp[2][k] = ga.fp[2][k]; g.fp[3][k] = ga.fp[3][k]; g.fq[0][k] = ga.fq[0][k]; g.fq[1][k] = ga.fq[1][k]; }
      sched0();
      g.run_steps<true>(acc, [&](int it, int i) {
        if (i == PS % 4 && __builtin_expect(it == (F / 64 - 2 * JA) / 4 - PS / 4, 0)) {
          wait_vm<0>();
          ST(4 + l * 4, 5);
          __builtin_amdgcn_raw_buffer_store_b32(one, rf, fo, 0, 16);
        }
      } CSA);
      bar();
      sched0();
      g.pend<0, 4>(acc);
      ST(4 + l * 4, 2);
      tile_add(acc, s, xa);
      u32x4 xr[4];
#pragma unroll
      for (int i = 0; i < 2; ++i)
#pragma unroll
        for (int jj = 0; jj < 2; ++jj) { xr[jj * 2 + i] = pack_pair(acc[2 * i][jj], acc[2 * i + 1][jj]); st16(ro, (jj ? oo2 : oo) + i * 64, xr[jj * 2 + i]); }
      ST(4 + l * 4, 3);
      if (l == 0) {
#pragma unroll
        for (int p = 0; p < 4; ++p) *(LAS u32x4*)(uintptr_t)(xa + p * 1024) = xr[p];
        if (NL == 1) sync_post(ws, 3, -1, wid);
      } else ST(8, 4);
    }
  }
  if (wid0 == 0 && blockIdx.x == 45) {
    wait_lgkm0();
    unsigned d = *(volatile LAS unsigned*)(uintptr_t)(19968 + PROBE * 4) - *(volatile LAS unsigned*)(uintptr_t)(19968 + PROBE0 * 4);
    const u64 e = __builtin_amdgcn_s_memrealtime() + PROBEF * (d < 20000 ? d : 0);
    while (__builtin_amdgcn_s_memrealtime() < e) __builtin_amdgcn_s_sleep(1);
  }
  return;
  if (wid0 == 0) {
    const u64 hi = __builtin_amdgcn_s_memrealtime() & ~0xffffffffull;
    wait_lgkm0();
    for (int e = threadIdx.x; e < 128; e += 64) ((gu64*)(ws + (200u << 20)))[((e >> 3) * 1024 + blockIdx.x) * 8 + (e & 7)] = hi | *(volatile LAS unsigned*)(uintptr_t)(19968 + e * 4);
  }
}

extern "C" void kernel_launch(void* const* d_in, const int* in_sizes, int n_in,
                              void* d_out, int out_size, void* d_ws, size_t ws_size,
                              hipStream_t stream) {
  (void)hipFuncSetAttribute((const void*)mega_kernel, hipFuncAttributeMaxDynamicSharedMemorySize, 163840);
  mega_kernel<<<256, 512, 163840, stream>>>((const u16*)d_in[0], (const u16*)d_in[1], (const u16*)d_in[2], (const u16*)d_in[4], (const u16*)d_in[6], (const u16*)d_in[5], (const u16*)d_in[7], (u16*)d_out, (char*)d_ws);
}
```

```cpp
#define LEAN
#define STAMP
#define PROBE 62
#define PROBE0 54
#define PROBEF 8
#include <hip/hip_runtime.h>
#include <hip/hip_bf16.h>
#include <stdint.h>

typedef unsigned short u16;
typedef unsigned long long u64;
typedef short bf16x8 __attribute__((ext_vector_type(8)));
typedef short bf16x4 __attribute__((ext_vector_type(4)));
typedef float f32x4 __attribute__((ext_vector_type(4)));
typedef float f32x2 __attribute__((ext_vector_type(2)));
typedef unsigned u32x4 __attribute__((ext_vector_type(4)));
typedef unsigned u32x2 __attribute__((ext_vector_type(2)));
typedef __bf16 bf16v2 __attribute__((ext_vector_type(2)));
#define LAS __attribute__((address_space(3)))
#define GAS __attribute__((address_space(1)))
#define DEVI __device__ __forceinline__
typedef __attribute__((address_space(1))) unsigned gu32;
typedef __attribute__((address_space(1))) unsigned long long gu64;

constexpr int S = 2048, D = 1024, F = 3584;
constexpr float EPS = 1e-5f;

DEVI float bf2f(u16 v) { return __uint_as_float(((unsigned)v) << 16); }
DEVI float bflo(unsigned v) { return __uint_as_float(v << 16); }
DEVI float bfhi(unsigned v) { return __uint_as_float(v & 0xffff0000u); }
DEVI unsigned pk2(float lo, float hi) { bf16v2 p; p.x = (__bf16)lo; p.y = (__bf16)hi; return __builtin_bit_cast(unsigned, p); }

DEVI const void* uni_ptr(const void* p) {
  const u64 v = (u64)p;
  return (const void*)(((u64)(unsigned)__builtin_amdgcn_readfirstlane((unsigned)(v >> 32)) << 32) | (unsigned)__builtin_amdgcn_readfirstlane((unsigned)v));
}
DEVI void dma16(const void* sbase, unsigned voff, unsigned ldsaddr) {
  ldsaddr = __builtin_amdgcn_readfirstlane(ldsaddr);
  asm volatile("s_mov_b32 m0, %2\n\ts_nop 0\n\tglobal_load_lds_dwordx4 %0, %1 sc1" :: "v"(voff), "s"(sbase), "s"(ldsaddr) : "memory");
}
template <int N, int IMM> DEVI void dma_grp(const void* sbase, const unsigned* v, unsigned lb) {
  static_assert(N == 1 || N == 2 || N == 4, "");
  if constexpr (N == 4)
    asm volatile("s_add_u32 m0, %1, %2\n\ts_nop 0\n\tglobal_load_lds_dwordx4 %3, %0 sc1\n\tglobal_load_lds_dwordx4 %4, %0 offset:1024 sc1\n\t"
                 "global_load_lds_dwordx4 %5, %0 offset:2048 sc1\n\tglobal_load_lds_dwordx4 %6, %0 offset:3072 sc1"
                 :: "s"(sbase), "s"(lb), "i"(IMM), "v"(v[0]), "v"(v[1]), "v"(v[2]), "v"(v[3]) : "memory", "scc");
  else if constexpr (N == 2)
    asm volatile("s_add_u32 m0, %1, %2\n\ts_nop 0\n\tglobal_load_lds_dwordx4 %3, %0 sc1\n\tglobal_load_lds_dwordx4 %4, %0 offset:1024 sc1"
                 :: "s"(sbase), "s"(lb), "i"(IMM), "v"(v[0]), "v"(v[1]) : "memory", "scc");
  else
    asm volatile("s_add_u32 m0, %1, %2\n\ts_nop 0\n\tglobal_load_lds_dwordx4 %3, %0 sc1" :: "s"(sbase), "s"(lb), "i"(IMM), "v"(v[0]) : "memory", "scc");
}
template <int N> DEVI void wait_vm() { asm volatile("s_waitcnt vmcnt(%0)" :: "i"(N) : "memory"); }
DEVI void wait_lgkm0() { asm volatile("" ::: "memory"); __builtin_amdgcn_s_waitcnt(0xC07F); asm volatile("" ::: "memory"); }
DEVI void mfma_ip(f32x4& c, const bf16x8& a, const bf16x8& b) { asm volatile("v_mfma_f32_16x16x32_bf16 %0, %1, %2, %0" : "+v"(c) : "v"(a), "v"(b)); }
DEVI void mfma_gap() { __builtin_amdgcn_sched_barrier(0); asm volatile("s_nop 7"); __builtin_amdgcn_sched_barrier(0); }
DEVI void bar() { asm volatile("" ::: "memory"); __builtin_amdgcn_s_barrier(); asm volatile("" ::: "memory"); }
DEVI void sched0() { __builtin_amdgcn_sched_barrier(0); }

DEVI bf16x8 lds_b128(unsigned addr) { return *(const LAS bf16x8*)(uintptr_t)addr; }
DEVI bf16x4 lds_tr4(unsigned addr) { return __builtin_amdgcn_ds_read_tr16_b64_v4i16((LAS bf16x4*)(uintptr_t)addr); }

typedef __amdgpu_buffer_rsrc_t rsrc_t;
DEVI rsrc_t mkrsrc(const void* p, unsigned bytes) { return __builtin_amdgcn_make_buffer_rsrc((void*)p, 0, bytes, 0x00020000); }
DEVI void st16(rsrc_t r, unsigned off, u32x4 v) { __builtin_amdgcn_raw_buffer_store_b128(v, r, off, 0, 16); }
DEVI void ld_raw(u32x4& v, rsrc_t r, unsigned off) { asm volatile("buffer_load_dwordx4 %0, %1, %2, 0 offen sc1 ; raw load" : "=&v"(v) : "v"(off), "s"(r) : "memory"); }
DEVI void ld_again(u32x4& v, rsrc_t r, unsigned off) { asm volatile("buffer_load_dwordx4 %0, %1, %2, 0 offen sc1\n\ts_waitcnt vmcnt(0)" : "=&v"(v) : "v"(off), "s"(r) : "memory"); }
DEVI void ld_ok(u32x4& v) { asm volatile("; raw ok %0" : "+v"(v)); }
#define RLX_AGENT __ATOMIC_RELAXED, __HIP_MEMORY_SCOPE_AGENT

template <int N> DEVI float ror16(float v) { return __int_as_float(__builtin_amdgcn_update_dpp(0, __float_as_int(v), 0x120 + N, 0xf, 0xf, false)); }
DEVI float red16(float v) { v += ror16<8>(v); v += ror16<4>(v); v += ror16<2>(v); v += ror16<1>(v); return v; }
DEVI void xch32(float v, float& a, float& b) { unsigned u = __float_as_uint(v); auto r = __builtin_amdgcn_permlane32_swap(u, u, false, false); unsigned x = r[0], y = r[1]; a = __uint_as_float(x); b = __uint_as_float(y); }
DEVI void xch16(float v, float& a, float& b) { unsigned u = __float_as_uint(v); auto r = __builtin_amdgcn_permlane16_swap(u, u, false, false); unsigned x = r[0], y = r[1]; a = __uint_as_float(x); b = __uint_as_float(y); }
DEVI void swp32(float& a, float& b) { auto r = __builtin_amdgcn_permlane32_swap(__float_as_uint(a), __float_as_uint(b), false, false); unsigned x = r[0], y = r[1]; a = __uint_as_float(x); b = __uint_as_float(y); }
DEVI void swp16(float& a, float& b) { auto r = __builtin_amdgcn_permlane16_swap(__float_as_uint(a), __float_as_uint(b), false, false); unsigned x = r[0], y = r[1]; a = __uint_as_float(x); b = __uint_as_float(y); }
DEVI float colsum4(float v) { float a, b; xch32(v, a, b); v = a + b; xch16(v, a, b); return a + b; }
DEVI float vmax(float a, float b) { float r; asm("v_max_f32 %0, %1, %2" : "=v"(r) : "v"(a), "v"(b)); return r; }
DEVI float vmin3(float a, float b, float c) { float r; asm("v_min3_f32 %0, %1, %2, %3" : "=v"(r) : "v"(a), "v"(b), "v"(c)); return r; }
DEVI float vmax3(float a, float b, float c) { float r; asm("v_max3_f32 %0, %1, %2, %3" : "=v"(r) : "v"(a), "v"(b), "v"(c)); return r; }

DEVI unsigned src_kc(int c, int ld) { int row = c >> 3, ch = (c & 7) ^ ((row >> 1) & 7); return (unsigned)(row * ld * 2 + ch * 16); }
DEVI int km_f(int k) { return ((k >> 1) & 1) | (((k >> 3) & 1) << 1); }
DEVI unsigned ph(unsigned x) { return ((x >> 7) << 8) | (x & 127u); }
DEVI unsigned rd_kc(int lane, int ks) { int r = lane & 15, g = lane >> 4; return (unsigned)(r * 128 + (((ks * 4 + g) ^ (r >> 1)) << 4)); }
DEVI unsigned rd_km(int lane, int ct) {
  int g = lane >> 4, q = (lane & 15) >> 2, p = lane & 3;
  int f = ((q >> 1) & 1) | ((g & 1) << 1);
  return (unsigned)((ct >> 2) * 8192 + (8 * g + q) * 128 + (((ct & 3) ^ f) << 5) + 8 * p);
}

DEVI void stamp(unsigned a) { unsigned x, y;
  asm volatile("s_memrealtime s[98:99]\n\tv_mov_b32 %1, %2\n\ts_waitcnt lgkmcnt(0)\n\tv_mov_b32 %0, s98\n\tds_write_b32 %1, %0" : "=&v"(x), "=&v"(y) : "s"(a) : "s98", "s99"); }
#define ST(k, i) do { if (__builtin_amdgcn_readfirstlane(threadIdx.x) == 0) stamp(19968 + ((k) * 8 + (i)) * 4); } while (0)
#define STL(k, i) do { if (__builtin_amdgcn_readfirstlane(threadIdx.x) == 256) stamp(19968 + ((k) * 8 + (i)) * 4); } while (0)

constexpr unsigned CODE_BYTES = 52224;
DEVI void code_warm(int part, int lane, unsigned scratch) {
  unsigned long long pc;
  asm volatile("s_getpc_b64 s[40:41]\n\ts_add_u32 s40, s40, mega_kernel@rel32@lo+4\n\ts_addc_u32 s41, s41, mega_kernel@rel32@hi+12\n\ts_mov_b64 %0, s[40:41]" : "=s"(pc) :: "s40", "s41", "scc");
  const unsigned off = (unsigned)(part * 64 + lane) * 128u;
  scratch = __builtin_amdgcn_readfirstlane(scratch);
  if (off < CODE_BYTES) asm volatile("s_nop 3\n\ts_mov_b32 m0, %2\n\ts_nop 0\n\tglobal_load_lds_dword %0, %1" :: "v"(off), "s"(pc), "s"(scratch) : "memory");
}

DEVI unsigned src_km(int lt, int ld) { const int k = lt >> 3; return (unsigned)(k * ld * 2 + (((lt & 7) ^ (km_f(k) << 1)) << 4)); }
DEVI void touch4(const void* sbase, unsigned voff, unsigned ldsaddr) {
  ldsaddr = __builtin_amdgcn_readfirstlane(ldsaddr); sbase = uni_ptr(sbase);
  asm volatile("s_nop 3\n\ts_mov_b32 m0, %2\n\ts_nop 0\n\tglobal_load_lds_dword %0, %1" :: "v"(voff), "s"(sbase), "s"(ldsaddr) : "memory");
}

template <int IMM> DEVI void spf1(const void* base, unsigned soff) { asm volatile("s_load_dword s101, %0, %1 offset:%2" :: "s"(base), "s"(soff), "i"(IMM) : "s101"); }
template <int IMM = 0> DEVI void spf2(const void* base, unsigned soff) { asm volatile("s_load_dwordx2 s[100:101], %0, %1 offset:%2" :: "s"(base), "s"(soff), "i"(IMM + 60) : "s100", "s101"); }
template <int ST> DEVI void spf2x6(const void* base, unsigned soff) {
  asm volatile("s_load_dwordx2 s[100:101], %0, %1 offset:%2\n\ts_load_dwordx2 s[100:101], %0, %1 offset:%3\n\ts_load_dwordx2 s[100:101], %0, %1 offset:%4\n\t"
               "s_load_dwordx2 s[100:101], %0, %1 offset:%5\n\ts_load_dwordx2 s[100:101], %0, %1 offset:%6\n\ts_load_dwordx2 s[100:101], %0, %1 offset:%7"
               :: "s"(base), "s"(soff), "i"(60), "i"(60 + ST), "i"(60 + 2 * ST), "i"(60 + 3 * ST), "i"(60 + 4 * ST), "i"(60 + 5 * ST) : "s100", "s101");
}
template <int ST> DEVI void spf1x12(const void* base, unsigned soff) {
  spf1<0>(base, soff); spf1<ST>(base, soff); spf1<2 * ST>(base, soff); spf1<3 * ST>(base, soff); spf1<4 * ST>(base, soff); spf1<5 * ST>(base, soff);
  spf1<6 * ST>(base, soff); spf1<7 * ST>(base, soff); spf1<8 * ST>(base, soff); spf1<9 * ST>(base, soff); spf1<10 * ST>(base, soff); spf1<11 * ST>(base, soff);
}
DEVI int lgkm_out() { int n; asm volatile("s_getreg_b32 %0, hwreg(HW_REG_IB_STS, 8, 4)" : "=s"(n)); return n; }
DEVI int slow_res(const void* w) { return (3 - ((unsigned)(uintptr_t)w >> 7)) & 7; }

#define MTD
#define MT(i)
#define MTOUT(k, w)
#define CSP
#define CSA
#define WNE 16
#define ANE 24
#define BNE 13
#define QNE 46
struct Plan { int n; signed char q[40], idx[40], k[40], h[40], g[40]; };
template <class O, bool PKM, bool QKM> constexpr Plan make_plan() {
  Plan p{};
  int nr = 0;
  for (int c = 0; c < O::NRO; ++c) nr += ((O::ro[c] >> 4) ? QKM : PKM) ? 4 : 2;
  int prev = 0;
  for (int c = 0; c < O::NRO; ++c) {
    const int q = O::ro[c] >> 4, i = O::ro[c] & 15;
    int av[2] = {0, 0}, k0 = -1;
    for (int m = 0; m < 2 * O::NP; ++m) {
      const int mm = (m + 2 * O::NPEND) % (2 * O::NP);
      if ((q ? O::pj[m >> 1] : O::pi[m >> 1]) == i && m < 2 * O::NPEND) av[((m >> 1) ^ m) & 1] = m;
      if ((q ? O::pj[mm >> 1] : O::pi[mm >> 1]) == i && k0 < 0) k0 = ((mm >> 1) ^ mm) & 1;
    }
    for (int kk = 0; kk < 2; ++kk)
      for (int h = 0; h < ((q ? QKM : PKM) ? 2 : 1); ++h) {
        const int k = k0 ^ kk;
        int g = p.n * O::NE / nr;
        if (g < av[k]) g = av[k];
        if (g < prev + (p.n && O::NE >= nr ? 1 : 0)) g = prev + (p.n && O::NE >= nr ? 1 : 0);
        p.q[p.n] = q; p.idx[p.n] = i; p.k[p.n] = k; p.h[p.n] = h; p.g[p.n] = g; prev = g; ++p.n;
      }
  }
  return p;
}
struct OBase { static constexpr int pidx(int i) { return i; } static constexpr int poff(int) { return 0; } static constexpr int qoff(int j) { return j * 2048; } static constexpr bool swp(int) { return false; } };
template <bool PF, int NE_> struct O44 : OBase {
  static constexpr int TP = 4, TQ = 4, NPA = 4, NP = 16, NPEND = 8, NRO = 8, NE = NE_;
  static constexpr signed char pi[16] = {2, 3, 3, 2, 2, 3, 3, 2, 0, 1, 1, 0, 0, 1, 1, 0}, pj[16] = {0, 0, 1, 1, 2, 2, 3, 3, 0, 0, 1, 1, 2, 2, 3, 3};
  static constexpr signed char ro[8] = {0, PF ? 1 : 16, PF ? 16 : 1, 17, 18, 19, 2, 3};
};
template <bool PF, int NE_> struct O42 : OBase {
  static constexpr int TP = 4, TQ = 2, NPA = 4, NP = 8, NPEND = 4, NRO = 6, NE = NE_;
  static constexpr signed char pi[8] = {2, 3, 3, 2, 0, 1, 1, 0}, pj[8] = {0, 0, 1, 1, 0, 0, 1, 1};
  static constexpr signed char ro[6] = {0, PF ? 1 : 16, PF ? 16 : 1, 17, 3, 2};
};
template <int TH> struct OQ {
  static constexpr int TP = 6, TQ = 5, NPA = 2, NP = 26, NPEND = 13, NRO = 11, NE = QNE;
  static constexpr signed char pi[26] = {3, 4, 5, 5, 4, 3, 3, 4, 5, 5, TH ? 5 : 3, 4, TH ? 3 : 4, 0, 1, 2, 2, 1, 0, 0, 1, 2, 2, TH ? 2 : 0, 1, TH ? 0 : 1};
  static constexpr signed char pj[26] = {0, 0, 0, 1, 1, 1, 2, 2, 2, TH ? 4 : 3, 3, 3, TH ? 3 : 4, 0, 0, 0, 1, 1, 1, 2, 2, 2, TH ? 4 : 3, 3, 3, TH ? 3 : 4};
  static constexpr signed char ro[11] = {0, 1, 16, 2, 17, 18, TH ? 20 : 19, TH ? 19 : 20, 3, 4, 5};
  static constexpr int pidx(int i) { return i / 3; }
  static constexpr int poff(int i) { return i % 3 * 8192; }
  static constexpr int qoff(int j) { return j == 4 ? 0 : (1 + 4 * TH + j) * 2048; }
  static constexpr bool swp(int i) { return i % 3 == 2; }
};
template <bool PKM, bool QKM, int PB, int QB, int XB, int NS, int NT, bool WISP, int BASE, int ROT, int WLD, int WSUB, int ASTEP, int RLX = 0, bool QF = false>
struct Gemm {
  static constexpr int SB = PB + QB + XB, CNT = (PB + QB) / 4096 + (XB ? 1 : 0);
  static constexpr int PO = QF ? QB : 0, QO = QF ? 0 : PB, WB = WISP ? PB : QB, AB = WISP ? QB : PB, WOFF = WISP ? PO : QO, AOFF = WISP ? QO + XB : PO;
  static constexpr int XL = XB / 64, H = NS - 1 - RLX, CA = AB / 4096 + (XB ? 1 : 0), WSTEP = 64 * WLD * 2;
  static_assert(H * CNT <= 63 && BASE + NS * SB <= 163840, "");
  struct Loader {
    static constexpr int NW = WB / 4096, NA = AB / 4096;
    const char* w; const char* ab; unsigned wv[NW], av[NA], xv, lwb, lab, lx; int rot, islot, t; bool xl;
    DEVI Loader(const void* w_, const void* ab_, int lw, int lane, int rot_ = 0)
        : w((const char*)w_ - 3072), ab((const char*)ab_ - 3072), lwb(BASE + WOFF + lw * (NW == 6 ? 2048 : WB / 4)), lab(BASE + AOFF + lw * (AB / 4)), lx(BASE + PB + lw * (XL * 16)),
          rot(rot_), islot((H + ROT) % NS), t(0), xl(lane < XL) {
      const unsigned w0 = (lane >> 3) * (WLD * 2), f0 = (lane & 7) ^ ((lane >> 3) & 2);
#pragma unroll
      for (int i = 0; i < NW; ++i) {
        const unsigned p = NW == 6 ? (i >> 1) * 8 + lw * 2 + (i & 1) : lw * NW + i;
        wv[i] = w0 + ((f0 ^ ((i & 1) << 2)) << 4) + (NW == 6 ? (i >> 1) * WSUB + (lw * 2 + (i & 1)) * (8 * WLD * 2) - (i & 1) * 1024 : (p >> 3) * WSUB + (p & 7) * (8 * WLD * 2) - i * 1024) + 3072;
        asm volatile("" : "+v"(wv[i]));
      }
      asm volatile("" : "+s"(w), "+s"(lwb));
      if (XB) asm volatile("" : "+s"(rot));
      else seta(0, 0, lw, lane);
    }
    DEVI void seta(unsigned a0, unsigned xv_, int lw, int lane) {
      xv = xv_ + 3072;
#pragma unroll
      for (int i = 0; i < NA; ++i) { const unsigned p = lw * NA + i; av[i] = (XB ? a0 + src_kc(p * 64 + lane, D) : p * 2048 + ph(lane * 16)) + 3072 - i * 1024; asm volatile("" : "+v"(av[i])); }
      asm volatile("" : "+s"(ab), "+s"(lab));
      if (XB) { asm volatile("" : "+v"(xv)); asm volatile("" : "+s"(lx)); }
    }
    DEVI int kord(int n) const { if constexpr (XB != 0) return (n + rot) & (NT - 1); else return n; }
    template <int SO> DEVI void issue_w(const char* p, unsigned so = 0) const {
      if constexpr (NW == 6) { dma_grp<2, SO>(p, wv, lwb + so); dma_grp<2, SO + 8192>(p, wv + 2, lwb + so); dma_grp<2, SO + 16384>(p, wv + 4, lwb + so); }
      else dma_grp<NW, SO>(p, wv, lwb + so);
    }
    template <int SO> DEVI void issue_a(const char* p, unsigned so = 0) const {
      if (XB) { if (xl) dma_grp<1, SO>(p, &xv, lx + so); }
      dma_grp<NA, SO>(p, av, lab + so);
    }
    template <int S0, bool W, bool A> DEVI void pre() const {
      if constexpr (S0 < H) {
        if (W) issue_w<((S0 + ROT) % NS) * SB>(w + (kord(0) + S0) * WSTEP);
        if (A) issue_a<((S0 + ROT) % NS) * SB>(ab + (kord(0) + S0) * ASTEP);
        pre<S0 + 1, W, A>();
      }
    }
    template <int T> DEVI void pre_w1() const { issue_w<((T + ROT) % NS) * SB>(w + (kord(0) + T) * WSTEP); sched0(); }
    DEVI void pre_w() const { pre<0, true, false>(); sched0(); }
    template <int T> DEVI void peel() const {
      if constexpr (T < H - 1) {
        wait_vm<(H - 1 - T) * CA + T * CNT>();
        bar();
        issue_w<((T + H + ROT) % NS) * SB>(w + (T + H) * WSTEP); issue_a<((T + H + ROT) % NS) * SB>(ab + (T + H) * ASTEP);
        peel<T + 1>();
      }
    }
    DEVI void start() {
      sched0();
      bar();
      pre<0, false, true>();
      sched0();
      if constexpr (H == 2) wait_vm<CA>();
      else { peel<0>(); islot = (2 * H - 1 + ROT) % NS; t = H - 1; }
    }
    template <class HK> DEVI void run(int tend, HK side) {
#pragma unroll 1
      for (; t < tend; ++t) {
        const char* wk = w + (t + H) * WSTEP; const char* ak = ab + (t + H) * ASTEP;
        unsigned lw_ = lwb + islot * SB, la_ = lab + islot * SB;
        islot = islot + 1 == NS ? 0 : islot + 1;
        asm volatile("" : "+s"(wk), "+s"(ak), "+s"(lw_), "+s"(la_));
        wait_vm<(H - 1) * CNT>();
        bar();
        dma_grp<NW, 0>(wk, wv, lw_);
        side(t);
        dma_grp<NA, 0>(ak, av, la_);
        sched0();
      }
    }
    template <int T, int X, class A, class B, class C> DEVI void step(A pre_, B side, C post) const {
      const int k = kord(T + H);
      const char* wk = w + k * WSTEP; const char* ak = ab + k * ASTEP;
      asm volatile("" : "+s"(wk), "+s"(ak));
      wait_vm<(H - 1) * CNT + X>();
      bar();
      pre_(BASE + ((T + ROT) % NS) * SB);
      issue_w<((T + H + ROT) % NS) * SB>(wk);
      sched0();
      side();
      sched0();
      issue_a<((T + H + ROT) % NS) * SB>(ak);
      post();
      sched0();
    }
    template <int I> DEVI void tail() const { if constexpr (I >= 0) { sched0(); wait_vm<I * CNT>(); bar(); tail<I - 1>(); } }
  };
  static constexpr int NWIN = (NS * SB + 65535) / 65536;
  template <class O> struct Comp {
    static constexpr int TP = O::TP, TQ = O::TQ, NM = 2 * O::NP, NPA = PKM ? O::NPA : 2, NQA = QKM ? TQ : 2;
    static constexpr Plan PL = make_plan<O, PKM, QKM>();
    unsigned prd[NWIN][NPA], qrd[NWIN][NQA];
    bf16x8 fp[TP][2], fq[TQ][2]; bf16x4 lo;
    DEVI void setwin() {
#pragma unroll
      for (int w = 1; w < NWIN; ++w) {
#pragma unroll
        for (int i = 0; i < NPA; ++i) { prd[w][i] = prd[0][i] + w * 65536; asm volatile("" : "+v"(prd[w][i])); }
#pragma unroll
        for (int i = 0; i < NQA; ++i) { qrd[w][i] = qrd[0][i] + w * 65536; asm volatile("" : "+v"(qrd[w][i])); }
      }
#pragma unroll
      for (int i = 0; i < NPA; ++i) asm volatile("" : "+v"(prd[0][i]));
#pragma unroll
      for (int i = 0; i < NQA; ++i) asm volatile("" : "+v"(qrd[0][i]));
    }
    DEVI void setup(int wp, int wq, int lane) {
      const unsigned b = BASE;
      if (PKM) {
#pragma unroll
        for (int i = 0; i < TP; ++i) prd[0][i] = b + rd_km(lane, wp * TP + i);
      } else { prd[0][0] = b + rd_kc(lane, 0) + wp * TP * 2048; prd[0][1] = b + rd_kc(lane, 1) + wp * TP * 2048; }
      if (QKM) {
#pragma unroll
        for (int j = 0; j < TQ; ++j) qrd[0][j] = b + rd_km(lane, wq * TQ + j);
      } else { qrd[0][0] = b + rd_kc(lane, 0) + wq * TQ * 2048; qrd[0][1] = b + rd_kc(lane, 1) + wq * TQ * 2048; }
    }
    template <int C, int N> DEVI static unsigned wa(const unsigned (&b)[NWIN][N], int i) { return b[C >> 16][i] + (C & 0xffff); }
    template <int SLOT, int R> DEVI void rd() {
      constexpr int q = PL.q[R], i = PL.idx[R], k = PL.k[R], h = PL.h[R];
      bf16x8& f = q ? fq[q ? i : 0][k] : fp[q ? 0 : i][k];
      if constexpr (q ? QKM : PKM) {
        unsigned a;
        if constexpr (q) a = wa<SLOT * SB + QO + k * 4096 + h * 512, NQA>(qrd, i); else a = wa<SLOT * SB + PO + O::poff(i) + k * 4096 + h * 512, NPA>(prd, O::pidx(i));
        const bf16x4 x = lds_tr4(a);
        if constexpr (h) f = __builtin_shufflevector(lo, x, 0, 1, 2, 3, 4, 5, 6, 7); else lo = x;
      } else if constexpr (q) f = lds_b128(wa<SLOT * SB + QO + O::qoff(i), 2>(qrd, k));
      else f = lds_b128(wa<SLOT * SB + PO + i * 2048, 2>(prd, k));
    }
    template <int SLOT, int M, int R> DEVI void rds() { if constexpr (R < PL.n) { if constexpr (PL.g[R] == M) rd<SLOT, R>(); if constexpr (PL.g[R] <= M) rds<SLOT, M, R + 1>(); } }
    template <int M, bool IP = false> DEVI void mm(f32x4 (&acc)[TP][TQ]) {
      constexpr int i = O::pi[M >> 1], j = O::pj[M >> 1], k = ((M >> 1) ^ M) & 1;
      if constexpr (IP) { if constexpr (O::swp(i)) mfma_ip(acc[i][j], fq[j][k], fp[i][k]); else mfma_ip(acc[i][j], fp[i][k], fq[j][k]); }
      else if constexpr (O::swp(i)) acc[i][j] = __builtin_amdgcn_mfma_f32_16x16x32_bf16(fq[j][k], fp[i][k], acc[i][j], 0, 0, 0);
      else acc[i][j] = __builtin_amdgcn_mfma_f32_16x16x32_bf16(fp[i][k], fq[j][k], acc[i][j], 0, 0, 0);
    }
    template <int A, int B> DEVI void pend(f32x4 (&acc)[TP][TQ]) { if constexpr (A < B) { mm<2 * A>(acc); mm<2 * A + 1>(acc); pend<A + 1, B>(acc); } }
    template <int SLOT, bool FIRST, int M> DEVI void body(f32x4 (&acc)[TP][TQ]) {
      if constexpr (M < NM) {
        if constexpr (!FIRST || M >= 2 * O::NPEND) { mm<M, true>(acc); sched0(); }
        rds<SLOT, M, 0>();
        sched0();
        body<SLOT, FIRST, M + 1>(acc);
      }
    }
    template <int SLOT, bool FIRST> DEVI void step(f32x4 (&acc)[TP][TQ]) { if (!RLX) wait_lgkm0(); bar(); sched0(); body<SLOT, FIRST, 0>(acc); }
    template <int I, int N, int T0, class MID> DEVI void steps(f32x4 (&acc)[TP][TQ], MID mid) {
      if constexpr (I < N) { mid(I); step<(I + T0 + ROT) % NS, false>(acc); steps<I + 1, N, T0>(acc, mid); }
    }
    DEVI void prep(f32x4 (&acc)[TP][TQ]) { setwin(); pin(acc); }
    DEVI void pin(f32x4 (&acc)[TP][TQ]) {
#pragma unroll
      for (int p = 0; p < O::NP; ++p) asm volatile("" : "+v"(acc[O::pi[p]][O::pj[p]]));
    }
    template <bool CONT, class MID> DEVI void run_steps(f32x4 (&acc)[TP][TQ], MID mid CSP) {
      constexpr int T0 = CONT ? 0 : 1;
      if constexpr (T0) { mid((NT - T0) / NS, -1); step<ROT % NS, true>(acc); }
#pragma unroll 1
      for (int itn = (NT - T0) / NS; itn > 0; --itn) {
        steps<0, NS, T0>(acc, [&](int i) { mid(itn, i); });
      }
      steps<0, (NT - T0) % NS, T0>(acc, [](int) {});
      wait_lgkm0();
      mfma_gap();
    }
  };
};

DEVI f32x4 gelu_arg(f32x4 u) { return u * (-2.302208198f + -0.1029432397f * (u * u)); }
DEVI f32x4 gelu_tanh(f32x4 u) {
  f32x4 a = gelu_arg(u);
#pragma unroll
  for (int e = 0; e < 4; ++e) a[e] = __builtin_amdgcn_exp2f(a[e]);
  a += 1.f;
#pragma unroll
  for (int e = 0; e < 4; ++e) a[e] = __builtin_amdgcn_rcpf(a[e]);
  return u * a;
}

template <int tilesP, int tilesQ, int gp, int gq> DEVI void tile_map(unsigned bid, int& tp, int& tq) {
  const unsigned xcd = bid & 7, idx = bid >> 3;
  constexpr unsigned gP = tilesP / gp, gQ = tilesQ / gq;
  static_assert(gP * gQ == 8, "");
  tp = (xcd % gP) * gp + idx % gp;
  tq = (xcd / gP) * gq + idx / gp;
}

template <int V> struct IC { static constexpr int value = V; };
template <int A, int B, class FN> DEVI void for_c(FN f) { if constexpr (A < B) { f(IC<A>{}); for_c<A + 1, B>(f); } }
DEVI float sel(bool c, float a, float b) { return c ? a : b; }
DEVI u32x2 pk4(const f32x4& v) { return (u32x2){pk2(v[0], v[1]), pk2(v[2], v[3])}; }
DEVI u32x4 cat4(u32x2 a, u32x2 b) { return (u32x4){a.x, a.y, b.x, b.y}; }
DEVI bf16x8 cat(u32x2 a, u32x2 b) { return __builtin_bit_cast(bf16x8, cat4(a, b)); }
DEVI f32x4 mfma(const bf16x8& a, const bf16x8& b, const f32x4& c) { return __builtin_amdgcn_mfma_f32_16x16x32_bf16(a, b, c, 0, 0, 0); }
DEVI u32x4 pack_pair(const f32x4& a, const f32x4& b) {
  const unsigned ax = pk2(a[0], a[1]), ay = pk2(a[2], a[3]), bx = pk2(b[0], b[1]), by = pk2(b[2], b[3]);
  auto rx = __builtin_amdgcn_permlane16_swap(ax, bx, false, false);
  auto ry = __builtin_amdgcn_permlane16_swap(ay, by, false, false);
  const unsigned x0 = rx[0], x1 = rx[1], y0 = ry[0], y1 = ry[1];
  return (u32x4){x0, y0, x1, y1};
}
DEVI int pair_chunk(int fq) { return ((fq & 1) << 1) | (fq >> 1); }
DEVI void sel_frags(bool paired, int fr, int fq, bf16x8 (&s)[2]) {
  const unsigned one = fr & 1 ? 0x3F800000u : 0x3F80u;
#pragma unroll
  for (int i = 0; i < 2; ++i) {
    const bool on = paired ? (fq & 1) == i && (fr >> 3) == (fq >> 1) : (fr >> 2) == fq;
    const int d = paired ? (fr & 7) >> 1 : i * 2 + ((fr >> 1) & 1);
    u32x4 v;
#pragma unroll
    for (int e = 0; e < 4; ++e) v[e] = on && d == e ? one : 0u;
    s[i] = __builtin_bit_cast(bf16x8, v);
  }
}
template <int TQ> DEVI void tile_add(f32x4 (&acc)[4][TQ], const bf16x8 (&s)[2], unsigned a) {
  bf16x8 t[4];
#pragma unroll
  for (int p = 0; p < 4; ++p) t[p] = lds_b128(a + p * 1024);
#pragma unroll
  for (int p = 0; p < 4; ++p)
#pragma unroll
    for (int i = 0; i < 2; ++i) acc[(p & 1) * 2 + i][p >> 1] = mfma(s[i], t[p], acc[(p & 1) * 2 + i][p >> 1]);
}
template <int C> DEVI float dppf(float v) { return __int_as_float(__builtin_amdgcn_update_dpp(0, __float_as_int(v), C, 0xf, 0xf, false)); }
DEVI float red8(float v) { v += dppf<0xB1>(v); v += dppf<0x4E>(v); v += dppf<0x141>(v); return v; }
DEVI float red4(float v) { v += dppf<0xB1>(v); v += dppf<0x4E>(v); return v; }

constexpr unsigned SENT = 0xBF800000u;
constexpr int SPIN_MAX = 1 << 21;

constexpr unsigned MB = 1u << 20;
constexpr unsigned WS_O = 0, WS_H = 8 * MB, WS_U = 16 * MB, WS_X2 = 32 * MB, WS_XP = 40 * MB  ;
constexpr unsigned WS_ST = 120 * MB, ST_CS = 2 * S * 64, WS_XCH = 122 * MB  , WS_EPOCH = 123 * MB, WS_ABORT = 123 * MB + 256  ;
DEVI unsigned tag2(unsigned t) { return t * 0x9E3779B1u ^ 0x5BD1E995u; }
constexpr int MEDGE = 256 * 64;
constexpr unsigned WS_MAIL = 124 * MB;

#define NL 2
#define QBR 4
#define FLS 3
#define NPL 6
#define SPON 1
#define PC0 0
#define PGAP 3
#define WSP 3
#define SWW 0
#define GSH 1
#define GSP 3
#define SSC 1
#define RB 1
#define SI 2
#define LPR 1
#define CPR 0
#define GLW 4
#define GL1 4
#define SW0 0
#define SW1 6
#define XE 0
#define E2 1
#define LB 3
#define TLQ 0
#define TLW 1
#define SPT 0
#define XD 1
#define XS 4
#define JA 22
#define PS 3
#define TBA 3
#define TC1 12
#define NW1 9
#define EW 12
#define ET 8
#define SPW 1
#define PA_1 3
#define PA_2 3
constexpr int L_X = 0, L_TAB = 16384, L_CS = L_TAB + 1152, L_TAG = L_CS + 768, L_BIAS = L_TAG + 128, L_SCR = L_BIAS + 512  , L_QKV = 20480;
static_assert(L_SCR + 1536 <= L_QKV, "");
typedef Gemm<true, false, 24576, 16384, 2048, 3, D / 64, true, 32768, 0, 3 * D, 2 * D, 128> GQ;
typedef Gemm<true, false, 16384, 8192, 0, 5, D / 64, true, 40960, 2, D, 128, 16384, 1, true> GW;
typedef Gemm<true, true, 16384, 16384, 0, 4, S / 64, true, 32768, 1, F, 128, 32768, 0, true> G1;
typedef Gemm<false, true, 16384, 16384, 0, 3, JA, false, 65536, (3 - JA % 3) % 3, S, 1024, 32768> G2A;
typedef Gemm<false, true, 16384, 8192, 0, 4, F / 64 - 2 * JA, false, 65536, 0, S, 128, 32768> G2;

DEVI unsigned get_tag() { return __builtin_amdgcn_readfirstlane(*(volatile LAS unsigned*)(uintptr_t)L_TAG) + 1u; }
DEVI bool spin_fail(int spin, char* ws) {
  if ((spin & 255) != 255) return false;
  gu32* ab = (gu32*)(ws + WS_ABORT);
  const unsigned tag = get_tag();
  if (spin >= SPIN_MAX) { __hip_atomic_store(ab, tag, RLX_AGENT); return true; }
  return __builtin_amdgcn_readfirstlane(__hip_atomic_load(ab, RLX_AGENT)) == tag;
}
DEVI void sync_post(char* ws, int edge, int post, int wid) {
  unsigned o = post >= 0 && wid == 0 ? (post >> 6) * 256u : MB, bit = 1u << (post & 31);
  const rsrc_t r = mkrsrc(ws + WS_MAIL + edge * (MEDGE * 4), MEDGE * 4);
  asm volatile("s_waitcnt vmcnt(0) lgkmcnt(0)" : "+v"(o), "+v"(bit) : "s"(r) : "memory");
  sched0();
  bar();
  ST(edge + 1, 6);
  if (wid == 0) asm volatile("buffer_atomic_or %0, %1, %2, 0 offen sc1" :: "v"(bit), "v"(o), "s"(r) : "memory");
  sched0();
}
#define POLL1(r) "s_waitcnt vmcnt(2)\n\tv_readfirstlane_b32 %[seen], " r "\n\tglobal_atomic_or " r ", %[z], %[z], %[p] sc0 sc1\n\t" \
  "s_andn2_b32 %[t], %[first], %[seen]\n\ts_cbranch_scc1 3f\n\ts_cmp_eq_u32 %[first], 0\n\ts_cbranch_scc1 4f\n\ts_barrier\n\ts_mov_b32 %[first], 0\n" \
  "4:\n\ts_andn2_b32 %[t], %[second], %[seen]\n\ts_cbranch_scc0 2f\n3:\n\t"
struct Gate {
  const char* p; unsigned miss, due, a, b, c;
  DEVI Gate() : p(nullptr), miss(0), due(0) { asm volatile("v_mov_b32 %0, 0\n\tv_mov_b32 %1, 0\n\tv_mov_b32 %2, 0" : "=v"(a), "=v"(b), "=v"(c)); }
  DEVI void ask() const { asm volatile("s_mov_b64 exec, 1\n\tglobal_atomic_or %0, %1, %1, %2 sc0 sc1\n\ts_mov_b64 exec, -1" :: "v"(a), "v"(0u), "s"(p) : "memory"); }
  template <int DELAY> DEVI void wait(char* ws, int edge, int line, unsigned all, unsigned first, unsigned second, int wid) {
    sched0();
    p = ws + WS_MAIL + (edge * MEDGE + line * 64) * 4;
    second &= all;
    if (wid == 3) {
      due = 1;
      if (DELAY) __builtin_amdgcn_s_sleep(DELAY);
      int spin = 255;
#pragma unroll 1
      for (;; spin += 256) {
        asm volatile("" : "+s"(spin));
        unsigned seen, t, n;
        asm volatile("s_mov_b64 exec, 1\n\ts_waitcnt vmcnt(0)\n\tglobal_atomic_or %[a], %[z], %[z], %[p] sc0 sc1\n\ts_sleep %[gap]\n\t"
                     "global_atomic_or %[b], %[z], %[z], %[p] sc0 sc1\n\ts_sleep %[gap]\n\tglobal_atomic_or %[c], %[z], %[z], %[p] sc0 sc1\n\ts_movk_i32 %[n], 128\n"
                     "1:\n\t" POLL1("%[a]") POLL1("%[b]") POLL1("%[c]") "s_sub_u32 %[n], %[n], 1\n\ts_cbranch_scc0 1b\n"
                     "2:\n\ts_mov_b64 exec, -1 ; raw polls %[a] %[b] %[c]"
                     : [seen] "=&s"(seen), [t] "=&s"(t), [n] "=&s"(n), [first] "+&s"(first)
                     : [p] "s"(p), [second] "s"(second), [gap] "i"(PGAP), [a] "v"(a), [b] "v"(b), [c] "v"(c), [z] "v"(0u) : "memory", "scc");
        miss = ~seen & all;
        if (__builtin_expect(!(miss & second) && !first, 1)) break;
        if (spin_fail(spin, ws)) { if (first) bar(); miss = 0; break; }
      }
    } else bar();
    ST(edge + 1, 4);
    ST(edge + 2, 0);
  }
  int ev;
  DEVI void arm(int ni, int cw) { ev = miss ? ni : cw >= 0 ? 2 : 0; asm volatile("" : "+s"(ev), "+s"(miss), "+s"(due)); }
  DEVI void head(int it, int cw, int lane, int wid) {
    if (__builtin_expect(it == ev, 0)) {
      asm volatile("" : "+v"(lane)); asm volatile("" : "+s"(cw), "+s"(wid));
      if (miss) ask();
      if (it == 2) code_warm(cw, lane, L_SCR + wid * 256);
      ev = cw >= 0 && it > 2 ? 2 : 0;
      asm volatile("" : "+s"(ev));
    }
  }
  DEVI void late(char* ws) {
    if (__builtin_expect(due != 0, 0)) {
      asm volatile("" : "+s"(ws));
      asm volatile("s_waitcnt vmcnt(0) ; raw settled %0 %1 %2" :: "v"(a), "v"(b), "v"(c) : "memory");
      for (int spin = 0; miss; ++spin) {
        unsigned seen;
        asm volatile("v_readfirstlane_b32 %0, %1" : "=s"(seen) : "v"(a));
        miss &= ~seen;
        if (!miss || spin_fail(spin, ws)) break;
        ask();
        wait_vm<0>();
      }
      miss = due = 0;
      asm volatile("" : "+s"(miss), "+s"(due));
    }
  }
};
DEVI unsigned halo_off(int s0, int lw, int lane) {
  const int c = lw * 32 + (lane & 31), row = c >> 3, ch = (c & 7) ^ ((row >> 1) & 7), tok = s0 - 16 + row;
  return (unsigned)((tok < 0 ? 0 : tok) * D * 2 + ch * 16);
}
DEVI int qkv_rot(int bid) { return (bid & 3) * 4 + (bid & 4) * 2; }
DEVI unsigned x_off(int tp, int tq, int w, int fr, int fq) { return (unsigned)(((tq * 64 + (w >> 1) * 32 + fr) * D + tp * 128 + (w & 1) * 64 + pair_chunk(fq) * 8) * 2); }

DEVI void pfb_q(const u16* w, int bid, int j, int n) {
  const int r = slow_res(w);
  if ((r >> 2) == (bid & 1)) {
    const unsigned o = (unsigned)(((bid & 2) * 4 + r) * 128 + (j >> 1) * (3 * D * 2) + (j & 1) * 64 + ((n + qkv_rot(bid)) & 15) * GQ::WSTEP);
    spf1<0>(w, o); spf1<2048>(w, o); spf1<4096>(w, o);
  }
}
constexpr int PFR = NPL * 128 * 1024, PF_END = TBA * 256 + NW1 * PFR / (F * 2) / 8 * 8;
static_assert(TBA * 256 * F * 2 + NW1 * PFR <= S * F * 2 && 3 * PFR >= D * D * 2, "");
struct Pfc {
  const char* p; const char* q; int pc;
  DEVI Pfc(const u16* wo, const u16* w1, unsigned g) : p((const char*)wo + g * (NPL * 128)), q((const char*)w1 + TBA * 256 * F * 2 + g * (NPL * 128)), pc(PC0) {}
  DEVI void go() {
    unsigned t, u;
    asm volatile("s_getreg_b32 %2, hwreg(HW_REG_IB_STS, 8, 4)\n\ts_cmp_lt_u32 %1, %5\n\ts_cselect_b32 %3, 0, 1\n\ts_or_b32 %2, %2, %3\n\ts_cbranch_scc1 1f\n\t"
                 "s_load_dwordx2 s[100:101], %0, 60\n\ts_load_dwordx2 s[100:101], %0, 188\n\ts_load_dwordx2 s[100:101], %0, 316\n\t"
                 "s_load_dwordx2 s[100:101], %0, 444\n\ts_load_dwordx2 s[100:101], %0, 572\n\ts_load_dwordx2 s[100:101], %0, 700\n\t"
                 ".if %8 == 7\n\ts_load_dwordx2 s[100:101], %0, 828\n\t.endif\n\t"
                 "s_mov_b32 %2, %7\n\ts_cmp_lt_u32 %1, 2\n\ts_cselect_b32 %2, %6, %2\n\ts_mov_b64 s[40:41], %0\n\ts_add_u32 s40, s40, %2\n\ts_addc_u32 s41, s41, 0\n\t"
                 "s_cmp_eq_u32 %1, 2\n\ts_cselect_b64 %0, %4, s[40:41]\n\ts_add_u32 %1, %1, 1\n1:"
                 : "+s"(p), "+s"(pc), "=&s"(t), "=&s"(u) : "s"(q), "i"(3 + NW1), "i"((D * D * 2 - PFR) / 2), "i"(PFR), "i"(NPL) : "s40", "s41", "s100", "s101", "scc");
  }
};
DEVI void spare_touch(const u16* w1, const u16* w2, int bid, int wid, int lane) {
  if (!SPON) return;
  const int j = ((bid >> 3) - 28) * 8 + wid, fr = lane & 15, fq = lane >> 4;
  const unsigned scr = L_QKV + wid * 256;
  u64 due = SPT ? __builtin_amdgcn_s_memrealtime() + (j & 7) * (SPT / 8) : 0;
  auto pace = [&]() { wait_vm<SPW>(); due += SPT; if (SPT) { while (__builtin_amdgcn_s_memrealtime() < due) __builtin_amdgcn_s_sleep(2); } };
#pragma unroll 1
  for (int r = PF_END + (j * 2 + ((bid >> 2) & 1)) * 4; r < S; r += 256) { if (fr < 14) touch4(w1 + (size_t)r * F + (bid & 3) * 896, fq * (F * 2) + fr * 128, scr); pace(); }
  if (SPON & 2) return;
#pragma unroll 1
  for (int r = (j * 2 + (bid & 1)) * 4; r < JA * 64; r += 256) { touch4(w2 + (size_t)(r + (bid & 2) * JA * 32) * S + (bid & 4) * 256, fq * (S * 2) + fr * 128, scr); pace(); }
#pragma unroll 1
  for (int r = JA * 128 + ((31 - j) * 2 + (bid & 1)) * 8; r < F; r += 512) { touch4(w2 + (size_t)r * S + ((bid & 7) >> 1) * 512, (lane >> 3) * (S * 2) + (lane & 7) * 128, scr); pace(); }
}

template <int TH> DEVI void qkv_attn(char* ws, int l, int h, int lane, int hp, int sb, int cw, int bid, int tp, int tq, const u16* wqkv) {
  asm volatile("; half %1" : "+v"(lane) : "i"(TH));
  const int fr = lane & 15, fq = lane >> 4;
  GQ::Comp<OQ<TH>> g;
#pragma unroll
  for (int c = 0; c < 2; ++c) { g.prd[0][c] = 32768 + rd_km(lane, h * 2 + c); g.qrd[0][c] = 32768 + rd_kc(lane, c); }
  f32x4 acc[6][5];
#pragma unroll
  for (int i = 0; i < 6; ++i)
#pragma unroll
    for (int j = 0; j < 5; ++j) acc[i][j] = (f32x4){0.f, 0.f, 0.f, 0.f};
  const int par = fr & 1, da = fr - 4 * fq - par;
  const bool ca = da >= 0, cb = da >= 2;
  const unsigned tm = L_TAB + (16 + 64 * TH) * 4, tc = L_CS + h * 128;
  unsigned am = tm + fr * 4 - 320, a4 = tm + fq * 16 - 320, aa = tm + 576 + (4 * fq + par - (ca ? 0 : 16)) * 4, ab = tm + 576 + (4 * fq + par + 2 - (cb ? 0 : 16)) * 4;
  unsigned c4 = tc + fq * 16, c1 = tc + 512 + fr * 4, xb = L_QKV + h * 3072 + lane * 16, hm = par ? 0xffff0000u : 0xffffu, m0 = ca ? 0u : hm, m1 = cb ? 0u : hm;
  unsigned oo = ph((unsigned)((((sb * 2 + TH) * 16 + hp) << 13) + fr * 128 + (((h * 4 + pair_chunk(fq)) ^ (fr >> 1)) << 4)));
  asm volatile("" : "+v"(am), "+v"(a4), "+v"(aa), "+v"(ab), "+v"(c4), "+v"(c1), "+v"(xb), "+v"(hm), "+v"(m0), "+v"(m1), "+v"(oo));
  g.prep(acc);
  if (l) {
    const int sbc = (tq >> 1) + fq;
    sync_post(ws, 3, !TH && (lane < 16 || (lane < 32 && (tq & 1) && sbc < 16)) ? (sbc * 16 + fr) * 64 + (tq - 2 * sbc + 1) * 8 + tp : -1, TH * 2 + h);
  }
  Gate gt;
  if (l) gt.wait<SW0>(ws, 3, sb * 16 + hp, sb ? 0xffffffu : 0xffff00u, (sb ? 0x10101u : 0x10100u) << ((qkv_rot(bid) & 15) >> 1), 0x30303u << ((qkv_rot(bid) & 15) >> 1), TH * 2 + h);
  else if (!TH || !h) wait_vm<0>();
  else if ((slow_res(wqkv) >> 2) == (bid & 1)) {
    if (ET) __builtin_amdgcn_s_sleep(ET);
    if ((lane & 3) < 3 && lane < 40) touch4(wqkv, (unsigned)(((qkv_rot(bid) * 64 + 128 + (bid >> 3) * 10 + (lane >> 2)) & 1023) * (3 * D * 2) + (lane & 3) * (D * 2) + ((bid & 2) * 4 + slow_res(wqkv)) * 128), L_SCR + 768);
  }
  g.template run_steps<false>(acc, [&](int it, int i) {
    if (i < 0) gt.arm(it, cw);
        if (i == 0) gt.head(it, cw, lane, TH * 2 + h);
    if (TH && i == 1) gt.late(ws);
  } CSA);
  const rsrc_t ro = mkrsrc(ws + WS_O, S * D * 4);
  MTD;
  MT(0);
  if (l == 0) bar();
  ST(1 + l * 4, 2);
  float mt[5], cv[2], rq[4], ra[4], rb[4]; f32x4 m4[5], cq[2], ck[2];
#pragma unroll
  for (int c = 0; c < 2; ++c) {
    ck[c] = *(const LAS f32x4*)(uintptr_t)(c4 + 256 + c * 64); cv[c] = *(const LAS float*)(uintptr_t)(c1 + c * 64);
    cq[c] = *(const LAS f32x4*)(uintptr_t)(c4 + c * 64);
  }
#pragma unroll
  for (int j = 4; j >= 0; --j) {
    const int o = 320 + (j == 4 ? -64 - 256 * TH : j * 64);
    if (j < 4 || !TH) mt[j] = *(const LAS float*)(uintptr_t)(am + o);
    if (j < 4 || TH) m4[j] = *(const LAS f32x4*)(uintptr_t)(a4 + o);
  }
#pragma unroll
  for (int j = 0; j < 4; ++j) { rq[j] = *(const LAS float*)(uintptr_t)(am + 896 + j * 64); ra[j] = *(const LAS float*)(uintptr_t)(aa + j * 64); rb[j] = *(const LAS float*)(uintptr_t)(ab + j * 64); }
  u32x2 kp[2][5], qp[2][4], vp[2][5];
  auto fk = [&](int j) { kp[0][(j + 1) % 5] = pk4(acc[1][j]); kp[1][(j + 1) % 5] = pk4(acc[4][j] + ck[1] * mt[j]); };
  auto fv = [&](int j) { vp[0][(j + 1) % 5] = pk4(acc[2][j]); vp[1][(j + 1) % 5] = pk4(acc[5][j] + m4[j] * cv[1]); };
  auto fqq = [&](int j) { qp[0][j] = pk4(acc[0][j]); qp[1][j] = pk4(acc[3][j] + cq[1] * mt[j]); };
  g.template pend<10, 13>(acc);
#pragma unroll
  for (int i = 0; i < 6; ++i) { __builtin_amdgcn_sched_group_barrier(0x100, 3, 0); __builtin_amdgcn_sched_group_barrier(0x8, 1, 0); }
  sched0();
  MT(2);
  for_c<0, 20>([&](auto mc) {
    constexpr int m = mc.value, n0 = m * 52 / 20, n1 = (m + 1) * 52 / 20;
    g.template mm<(18 - (m & ~1)) + (m & 1)>(acc);
    sched0();
#pragma unroll
    for (int n = n0; n < n1; ++n) {
      const int t = 12 - (n >> 2), e = n & 3, j = t == 12 ? 4 : t / 3, i = t == 12 ? 1 + TH : t % 3;
      float x = acc[i][j][e];
      asm volatile("v_fmac_f32 %0, %1, %2" : "+v"(x) : "v"(i == 2 ? m4[j][e] : i ? ck[0][e] : cq[0][e]), "v"(i == 2 ? cv[0] : mt[j]));
      acc[i][j][e] = x;
    }
    sched0();
  });
  volatile LAS unsigned* xf = (volatile LAS unsigned*)(uintptr_t)(L_TAG + 16 + (h * 2 + TH) * 4);
  if (TH) { fv(4); *(volatile LAS bf16x8*)(uintptr_t)(xb + 2048) = cat(vp[0][0], vp[1][0]); *xf = l + 1; fk(3); }
  else { fk(3); fv(3); *(volatile LAS bf16x8*)(uintptr_t)xb = cat(kp[0][4], kp[1][4]); *(volatile LAS bf16x8*)(uintptr_t)(xb + 1024) = cat(vp[0][4], vp[1][4]); *xf = l + 1; }
  sched0();
  fqq(3); fk(2); fqq(2); fk(1);
  sched0();
  const f32x4 z = {0.f, 0.f, 0.f, 0.f};
  f32x4 sc[4][2]; float a[4], b[4], m[4];
  auto scores = [&](int j) {
#pragma unroll
    for (int u = 1; u >= 0; --u) sc[j][u] = mfma(cat(kp[0][j + u], kp[1][j + u]), cat(qp[0][j], qp[1][j]), z);
  };
  auto pick = [&](int j) {
    const f32x4 s0 = sc[j][0], s1 = sc[j][1];
    a[j] *= sel(par, sel(ca, s1[1], s0[1]), sel(ca, s1[0], s0[0]));
    b[j] *= sel(par, sel(cb, s1[3], s0[3]), sel(cb, s1[2], s0[2]));
    if (!TH && j == 0) { a[j] = sel(ca || sb, a[j], -1e9f * 1.4426950408889634f); b[j] = sel(cb || sb, b[j], -1e9f * 1.4426950408889634f); }
    m[j] = __builtin_fmaxf(a[j], b[j]);
  };
  auto expo = [&](int j) { a[j] = __builtin_amdgcn_exp2f(a[j] - m[j]); b[j] = __builtin_amdgcn_exp2f(b[j] - m[j]); m[j] = a[j] + b[j]; };
  auto pv = [&](int j) {
    const unsigned pa = pk2(a[j] * ra[j], a[j] * ra[j]), pb = pk2(b[j] * rb[j], b[j] * rb[j]);
    const bf16x8 pf = __builtin_bit_cast(bf16x8, (u32x4){pa & m0, pb & m1, pa & (m0 ^ hm), pb & (m1 ^ hm)});
#pragma unroll
    for (int c = 0; c < 2; ++c) sc[j][c] = mfma(cat(vp[c][j], vp[c][j + 1]), pf, z);
  };
  auto outp = [&](int j) { st16(ro, oo + j * 4096, pack_pair(sc[j][0] * m[j], sc[j][1] * m[j])); };
  unsigned f; u32x4 xv, xk;
  auto xrd = [&]() {
    f = xf[1 - 2 * TH]; xv = *(volatile LAS u32x4*)(uintptr_t)(xb + (TH ? 1024 : 2048));
    if (TH) xk = *(volatile LAS u32x4*)(uintptr_t)xb;
  };
  auto xget = [&]() {
    sched0();
    while (__builtin_expect(__builtin_amdgcn_readfirstlane(f) != l + 1, 0)) xrd();
    vp[0][0] = (u32x2){xv[0], xv[1]}; vp[1][0] = (u32x2){xv[2], xv[3]};
    if (TH) { kp[0][0] = (u32x2){xk[0], xk[1]}; kp[1][0] = (u32x2){xk[2], xk[3]}; }
  };
  MT(3);
  {
    fqq(1); fk(0); fqq(0);
    if (!TH) fk(4);
    xrd();
    sched0();
    if (TH) fv(3);
    fv(2); fv(1); fv(0);
#pragma unroll
    for (int c = 0; c < 2; ++c)
#pragma unroll
      for (int j = 0; j < 4; ++j) asm volatile("" : "+v"(qp[c][j]), "+v"(kp[c][j + 1]), "+v"(vp[c][j + 1]));
    if (!TH) asm volatile("" : "+v"(kp[0][0]), "+v"(kp[1][0]));
    MT(4);
    xget();
    scores(3); scores(2); scores(1); scores(0);
#pragma unroll
    for (int j = 0; j < 4; ++j) { const float r = rq[j] * (0.17677669529663687f * 1.4426950408889634f); a[j] = r * ra[j]; b[j] = r * rb[j]; }
#pragma unroll
    for (int i = 0; i < 8; ++i) { __builtin_amdgcn_sched_group_barrier(0x8, 1, 0); __builtin_amdgcn_sched_group_barrier(0x2, 2, 0); }
    sched0();
    pick(3); pick(2); pick(1); pick(0);
    float u, w;
    swp32(m[0], m[2]); swp32(m[1], m[3]); u = m[0]; w = m[1];
    asm volatile("v_max_f32 %0, %0, %2\n\tv_max_f32 %1, %1, %3\n\ts_nop 1" : "+v"(u), "+v"(w) : "v"(m[2]), "v"(m[3]));
    swp16(u, w); u = vmax(u, w);
    xch16(u, u, w); xch32(u, m[0], m[2]); xch32(w, m[1], m[3]);
    MT(5);
    expo(3); expo(2); expo(1); expo(0);
    pv(3); pv(2); pv(1); pv(0);
    swp32(m[0], m[2]); swp32(m[1], m[3]); u = m[0] + m[2]; w = m[1] + m[3];
    swp16(u, w); u = __builtin_amdgcn_rcpf(u + w);
    xch16(u, u, w); xch32(u, m[0], m[2]); xch32(w, m[1], m[3]);
    MT(6);
    outp(3); outp(2); outp(1); outp(0);
  }
  MT(7);
  MTOUT(10 + TH, TH * 2);
}

extern "C" __global__ __launch_bounds__(512, 2) void mega_kernel(const u16* x0, const u16* wqkv, const u16* wo, const u16* w1, const u16* w2, const u16* b1, const u16* b2, u16* out, char* ws) {
  const int wid0 = __builtin_amdgcn_readfirstlane(threadIdx.x >> 6);
  ST(0, 7);
  STL(0, 6);
  asm volatile("s_getpc_b64 s[40:41]\n\ts_andn2_b32 s40, s40, 63\n\ts_load_dwordx2 s[100:101], s[40:41], %0 offset:124\n\ts_load_dwordx2 s[100:101], s[40:41], %0 offset:252"
               :: "s"(__builtin_amdgcn_readfirstlane(threadIdx.x) << 2) : "s40", "s41", "s100", "s101", "scc");
  sched0();
#define PHASE_VARS \
      int tid = threadIdx.x, bid = blockIdx.x, wid = wid0; \
      asm volatile("" : "+v"(tid)); asm volatile("" : "+s"(bid), "+s"(wid), "+s"(ws)); \
      const int lw = wid - 4, lt = tid - 256, lane = tid & 63, fr = lane & 15, fq = lane >> 4, wp = wid & 1, wq = wid >> 1; \
      const unsigned j = (bid >> 3) * 4 + lw; \
      int hp, sb; tile_map<16, 16, 4, 8>(bid, hp, sb);            \
      int tp, tq; tile_map<8, 32, 4, 8>(bid, tp, tq);             \
      int fp, fq1; tile_map<28, 8, 7, 4>(bid, fp, fq1);           \
      const int cw = bid < 16 ? wid + 4 * (bid >> 3) : -1; \
      (void)cw; (void)j; (void)lt; (void)wp; (void)wq; (void)lw; (void)fr; (void)fq; (void)hp; (void)sb; (void)tp; (void)tq; (void)fp; (void)fq1;
#define LOADER_VARS PHASE_VARS asm volatile("" : "+s"(wqkv), "+s"(wo), "+s"(w1), "+s"(w2));

  if (__builtin_expect(wid0 >= 4, 1)) {
    if (LPR) __builtin_amdgcn_s_setprio(3);
    const u16* xin = x0;
    Pfc pf(wo, w1, blockIdx.x * 4 + wid0 - 4);
#pragma unroll 1
    for (int l = 0; l < NL; ++l) {
      {
        LOADER_VARS
        const int s0 = sb * 128;
        GQ::Loader L(wqkv + hp * 64, xin, lw, lane, qkv_rot(bid));
        if (l == 0) L.pre_w1<0>();
        L.seta((unsigned)(s0 * D * 2), halo_off(s0, lw, lane), lw, lane);
        if (l == 0) { L.issue_a<0>(L.ab + L.kord(0) * 128); if (!SI) L.pre<1, true, true>(); }
        sched0();
        const rsrc_t rs = mkrsrc(ws + WS_ST, MB);
        f32x4 c = {0.f, 0.f, 0.f, 0.f}; f32x2 s1 = {0.f, 0.f}, s2 = {0.f, 0.f}, o = {0.f, 0.f}; float pv = 0.f;
        bf16x8 ones = {0x3F80, 0x3F80, 0x3F80, 0x3F80, 0x3F80, 0x3F80, 0x3F80, 0x3F80};
        const int ct = lw & 1 ? 8 + (sb & 3) : sb & 7; const bool cs = l == 0 && lw >= 2;
        int tpf = (slow_res(wqkv) >> 2) != (bid & 1) ? 0 : lw < 2 ? 10 : 9;
        unsigned ra = GQ::AOFF + ((hp & 3) * 32 + lw * 8) * 128 + lane * 16, rb = rd_km(lane, ct);
        unsigned so = lane & 7 ? MB : ph((unsigned)(((l * S + s0 + (hp & 3) * 32 + lw * 8 + (lane >> 3)) * 4 + (hp >> 2)) * 16));
        unsigned co = fr >= 2 || !cs ? MB : ph((unsigned)(ST_CS + ((hp * 96 + ct * 8 + fq * 2 + fr) * 2 + (sb >> 3)) * 16));
        asm volatile("" : "+v"(ra), "+v"(rb), "+v"(so), "+v"(co), "+v"(c), "+v"(s1), "+v"(s2), "+v"(ones), "+v"(o));
        asm volatile("" : "+s"(tpf));
        if (SI && l == 0) L.pre_w1<1>();
        sched0();
        unsigned to[3], xo = x_off(tp, tq, lw, fr, fq), q2 = ((bid & 2) * 4 + slow_res(wqkv)) * 128 + ((bid >> 3) * 2 + (lw & 1)) * (3 * D * 2);
#pragma unroll
        for (int i = 0; i < 3; ++i) {
          const int r = s0 - 16 + (i == 2 && lw ? 64 : i * 64) + (lt >> 2);
          to[i] = ph((unsigned)(((l * S + (r < 0 ? 0 : r)) * 4 + (lt & 3)) * 16));
        }
        unsigned ta = (lt & 3) < (lw ? 2 : 3) ? L_TAB + ((lt & 3) * 64 + (lt >> 2)) * 4 : L_SCR, ts = ph((unsigned)(ST_CS + (hp * 192 + (lw ? lt - 64 : lt)) * 16));
        asm volatile("" : "+v"(to[0]), "+v"(to[1]), "+v"(to[2]), "+v"(xo), "+v"(ta), "+v"(ts));
        asm volatile("" : "+s"(q2));
        if (SI == 1 && l == 0) L.pre<1, false, true>();
        sched0();
        const GW::Loader W(wo + tp * 128, ws, lw, lane);
        if (SI == 2 && l == 0) L.pre<1, false, true>();
        sched0();
        auto pfq = [&](int n) { const unsigned o = q2 + L.kord(n) * GQ::WSTEP; spf2<0>(wqkv, o); spf2<2048>(wqkv, o); spf2<4096>(wqkv, o); };
        if (l) {
          if (QBR == 3) bar();
          if (!(EW & 8)) L.pre_w();
          if (tpf) { pfq(2 + (lw >> 1)); pfq(4 + (lw >> 1)); }
          if (QBR == 4) bar();
          L.start();
        }
        else STL(0, 2);
        if (LPR == 1) __builtin_amdgcn_s_setprio(0);
        unsigned tag, tg2; u32x4 gs, a[3];
        auto rowst = [&](int i) {
          const float m0 = __uint_as_float(a[i][0]), m = red4(m0) * 0.25f, q = red4(__uint_as_float(a[i][2]) + 256.f * (m0 - m) * (m0 - m));
          o = (f32x2){sel((lt & 3) == i, m, o.x), sel((lt & 3) == i, q, o.y)};
          asm volatile("" : "+v"(o));
        };
        for_c<0, 16 - GQ::H>([&](auto tc) {
          constexpr int t = tc.value;
          u32x4 v; unsigned tv;
          L.step<t, t == 5 || t == 6 ? 1 : t == 9 || t == 10 ? 4 : 0>([&](unsigned ls) {
            if (t < 4) v = *(const LAS u32x4*)(uintptr_t)(ls + ra);
            if (t == 3) tv = *(volatile LAS unsigned*)(uintptr_t)L_TAG;
          }, [&]() {
            if (t < 8 && cs) {
              constexpr int ls = 32768 + t % 3 * GQ::SB;
              bf16x4 x[4];
#pragma unroll
              for (int e = 0; e < 4; ++e) x[e] = lds_tr4(rb + (ls & ~0x7fff) + (ls & 0x7fff) + (e >> 1) * 4096 + (e & 1) * 512);
#pragma unroll
              for (int ks = 0; ks < 2; ++ks) c = mfma(__builtin_shufflevector(x[2 * ks], x[2 * ks + 1], 0, 1, 2, 3, 4, 5, 6, 7), ones, c);
            } else if (t >= 4) {
              if (t >= 10 || t >= tpf) { if (t < TC1) pf.go(); }
              else if (lw < 2) pfq(t + 4);
              else if (t == 8) { pfq(14); pfq(15); }
            }
            if (t < 4) {
              if (t == 0) { pv = bflo(v[0]); asm volatile("" : "+v"(pv)); }
#pragma unroll
              for (int e = 0; e < 4; ++e) {
                if (SSC) {
                  float x = bflo(v[e]), y = bfhi(v[e]), a0 = s1.x, a1 = s1.y, b0 = s2.x, b1 = s2.y;
                  asm volatile("v_sub_f32 %0, %0, %6\n\tv_sub_f32 %1, %1, %6\n\tv_add_f32 %2, %2, %0\n\tv_add_f32 %3, %3, %1\n\tv_fmac_f32 %4, %0, %0\n\tv_fmac_f32 %5, %1, %1"
                               : "+v"(x), "+v"(y), "+v"(a0), "+v"(a1), "+v"(b0), "+v"(b1) : "v"(pv));
                  s1 = (f32x2){a0, a1}; s2 = (f32x2){b0, b1};
                } else { const f32x2 d = (f32x2){bflo(v[e]), bfhi(v[e])} - pv; s1 += d; s2 += d * d; }
              }
              asm volatile("" : "+v"(s1), "+v"(s2));
            }
            if (t == 3) { tag = __builtin_amdgcn_readfirstlane(tv) + 1u; tg2 = tag2(tag); if (tpf == 10) pfq(7); if (l && tpf == 9) pfq(6); }
            if (t == 4) {
              const float d1 = (s1.x + s1.y) * (1.f / 32.f), ml = pv + d1, m = red8(ml) * 0.125f;
              const float q = red8(vmax((s2.x + s2.y) - 32.f * d1 * d1, 0.f) + 32.f * (ml - m) * (ml - m));
              gs = (u32x4){__float_as_uint(m), tag, __float_as_uint(q), tg2};
              asm volatile("" : "+v"(gs));
            }
            if (t == 8) { gs = (u32x4){__float_as_uint(sel(fr, c[2], c[0])), tag, __float_as_uint(sel(fr, c[3], c[1])), tg2}; asm volatile("" : "+v"(gs)); }
            if (t == (RB ? 12 : 11)) {
#pragma unroll
              for (int i = 0; i < 3; ++i) ld_ok(a[i]);
              for (int spin = 0;; ++spin) {
                unsigned bad = 0;
#pragma unroll
                for (int i = 0; i < 3; ++i) bad |= (a[i][1] ^ tag) | (a[i][3] ^ tg2);
                if (__builtin_expect(__all(bad == 0), 1) || spin_fail(spin, ws)) break;
                __builtin_amdgcn_s_sleep(8);
#pragma unroll
                for (int i = 0; i < 3; ++i) ld_again(a[i], rs, to[i]);
              }
              if (!RB) rowst(0);
            }
            if (t == 12 && !RB) { rowst(1); rowst(2); }
            if (t == 13 && RB == 2) rowst(0);
            if (t == 13 && !RB) { LAS float* p = (LAS float*)(uintptr_t)ta; p[0] = -o.x; p[144] = __builtin_amdgcn_rsqf(o.y * (1.f / D) + EPS); }
          }, [&]() {
            if (t == 4) st16(rs, so, gs);
            if (t == 8) {
              st16(rs, co, gs);
              sched0();
#pragma unroll
              for (int i = 0; i < 3; ++i) ld_raw(a[i], rs, to[i]);
            }
          });
        });
        if (l == 0) {
#pragma unroll
          for (int p = 0; p < 4; ++p) dma16((const char*)xin + (p >> 1) * 32768 + (p & 1) * 64, xo, L_X + lw * 4096 + p * 1024);
          ld_raw(gs, rs, ts);
          wait_vm<GQ::CNT + 5>();
        } else wait_vm<GQ::CNT>();
        bar();
        W.pre_w1<0>();
        if (RB) { if (RB == 1) rowst(0); rowst(1); rowst(2); LAS float* p = (LAS float*)(uintptr_t)ta; p[0] = -o.x; p[144] = __builtin_amdgcn_rsqf(o.y * (1.f / D) + EPS); }
        wait_lgkm0();
        if (l == 0) wait_vm<9>(); else wait_vm<4>();
        bar();
        W.pre_w1<1>(); W.pre_w1<2>();
        if (l == 0) {
          wait_vm<12>();
          ld_ok(gs);
          for (int spin = 0;; ++spin) {
            if (__builtin_expect(__all(((gs[1] ^ tag) | (gs[3] ^ tg2)) == 0), 1) || spin_fail(spin, ws)) break;
            __builtin_amdgcn_s_sleep(8);
            ld_again(gs, rs, ts);
          }
          float c0 = __uint_as_float(gs[0]), c1 = __uint_as_float(gs[2]);
          c0 += dppf<0xB1>(c0); c1 += dppf<0xB1>(c1);
          *(LAS float*)(uintptr_t)(L_CS + (lw ? lt - 64 : lt) * 4) = sel(lt & 1, c1, c0);
        }
        if (l == 0 && lw < TBA) { if (fr < 14) touch4(w1 + (size_t)(((bid >> 3) * TBA + lw) * 8 + (bid & 4)) * F + (bid & 3) * 896, fq * (F * 2) + fr * 128, L_SCR + lw * 256); }
        if (l == 0) { wait_lgkm0(); bar(); }
      }
      {
        LOADER_VARS
        GW::Loader L(wo + tp * 128, ws + WS_O + (size_t)tq * 32 * 8192, lw, lane);
        const G1::Loader W(w1 + fp * 128, ws, lw, lane);
        unsigned po = ((bid & 1) * 8 + (slow_res(wo) ^ (j & 1))) * 128 + (j >> 1) * (D * 2);
#pragma unroll
        for (int k = GW::H; k < 10; ++k) spf2(wo, po + k * GW::WSTEP);
        asm volatile("" : "+s"(po));
        if (!(LB & 1)) { sched0(); bar(); } else wait_lgkm0();
#pragma unroll
        for (int k = 10; k < 16; ++k) spf2(wo, po + k * GW::WSTEP);
#pragma unroll
        for (int r = 0; r < TLQ; ++r) { if (pf.pc < 3 + NW1) { wait_lgkm0(); pf.go(); } }
        if (LB & 1) { sched0(); bar(); }
        L.start();
        L.run(16 - GW::H, [&](int) { pf.go(); });
        pf.go();
        if (E2 && bid < 224) {
          wait_vm<2 * GW::CNT>(); bar(); W.pre_w1<1>();
          wait_vm<GW::CNT + 4>(); bar(); W.pre_w1<2>();
          wait_vm<8>(); bar();
          bar(); W.pre_w1<0>();
        } else { L.tail<GW::H - 1>(); bar(); }
      }
      {
        LOADER_VARS
        if (bid < 224) {
          G1::Loader L(w1 + fp * 128, ws + WS_H + (size_t)fq1 * 64 * 16384, lw, lane);
          if (!E2) L.pre_w();
          const G2A::Loader W((const char*)(w2 + (tq & 23) * 64) + (tq & 8 ? JA * G2::WSTEP : 0), ws, lw, lane);
          const int g0 = (bid & 3) * 14, ga = (g0 + ((slow_res(w1) - g0) & 7)) ^ (j & 1), gb = ga + 8 < g0 + 14 ? ga + 8 : ga;
          const unsigned j2 = j < 32 ? 112 + (j & 15) : j;
          unsigned pa = (j >> 1) * (F * 2) + ga * 128, pb = (j >> 1) * (F * 2) + gb * 128, pd = (j2 >> 1) * (F * 2) + (j & 16 ? gb : ga) * 128;
          asm volatile("" : "+s"(pa), "+s"(pb), "+s"(pd));
          auto pfb = [&](int k) { spf2(w1, pa + k * G1::WSTEP); spf2(w1, pb + k * G1::WSTEP); spf2(w1, pd + k * G1::WSTEP); };
#pragma unroll
          for (int k = G1::H; k < G1::H + PA_1; ++k) pfb(k);
#pragma unroll
          for (int r = 0; r < TLW; ++r) { if (pf.pc < 3 + NW1) { wait_lgkm0(); pf.go(); } }
          sched0();
          bar();
          if (!(LB & 2)) pf.go();
          L.start();
          L.run(32 - G1::H, [&](int t) {
            if (pf.pc < 3 + NW1) pf.go();
            else if (t < 32 - G1::H - PA_1) pfb(t + G1::H + PA_1);
          });
          if (EW & 4) {
            wait_vm<2 * G1::CNT>(); bar(); W.pre_w1<1>();
            wait_vm<G1::CNT + 4>(); bar();
            wait_vm<4>(); bar(); W.pre_w1<0>();
          } else L.tail<G1::H - 1>();
          bar();
        } else {
          wait_lgkm0(); pf.go();
          bar();
          if (l == 0) {
            __builtin_amdgcn_s_sleep(24);
            while (pf.pc < 3 + NW1) { wait_lgkm0(); pf.go(); }
            spare_touch(w1, w2, bid, wid, lane);
          }
        }
        pf.pc = 99;
      }
      {
        LOADER_VARS
        constexpr int NB = F / 64 - 2 * JA;
        const char* ub = ws + WS_U + (size_t)tp * 112 * 16384;
        G2A::Loader A((const char*)(w2 + (tq & 23) * 64) + (tq & 8 ? JA * G2::WSTEP : 0), ub + (tq & 8 ? JA * 32768 : 0), lw, lane);
        if (!(EW & 4) || bid >= 224) A.pre_w();
        const GQ::Loader W(wqkv + hp * 64, ws, lw, lane, qkv_rot(bid));
        unsigned pb = ((bid & 4) * 4 + (j & 1) * 8 + slow_res(w2)) * 128 + (j >> 1) * (S * 2) + JA * G2::WSTEP, pa = pb - (bid & 2 ? 0 : JA * G2::WSTEP);
        asm volatile("" : "+s"(pa), "+s"(pb));
        auto side = [&](int n) { n += G2A::H + PA_2; if (n < JA + NB) spf2(w2, (n < JA ? pa : pb) + n * G2::WSTEP); };
#pragma unroll
        for (int k = 0; k < PA_2; ++k) side(k - PA_2);
        G2::Loader L((const char*)(w2 + tq * 64) + 2 * JA * G2::WSTEP, ub + 2 * JA * 32768, lw, lane);
        gu32* xf = (gu32*)(ws + WS_MAIL) + (l * 4 + 2) * MEDGE + (tq * 8 + tp) * 64 + 28 + lw;
        const char* xp = ws + WS_XP + (unsigned)(((bid ^ 2) * 4 + lw) << 14);
        unsigned fl = L_QKV + lw * 256, xv = ph(lane * 16);
        asm volatile("" : "+s"(xf), "+s"(xp), "+s"(fl)); asm volatile("" : "+v"(xv));
        sched0();
        bar();
        A.start();
        A.run(JA - G2A::H, [&](int t) { side(t); });
        auto stage = [&](auto s) { L.issue_w<s.value * G2::SB>(L.w + s.value * G2::WSTEP); L.issue_a<s.value * G2::SB>(L.ab + s.value * 32768); sched0(); };
        wait_vm<G2A::CNT>(); bar(); stage(IC<0>{}); side(JA - 2);
        wait_vm<G2::CNT>(); bar(); stage(IC<1>{}); side(JA - 1);
        wait_vm<G2::CNT>(); bar(); stage(IC<2>{}); stage(IC<3>{}); side(JA);
        L.t = 1; L.islot = 0;
        L.run(NB - G2::H, [&](int t) {
          if (t == NB - G2::H - FLS) asm volatile("s_mov_b32 m0, %2\n\ts_nop 0\n\tglobal_load_lds_dword %0, %1 sc1" :: "v"(0u), "s"(xf), "s"(fl) : "memory");
          side(JA + t);
        });
        auto xdma = [&]() {
#pragma unroll
          for (int i = 0; i < 4; ++i) dma16(xp + i * 2048, xv, L_X + lw * 4096 + i * 1024);
          sched0();
        };
        sched0(); wait_vm<2 * G2::CNT>(); bar();
        if (__builtin_amdgcn_readfirstlane(*(volatile LAS unsigned*)(uintptr_t)fl)) {
          xdma();
          if ((EW & 8) && l == 0) { W.pre_w1<0>(); wait_vm<G2::CNT + 10>(); bar(); W.pre_w1<1>(); wait_vm<16>(); bar(); wait_vm<12>(); }
          else { wait_vm<G2::CNT + 4>(); bar(); wait_vm<4>(); bar(); wait_vm<0>(); }
        } else {
          wait_vm<G2::CNT>(); bar(); wait_vm<0>(); bar();
          for (int spin = 0;; ++spin) {
            if (__builtin_amdgcn_readfirstlane(__hip_atomic_load(xf, RLX_AGENT)) || spin_fail(spin, ws)) break;
            __builtin_amdgcn_s_sleep(2);
          }
          STL(4 + l * 4, 7);
          xdma();
          if ((EW & 8) && l == 0) { W.pre_w(); wait_vm<12>(); } else wait_vm<0>();
        }
        bar();
        xin = (const u16*)(ws + WS_X2);
        if (NL == 1) bar();
      }
    }
    return;
  }

  {
    ST(0, 0);
    PHASE_VARS
    if (cw >= 0) code_warm(cw, lane, L_SCR + wid * 256);
    pfb_q(wqkv, bid, (bid >> 3) * 4 + wid, 0); pfb_q(wqkv, bid, (bid >> 3) * 4 + wid, 1);
    if (wid < 3) {
      if (tid == 0) asm volatile("s_mov_b32 m0, %2\n\ts_nop 0\n\tglobal_load_lds_dword %0, %1 sc1" :: "v"(WS_EPOCH), "s"(ws), "s"(L_TAG) : "memory");
      const rsrc_t rz = mkrsrc(ws + WS_XCH, 3 * MB);
      if (tid < 128) st16(rz, ph((((tid >> 6) * 256 + tq * 8 + tp) * 64 + lane) * 16), (u32x4){0u, SENT, 0u, SENT});
      else {
        const int e = (tid >> 3) & 7, line = e == 0 || e == 7 ? (2 * sb + (e & 1)) * 8 + (hp & 7) : e * 256 + bid;
        st16(rz, WS_MAIL - WS_XCH + line * 256 + (tid & 7) * 16, (u32x4){0u, 0u, 0u, 0u});
      }
      if (wid == 1 && bid < 224) touch4(b1 + fp * 128, lane * 4, L_BIAS);
      if (wid == 2) touch4(b2 + tq * 64, (lane & 31) * 4, L_BIAS + 256);
      if (wid == 0) *(LAS u32x4*)(uintptr_t)(L_TAG + 16) = (u32x4){0u, 0u, 0u, 0u};
    }
    if (CPR) __builtin_amdgcn_s_setprio(CPR);
    ST(0, 4);
    ST(1, 0);
  }
#pragma unroll 1
  for (int l = 0; l < NL; ++l) {
    {
      PHASE_VARS
      if (wid & 2) qkv_attn<1>(ws, l, wp, lane, hp, sb, cw, bid, tp, tq, wqkv);
      else qkv_attn<0>(ws, l, wp, lane, hp, sb, cw, bid, tp, tq, wqkv);
      ST(1 + l * 4, 5);
    }
    {
      PHASE_VARS
      GW::Comp<O42<false, WNE>> g; g.setup(wp, wq, lane);
      g.setwin();
      const rsrc_t rx = mkrsrc(ws + WS_XCH + l * (MB / 2), MB / 2), rh = mkrsrc(ws + WS_H, S * D * 4);
      unsigned xa = L_X + wid * 4096 + lane * 16;
      unsigned xs = fq != fr >> 2 ? MB : ph((unsigned)(((tq * 8 + tp) * 64 + wp * 32 + wq * 16 + fr) * 16)), xl = ph((unsigned)((tq * 512 + fq * 128 + wq * 16 + fr) * 16));
      const unsigned h0 = ph((unsigned)(((tp * 32 + tq) << 14) + wp * 8192 + (wq * 32 + fr) * 128 + ((pair_chunk(fq) ^ (km_f(fr) << 1)) << 4)));
      u32x4 ho = {h0, h0 ^ 64, h0 + 4096, (h0 ^ 64) + 4096};
      bf16x8 ones = {0x3F80, 0x3F80, 0x3F80, 0x3F80, 0x3F80, 0x3F80, 0x3F80, 0x3F80};
      asm volatile("" : "+v"(xa), "+v"(xs), "+v"(xl), "+v"(ho), "+v"(ones));
      Gate gt;
      auto edge = [&]() {
        sync_post(ws, l * 4, lane < 16 ? (sb * 16 + lane) * 64 + hp : -1, wid);
        gt.wait<0>(ws, l * 4, tq * 8 + tp, 0xffffu, (1u << GW::H) - 1, (1u << (GW::H + GLW)) - 1, wid);
        sched0();
      };
      if (WSP == 3) edge();
      f32x4 acc[4][2];
#pragma unroll
      for (int i = 0; i < 4; ++i)
#pragma unroll
        for (int jj = 0; jj < 2; ++jj) acc[i][jj] = (f32x4){0.f, 0.f, 0.f, 0.f};
      { bf16x8 s[2]; sel_frags(true, fr, fq, s); tile_add(acc, s, xa); }
      g.pin(acc);
      if (WSP == 2) edge(); else mfma_gap();
      g.run_steps<false>(acc, [&](int it, int i) {
        if (i < 0) gt.arm(it, cw);
        if (i == 0) gt.head(it, cw, lane, wid);
        if (i == GLW - 1) gt.late(ws);
      } CSA);
      MTD;
      MT(0);
      bar();
      sched0();
      MT(1);
      g.pend<0, 4>(acc);
      ST(2 + l * 4, 2);
      u32x4 pk[2][2]; float mp[2];
      {
        f32x4 sm[2], gm[2];
#pragma unroll
        for (int i = 0; i < 2; ++i)
#pragma unroll
          for (int jj = 0; jj < 2; ++jj) {
            pk[jj][i] = cat4(pk4(acc[2 * i][jj]), pk4(acc[2 * i + 1][jj])); asm volatile("" : "+v"(pk[jj][i]));
            const bf16x8 x = __builtin_bit_cast(bf16x8, pk[jj][i]);
            gm[jj] = mfma(x, x, i ? gm[jj] : (f32x4){0.f, 0.f, 0.f, 0.f}); sm[jj] = mfma(ones, x, i ? sm[jj] : (f32x4){0.f, 0.f, 0.f, 0.f});
          }
        u32x4 st;
#pragma unroll
        for (int jj = 0; jj < 2; ++jj) {
          const f32x4 gv = gm[jj];
          st[2 * jj] = __float_as_uint(sm[jj][0]); st[2 * jj + 1] = __float_as_uint(sel(fr & 2, sel(fr & 1, gv[3], gv[2]), sel(fr & 1, gv[1], gv[0])));
          mp[jj] = sm[jj][0] * (1.f / 64.f);
        }
        st16(rx, xs, st);
      }
      sched0();
      MT(2);
      f32x4 gr[4];
      auto gld = [&]() {
        asm volatile("" ::: "memory");
#pragma unroll
        for (int p = 0; p < 4; ++p) gr[p] = __builtin_bit_cast(f32x4, __builtin_amdgcn_raw_buffer_load_b128(rx, xl + p * 1024, 0, 16));
        sched0();
      };
      if (XE == 1) gld();
#pragma unroll
      for (int p = 0; p < 4; ++p) *(LAS u32x4*)(uintptr_t)(xa + p * 1024) = pk[p >> 1][p & 1];
      asm volatile("" : "+v"(mp[0]), "+v"(mp[1]));
#pragma unroll
      for (int jj = 0; jj < 2; ++jj)
#pragma unroll
        for (int i = 0; i < 2; ++i) {
          const auto rx_ = __builtin_amdgcn_permlane16_swap(pk[jj][i][0], pk[jj][i][2], false, false), ry_ = __builtin_amdgcn_permlane16_swap(pk[jj][i][1], pk[jj][i][3], false, false);
          const unsigned x0 = rx_[0], x1 = rx_[1], y0 = ry_[0], y1 = ry_[1];
          acc[2 * i][jj] = (f32x4){bflo(x0), bfhi(x0), bflo(y0), bfhi(y0)} - mp[jj]; acc[2 * i + 1][jj] = (f32x4){bflo(x1), bfhi(x1), bflo(y1), bfhi(y1)} - mp[jj];
          asm volatile("" : "+v"(acc[2 * i][jj]), "+v"(acc[2 * i + 1][jj]));
          if (XE == 2 + jj * 2 + i) { sched0(); gld(); }
        }
      if (!XE) { if (XD) __builtin_amdgcn_s_sleep(XD); gld(); }
      MT(3);
      for (int spin = 0;; ++spin) {
        const float lo = vmin3(vmin3(gr[0][1], gr[0][3], gr[1][1]), vmin3(gr[1][3], gr[2][1], gr[2][3]), vmin3(gr[3][1], gr[3][3], gr[3][3]));
        if (!__any(lo < 0.f) || spin_fail(spin, ws)) break;
        if (XS) __builtin_amdgcn_s_sleep(XS);
        gld();
      }
      ST(2 + l * 4, 5);
      MT(4);
      {
        const f32x4 t = (gr[0] + gr[1]) + (gr[2] + gr[3]);
        float m[2] = {t[0], t[2]}, q[2] = {t[1], t[3]}, u, w, v, y;
        swp32(m[0], m[1]); swp32(q[0], q[1]);
        xch16(m[0] + m[1], u, w); xch16(q[0] + q[1], v, y);
        const float mean = (u + w) * (1.f / D), var = __builtin_fmaxf((v + y) * (1.f / D) - mean * mean, 0.f), r1 = __builtin_amdgcn_rsqf(var + EPS);
        xch32(mean, m[0], m[1]);
        xch32(r1 * __builtin_amdgcn_rsqf(var * r1 * r1 + EPS), q[0], q[1]);
        ST(2 + l * 4, 3);
        MT(5);
#pragma unroll
        for (int jj = 0; jj < 2; ++jj) {
          const float c = (mp[jj] - m[jj]) * q[jj];
#pragma unroll
          for (int i = 0; i < 2; ++i) st16(rh, ho[jj * 2 + i], cat4(pk4(acc[2 * i][jj] * q[jj] + c), pk4(acc[2 * i + 1][jj] * q[jj] + c)));
        }
      }
      MT(6);
      MT(7);
      MTOUT(12, 0);
    }
    {
      PHASE_VARS
      const int post = lane < 28 ? (tp * 28 + lane) * 64 + tq : -1;
      if (bid >= 224) {
        sync_post(ws, l * 4 + 1, post, wid);
        if (l == 0 && bid == 255 && wid == 0) { const unsigned tag = get_tag(); if (lane == 0) __hip_atomic_store((gu32*)(ws + WS_EPOCH), tag, RLX_AGENT); }
        ST(2 + l * 4, 4);
        ST(3 + l * 4, 0);
        if (l == 0) { __builtin_amdgcn_s_sleep(24); spare_touch(w1, w2, bid, wid, lane); }
      } else {
        G1::Comp<O44<false, 32>> g; g.setup(wp, wq, lane);
        f32x4 acc[4][4];
#pragma unroll
        for (int i = 0; i < 4; ++i) { const u32x2 b = *(const LAS u32x2*)(uintptr_t)(L_BIAS + (wp * 4 + i) * 32 + fq * 8);
#pragma unroll
          for (int jj = 0; jj < 4; ++jj) acc[i][jj] = (f32x4){bflo(b.x), bfhi(b.x), bflo(b.y), bfhi(b.y)}; }
        const rsrc_t ro = mkrsrc(ws + WS_U, D * F * 4);
        const unsigned u0 = ph((unsigned)(((fq1 * 56 + fp * 2 + wp) << 14) + (wq * 64 + fr) * 128 + ((pair_chunk(fq) ^ (fr >> 1)) << 4)));
        u32x4 uo = {u0, u0 + 4096, u0 + 8192, u0 + 12288}, uo2 = uo ^ 64;
        asm volatile("" : "+v"(uo), "+v"(uo2));
        g.prep(acc);
        sync_post(ws, l * 4 + 1, post, wid);
        Gate gt; gt.wait<SW0>(ws, l * 4 + 1, fq1 * 28 + fp, ~0u, (1u << G1::H) - 1, (1u << (G1::H + GL1)) - 1, wid);
        g.run_steps<false>(acc, [&](int it, int i) {
          if (i < 0) gt.arm(it, cw);
        if (i == 0) gt.head(it, cw, lane, wid);
          if (i == GL1 - 1) gt.late(ws);
        } CSA);
        bar();
        sched0();
        ST(3 + l * 4, 2);
        if (GSH) {
          for_c<0, 2>([&](auto jc) {
            constexpr int jj = jc.value;
            f32x4 a[2];
#pragma unroll
            for (int i = 0; i < 2; ++i) { a[i] = gelu_arg(acc[i][jj]); asm volatile("" : "+v"(a[i])); }
            sched0();
            for_c<0, 4>([&](auto ec) { constexpr int e = ec.value; g.mm<jj * 8 + e>(acc); sched0(); a[0][e] = __builtin_amdgcn_exp2f(a[0][e]); a[1][e] = __builtin_amdgcn_exp2f(a[1][e]); asm volatile("" : "+v"(a[0]), "+v"(a[1])); sched0(); });
            a[0] += 1.f; a[1] += 1.f; asm volatile("" : "+v"(a[0]), "+v"(a[1]));
            sched0();
            for_c<0, 4>([&](auto ec) { constexpr int e = ec.value; g.mm<jj * 8 + 4 + e>(acc); sched0(); a[0][e] = __builtin_amdgcn_rcpf(a[0][e]); a[1][e] = __builtin_amdgcn_rcpf(a[1][e]); asm volatile("" : "+v"(a[0]), "+v"(a[1])); sched0(); });
            st16(ro, uo[jj], pack_pair(acc[0][jj] * a[0], acc[1][jj] * a[1]));
          });
        } else g.pend<0, 8>(acc);
#pragma unroll
        for (int n = GSH ? 2 : 0; n < 8; ++n) { const int i = n >> 2, jj = n & 3; st16(ro, (i ? uo2 : uo)[jj], pack_pair(gelu_tanh(acc[2 * i][jj]), gelu_tanh(acc[2 * i + 1][jj]))); }
        ST(3 + l * 4, 3);
      }
    }
    {
      PHASE_VARS
      asm volatile("" : "+s"(out));
      G2A::Comp<O44<true, ANE>> ga; ga.setup(wp, wq, lane);
#pragma unroll
      for (int jj = 0; jj < 4; ++jj) ga.qrd[0][jj] = 65536 + rd_km(lane, wq * 2 + (jj & 1)) + (((jj >> 1) ^ (tq >> 3)) & 1) * 8192;
      G2::Comp<O42<true, BNE>> g; g.setup(wp, wq, lane);
      g.setwin();
      ga.setwin();
      Gate gt;
      auto edge = [&]() {
        sync_post(ws, l * 4 + 2, bid < 224 && lane < 32 ? (lane * 8 + fq1) * 64 + fp : -1, wid);
        gt.wait<0>(ws, l * 4 + 2, tq * 8 + tp, 0xfffffffu, tq & 8 ? 1u << JA / 2 : 1u, tq & 8 ? 7u << JA / 2 : 7u, wid);
        sched0();
      };
      if (GSP == 3) edge();
      f32x4 ac4[4][4], acc[4][2];
#pragma unroll
      for (int jj = 0; jj < 2; ++jj) { const float b = bf2f(*(const LAS u16*)(uintptr_t)(L_BIAS + 256 + (wq * 2 + jj) * 32 + fr * 2));
#pragma unroll
        for (int i = 0; i < 4; ++i) { ac4[i][jj] = (f32x4){b, b, b, b}; ac4[i][jj + 2] = (f32x4){0.f, 0.f, 0.f, 0.f}; } }
      unsigned xa = L_X + wid * 4096 + lane * 16, xo = (unsigned)((bid * 4 + wid) << 14) + ph(lane * 16), oo = x_off(tp, tq, wid, fr, fq), xo2 = xo + 4096, oo2 = oo + 32768;
      unsigned fo = lane ? MB : (((tq ^ 8) * 8 + tp) * 64 + 28 + wid) * 4u, one = 1u;
      bf16x8 s[2]; sel_frags(false, fr, fq, s);
      if (GSP == 4) { asm volatile("" : "+v"(xa), "+v"(xo), "+v"(oo), "+v"(fo), "+v"(s[0]), "+v"(s[1])); edge(); }
      tile_add(ac4, s, xa);
      const rsrc_t rx = mkrsrc(ws + WS_XP, 16 * MB), rf = mkrsrc(ws + WS_MAIL + (l * 4 + 2) * (MEDGE * 4), MEDGE * 4), ro = mkrsrc(l ? (const void*)out : (const void*)(ws + WS_X2), S * D * 2);
      asm volatile("" : "+v"(xa), "+v"(xo), "+v"(oo), "+v"(xo2), "+v"(oo2), "+v"(fo), "+v"(one), "+v"(s[0]), "+v"(s[1]));
      ga.pin(ac4);
      static_assert(JA % 2 == 0 && G2A::H == 2, "");
      if (GSP == 2) edge(); else mfma_gap();
      ga.run_steps<false>(ac4, [&](int it, int i) {
        if (i < 0) gt.arm(it, cw);
        if (i == 0) gt.head(it, cw, lane, wid);
        if (i == 2) gt.late(ws);
      } CSA);
      ga.pend<4, 8>(ac4);
      ST(4 + l * 4, 1);
#pragma unroll
      for (int i = 0; i < 2; ++i)
#pragma unroll
        for (int jj = 0; jj < 2; ++jj) st16(rx, (jj ? xo2 : xo) + i * 2048, cat4(pk4(ac4[2 * i][jj + 2]), pk4(ac4[2 * i + 1][jj + 2])));
#pragma unroll
      for (int i = 0; i < 4; ++i) { acc[i][0] = ac4[i][0]; acc[i][1] = ac4[i][1]; }
#pragma unroll
      for (int k = 0; k < 2; ++k) { g.fp[2][k] = ga.fp[2][k]; g.fp[3][k] = ga.fp[3][k]; g.fq[0][k] = ga.fq[0][k]; g.fq[1][k] = ga.fq[1][k]; }
      sched0();
      g.run_steps<true>(acc, [&](int it, int i) {
        if (i == PS % 4 && __builtin_expect(it == (F / 64 - 2 * JA) / 4 - PS / 4, 0)) {
          wait_vm<0>();
          ST(4 + l * 4, 5);
          __builtin_amdgcn_raw_buffer_store_b32(one, rf, fo, 0, 16);
        }
      } CSA);
      bar();
      sched0();
      g.pend<0, 4>(acc);
      ST(4 + l * 4, 2);
      tile_add(acc, s, xa);
      u32x4 xr[4];
#pragma unroll
      for (int i = 0; i < 2; ++i)
#pragma unroll
        for (int jj = 0; jj < 2; ++jj) { xr[jj * 2 + i] = pack_pair(acc[2 * i][jj], acc[2 * i + 1][jj]); st16(ro, (jj ? oo2 : oo) + i * 64, xr[jj * 2 + i]); }
      ST(4 + l * 4, 3);
      if (l == 0) {
#pragma unroll
        for (int p = 0; p < 4; ++p) *(LAS u32x4*)(uintptr_t)(xa + p * 1024) = xr[p];
        if (NL == 1) sync_post(ws, 3, -1, wid);
      } else ST(8, 4);
    }
  }
  if (wid0 == 0 && blockIdx.x == 45) {
    wait_lgkm0();
    unsigned d = *(volatile LAS unsigned*)(uintptr_t)(19968 + PROBE * 4) - *(volatile LAS unsigned*)(uintptr_t)(19968 + PROBE0 * 4);
    const u64 e = __builtin_amdgcn_s_memrealtime() + PROBEF * (d < 20000 ? d : 0);
    while (__builtin_amdgcn_s_memrealtime() < e) __builtin_amdgcn_s_sleep(1);
  }
  return;
  if (wid0 == 0) {
    const u64 hi = __builtin_amdgcn_s_memrealtime() & ~0xffffffffull;
    wait_lgkm0();
    for (int e = threadIdx.x; e < 128; e += 64) ((gu64*)(ws + (200u << 20)))[((e >> 3) * 1024 + blockIdx.x) * 8 + (e & 7)] = hi | *(volatile LAS unsigned*)(uintptr_t)(19968 + e * 4);
  }
}

extern "C" void kernel_launch(void* const* d_in, const int* in_sizes, int n_in,
                              void* d_out, int out_size, void* d_ws, size_t ws_size,
                              hipStream_t stream) {
  (void)hipFuncSetAttribute((const void*)mega_kernel, hipFuncAttributeMaxDynamicSharedMemorySize, 163840);
  mega_kernel<<<256, 512, 163840, stream>>>((const u16*)d_in[0], (const u16*)d_in[1], (const u16*)d_in[2], (const u16*)d_in[4], (const u16*)d_in[6], (const u16*)d_in[5], (const u16*)d_in[7], (u16*)d_out, (char*)d_ws);
}
```

```cpp
#include <hip/hip_runtime.h>
#include <hip/hip_bf16.h>
#include <stdint.h>

typedef unsigned short u16;
typedef unsigned long long u64;
typedef short bf16x8 __attribute__((ext_vector_type(8)));
typedef short bf16x4 __attribute__((ext_vector_type(4)));
typedef float f32x4 __attribute__((ext_vector_type(4)));
typedef float f32x2 __attribute__((ext_vector_type(2)));
typedef unsigned u32x4 __attribute__((ext_vector_type(4)));
typedef unsigned u32x2 __attribute__((ext_vector_type(2)));
typedef __bf16 bf16v2 __attribute__((ext_vector_type(2)));
#define LAS __attribute__((address_space(3)))
#define GAS __attribute__((address_space(1)))
#define DEVI __device__ __forceinline__
typedef __attribute__((address_space(1))) unsigned gu32;
typedef __attribute__((address_space(1))) unsigned long long gu64;

constexpr int S = 2048, D = 1024, F = 3584;
constexpr float EPS = 1e-5f;

DEVI float bf2f(u16 v) { return __uint_as_float(((unsigned)v) << 16); }
DEVI float bflo(unsigned v) { return __uint_as_float(v << 16); }
DEVI float bfhi(unsigned v) { return __uint_as_float(v & 0xffff0000u); }
DEVI unsigned pk2(float lo, float hi) { bf16v2 p; p.x = (__bf16)lo; p.y = (__bf16)hi; return __builtin_bit_cast(unsigned, p); }

DEVI const void* uni_ptr(const void* p) {
  const u64 v = (u64)p;
  return (const void*)(((u64)(unsigned)__builtin_amdgcn_readfirstlane((unsigned)(v >> 32)) << 32) | (unsigned)__builtin_amdgcn_readfirstlane((unsigned)v));
}
DEVI void dma16(const void* sbase, unsigned voff, unsigned ldsaddr) {
  ldsaddr = __builtin_amdgcn_readfirstlane(ldsaddr);
  asm volatile("s_mov_b32 m0, %2\n\ts_nop 0\n\tglobal_load_lds_dwordx4 %0, %1 sc1" :: "v"(voff), "s"(sbase), "s"(ldsaddr) : "memory");
}
template <int N, int IMM> DEVI void dma_grp(const void* sbase, const unsigned* v, unsigned lb) {
  static_assert(N == 1 || N == 2 || N == 4, "");
  if constexpr (N == 4)
    asm volatile("s_add_u32 m0, %1, %2\n\ts_nop 0\n\tglobal_load_lds_dwordx4 %3, %0 sc1\n\tglobal_load_lds_dwordx4 %4, %0 offset:1024 sc1\n\t"
                 "global_load_lds_dwordx4 %5, %0 offset:2048 sc1\n\tglobal_load_lds_dwordx4 %6, %0 offset:3072 sc1"
                 :: "s"(sbase), "s"(lb), "i"(IMM), "v"(v[0]), "v"(v[1]), "v"(v[2]), "v"(v[3]) : "memory", "scc");
  else if constexpr (N == 2)
    asm volatile("s_add_u32 m0, %1, %2\n\ts_nop 0\n\tglobal_load_lds_dwordx4 %3, %0 sc1\n\tglobal_load_lds_dwordx4 %4, %0 offset:1024 sc1"
                 :: "s"(sbase), "s"(lb), "i"(IMM), "v"(v[0]), "v"(v[1]) : "memory", "scc");
  else
    asm volatile("s_add_u32 m0, %1, %2\n\ts_nop 0\n\tglobal_load_lds_dwordx4 %3, %0 sc1" :: "s"(sbase), "s"(lb), "i"(IMM), "v"(v[0]) : "memory", "scc");
}
template <int N> DEVI void wait_vm() { asm volatile("s_waitcnt vmcnt(%0)" :: "i"(N) : "memory"); }
DEVI void wait_lgkm0() { asm volatile("" ::: "memory"); __builtin_amdgcn_s_waitcnt(0xC07F); asm volatile("" ::: "memory"); }
DEVI void mfma_ip(f32x4& c, const bf16x8& a, const bf16x8& b) { asm volatile("v_mfma_f32_16x16x32_bf16 %0, %1, %2, %0" : "+v"(c) : "v"(a), "v"(b)); }
DEVI void mfma_gap() { __builtin_amdgcn_sched_barrier(0); asm volatile("s_nop 7"); __builtin_amdgcn_sched_barrier(0); }
DEVI void bar() { asm volatile("" ::: "memory"); __builtin_amdgcn_s_barrier(); asm volatile("" ::: "memory"); }
DEVI void sched0() { __builtin_amdgcn_sched_barrier(0); }

DEVI bf16x8 lds_b128(unsigned addr) { return *(const LAS bf16x8*)(uintptr_t)addr; }
DEVI bf16x4 lds_tr4(unsigned addr) { return __builtin_amdgcn_ds_read_tr16_b64_v4i16((LAS bf16x4*)(uintptr_t)addr); }

typedef __amdgpu_buffer_rsrc_t rsrc_t;
DEVI rsrc_t mkrsrc(const void* p, unsigned bytes) { return __builtin_amdgcn_make_buffer_rsrc((void*)p, 0, bytes, 0x00020000); }
DEVI void st16(rsrc_t r, unsigned off, u32x4 v) { __builtin_amdgcn_raw_buffer_store_b128(v, r, off, 0, 16); }
DEVI void ld_raw(u32x4& v, rsrc_t r, unsigned off) { asm volatile("buffer_load_dwordx4 %0, %1, %2, 0 offen sc1 ; raw load" : "=&v"(v) : "v"(off), "s"(r) : "memory"); }
DEVI void ld_again(u32x4& v, rsrc_t r, unsigned off) { asm volatile("buffer_load_dwordx4 %0, %1, %2, 0 offen sc1\n\ts_waitcnt vmcnt(0)" : "=&v"(v) : "v"(off), "s"(r) : "memory"); }
DEVI void ld_ok(u32x4& v) { asm volatile("; raw ok %0" : "+v"(v)); }
#define RLX_AGENT __ATOMIC_RELAXED, __HIP_MEMORY_SCOPE_AGENT

template <int N> DEVI float ror16(float v) { return __int_as_float(__builtin_amdgcn_update_dpp(0, __float_as_int(v), 0x120 + N, 0xf, 0xf, false)); }
DEVI float red16(float v) { v += ror16<8>(v); v += ror16<4>(v); v += ror16<2>(v); v += ror16<1>(v); return v; }
DEVI void xch32(float v, float& a, float& b) { unsigned u = __float_as_uint(v); auto r = __builtin_amdgcn_permlane32_swap(u, u, false, false); unsigned x = r[0], y = r[1]; a = __uint_as_float(x); b = __uint_as_float(y); }
DEVI void xch16(float v, float& a, float& b) { unsigned u = __float_as_uint(v); auto r = __builtin_amdgcn_permlane16_swap(u, u, false, false); unsigned x = r[0], y = r[1]; a = __uint_as_float(x); b = __uint_as_float(y); }
DEVI void swp32(float& a, float& b) { auto r = __builtin_amdgcn_permlane32_swap(__float_as_uint(a), __float_as_uint(b), false, false); unsigned x = r[0], y = r[1]; a = __uint_as_float(x); b = __uint_as_float(y); }
DEVI void swp16(float& a, float& b) { auto r = __builtin_amdgcn_permlane16_swap(__float_as_uint(a), __float_as_uint(b), false, false); unsigned x = r[0], y = r[1]; a = __uint_as_float(x); b = __uint_as_float(y); }
DEVI float colsum4(float v) { float a, b; xch32(v, a, b); v = a + b; xch16(v, a, b); return a + b; }
DEVI float vmax(float a, float b) { float r; asm("v_max_f32 %0, %1, %2" : "=v"(r) : "v"(a), "v"(b)); return r; }
DEVI float vmin3(float a, float b, float c) { float r; asm("v_min3_f32 %0, %1, %2, %3" : "=v"(r) : "v"(a), "v"(b), "v"(c)); return r; }
DEVI float vmax3(float a, float b, float c) { float r; asm("v_max3_f32 %0, %1, %2, %3" : "=v"(r) : "v"(a), "v"(b), "v"(c)); return r; }

DEVI unsigned src_kc(int c, int ld) { int row = c >> 3, ch = (c & 7) ^ ((row >> 1) & 7); return (unsigned)(row * ld * 2 + ch * 16); }
DEVI int km_f(int k) { return ((k >> 1) & 1) | (((k >> 3) & 1) << 1); }
DEVI unsigned ph(unsigned x) { return ((x >> 7) << 8) | (x & 127u); }
DEVI unsigned rd_kc(int lane, int ks) { int r = lane & 15, g = lane >> 4; return (unsigned)(r * 128 + (((ks * 4 + g) ^ (r >> 1)) << 4)); }
DEVI unsigned rd_km(int lane, int ct) {
  int g = lane >> 4, q = (lane & 15) >> 2, p = lane & 3;
  int f = ((q >> 1) & 1) | ((g & 1) << 1);
  return (unsigned)((ct >> 2) * 8192 + (8 * g + q) * 128 + (((ct & 3) ^ f) << 5) + 8 * p);
}


constexpr unsigned CODE_BYTES = 53760;
DEVI void code_warm(int part, int lane, unsigned scratch) {
  unsigned long long pc;
  asm volatile("s_getpc_b64 s[40:41]\n\ts_add_u32 s40, s40, mega_kernel@rel32@lo+4\n\ts_addc_u32 s41, s41, mega_kernel@rel32@hi+12\n\ts_mov_b64 %0, s[40:41]" : "=s"(pc) :: "s40", "s41", "scc");
  const unsigned off = (unsigned)(part * 64 + lane) * 128u;
  scratch = __builtin_amdgcn_readfirstlane(scratch);
  if (off < CODE_BYTES) asm volatile("s_nop 3\n\ts_mov_b32 m0, %2\n\ts_nop 0\n\tglobal_load_lds_dword %0, %1" :: "v"(off), "s"(pc), "s"(scratch) : "memory");
}

DEVI unsigned src_km(int lt, int ld) { const int k = lt >> 3; return (unsigned)(k * ld * 2 + (((lt & 7) ^ (km_f(k) << 1)) << 4)); }
DEVI void touch4(const void* sbase, unsigned voff, unsigned ldsaddr) {
  ldsaddr = __builtin_amdgcn_readfirstlane(ldsaddr); sbase = uni_ptr(sbase);
  asm volatile("s_nop 3\n\ts_mov_b32 m0, %2\n\ts_nop 0\n\tglobal_load_lds_dword %0, %1" :: "v"(voff), "s"(sbase), "s"(ldsaddr) : "memory");
}

template <int IMM> DEVI void spf1(const void* base, unsigned soff) { asm volatile("s_load_dword s101, %0, %1 offset:%2" :: "s"(base), "s"(soff), "i"(IMM) : "s101"); }
template <int IMM = 0> DEVI void spf2(const void* base, unsigned soff) { asm volatile("s_load_dwordx2 s[100:101], %0, %1 offset:%2" :: "s"(base), "s"(soff), "i"(IMM + 60) : "s100", "s101"); }
template <int ST> DEVI void spf2x6(const void* base, unsigned soff) {
  asm volatile("s_load_dwordx2 s[100:101], %0, %1 offset:%2\n\ts_load_dwordx2 s[100:101], %0, %1 offset:%3\n\ts_load_dwordx2 s[100:101], %0, %1 offset:%4\n\t"
               "s_load_dwordx2 s[100:101], %0, %1 offset:%5\n\ts_load_dwordx2 s[100:101], %0, %1 offset:%6\n\ts_load_dwordx2 s[100:101], %0, %1 offset:%7"
               :: "s"(base), "s"(soff), "i"(60), "i"(60 + ST), "i"(60 + 2 * ST), "i"(60 + 3 * ST), "i"(60 + 4 * ST), "i"(60 + 5 * ST) : "s100", "s101");
}
template <int ST> DEVI void spf1x12(const void* base, unsigned soff) {
  spf1<0>(base, soff); spf1<ST>(base, soff); spf1<2 * ST>(base, soff); spf1<3 * ST>(base, soff); spf1<4 * ST>(base, soff); spf1<5 * ST>(base, soff);
  spf1<6 * ST>(base, soff); spf1<7 * ST>(base, soff); spf1<8 * ST>(base, soff); spf1<9 * ST>(base, soff); spf1<10 * ST>(base, soff); spf1<11 * ST>(base, soff);
}
DEVI int lgkm_out() { int n; asm volatile("s_getreg_b32 %0, hwreg(HW_REG_IB_STS, 8, 4)" : "=s"(n)); return n; }
DEVI int slow_res(const void* w) { return (3 - ((unsigned)(uintptr_t)w >> 7)) & 7; }

constexpr int WNE = 16, ANE = 24, BNE = 13, QNE = 46;
struct Plan { int n; signed char q[40], idx[40], k[40], h[40], g[40]; };
template <class O, bool PKM, bool QKM> constexpr Plan make_plan() {
  Plan p{};
  int nr = 0;
  for (int c = 0; c < O::NRO; ++c) nr += ((O::ro[c] >> 4) ? QKM : PKM) ? 4 : 2;
  int prev = 0;
  for (int c = 0; c < O::NRO; ++c) {
    const int q = O::ro[c] >> 4, i = O::ro[c] & 15;
    int av[2] = {0, 0}, k0 = -1;
    for (int m = 0; m < 2 * O::NP; ++m) {
      const int mm = (m + 2 * O::NPEND) % (2 * O::NP);
      if ((q ? O::pj[m >> 1] : O::pi[m >> 1]) == i && m < 2 * O::NPEND) av[((m >> 1) ^ m) & 1] = m;
      if ((q ? O::pj[mm >> 1] : O::pi[mm >> 1]) == i && k0 < 0) k0 = ((mm >> 1) ^ mm) & 1;
    }
    for (int kk = 0; kk < 2; ++kk)
      for (int h = 0; h < ((q ? QKM : PKM) ? 2 : 1); ++h) {
        const int k = k0 ^ kk;
        int g = p.n * O::NE / nr;
        if (g < av[k]) g = av[k];
        if (g < prev + (p.n && O::NE >= nr ? 1 : 0)) g = prev + (p.n && O::NE >= nr ? 1 : 0);
        p.q[p.n] = q; p.idx[p.n] = i; p.k[p.n] = k; p.h[p.n] = h; p.g[p.n] = g; prev = g; ++p.n;
      }
  }
  return p;
}
struct OBase { static constexpr int pidx(int i) { return i; } static constexpr int poff(int) { return 0; } static constexpr int qoff(int j) { return j * 2048; } static constexpr bool swp(int) { return false; } };
template <bool PF, int NE_> struct O44 : OBase {
  static constexpr int TP = 4, TQ = 4, NPA = 4, NP = 16, NPEND = 8, NRO = 8, NE = NE_;
  static constexpr signed char pi[16] = {2, 3, 3, 2, 2, 3, 3, 2, 0, 1, 1, 0, 0, 1, 1, 0}, pj[16] = {0, 0, 1, 1, 2, 2, 3, 3, 0, 0, 1, 1, 2, 2, 3, 3};
  static constexpr signed char ro[8] = {0, PF ? 1 : 16, PF ? 16 : 1, 17, 18, 19, 2, 3};
};
template <bool PF, int NE_> struct O42 : OBase {
  static constexpr int TP = 4, TQ = 2, NPA = 4, NP = 8, NPEND = 4, NRO = 6, NE = NE_;
  static constexpr signed char pi[8] = {2, 3, 3, 2, 0, 1, 1, 0}, pj[8] = {0, 0, 1, 1, 0, 0, 1, 1};
  static constexpr signed char ro[6] = {0, PF ? 1 : 16, PF ? 16 : 1, 17, 3, 2};
};
template <int TH> struct OQ {
  static constexpr int TP = 6, TQ = 5, NPA = 2, NP = 26, NPEND = 13, NRO = 11, NE = QNE;
  static constexpr signed char pi[26] = {3, 4, 5, 5, 4, 3, 3, 4, 5, 5, TH ? 5 : 3, 4, TH ? 3 : 4, 0, 1, 2, 2, 1, 0, 0, 1, 2, 2, TH ? 2 : 0, 1, TH ? 0 : 1};
  static constexpr signed char pj[26] = {0, 0, 0, 1, 1, 1, 2, 2, 2, TH ? 4 : 3, 3, 3, TH ? 3 : 4, 0, 0, 0, 1, 1, 1, 2, 2, 2, TH ? 4 : 3, 3, 3, TH ? 3 : 4};
  static constexpr signed char ro[11] = {0, 1, 16, 2, 17, 18, TH ? 20 : 19, TH ? 19 : 20, 3, 4, 5};
  static constexpr int pidx(int i) { return i / 3; }
  static constexpr int poff(int i) { return i % 3 * 8192; }
  static constexpr int qoff(int j) { return j == 4 ? 0 : (1 + 4 * TH + j) * 2048; }
  static constexpr bool swp(int i) { return i % 3 == 2; }
};
template <bool PKM, bool QKM, int PB, int QB, int XB, int NS, int NT, bool WISP, int BASE, int ROT, int WLD, int WSUB, int ASTEP, int RLX = 0, bool QF = false>
struct Gemm {
  static constexpr int SB = PB + QB + XB, CNT = (PB + QB) / 4096 + (XB ? 1 : 0);
  static constexpr int PO = QF ? QB : 0, QO = QF ? 0 : PB, WB = WISP ? PB : QB, AB = WISP ? QB : PB, WOFF = WISP ? PO : QO, AOFF = WISP ? QO + XB : PO;
  static constexpr int XL = XB / 64, H = NS - 1 - RLX, CA = AB / 4096 + (XB ? 1 : 0), WSTEP = 64 * WLD * 2;
  static_assert(H * CNT <= 63 && BASE + NS * SB <= 163840, "");
  struct Loader {
    static constexpr int NW = WB / 4096, NA = AB / 4096;
    const char* w; const char* ab; unsigned wv[NW], av[NA], xv, lwb, lab, lx; int rot, islot, t; bool xl;
    DEVI Loader(const void* w_, const void* ab_, int lw, int lane, int rot_ = 0)
        : w((const char*)w_ - 3072), ab((const char*)ab_ - 3072), lwb(BASE + WOFF + lw * (NW == 6 ? 2048 : WB / 4)), lab(BASE + AOFF + lw * (AB / 4)), lx(BASE + PB + lw * (XL * 16)),
          rot(rot_), islot((H + ROT) % NS), t(0), xl(lane < XL) {
      const unsigned w0 = (lane >> 3) * (WLD * 2), f0 = (lane & 7) ^ ((lane >> 3) & 2);
#pragma unroll
      for (int i = 0; i < NW; ++i) {
        const unsigned p = NW == 6 ? (i >> 1) * 8 + lw * 2 + (i & 1) : lw * NW + i;
        wv[i] = w0 + ((f0 ^ ((i & 1) << 2)) << 4) + (NW == 6 ? (i >> 1) * WSUB + (lw * 2 + (i & 1)) * (8 * WLD * 2) - (i & 1) * 1024 : (p >> 3) * WSUB + (p & 7) * (8 * WLD * 2) - i * 1024) + 3072;
        asm volatile("" : "+v"(wv[i]));
      }
      asm volatile("" : "+s"(w), "+s"(lwb));
      if (XB) asm volatile("" : "+s"(rot));
      else seta(0, 0, lw, lane);
    }
    DEVI void seta(unsigned a0, unsigned xv_, int lw, int lane) {
      xv = xv_ + 3072;
#pragma unroll
      for (int i = 0; i < NA; ++i) { const unsigned p = lw * NA + i; av[i] = (XB ? a0 + src_kc(p * 64 + lane, D) : p * 2048 + ph(lane * 16)) + 3072 - i * 1024; asm volatile("" : "+v"(av[i])); }
      asm volatile("" : "+s"(ab), "+s"(lab));
      if (XB) { asm volatile("" : "+v"(xv)); asm volatile("" : "+s"(lx)); }
    }
    DEVI int kord(int n) const { if constexpr (XB != 0) return (n + rot) & (NT - 1); else return n; }
    template <int SO> DEVI void issue_w(const char* p, unsigned so = 0) const {
      if constexpr (NW == 6) { dma_grp<2, SO>(p, wv, lwb + so); dma_grp<2, SO + 8192>(p, wv + 2, lwb + so); dma_grp<2, SO + 16384>(p, wv + 4, lwb + so); }
      else dma_grp<NW, SO>(p, wv, lwb + so);
    }
    template <int SO> DEVI void issue_a(const char* p, unsigned so = 0) const {
      if (XB) { if (xl) dma_grp<1, SO>(p, &xv, lx + so); }
      dma_grp<NA, SO>(p, av, lab + so);
    }
    template <int S0, bool W, bool A> DEVI void pre() const {
      if constexpr (S0 < H) {
        if (W) issue_w<((S0 + ROT) % NS) * SB>(w + (kord(0) + S0) * WSTEP);
        if (A) issue_a<((S0 + ROT) % NS) * SB>(ab + (kord(0) + S0) * ASTEP);
        pre<S0 + 1, W, A>();
      }
    }
    template <int T> DEVI void pre_w1() const { issue_w<((T + ROT) % NS) * SB>(w + (kord(0) + T) * WSTEP); sched0(); }
    DEVI void pre_w() const { pre<0, true, false>(); sched0(); }
    template <int T> DEVI void peel() const {
      if constexpr (T < H - 1) {
        wait_vm<(H - 1 - T) * CA + T * CNT>();
        bar();
        issue_w<((T + H + ROT) % NS) * SB>(w + (T + H) * WSTEP); issue_a<((T + H + ROT) % NS) * SB>(ab + (T + H) * ASTEP);
        peel<T + 1>();
      }
    }
    DEVI void start() {
      sched0();
      bar();
      pre<0, false, true>();
      sched0();
      if constexpr (H == 2) wait_vm<CA>();
      else { peel<0>(); islot = (2 * H - 1 + ROT) % NS; t = H - 1; }
    }
    template <class HK> DEVI void run(int tend, HK side) {
#pragma unroll 1
      for (; t < tend; ++t) {
        const char* wk = w + (t + H) * WSTEP; const char* ak = ab + (t + H) * ASTEP;
        unsigned lw_ = lwb + islot * SB, la_ = lab + islot * SB;
        islot = islot + 1 == NS ? 0 : islot + 1;
        asm volatile("" : "+s"(wk), "+s"(ak), "+s"(lw_), "+s"(la_));
        wait_vm<(H - 1) * CNT>();
        bar();
        dma_grp<NW, 0>(wk, wv, lw_);
        side(t);
        dma_grp<NA, 0>(ak, av, la_);
        sched0();
      }
    }
    template <int T, int X, class A, class B, class C> DEVI void step(A pre_, B side, C post) const {
      const int k = kord(T + H);
      const char* wk = w + k * WSTEP; const char* ak = ab + k * ASTEP;
      asm volatile("" : "+s"(wk), "+s"(ak));
      wait_vm<(H - 1) * CNT + X>();
      bar();
      pre_(BASE + ((T + ROT) % NS) * SB);
      issue_w<((T + H + ROT) % NS) * SB>(wk);
      sched0();
      side();
      sched0();
      issue_a<((T + H + ROT) % NS) * SB>(ak);
      post();
      sched0();
    }
    template <int I> DEVI void tail() const { if constexpr (I >= 0) { sched0(); wait_vm<I * CNT>(); bar(); tail<I - 1>(); } }
  };
  static constexpr int NWIN = (NS * SB + 65535) / 65536;
  template <class O> struct Comp {
    static constexpr int TP = O::TP, TQ = O::TQ, NM = 2 * O::NP, NPA = PKM ? O::NPA : 2, NQA = QKM ? TQ : 2;
    static constexpr Plan PL = make_plan<O, PKM, QKM>();
    unsigned prd[NWIN][NPA], qrd[NWIN][NQA];
    bf16x8 fp[TP][2], fq[TQ][2]; bf16x4 lo;
    DEVI void setwin() {
#pragma unroll
      for (int w = 1; w < NWIN; ++w) {
#pragma unroll
        for (int i = 0; i < NPA; ++i) { prd[w][i] = prd[0][i] + w * 65536; asm volatile("" : "+v"(prd[w][i])); }
#pragma unroll
        for (int i = 0; i < NQA; ++i) { qrd[w][i] = qrd[0][i] + w * 65536; asm volatile("" : "+v"(qrd[w][i])); }
      }
#pragma unroll
      for (int i = 0; i < NPA; ++i) asm volatile("" : "+v"(prd[0][i]));
#pragma unroll
      for (int i = 0; i < NQA; ++i) asm volatile("" : "+v"(qrd[0][i]));
    }
    DEVI void setup(int wp, int wq, int lane) {
      const unsigned b = BASE;
      if (PKM) {
#pragma unroll
        for (int i = 0; i < TP; ++i) prd[0][i] = b + rd_km(lane, wp * TP + i);
      } else { prd[0][0] = b + rd_kc(lane, 0) + wp * TP * 2048; prd[0][1] = b + rd_kc(lane, 1) + wp * TP * 2048; }
      if (QKM) {
#pragma unroll
        for (int j = 0; j < TQ; ++j) qrd[0][j] = b + rd_km(lane, wq * TQ + j);
      } else { qrd[0][0] = b + rd_kc(lane, 0) + wq * TQ * 2048; qrd[0][1] = b + rd_kc(lane, 1) + wq * TQ * 2048; }
    }
    template <int C, int N> DEVI static unsigned wa(const unsigned (&b)[NWIN][N], int i) { return b[C >> 16][i] + (C & 0xffff); }
    template <int SLOT, int R> DEVI void rd() {
      constexpr int q = PL.q[R], i = PL.idx[R], k = PL.k[R], h = PL.h[R];
      bf16x8& f = q ? fq[q ? i : 0][k] : fp[q ? 0 : i][k];
      if constexpr (q ? QKM : PKM) {
        unsigned a;
        if constexpr (q) a = wa<SLOT * SB + QO + k * 4096 + h * 512, NQA>(qrd, i); else a = wa<SLOT * SB + PO + O::poff(i) + k * 4096 + h * 512, NPA>(prd, O::pidx(i));
        const bf16x4 x = lds_tr4(a);
        if constexpr (h) f = __builtin_shufflevector(lo, x, 0, 1, 2, 3, 4, 5, 6, 7); else lo = x;
      } else if constexpr (q) f = lds_b128(wa<SLOT * SB + QO + O::qoff(i), 2>(qrd, k));
      else f = lds_b128(wa<SLOT * SB + PO + i * 2048, 2>(prd, k));
    }
    template <int SLOT, int M, int R> DEVI void rds() { if constexpr (R < PL.n) { if constexpr (PL.g[R] == M) rd<SLOT, R>(); if constexpr (PL.g[R] <= M) rds<SLOT, M, R + 1>(); } }
    template <int M, bool IP = false> DEVI void mm(f32x4 (&acc)[TP][TQ]) {
      constexpr int i = O::pi[M >> 1], j = O::pj[M >> 1], k = ((M >> 1) ^ M) & 1;
      if constexpr (IP) { if constexpr (O::swp(i)) mfma_ip(acc[i][j], fq[j][k], fp[i][k]); else mfma_ip(acc[i][j], fp[i][k], fq[j][k]); }
      else if constexpr (O::swp(i)) acc[i][j] = __builtin_amdgcn_mfma_f32_16x16x32_bf16(fq[j][k], fp[i][k], acc[i][j], 0, 0, 0);
      else acc[i][j] = __builtin_amdgcn_mfma_f32_16x16x32_bf16(fp[i][k], fq[j][k], acc[i][j], 0, 0, 0);
    }
    template <int A, int B> DEVI void pend(f32x4 (&acc)[TP][TQ]) { if constexpr (A < B) { mm<2 * A>(acc); mm<2 * A + 1>(acc); pend<A + 1, B>(acc); } }
    template <int SLOT, bool FIRST, int M> DEVI void body(f32x4 (&acc)[TP][TQ]) {
      if constexpr (M < NM) {
        if constexpr (!FIRST || M >= 2 * O::NPEND) { mm<M, true>(acc); sched0(); }
        rds<SLOT, M, 0>();
        sched0();
        body<SLOT, FIRST, M + 1>(acc);
      }
    }
    template <int SLOT, bool FIRST> DEVI void step(f32x4 (&acc)[TP][TQ]) { if (!RLX) wait_lgkm0(); bar(); sched0(); body<SLOT, FIRST, 0>(acc); }
    template <int I, int N, int T0, class MID> DEVI void steps(f32x4 (&acc)[TP][TQ], MID mid) {
      if constexpr (I < N) { mid(I); step<(I + T0 + ROT) % NS, false>(acc); steps<I + 1, N, T0>(acc, mid); }
    }
    DEVI void prep(f32x4 (&acc)[TP][TQ]) { setwin(); pin(acc); }
    DEVI void pin(f32x4 (&acc)[TP][TQ]) {
#pragma unroll
      for (int p = 0; p < O::NP; ++p) asm volatile("" : "+v"(acc[O::pi[p]][O::pj[p]]));
    }
    template <bool CONT, class MID> DEVI void run_steps(f32x4 (&acc)[TP][TQ], MID mid) {
      constexpr int T0 = CONT ? 0 : 1;
      if constexpr (T0) { mid((NT - T0) / NS, -1); step<ROT % NS, true>(acc); }
#pragma unroll 1
      for (int itn = (NT - T0) / NS; itn > 0; --itn) {
        steps<0, NS, T0>(acc, [&](int i) { mid(itn, i); });
      }
      steps<0, (NT - T0) % NS, T0>(acc, [](int) {});
      wait_lgkm0();
      mfma_gap();
    }
  };
};

DEVI f32x4 gelu_arg(f32x4 u) { return u * (-2.302208198f + -0.1029432397f * (u * u)); }
DEVI f32x4 gelu_tanh(f32x4 u) {
  f32x4 a = gelu_arg(u);
#pragma unroll
  for (int e = 0; e < 4; ++e) a[e] = __builtin_amdgcn_exp2f(a[e]);
  a += 1.f;
#pragma unroll
  for (int e = 0; e < 4; ++e) a[e] = __builtin_amdgcn_rcpf(a[e]);
  return u * a;
}

template <int tilesP, int tilesQ, int gp, int gq> DEVI void tile_map(unsigned bid, int& tp, int& tq) {
  const unsigned xcd = bid & 7, idx = bid >> 3;
  constexpr unsigned gP = tilesP / gp, gQ = tilesQ / gq;
  static_assert(gP * gQ == 8, "");
  tp = (xcd % gP) * gp + idx % gp;
  tq = (xcd / gP) * gq + idx / gp;
}

template <int V> struct IC { static constexpr int value = V; };
template <int A, int B, class FN> DEVI void for_c(FN f) { if constexpr (A < B) { f(IC<A>{}); for_c<A + 1, B>(f); } }
DEVI float sel(bool c, float a, float b) { return c ? a : b; }
DEVI u32x2 pk4(const f32x4& v) { return (u32x2){pk2(v[0], v[1]), pk2(v[2], v[3])}; }
DEVI u32x4 cat4(u32x2 a, u32x2 b) { return (u32x4){a.x, a.y, b.x, b.y}; }
DEVI bf16x8 cat(u32x2 a, u32x2 b) { return __builtin_bit_cast(bf16x8, cat4(a, b)); }
DEVI f32x4 mfma(const bf16x8& a, const bf16x8& b, const f32x4& c) { return __builtin_amdgcn_mfma_f32_16x16x32_bf16(a, b, c, 0, 0, 0); }
DEVI u32x4 pack_pair(const f32x4& a, const f32x4& b) {
  const unsigned ax = pk2(a[0], a[1]), ay = pk2(a[2], a[3]), bx = pk2(b[0], b[1]), by = pk2(b[2], b[3]);
  auto rx = __builtin_amdgcn_permlane16_swap(ax, bx, false, false);
  auto ry = __builtin_amdgcn_permlane16_swap(ay, by, false, false);
  const unsigned x0 = rx[0], x1 = rx[1], y0 = ry[0], y1 = ry[1];
  return (u32x4){x0, y0, x1, y1};
}
DEVI int pair_chunk(int fq) { return ((fq & 1) << 1) | (fq >> 1); }
DEVI void sel_frags(bool paired, int fr, int fq, bf16x8 (&s)[2]) {
  const unsigned one = fr & 1 ? 0x3F800000u : 0x3F80u;
#pragma unroll
  for (int i = 0; i < 2; ++i) {
    const bool on = paired ? (fq & 1) == i && (fr >> 3) == (fq >> 1) : (fr >> 2) == fq;
    const int d = paired ? (fr & 7) >> 1 : i * 2 + ((fr >> 1) & 1);
    u32x4 v;
#pragma unroll
    for (int e = 0; e < 4; ++e) v[e] = on && d == e ? one : 0u;
    s[i] = __builtin_bit_cast(bf16x8, v);
  }
}
template <int TQ> DEVI void tile_add(f32x4 (&acc)[4][TQ], const bf16x8 (&s)[2], unsigned a) {
  bf16x8 t[4];
#pragma unroll
  for (int p = 0; p < 4; ++p) t[p] = lds_b128(a + p * 1024);
#pragma unroll
  for (int p = 0; p < 4; ++p)
#pragma unroll
    for (int i = 0; i < 2; ++i) acc[(p & 1) * 2 + i][p >> 1] = mfma(s[i], t[p], acc[(p & 1) * 2 + i][p >> 1]);
}
template <int C> DEVI float dppf(float v) { return __int_as_float(__builtin_amdgcn_update_dpp(0, __float_as_int(v), C, 0xf, 0xf, false)); }
DEVI float red8(float v) { v += dppf<0xB1>(v); v += dppf<0x4E>(v); v += dppf<0x141>(v); return v; }
DEVI float red4(float v) { v += dppf<0xB1>(v); v += dppf<0x4E>(v); return v; }

constexpr unsigned SENT = 0xBF800000u;
constexpr int SPIN_MAX = 1 << 21;

constexpr unsigned MB = 1u << 20;
constexpr unsigned WS_O = 0, WS_H = 8 * MB, WS_U = 16 * MB, WS_X2 = 32 * MB, WS_XP = 40 * MB  ;
constexpr unsigned WS_ST = 120 * MB, ST_CS = 2 * S * 64, WS_XCH = 122 * MB  , WS_EPOCH = 123 * MB, WS_ABORT = 123 * MB + 256  ;
DEVI unsigned tag2(unsigned t) { return t * 0x9E3779B1u ^ 0x5BD1E995u; }
constexpr int MEDGE = 256 * 64;
constexpr unsigned WS_MAIL = 124 * MB;

constexpr int NL = 2, QBR = 4, FLS = 3, NPL = 6, SPON = 1, PC0 = 0, PGAP = 3, WSP = 3, SWW = 0, GSH = 1, GSP = 3, SSC = 1, RB = 1, SI = 1, LPR = 1, CPR = 0, GLW = 4, GL1 = 4, SW0 = 0, SW1 = 6, XE = 0, E2 = 1, LB = 2, TLQ = 0, TLW = 1, SPT = 0, SLS = 0, SW2 = 0, XD = 1, XS = 4, JA = 22, PS = 3, TBA = 3, TC1 = 13, NW1 = 4, EW = 12, ET = 3, SPW = 1, PA_1 = 3, PA_2 = 3;
constexpr int L_X = 0, L_TAB = 16384, L_CS = L_TAB + 1152, L_TAG = L_CS + 768, L_BIAS = L_TAG + 128, L_SCR = L_BIAS + 512  , L_QKV = 20480;
static_assert(L_SCR + 1536 <= L_QKV, "");
typedef Gemm<true, false, 24576, 16384, 2048, 3, D / 64, true, 32768, 0, 3 * D, 2 * D, 128> GQ;
typedef Gemm<true, false, 16384, 8192, 0, 5, D / 64, true, 40960, 2, D, 128, 16384, 1, true> GW;
typedef Gemm<true, true, 16384, 16384, 0, 4, S / 64, true, 32768, 1, F, 128, 32768, 0, true> G1;
typedef Gemm<false, true, 16384, 16384, 0, 3, JA, false, 65536, (3 - JA % 3) % 3, S, 1024, 32768> G2A;
typedef Gemm<false, true, 16384, 8192, 0, 4, F / 64 - 2 * JA, false, 65536, 0, S, 128, 32768> G2;

DEVI unsigned get_tag() { return __builtin_amdgcn_readfirstlane(*(volatile LAS unsigned*)(uintptr_t)L_TAG) + 1u; }
DEVI bool spin_fail(int spin, char* ws) {
  if ((spin & 255) != 255) return false;
  gu32* ab = (gu32*)(ws + WS_ABORT);
  const unsigned tag = get_tag();
  if (spin >= SPIN_MAX) { __hip_atomic_store(ab, tag, RLX_AGENT); return true; }
  return __builtin_amdgcn_readfirstlane(__hip_atomic_load(ab, RLX_AGENT)) == tag;
}
DEVI void sync_post(char* ws, int edge, int post, int wid) {
  unsigned o = post >= 0 && wid == 0 ? (post >> 6) * 256u : MB, bit = 1u << (post & 31);
  const rsrc_t r = mkrsrc(ws + WS_MAIL + edge * (MEDGE * 4), MEDGE * 4);
  asm volatile("s_waitcnt vmcnt(0) lgkmcnt(0)" : "+v"(o), "+v"(bit) : "s"(r) : "memory");
  sched0();
  bar();
  if (wid == 0) asm volatile("buffer_atomic_or %0, %1, %2, 0 offen sc1" :: "v"(bit), "v"(o), "s"(r) : "memory");
  sched0();
}
#define POLL1(r) "s_waitcnt vmcnt(2)\n\tv_readfirstlane_b32 %[seen], " r "\n\tglobal_atomic_or " r ", %[z], %[z], %[p] sc0 sc1\n\t" \
  "s_andn2_b32 %[t], %[first], %[seen]\n\ts_cbranch_scc1 3f\n\ts_cmp_eq_u32 %[first], 0\n\ts_cbranch_scc1 4f\n\ts_barrier\n\ts_mov_b32 %[first], 0\n" \
  "4:\n\ts_andn2_b32 %[t], %[second], %[seen]\n\ts_cbranch_scc0 2f\n3:\n\t"
struct Gate {
  const char* p; unsigned miss, due, a, b, c;
  DEVI Gate() : p(nullptr), miss(0), due(0) { asm volatile("v_mov_b32 %0, 0\n\tv_mov_b32 %1, 0\n\tv_mov_b32 %2, 0" : "=v"(a), "=v"(b), "=v"(c)); }
  DEVI void ask() const { asm volatile("s_mov_b64 exec, 1\n\tglobal_atomic_or %0, %1, %1, %2 sc0 sc1\n\ts_mov_b64 exec, -1" :: "v"(a), "v"(0u), "s"(p) : "memory"); }
  template <int DELAY> DEVI void wait(char* ws, int edge, int line, unsigned all, unsigned first, unsigned second, int wid) {
    sched0();
    p = ws + WS_MAIL + (edge * MEDGE + line * 64) * 4;
    second &= all;
    if (wid == 3) {
      due = 1;
      if (DELAY) __builtin_amdgcn_s_sleep(DELAY);
      int spin = 255;
#pragma unroll 1
      for (;; spin += 256) {
        asm volatile("" : "+s"(spin));
        unsigned seen, t, n;
        asm volatile("s_mov_b64 exec, 1\n\ts_waitcnt vmcnt(0)\n\tglobal_atomic_or %[a], %[z], %[z], %[p] sc0 sc1\n\ts_sleep %[gap]\n\t"
                     "global_atomic_or %[b], %[z], %[z], %[p] sc0 sc1\n\ts_sleep %[gap]\n\tglobal_atomic_or %[c], %[z], %[z], %[p] sc0 sc1\n\ts_movk_i32 %[n], 128\n"
                     "1:\n\t" POLL1("%[a]") POLL1("%[b]") POLL1("%[c]") "s_sub_u32 %[n], %[n], 1\n\ts_cbranch_scc0 1b\n"
                     "2:\n\ts_mov_b64 exec, -1 ; raw polls %[a] %[b] %[c]"
                     : [seen] "=&s"(seen), [t] "=&s"(t), [n] "=&s"(n), [first] "+&s"(first)
                     : [p] "s"(p), [second] "s"(second), [gap] "i"(PGAP), [a] "v"(a), [b] "v"(b), [c] "v"(c), [z] "v"(0u) : "memory", "scc");
        miss = ~seen & all;
        if (__builtin_expect(!(miss & second) && !first, 1)) break;
        if (spin_fail(spin, ws)) { if (first) bar(); miss = 0; break; }
      }
    } else bar();
  }
  int ev;
  DEVI void arm(int ni, int cw) { ev = miss ? ni : cw >= 0 ? 2 : 0; asm volatile("" : "+s"(ev), "+s"(miss), "+s"(due)); }
  DEVI void head(int it, int cw, int lane, int wid) {
    if (__builtin_expect(it == ev, 0)) {
      asm volatile("" : "+v"(lane)); asm volatile("" : "+s"(cw), "+s"(wid));
      if (miss) ask();
      if (it == 2) code_warm(cw, lane, L_SCR + wid * 256);
      ev = cw >= 0 && it > 2 ? 2 : 0;
      asm volatile("" : "+s"(ev));
    }
  }
  DEVI void late(char* ws) {
    if (__builtin_expect(due != 0, 0)) {
      asm volatile("" : "+s"(ws));
      asm volatile("s_waitcnt vmcnt(0) ; raw settled %0 %1 %2" :: "v"(a), "v"(b), "v"(c) : "memory");
      for (int spin = 0; miss; ++spin) {
        unsigned seen;
        asm volatile("v_readfirstlane_b32 %0, %1" : "=s"(seen) : "v"(a));
        miss &= ~seen;
        if (!miss || spin_fail(spin, ws)) break;
        ask();
        wait_vm<0>();
      }
      miss = due = 0;
      asm volatile("" : "+s"(miss), "+s"(due));
    }
  }
};
DEVI unsigned halo_off(int s0, int lw, int lane) {
  const int c = lw * 32 + (lane & 31), row = c >> 3, ch = (c & 7) ^ ((row >> 1) & 7), tok = s0 - 16 + row;
  return (unsigned)((tok < 0 ? 0 : tok) * D * 2 + ch * 16);
}
DEVI int qkv_rot(int bid) { return (bid & 3) * 4 + (bid & 4) * 2; }
DEVI unsigned x_off(int tp, int tq, int w, int fr, int fq) { return (unsigned)(((tq * 64 + (w >> 1) * 32 + fr) * D + tp * 128 + (w & 1) * 64 + pair_chunk(fq) * 8) * 2); }

DEVI void pfb_q(const u16* w, int bid, int j, int n) {
  const int r = slow_res(w);
  if ((r >> 2) == (bid & 1)) {
    const unsigned o = (unsigned)(((bid & 2) * 4 + r) * 128 + (j >> 1) * (3 * D * 2) + (j & 1) * 64 + ((n + qkv_rot(bid)) & 15) * GQ::WSTEP);
    spf1<0>(w, o); spf1<2048>(w, o); spf1<4096>(w, o);
  }
}
constexpr int PFR = NPL * 128 * 1024, PF_END = TBA * 256 + NW1 * PFR / (F * 2) / 8 * 8;
static_assert(TBA * 256 * F * 2 + NW1 * PFR <= S * F * 2 && 3 * PFR >= D * D * 2, "");
struct Pfc {
  const char* p; const char* q; int pc;
  DEVI Pfc(const u16* wo, const u16* w1, unsigned g) : p((const char*)wo + g * (NPL * 128)), q((const char*)w1 + TBA * 256 * F * 2 + g * (NPL * 128)), pc(PC0) {}
  DEVI void go() {
    unsigned t, u;
    asm volatile("s_getreg_b32 %2, hwreg(HW_REG_IB_STS, 8, 4)\n\ts_cmp_lt_u32 %1, %5\n\ts_cselect_b32 %3, 0, 1\n\ts_or_b32 %2, %2, %3\n\ts_cbranch_scc1 1f\n\t"
                 "s_load_dwordx2 s[100:101], %0, 60\n\ts_load_dwordx2 s[100:101], %0, 188\n\ts_load_dwordx2 s[100:101], %0, 316\n\t"
                 "s_load_dwordx2 s[100:101], %0, 444\n\ts_load_dwordx2 s[100:101], %0, 572\n\ts_load_dwordx2 s[100:101], %0, 700\n\t"
                 ".if %8 == 7\n\ts_load_dwordx2 s[100:101], %0, 828\n\t.endif\n\t"
                 "s_mov_b32 %2, %7\n\ts_cmp_lt_u32 %1, 2\n\ts_cselect_b32 %2, %6, %2\n\ts_mov_b64 s[40:41], %0\n\ts_add_u32 s40, s40, %2\n\ts_addc_u32 s41, s41, 0\n\t"
                 "s_cmp_eq_u32 %1, 2\n\ts_cselect_b64 %0, %4, s[40:41]\n\ts_add_u32 %1, %1, 1\n1:"
                 : "+s"(p), "+s"(pc), "=&s"(t), "=&s"(u) : "s"(q), "i"(3 + NW1), "i"((D * D * 2 - PFR) / 2), "i"(PFR), "i"(NPL) : "s40", "s41", "s100", "s101", "scc");
  }
};
template <class FN> DEVI void spare_touch(const u16* w1, const u16* w2, int bid, int wid, int lane, FN side) {
  if (!SPON) return;
  const int j = ((bid >> 3) - 28) * 8 + wid, fr = lane & 15, fq = lane >> 4;
  const unsigned scr = L_QKV + wid * 256;
  u64 due = SPT ? __builtin_amdgcn_s_memrealtime() + (j & 7) * (SPT / 8) : 0;
  auto pace = [&]() { side(); wait_vm<SPW>(); due += SPT; if (SPT) { while (__builtin_amdgcn_s_memrealtime() < due) __builtin_amdgcn_s_sleep(2); } };
#pragma unroll 1
  for (int r = PF_END + (j * 2 + ((bid >> 2) & 1)) * 4; r < S; r += 256) { if (fr < 14) touch4(w1 + (size_t)r * F + (bid & 3) * 896, fq * (F * 2) + fr * 128, scr); pace(); }
  if (SPON & 2) return;
  if (SW2 && wid < 4) __builtin_amdgcn_s_sleep(SW2);
#pragma unroll 1
  for (int r = (j * 2 + (bid & 1)) * 4; r < JA * 64; r += 256) { touch4(w2 + (size_t)(r + (bid & 2) * JA * 32) * S + (bid & 4) * 256, fq * (S * 2) + fr * 128, scr); pace(); }
#pragma unroll 1
  for (int r = JA * 128 + ((31 - j) * 2 + (bid & 1)) * 8; r < F; r += 512) { touch4(w2 + (size_t)r * S + ((bid & 7) >> 1) * 512, (lane >> 3) * (S * 2) + (lane & 7) * 128, scr); pace(); }
}

template <int TH> DEVI void qkv_attn(char* ws, int l, int h, int lane, int hp, int sb, int cw, int bid, int tp, int tq, const u16* wqkv) {
  asm volatile("; half %1" : "+v"(lane) : "i"(TH));
  const int fr = lane & 15, fq = lane >> 4;
  GQ::Comp<OQ<TH>> g;
#pragma unroll
  for (int c = 0; c < 2; ++c) { g.prd[0][c] = 32768 + rd_km(lane, h * 2 + c); g.qrd[0][c] = 32768 + rd_kc(lane, c); }
  f32x4 acc[6][5];
#pragma unroll
  for (int i = 0; i < 6; ++i)
#pragma unroll
    for (int j = 0; j < 5; ++j) acc[i][j] = (f32x4){0.f, 0.f, 0.f, 0.f};
  const int par = fr & 1, da = fr - 4 * fq - par;
  const bool ca = da >= 0, cb = da >= 2;
  const unsigned tm = L_TAB + (16 + 64 * TH) * 4, tc = L_CS + h * 128;
  unsigned am = tm + fr * 4 - 320, a4 = tm + fq * 16 - 320, aa = tm + 576 + (4 * fq + par - (ca ? 0 : 16)) * 4, ab = tm + 576 + (4 * fq + par + 2 - (cb ? 0 : 16)) * 4;
  unsigned c4 = tc + fq * 16, c1 = tc + 512 + fr * 4, xb = L_QKV + h * 3072 + lane * 16, hm = par ? 0xffff0000u : 0xffffu, m0 = ca ? 0u : hm, m1 = cb ? 0u : hm;
  unsigned oo = ph((unsigned)((((sb * 2 + TH) * 16 + hp) << 13) + fr * 128 + (((h * 4 + pair_chunk(fq)) ^ (fr >> 1)) << 4)));
  asm volatile("" : "+v"(am), "+v"(a4), "+v"(aa), "+v"(ab), "+v"(c4), "+v"(c1), "+v"(xb), "+v"(hm), "+v"(m0), "+v"(m1), "+v"(oo));
  g.prep(acc);
  if (l) {
    const int sbc = (tq >> 1) + fq;
    sync_post(ws, 3, !TH && (lane < 16 || (lane < 32 && (tq & 1) && sbc < 16)) ? (sbc * 16 + fr) * 64 + (tq - 2 * sbc + 1) * 8 + tp : -1, TH * 2 + h);
  }
  Gate gt;
  if (l) gt.wait<SW0>(ws, 3, sb * 16 + hp, sb ? 0xffffffu : 0xffff00u, (sb ? 0x10101u : 0x10100u) << ((qkv_rot(bid) & 15) >> 1), 0x30303u << ((qkv_rot(bid) & 15) >> 1), TH * 2 + h);
  else if (!TH || !h) wait_vm<0>();
  else if ((slow_res(wqkv) >> 2) == (bid & 1)) {
    if (ET) __builtin_amdgcn_s_sleep(ET);
    if ((lane & 3) < 3 && lane < 40) touch4(wqkv, (unsigned)(((qkv_rot(bid) * 64 + 128 + (bid >> 3) * 10 + (lane >> 2)) & 1023) * (3 * D * 2) + (lane & 3) * (D * 2) + ((bid & 2) * 4 + slow_res(wqkv)) * 128), L_SCR + 768);
  }
  g.template run_steps<false>(acc, [&](int it, int i) {
    if (i < 0) gt.arm(it, cw);
        if (i == 0) gt.head(it, cw, lane, TH * 2 + h);
    if (TH && i == 1) gt.late(ws);
  });
  const rsrc_t ro = mkrsrc(ws + WS_O, S * D * 4);
  if (l == 0) bar();
  float mt[5], cv[2], rq[4], ra[4], rb[4]; f32x4 m4[5], cq[2], ck[2];
#pragma unroll
  for (int c = 0; c < 2; ++c) {
    ck[c] = *(const LAS f32x4*)(uintptr_t)(c4 + 256 + c * 64); cv[c] = *(const LAS float*)(uintptr_t)(c1 + c * 64);
    cq[c] = *(const LAS f32x4*)(uintptr_t)(c4 + c * 64);
  }
#pragma unroll
  for (int j = 4; j >= 0; --j) {
    const int o = 320 + (j == 4 ? -64 - 256 * TH : j * 64);
    if (j < 4 || !TH) mt[j] = *(const LAS float*)(uintptr_t)(am + o);
    if (j < 4 || TH) m4[j] = *(const LAS f32x4*)(uintptr_t)(a4 + o);
  }
#pragma unroll
  for (int j = 0; j < 4; ++j) { rq[j] = *(const LAS float*)(uintptr_t)(am + 896 + j * 64); ra[j] = *(const LAS float*)(uintptr_t)(aa + j * 64); rb[j] = *(const LAS float*)(uintptr_t)(ab + j * 64); }
  u32x2 kp[2][5], qp[2][4], vp[2][5];
  auto fk = [&](int j) { kp[0][(j + 1) % 5] = pk4(acc[1][j]); kp[1][(j + 1) % 5] = pk4(acc[4][j] + ck[1] * mt[j]); };
  auto fv = [&](int j) { vp[0][(j + 1) % 5] = pk4(acc[2][j]); vp[1][(j + 1) % 5] = pk4(acc[5][j] + m4[j] * cv[1]); };
  auto fqq = [&](int j) { qp[0][j] = pk4(acc[0][j]); qp[1][j] = pk4(acc[3][j] + cq[1] * mt[j]); };
  g.template pend<10, 13>(acc);
#pragma unroll
  for (int i = 0; i < 6; ++i) { __builtin_amdgcn_sched_group_barrier(0x100, 3, 0); __builtin_amdgcn_sched_group_barrier(0x8, 1, 0); }
  sched0();
  for_c<0, 20>([&](auto mc) {
    constexpr int m = mc.value, n0 = m * 52 / 20, n1 = (m + 1) * 52 / 20;
    g.template mm<(18 - (m & ~1)) + (m & 1)>(acc);
    sched0();
#pragma unroll
    for (int n = n0; n < n1; ++n) {
      const int t = 12 - (n >> 2), e = n & 3, j = t == 12 ? 4 : t / 3, i = t == 12 ? 1 + TH : t % 3;
      float x = acc[i][j][e];
      asm volatile("v_fmac_f32 %0, %1, %2" : "+v"(x) : "v"(i == 2 ? m4[j][e] : i ? ck[0][e] : cq[0][e]), "v"(i == 2 ? cv[0] : mt[j]));
      acc[i][j][e] = x;
    }
    sched0();
  });
  volatile LAS unsigned* xf = (volatile LAS unsigned*)(uintptr_t)(L_TAG + 16 + (h * 2 + TH) * 4);
  if (TH) { fv(4); *(volatile LAS bf16x8*)(uintptr_t)(xb + 2048) = cat(vp[0][0], vp[1][0]); *xf = l + 1; fk(3); }
  else { fk(3); fv(3); *(volatile LAS bf16x8*)(uintptr_t)xb = cat(kp[0][4], kp[1][4]); *(volatile LAS bf16x8*)(uintptr_t)(xb + 1024) = cat(vp[0][4], vp[1][4]); *xf = l + 1; }
  sched0();
  fqq(3); fk(2); fqq(2); fk(1);
  sched0();
  const f32x4 z = {0.f, 0.f, 0.f, 0.f};
  f32x4 sc[4][2]; float a[4], b[4], m[4];
  auto scores = [&](int j) {
#pragma unroll
    for (int u = 1; u >= 0; --u) sc[j][u] = mfma(cat(kp[0][j + u], kp[1][j + u]), cat(qp[0][j], qp[1][j]), z);
  };
  auto pick = [&](int j) {
    const f32x4 s0 = sc[j][0], s1 = sc[j][1];
    a[j] *= sel(par, sel(ca, s1[1], s0[1]), sel(ca, s1[0], s0[0]));
    b[j] *= sel(par, sel(cb, s1[3], s0[3]), sel(cb, s1[2], s0[2]));
    if (!TH && j == 0) { a[j] = sel(ca || sb, a[j], -1e9f * 1.4426950408889634f); b[j] = sel(cb || sb, b[j], -1e9f * 1.4426950408889634f); }
    m[j] = __builtin_fmaxf(a[j], b[j]);
  };
  auto expo = [&](int j) { a[j] = __builtin_amdgcn_exp2f(a[j] - m[j]); b[j] = __builtin_amdgcn_exp2f(b[j] - m[j]); m[j] = a[j] + b[j]; };
  auto pv = [&](int j) {
    const unsigned pa = pk2(a[j] * ra[j], a[j] * ra[j]), pb = pk2(b[j] * rb[j], b[j] * rb[j]);
    const bf16x8 pf = __builtin_bit_cast(bf16x8, (u32x4){pa & m0, pb & m1, pa & (m0 ^ hm), pb & (m1 ^ hm)});
#pragma unroll
    for (int c = 0; c < 2; ++c) sc[j][c] = mfma(cat(vp[c][j], vp[c][j + 1]), pf, z);
  };
  auto outp = [&](int j) { st16(ro, oo + j * 4096, pack_pair(sc[j][0] * m[j], sc[j][1] * m[j])); };
  unsigned f; u32x4 xv, xk;
  auto xrd = [&]() {
    f = xf[1 - 2 * TH]; xv = *(volatile LAS u32x4*)(uintptr_t)(xb + (TH ? 1024 : 2048));
    if (TH) xk = *(volatile LAS u32x4*)(uintptr_t)xb;
  };
  auto xget = [&]() {
    sched0();
    while (__builtin_expect(__builtin_amdgcn_readfirstlane(f) != l + 1, 0)) xrd();
    vp[0][0] = (u32x2){xv[0], xv[1]}; vp[1][0] = (u32x2){xv[2], xv[3]};
    if (TH) { kp[0][0] = (u32x2){xk[0], xk[1]}; kp[1][0] = (u32x2){xk[2], xk[3]}; }
  };
  {
    fqq(1); fk(0); fqq(0);
    if (!TH) fk(4);
    xrd();
    sched0();
    if (TH) fv(3);
    fv(2); fv(1); fv(0);
#pragma unroll
    for (int c = 0; c < 2; ++c)
#pragma unroll
      for (int j = 0; j < 4; ++j) asm volatile("" : "+v"(qp[c][j]), "+v"(kp[c][j + 1]), "+v"(vp[c][j + 1]));
    if (!TH) asm volatile("" : "+v"(kp[0][0]), "+v"(kp[1][0]));
    xget();
    scores(3); scores(2); scores(1); scores(0);
#pragma unroll
    for (int j = 0; j < 4; ++j) { const float r = rq[j] * (0.17677669529663687f * 1.4426950408889634f); a[j] = r * ra[j]; b[j] = r * rb[j]; }
#pragma unroll
    for (int i = 0; i < 8; ++i) { __builtin_amdgcn_sched_group_barrier(0x8, 1, 0); __builtin_amdgcn_sched_group_barrier(0x2, 2, 0); }
    sched0();
    pick(3); pick(2); pick(1); pick(0);
    float u, w;
    swp32(m[0], m[2]); swp32(m[1], m[3]); u = m[0]; w = m[1];
    asm volatile("v_max_f32 %0, %0, %2\n\tv_max_f32 %1, %1, %3\n\ts_nop 1" : "+v"(u), "+v"(w) : "v"(m[2]), "v"(m[3]));
    swp16(u, w); u = vmax(u, w);
    xch16(u, u, w); xch32(u, m[0], m[2]); xch32(w, m[1], m[3]);
    expo(3); expo(2); expo(1); expo(0);
    pv(3); pv(2); pv(1); pv(0);
    swp32(m[0], m[2]); swp32(m[1], m[3]); u = m[0] + m[2]; w = m[1] + m[3];
    swp16(u, w); u = __builtin_amdgcn_rcpf(u + w);
    xch16(u, u, w); xch32(u, m[0], m[2]); xch32(w, m[1], m[3]);
    outp(3); outp(2); outp(1); outp(0);
  }
}

extern "C" __global__ __launch_bounds__(512, 2) void mega_kernel(const u16* x0, const u16* wqkv, const u16* wo, const u16* w1, const u16* w2, const u16* b1, const u16* b2, u16* out, char* ws) {
  const int wid0 = __builtin_amdgcn_readfirstlane(threadIdx.x >> 6);
  asm volatile("s_getpc_b64 s[40:41]\n\ts_andn2_b32 s40, s40, 63\n\ts_load_dwordx2 s[100:101], s[40:41], %0 offset:124\n\ts_load_dwordx2 s[100:101], s[40:41], %0 offset:252"
               :: "s"(__builtin_amdgcn_readfirstlane(threadIdx.x) << 2) : "s40", "s41", "s100", "s101", "scc");
  sched0();
#define PHASE_VARS \
      int tid = threadIdx.x, bid = blockIdx.x, wid = wid0; \
      asm volatile("" : "+v"(tid)); asm volatile("" : "+s"(bid), "+s"(wid), "+s"(ws)); \
      const int lw = wid - 4, lt = tid - 256, lane = tid & 63, fr = lane & 15, fq = lane >> 4, wp = wid & 1, wq = wid >> 1; \
      const unsigned j = (bid >> 3) * 4 + lw; \
      int hp, sb; tile_map<16, 16, 4, 8>(bid, hp, sb);            \
      int tp, tq; tile_map<8, 32, 4, 8>(bid, tp, tq);             \
      int fp, fq1; tile_map<28, 8, 7, 4>(bid, fp, fq1);           \
      const int cw = bid < 16 ? wid + 4 * (bid >> 3) : -1; \
      (void)cw; (void)j; (void)lt; (void)wp; (void)wq; (void)lw; (void)fr; (void)fq; (void)hp; (void)sb; (void)tp; (void)tq; (void)fp; (void)fq1;
#define LOADER_VARS PHASE_VARS asm volatile("" : "+s"(wqkv), "+s"(wo), "+s"(w1), "+s"(w2));

  if (__builtin_expect(wid0 >= 4, 1)) {
    if (LPR) __builtin_amdgcn_s_setprio(3);
    const u16* xin = x0;
    Pfc pf(wo, w1, blockIdx.x * 4 + wid0 - 4);
#pragma unroll 1
    for (int l = 0; l < NL; ++l) {
      {
        LOADER_VARS
        const int s0 = sb * 128;
        GQ::Loader L(wqkv + hp * 64, xin, lw, lane, qkv_rot(bid));
        if (l == 0) L.pre_w1<0>();
        L.seta((unsigned)(s0 * D * 2), halo_off(s0, lw, lane), lw, lane);
        if (l == 0) { L.issue_a<0>(L.ab + L.kord(0) * 128); if (!SI) L.pre<1, true, true>(); }
        sched0();
        const rsrc_t rs = mkrsrc(ws + WS_ST, MB);
        f32x4 c = {0.f, 0.f, 0.f, 0.f}; f32x2 s1 = {0.f, 0.f}, s2 = {0.f, 0.f}, o = {0.f, 0.f}; float pv = 0.f;
        bf16x8 ones = {0x3F80, 0x3F80, 0x3F80, 0x3F80, 0x3F80, 0x3F80, 0x3F80, 0x3F80};
        const int ct = lw & 1 ? 8 + (sb & 3) : sb & 7; const bool cs = l == 0 && lw >= 2;
        int tpf = (slow_res(wqkv) >> 2) != (bid & 1) ? 0 : lw < 2 ? 10 : 9;
        unsigned ra = GQ::AOFF + ((hp & 3) * 32 + lw * 8) * 128 + lane * 16, rb = rd_km(lane, ct);
        unsigned so = lane & 7 ? MB : ph((unsigned)(((l * S + s0 + (hp & 3) * 32 + lw * 8 + (lane >> 3)) * 4 + (hp >> 2)) * 16));
        unsigned co = fr >= 2 || !cs ? MB : ph((unsigned)(ST_CS + ((hp * 96 + ct * 8 + fq * 2 + fr) * 2 + (sb >> 3)) * 16));
        asm volatile("" : "+v"(ra), "+v"(rb), "+v"(so), "+v"(co), "+v"(c), "+v"(s1), "+v"(s2), "+v"(ones), "+v"(o));
        asm volatile("" : "+s"(tpf));
        if (SI && l == 0) L.pre_w1<1>();
        sched0();
        unsigned to[3], xo = x_off(tp, tq, lw, fr, fq), q2 = ((bid & 2) * 4 + slow_res(wqkv)) * 128 + ((bid >> 3) * 2 + (lw & 1)) * (3 * D * 2);
#pragma unroll
        for (int i = 0; i < 3; ++i) {
          const int r = s0 - 16 + (i == 2 && lw ? 64 : i * 64) + (lt >> 2);
          to[i] = ph((unsigned)(((l * S + (r < 0 ? 0 : r)) * 4 + (lt & 3)) * 16));
        }
        unsigned ta = (lt & 3) < (lw ? 2 : 3) ? L_TAB + ((lt & 3) * 64 + (lt >> 2)) * 4 : L_SCR, ts = ph((unsigned)(ST_CS + (hp * 192 + (lw ? lt - 64 : lt)) * 16));
        asm volatile("" : "+v"(to[0]), "+v"(to[1]), "+v"(to[2]), "+v"(xo), "+v"(ta), "+v"(ts));
        asm volatile("" : "+s"(q2));
        if (SI == 1 && l == 0) L.pre<1, false, true>();
        sched0();
        const GW::Loader W(wo + tp * 128, ws, lw, lane);
        if (SI == 2 && l == 0) L.pre<1, false, true>();
        sched0();
        auto pfq = [&](int n) { const unsigned o = q2 + L.kord(n) * GQ::WSTEP; spf2<0>(wqkv, o); spf2<2048>(wqkv, o); spf2<4096>(wqkv, o); };
        if (l) {
          if (QBR == 3) bar();
          if (!(EW & 8)) L.pre_w();
          if (tpf) { pfq(2 + (lw >> 1)); pfq(4 + (lw >> 1)); }
          if (QBR == 4) bar();
          L.start();
        }
        if (LPR == 1) __builtin_amdgcn_s_setprio(0);
        unsigned tag, tg2; u32x4 gs, a[3];
        auto rowst = [&](int i) {
          const float m0 = __uint_as_float(a[i][0]), m = red4(m0) * 0.25f, q = red4(__uint_as_float(a[i][2]) + 256.f * (m0 - m) * (m0 - m));
          o = (f32x2){sel((lt & 3) == i, m, o.x), sel((lt & 3) == i, q, o.y)};
          asm volatile("" : "+v"(o));
        };
        for_c<0, 16 - GQ::H>([&](auto tc) {
          constexpr int t = tc.value;
          u32x4 v; unsigned tv;
          L.step<t, t == 5 || t == 6 ? 1 : t == 9 || t == 10 ? 4 : 0>([&](unsigned ls) {
            if (t < 4) v = *(const LAS u32x4*)(uintptr_t)(ls + ra);
            if (t == 3) tv = *(volatile LAS unsigned*)(uintptr_t)L_TAG;
          }, [&]() {
            if (t < 8 && cs) {
              constexpr int ls = 32768 + t % 3 * GQ::SB;
              bf16x4 x[4];
#pragma unroll
              for (int e = 0; e < 4; ++e) x[e] = lds_tr4(rb + (ls & ~0x7fff) + (ls & 0x7fff) + (e >> 1) * 4096 + (e & 1) * 512);
#pragma unroll
              for (int ks = 0; ks < 2; ++ks) c = mfma(__builtin_shufflevector(x[2 * ks], x[2 * ks + 1], 0, 1, 2, 3, 4, 5, 6, 7), ones, c);
            } else if (t >= 4) {
              if (t >= 10 || t >= tpf) { if (t < TC1) pf.go(); }
              else if (lw < 2) pfq(t + 4);
              else if (t == 8) { pfq(14); pfq(15); }
            }
            if (t < 4) {
              if (t == 0) { pv = bflo(v[0]); asm volatile("" : "+v"(pv)); }
#pragma unroll
              for (int e = 0; e < 4; ++e) {
                if (SSC) {
                  float x = bflo(v[e]), y = bfhi(v[e]), a0 = s1.x, a1 = s1.y, b0 = s2.x, b1 = s2.y;
                  asm volatile("v_sub_f32 %0, %0, %6\n\tv_sub_f32 %1, %1, %6\n\tv_add_f32 %2, %2, %0\n\tv_add_f32 %3, %3, %1\n\tv_fmac_f32 %4, %0, %0\n\tv_fmac_f32 %5, %1, %1"
                               : "+v"(x), "+v"(y), "+v"(a0), "+v"(a1), "+v"(b0), "+v"(b1) : "v"(pv));
                  s1 = (f32x2){a0, a1}; s2 = (f32x2){b0, b1};
                } else { const f32x2 d = (f32x2){bflo(v[e]), bfhi(v[e])} - pv; s1 += d; s2 += d * d; }
              }
              asm volatile("" : "+v"(s1), "+v"(s2));
            }
            if (t == 3) { tag = __builtin_amdgcn_readfirstlane(tv) + 1u; tg2 = tag2(tag); if (tpf == 10) pfq(7); if (l && tpf == 9) pfq(6); }
            if (t == 4) {
              const float d1 = (s1.x + s1.y) * (1.f / 32.f), ml = pv + d1, m = red8(ml) * 0.125f;
              const float q = red8(vmax((s2.x + s2.y) - 32.f * d1 * d1, 0.f) + 32.f * (ml - m) * (ml - m));
              gs = (u32x4){__float_as_uint(m), tag, __float_as_uint(q), tg2};
              asm volatile("" : "+v"(gs));
            }
            if (t == 8) { gs = (u32x4){__float_as_uint(sel(fr, c[2], c[0])), tag, __float_as_uint(sel(fr, c[3], c[1])), tg2}; asm volatile("" : "+v"(gs)); }
            if (t == (RB ? 12 : 11)) {
#pragma unroll
              for (int i = 0; i < 3; ++i) ld_ok(a[i]);
              for (int spin = 0;; ++spin) {
                unsigned bad = 0;
#pragma unroll
                for (int i = 0; i < 3; ++i) bad |= (a[i][1] ^ tag) | (a[i][3] ^ tg2);
                if (__builtin_expect(__all(bad == 0), 1) || spin_fail(spin, ws)) break;
                __builtin_amdgcn_s_sleep(8);
#pragma unroll
                for (int i = 0; i < 3; ++i) ld_again(a[i], rs, to[i]);
              }
              if (!RB) rowst(0);
            }
            if (t == 12 && !RB) { rowst(1); rowst(2); }
            if (t == 13 && RB == 2) rowst(0);
            if (t == 13 && !RB) { LAS float* p = (LAS float*)(uintptr_t)ta; p[0] = -o.x; p[144] = __builtin_amdgcn_rsqf(o.y * (1.f / D) + EPS); }
          }, [&]() {
            if (t == 4) st16(rs, so, gs);
            if (t == 8) {
              st16(rs, co, gs);
              sched0();
#pragma unroll
              for (int i = 0; i < 3; ++i) ld_raw(a[i], rs, to[i]);
            }
          });
        });
        if (l == 0) {
#pragma unroll
          for (int p = 0; p < 4; ++p) dma16((const char*)xin + (p >> 1) * 32768 + (p & 1) * 64, xo, L_X + lw * 4096 + p * 1024);
          ld_raw(gs, rs, ts);
          wait_vm<GQ::CNT + 5>();
        } else wait_vm<GQ::CNT>();
        bar();
        W.pre_w1<0>();
        if (RB) { if (RB == 1) rowst(0); rowst(1); rowst(2); LAS float* p = (LAS float*)(uintptr_t)ta; p[0] = -o.x; p[144] = __builtin_amdgcn_rsqf(o.y * (1.f / D) + EPS); }
        wait_lgkm0();
        if (l == 0) wait_vm<9>(); else wait_vm<4>();
        bar();
        W.pre_w1<1>(); W.pre_w1<2>();
        if (l == 0) {
          wait_vm<12>();
          ld_ok(gs);
          for (int spin = 0;; ++spin) {
            if (__builtin_expect(__all(((gs[1] ^ tag) | (gs[3] ^ tg2)) == 0), 1) || spin_fail(spin, ws)) break;
            __builtin_amdgcn_s_sleep(8);
            ld_again(gs, rs, ts);
          }
          float c0 = __uint_as_float(gs[0]), c1 = __uint_as_float(gs[2]);
          c0 += dppf<0xB1>(c0); c1 += dppf<0xB1>(c1);
          *(LAS float*)(uintptr_t)(L_CS + (lw ? lt - 64 : lt) * 4) = sel(lt & 1, c1, c0);
        }
        if (l == 0 && lw < TBA) { if (fr < 14) touch4(w1 + (size_t)(((bid >> 3) * TBA + lw) * 8 + (bid & 4)) * F + (bid & 3) * 896, fq * (F * 2) + fr * 128, L_SCR + lw * 256); }
        if (l == 0) { wait_lgkm0(); bar(); }
      }
      {
        LOADER_VARS
        GW::Loader L(wo + tp * 128, ws + WS_O + (size_t)tq * 32 * 8192, lw, lane);
        const G1::Loader W(w1 + fp * 128, ws, lw, lane);
        unsigned po = ((bid & 1) * 8 + (slow_res(wo) ^ (j & 1))) * 128 + (j >> 1) * (D * 2);
#pragma unroll
        for (int k = GW::H; k < 10; ++k) spf2(wo, po + k * GW::WSTEP);
        asm volatile("" : "+s"(po));
        if (!(LB & 1)) { sched0(); bar(); } else wait_lgkm0();
#pragma unroll
        for (int k = 10; k < 16; ++k) spf2(wo, po + k * GW::WSTEP);
#pragma unroll
        for (int r = 0; r < TLQ; ++r) { if (pf.pc < 3 + NW1) { wait_lgkm0(); pf.go(); } }
        if (LB & 1) { sched0(); bar(); }
        L.start();
        L.run(16 - GW::H, [&](int) { pf.go(); });
        pf.go();
        if (E2 && bid < 224) {
          wait_vm<2 * GW::CNT>(); bar(); W.pre_w1<1>();
          wait_vm<GW::CNT + 4>(); bar(); W.pre_w1<2>();
          wait_vm<8>(); bar();
          bar(); W.pre_w1<0>();
        } else { L.tail<GW::H - 1>(); bar(); }
      }
      {
        LOADER_VARS
        if (bid < 224) {
          G1::Loader L(w1 + fp * 128, ws + WS_H + (size_t)fq1 * 64 * 16384, lw, lane);
          if (!E2) L.pre_w();
          const G2A::Loader W((const char*)(w2 + (tq & 23) * 64) + (tq & 8 ? JA * G2::WSTEP : 0), ws, lw, lane);
          const int g0 = (bid & 3) * 14, ga = (g0 + ((slow_res(w1) - g0) & 7)) ^ (j & 1), gb = ga + 8 < g0 + 14 ? ga + 8 : ga;
          const unsigned j2 = j < 32 ? 112 + (j & 15) : j;
          unsigned pa = (j >> 1) * (F * 2) + ga * 128, pb = (j >> 1) * (F * 2) + gb * 128, pd = (j2 >> 1) * (F * 2) + (j & 16 ? gb : ga) * 128;
          asm volatile("" : "+s"(pa), "+s"(pb), "+s"(pd));
          auto pfb = [&](int k) { spf2(w1, pa + k * G1::WSTEP); spf2(w1, pb + k * G1::WSTEP); spf2(w1, pd + k * G1::WSTEP); };
#pragma unroll
          for (int k = G1::H; k < G1::H + PA_1; ++k) pfb(k);
#pragma unroll
          for (int r = 0; r < TLW; ++r) { if (pf.pc < 3 + NW1) { wait_lgkm0(); pf.go(); } }
          sched0();
          bar();
          if (!(LB & 2)) pf.go();
          L.start();
          L.run(32 - G1::H, [&](int t) {
            if (pf.pc < 3 + NW1) pf.go();
            else if (t < 32 - G1::H - PA_1) pfb(t + G1::H + PA_1);
          });
          if (EW & 4) {
            wait_vm<2 * G1::CNT>(); bar(); W.pre_w1<1>();
            wait_vm<G1::CNT + 4>(); bar();
            wait_vm<4>(); bar(); W.pre_w1<0>();
          } else L.tail<G1::H - 1>();
          bar();
        } else {
          wait_lgkm0(); pf.go();
          bar();
          if (l == 0) {
            __builtin_amdgcn_s_sleep(24);
            while (!SLS && pf.pc < 3 + NW1) { wait_lgkm0(); pf.go(); }
            spare_touch(w1, w2, bid, wid, lane, [&]() { if (SLS) { wait_lgkm0(); pf.go(); } });
          }
        }
        pf.pc = 99;
      }
      {
        LOADER_VARS
        constexpr int NB = F / 64 - 2 * JA;
        const char* ub = ws + WS_U + (size_t)tp * 112 * 16384;
        G2A::Loader A((const char*)(w2 + (tq & 23) * 64) + (tq & 8 ? JA * G2::WSTEP : 0), ub + (tq & 8 ? JA * 32768 : 0), lw, lane);
        if (!(EW & 4) || bid >= 224) A.pre_w();
        const GQ::Loader W(wqkv + hp * 64, ws, lw, lane, qkv_rot(bid));
        unsigned pb = ((bid & 4) * 4 + (j & 1) * 8 + slow_res(w2)) * 128 + (j >> 1) * (S * 2) + JA * G2::WSTEP, pa = pb - (bid & 2 ? 0 : JA * G2::WSTEP);
        asm volatile("" : "+s"(pa), "+s"(pb));
        auto side = [&](int n) { n += G2A::H + PA_2; if (n < JA + NB) spf2(w2, (n < JA ? pa : pb) + n * G2::WSTEP); };
#pragma unroll
        for (int k = 0; k < PA_2; ++k) side(k - PA_2);
        G2::Loader L((const char*)(w2 + tq * 64) + 2 * JA * G2::WSTEP, ub + 2 * JA * 32768, lw, lane);
        gu32* xf = (gu32*)(ws + WS_MAIL) + (l * 4 + 2) * MEDGE + (tq * 8 + tp) * 64 + 28 + lw;
        const char* xp = ws + WS_XP + (unsigned)(((bid ^ 2) * 4 + lw) << 14);
        unsigned fl = L_QKV + lw * 256, xv = ph(lane * 16);
        asm volatile("" : "+s"(xf), "+s"(xp), "+s"(fl)); asm volatile("" : "+v"(xv));
        sched0();
        bar();
        A.start();
        A.run(JA - G2A::H, [&](int t) { side(t); });
        auto stage = [&](auto s) { L.issue_w<s.value * G2::SB>(L.w + s.value * G2::WSTEP); L.issue_a<s.value * G2::SB>(L.ab + s.value * 32768); sched0(); };
        wait_vm<G2A::CNT>(); bar(); stage(IC<0>{}); side(JA - 2);
        wait_vm<G2::CNT>(); bar(); stage(IC<1>{}); side(JA - 1);
        wait_vm<G2::CNT>(); bar(); stage(IC<2>{}); stage(IC<3>{}); side(JA);
        L.t = 1; L.islot = 0;
        L.run(NB - G2::H, [&](int t) {
          if (t == NB - G2::H - FLS) asm volatile("s_mov_b32 m0, %2\n\ts_nop 0\n\tglobal_load_lds_dword %0, %1 sc1" :: "v"(0u), "s"(xf), "s"(fl) : "memory");
          side(JA + t);
        });
        auto xdma = [&]() {
#pragma unroll
          for (int i = 0; i < 4; ++i) dma16(xp + i * 2048, xv, L_X + lw * 4096 + i * 1024);
          sched0();
        };
        sched0(); wait_vm<2 * G2::CNT>(); bar();
        if (__builtin_amdgcn_readfirstlane(*(volatile LAS unsigned*)(uintptr_t)fl)) {
          xdma();
          if ((EW & 8) && l == 0) { W.pre_w1<0>(); wait_vm<G2::CNT + 10>(); bar(); W.pre_w1<1>(); wait_vm<16>(); bar(); wait_vm<12>(); }
          else { wait_vm<G2::CNT + 4>(); bar(); wait_vm<4>(); bar(); wait_vm<0>(); }
        } else {
          wait_vm<G2::CNT>(); bar(); wait_vm<0>(); bar();
          for (int spin = 0;; ++spin) {
            if (__builtin_amdgcn_readfirstlane(__hip_atomic_load(xf, RLX_AGENT)) || spin_fail(spin, ws)) break;
            __builtin_amdgcn_s_sleep(2);
          }
          xdma();
          if ((EW & 8) && l == 0) { W.pre_w(); wait_vm<12>(); } else wait_vm<0>();
        }
        bar();
        xin = (const u16*)(ws + WS_X2);
        if (NL == 1) bar();
      }
    }
    return;
  }

  {
    PHASE_VARS
    if (cw >= 0) code_warm(cw, lane, L_SCR + wid * 256);
    pfb_q(wqkv, bid, (bid >> 3) * 4 + wid, 0); pfb_q(wqkv, bid, (bid >> 3) * 4 + wid, 1);
    if (wid < 3) {
      if (tid == 0) asm volatile("s_mov_b32 m0, %2\n\ts_nop 0\n\tglobal_load_lds_dword %0, %1 sc1" :: "v"(WS_EPOCH), "s"(ws), "s"(L_TAG) : "memory");
      const rsrc_t rz = mkrsrc(ws + WS_XCH, 3 * MB);
      if (tid < 128) st16(rz, ph((((tid >> 6) * 256 + tq * 8 + tp) * 64 + lane) * 16), (u32x4){0u, SENT, 0u, SENT});
      else {
        const int e = (tid >> 3) & 7, line = e == 0 || e == 7 ? (2 * sb + (e & 1)) * 8 + (hp & 7) : e * 256 + bid;
        st16(rz, WS_MAIL - WS_XCH + line * 256 + (tid & 7) * 16, (u32x4){0u, 0u, 0u, 0u});
      }
      if (wid == 1 && bid < 224) touch4(b1 + fp * 128, lane * 4, L_BIAS);
      if (wid == 2) touch4(b2 + tq * 64, (lane & 31) * 4, L_BIAS + 256);
      if (wid == 0) *(LAS u32x4*)(uintptr_t)(L_TAG + 16) = (u32x4){0u, 0u, 0u, 0u};
    }
    if (CPR) __builtin_amdgcn_s_setprio(CPR);
  }
#pragma unroll 1
  for (int l = 0; l < NL; ++l) {
    {
      PHASE_VARS
      if (wid & 2) qkv_attn<1>(ws, l, wp, lane, hp, sb, cw, bid, tp, tq, wqkv);
      else qkv_attn<0>(ws, l, wp, lane, hp, sb, cw, bid, tp, tq, wqkv);
    }
    {
      PHASE_VARS
      GW::Comp<O42<false, WNE>> g; g.setup(wp, wq, lane);
      g.setwin();
      const rsrc_t rx = mkrsrc(ws + WS_XCH + l * (MB / 2), MB / 2), rh = mkrsrc(ws + WS_H, S * D * 4);
      unsigned xa = L_X + wid * 4096 + lane * 16;
      unsigned xs = fq != fr >> 2 ? MB : ph((unsigned)(((tq * 8 + tp) * 64 + wp * 32 + wq * 16 + fr) * 16)), xl = ph((unsigned)((tq * 512 + fq * 128 + wq * 16 + fr) * 16));
      const unsigned h0 = ph((unsigned)(((tp * 32 + tq) << 14) + wp * 8192 + (wq * 32 + fr) * 128 + ((pair_chunk(fq) ^ (km_f(fr) << 1)) << 4)));
      u32x4 ho = {h0, h0 ^ 64, h0 + 4096, (h0 ^ 64) + 4096};
      bf16x8 ones = {0x3F80, 0x3F80, 0x3F80, 0x3F80, 0x3F80, 0x3F80, 0x3F80, 0x3F80};
      asm volatile("" : "+v"(xa), "+v"(xs), "+v"(xl), "+v"(ho), "+v"(ones));
      Gate gt;
      auto edge = [&]() {
        sync_post(ws, l * 4, lane < 16 ? (sb * 16 + lane) * 64 + hp : -1, wid);
        gt.wait<0>(ws, l * 4, tq * 8 + tp, 0xffffu, (1u << GW::H) - 1, (1u << (GW::H + GLW)) - 1, wid);
        sched0();
      };
      if (WSP == 3) edge();
      f32x4 acc[4][2];
#pragma unroll
      for (int i = 0; i < 4; ++i)
#pragma unroll
        for (int jj = 0; jj < 2; ++jj) acc[i][jj] = (f32x4){0.f, 0.f, 0.f, 0.f};
      { bf16x8 s[2]; sel_frags(true, fr, fq, s); tile_add(acc, s, xa); }
      g.pin(acc);
      if (WSP == 2) edge(); else mfma_gap();
      g.run_steps<false>(acc, [&](int it, int i) {
        if (i < 0) gt.arm(it, cw);
        if (i == 0) gt.head(it, cw, lane, wid);
        if (i == GLW - 1) gt.late(ws);
      });
      bar();
      sched0();
      g.pend<0, 4>(acc);
      u32x4 pk[2][2]; float mp[2];
      {
        f32x4 sm[2], gm[2];
#pragma unroll
        for (int i = 0; i < 2; ++i)
#pragma unroll
          for (int jj = 0; jj < 2; ++jj) {
            pk[jj][i] = cat4(pk4(acc[2 * i][jj]), pk4(acc[2 * i + 1][jj])); asm volatile("" : "+v"(pk[jj][i]));
            const bf16x8 x = __builtin_bit_cast(bf16x8, pk[jj][i]);
            gm[jj] = mfma(x, x, i ? gm[jj] : (f32x4){0.f, 0.f, 0.f, 0.f}); sm[jj] = mfma(ones, x, i ? sm[jj] : (f32x4){0.f, 0.f, 0.f, 0.f});
          }
        u32x4 st;
#pragma unroll
        for (int jj = 0; jj < 2; ++jj) {
          const f32x4 gv = gm[jj];
          st[2 * jj] = __float_as_uint(sm[jj][0]); st[2 * jj + 1] = __float_as_uint(sel(fr & 2, sel(fr & 1, gv[3], gv[2]), sel(fr & 1, gv[1], gv[0])));
          mp[jj] = sm[jj][0] * (1.f / 64.f);
        }
        st16(rx, xs, st);
      }
      sched0();
      f32x4 gr[4];
      auto gld = [&]() {
        asm volatile("" ::: "memory");
#pragma unroll
        for (int p = 0; p < 4; ++p) gr[p] = __builtin_bit_cast(f32x4, __builtin_amdgcn_raw_buffer_load_b128(rx, xl + p * 1024, 0, 16));
        sched0();
      };
      if (XE == 1) gld();
#pragma unroll
      for (int p = 0; p < 4; ++p) *(LAS u32x4*)(uintptr_t)(xa + p * 1024) = pk[p >> 1][p & 1];
      asm volatile("" : "+v"(mp[0]), "+v"(mp[1]));
#pragma unroll
      for (int jj = 0; jj < 2; ++jj)
#pragma unroll
        for (int i = 0; i < 2; ++i) {
          const auto rx_ = __builtin_amdgcn_permlane16_swap(pk[jj][i][0], pk[jj][i][2], false, false), ry_ = __builtin_amdgcn_permlane16_swap(pk[jj][i][1], pk[jj][i][3], false, false);
          const unsigned x0 = rx_[0], x1 = rx_[1], y0 = ry_[0], y1 = ry_[1];
          acc[2 * i][jj] = (f32x4){bflo(x0), bfhi(x0), bflo(y0), bfhi(y0)} - mp[jj]; acc[2 * i + 1][jj] = (f32x4){bflo(x1), bfhi(x1), bflo(y1), bfhi(y1)} - mp[jj];
          asm volatile("" : "+v"(acc[2 * i][jj]), "+v"(acc[2 * i + 1][jj]));
          if (XE == 2 + jj * 2 + i) { sched0(); gld(); }
        }
      if (!XE) { if (XD) __builtin_amdgcn_s_sleep(XD); gld(); }
      for (int spin = 0;; ++spin) {
        const float lo = vmin3(vmin3(gr[0][1], gr[0][3], gr[1][1]), vmin3(gr[1][3], gr[2][1], gr[2][3]), vmin3(gr[3][1], gr[3][3], gr[3][3]));
        if (!__any(lo < 0.f) || spin_fail(spin, ws)) break;
        if (XS) __builtin_amdgcn_s_sleep(XS);
        gld();
      }
      {
        const f32x4 t = (gr[0] + gr[1]) + (gr[2] + gr[3]);
        float m[2] = {t[0], t[2]}, q[2] = {t[1], t[3]}, u, w, v, y;
        swp32(m[0], m[1]); swp32(q[0], q[1]);
        xch16(m[0] + m[1], u, w); xch16(q[0] + q[1], v, y);
        const float mean = (u + w) * (1.f / D), var = __builtin_fmaxf((v + y) * (1.f / D) - mean * mean, 0.f), r1 = __builtin_amdgcn_rsqf(var + EPS);
        xch32(mean, m[0], m[1]);
        xch32(r1 * __builtin_amdgcn_rsqf(var * r1 * r1 + EPS), q[0], q[1]);
#pragma unroll
        for (int jj = 0; jj < 2; ++jj) {
          const float c = (mp[jj] - m[jj]) * q[jj];
#pragma unroll
          for (int i = 0; i < 2; ++i) st16(rh, ho[jj * 2 + i], cat4(pk4(acc[2 * i][jj] * q[jj] + c), pk4(acc[2 * i + 1][jj] * q[jj] + c)));
        }
      }
    }
    {
      PHASE_VARS
      const int post = lane < 28 ? (tp * 28 + lane) * 64 + tq : -1;
      if (bid >= 224) {
        sync_post(ws, l * 4 + 1, post, wid);
        if (l == 0 && bid == 255 && wid == 0) { const unsigned tag = get_tag(); if (lane == 0) __hip_atomic_store((gu32*)(ws + WS_EPOCH), tag, RLX_AGENT); }
        if (l == 0) { __builtin_amdgcn_s_sleep(24); spare_touch(w1, w2, bid, wid, lane, []() {}); }
      } else {
        G1::Comp<O44<false, 32>> g; g.setup(wp, wq, lane);
        f32x4 acc[4][4];
#pragma unroll
        for (int i = 0; i < 4; ++i) { const u32x2 b = *(const LAS u32x2*)(uintptr_t)(L_BIAS + (wp * 4 + i) * 32 + fq * 8);
#pragma unroll
          for (int jj = 0; jj < 4; ++jj) acc[i][jj] = (f32x4){bflo(b.x), bfhi(b.x), bflo(b.y), bfhi(b.y)}; }
        const rsrc_t ro = mkrsrc(ws + WS_U, D * F * 4);
        const unsigned u0 = ph((unsigned)(((fq1 * 56 + fp * 2 + wp) << 14) + (wq * 64 + fr) * 128 + ((pair_chunk(fq) ^ (fr >> 1)) << 4)));
        u32x4 uo = {u0, u0 + 4096, u0 + 8192, u0 + 12288}, uo2 = uo ^ 64;
        asm volatile("" : "+v"(uo), "+v"(uo2));
        g.prep(acc);
        sync_post(ws, l * 4 + 1, post, wid);
        Gate gt; gt.wait<SW0>(ws, l * 4 + 1, fq1 * 28 + fp, ~0u, (1u << G1::H) - 1, (1u << (G1::H + GL1)) - 1, wid);
        g.run_steps<false>(acc, [&](int it, int i) {
          if (i < 0) gt.arm(it, cw);
        if (i == 0) gt.head(it, cw, lane, wid);
          if (i == GL1 - 1) gt.late(ws);
        });
        bar();
        sched0();
        if (GSH) {
          for_c<0, 2>([&](auto jc) {
            constexpr int jj = jc.value;
            f32x4 a[2];
#pragma unroll
            for (int i = 0; i < 2; ++i) { a[i] = gelu_arg(acc[i][jj]); asm volatile("" : "+v"(a[i])); }
            sched0();
            for_c<0, 4>([&](auto ec) { constexpr int e = ec.value; g.mm<jj * 8 + e>(acc); sched0(); a[0][e] = __builtin_amdgcn_exp2f(a[0][e]); a[1][e] = __builtin_amdgcn_exp2f(a[1][e]); asm volatile("" : "+v"(a[0]), "+v"(a[1])); sched0(); });
            a[0] += 1.f; a[1] += 1.f; asm volatile("" : "+v"(a[0]), "+v"(a[1]));
            sched0();
            for_c<0, 4>([&](auto ec) { constexpr int e = ec.value; g.mm<jj * 8 + 4 + e>(acc); sched0(); a[0][e] = __builtin_amdgcn_rcpf(a[0][e]); a[1][e] = __builtin_amdgcn_rcpf(a[1][e]); asm volatile("" : "+v"(a[0]), "+v"(a[1])); sched0(); });
            st16(ro, uo[jj], pack_pair(acc[0][jj] * a[0], acc[1][jj] * a[1]));
          });
        } else g.pend<0, 8>(acc);
#pragma unroll
        for (int n = GSH ? 2 : 0; n < 8; ++n) { const int i = n >> 2, jj = n & 3; st16(ro, (i ? uo2 : uo)[jj], pack_pair(gelu_tanh(acc[2 * i][jj]), gelu_tanh(acc[2 * i + 1][jj]))); }
      }
    }
    {
      PHASE_VARS
      asm volatile("" : "+s"(out));
      G2A::Comp<O44<true, ANE>> ga; ga.setup(wp, wq, lane);
#pragma unroll
      for (int jj = 0; jj < 4; ++jj) ga.qrd[0][jj] = 65536 + rd_km(lane, wq * 2 + (jj & 1)) + (((jj >> 1) ^ (tq >> 3)) & 1) * 8192;
      G2::Comp<O42<true, BNE>> g; g.setup(wp, wq, lane);
      g.setwin();
      ga.setwin();
      Gate gt;
      auto edge = [&]() {
        sync_post(ws, l * 4 + 2, bid < 224 && lane < 32 ? (lane * 8 + fq1) * 64 + fp : -1, wid);
        gt.wait<0>(ws, l * 4 + 2, tq * 8 + tp, 0xfffffffu, tq & 8 ? 1u << JA / 2 : 1u, tq & 8 ? 7u << JA / 2 : 7u, wid);
        sched0();
      };
      if (GSP == 3) edge();
      f32x4 ac4[4][4], acc[4][2];
#pragma unroll
      for (int jj = 0; jj < 2; ++jj) { const float b = bf2f(*(const LAS u16*)(uintptr_t)(L_BIAS + 256 + (wq * 2 + jj) * 32 + fr * 2));
#pragma unroll
        for (int i = 0; i < 4; ++i) { ac4[i][jj] = (f32x4){b, b, b, b}; ac4[i][jj + 2] = (f32x4){0.f, 0.f, 0.f, 0.f}; } }
      unsigned xa = L_X + wid * 4096 + lane * 16, xo = (unsigned)((bid * 4 + wid) << 14) + ph(lane * 16), oo = x_off(tp, tq, wid, fr, fq), xo2 = xo + 4096, oo2 = oo + 32768;
      unsigned fo = lane ? MB : (((tq ^ 8) * 8 + tp) * 64 + 28 + wid) * 4u, one = 1u;
      bf16x8 s[2]; sel_frags(false, fr, fq, s);
      if (GSP == 4) { asm volatile("" : "+v"(xa), "+v"(xo), "+v"(oo), "+v"(fo), "+v"(s[0]), "+v"(s[1])); edge(); }
      tile_add(ac4, s, xa);
      const rsrc_t rx = mkrsrc(ws + WS_XP, 16 * MB), rf = mkrsrc(ws + WS_MAIL + (l * 4 + 2) * (MEDGE * 4), MEDGE * 4), ro = mkrsrc(l ? (const void*)out : (const void*)(ws + WS_X2), S * D * 2);
      asm volatile("" : "+v"(xa), "+v"(xo), "+v"(oo), "+v"(xo2), "+v"(oo2), "+v"(fo), "+v"(one), "+v"(s[0]), "+v"(s[1]));
      ga.pin(ac4);
      static_assert(JA % 2 == 0 && G2A::H == 2, "");
      if (GSP == 2) edge(); else mfma_gap();
      ga.run_steps<false>(ac4, [&](int it, int i) {
        if (i < 0) gt.arm(it, cw);
        if (i == 0) gt.head(it, cw, lane, wid);
        if (i == 2) gt.late(ws);
      });
      ga.pend<4, 8>(ac4);
#pragma unroll
      for (int i = 0; i < 2; ++i)
#pragma unroll
        for (int jj = 0; jj < 2; ++jj) st16(rx, (jj ? xo2 : xo) + i * 2048, cat4(pk4(ac4[2 * i][jj + 2]), pk4(ac4[2 * i + 1][jj + 2])));
#pragma unroll
      for (int i = 0; i < 4; ++i) { acc[i][0] = ac4[i][0]; acc[i][1] = ac4[i][1]; }
#pragma unroll
      for (int k = 0; k < 2; ++k) { g.fp[2][k] = ga.fp[2][k]; g.fp[3][k] = ga.fp[3][k]; g.fq[0][k] = ga.fq[0][k]; g.fq[1][k] = ga.fq[1][k]; }
      sched0();
      g.run_steps<true>(acc, [&](int it, int i) {
        if (i == PS % 4 && __builtin_expect(it == (F / 64 - 2 * JA) / 4 - PS / 4, 0)) {
          wait_vm<0>();
          __builtin_amdgcn_raw_buffer_store_b32(one, rf, fo, 0, 16);
        }
      });
      bar();
      sched0();
      g.pend<0, 4>(acc);
      tile_add(acc, s, xa);
      u32x4 xr[4];
#pragma unroll
      for (int i = 0; i < 2; ++i)
#pragma unroll
        for (int jj = 0; jj < 2; ++jj) { xr[jj * 2 + i] = pack_pair(acc[2 * i][jj], acc[2 * i + 1][jj]); st16(ro, (jj ? oo2 : oo) + i * 64, xr[jj * 2 + i]); }
      if (l == 0) {
#pragma unroll
        for (int p = 0; p < 4; ++p) *(LAS u32x4*)(uintptr_t)(xa + p * 1024) = xr[p];
        if (NL == 1) sync_post(ws, 3, -1, wid);
      }
    }
  }
}

extern "C" void kernel_launch(void* const* d_in, const int* in_sizes, int n_in,
                              void* d_out, int out_size, void* d_ws, size_t ws_size,
                              hipStream_t stream) {
  (void)hipFuncSetAttribute((const void*)mega_kernel, hipFuncAttributeMaxDynamicSharedMemorySize, 163840);
  mega_kernel<<<256, 512, 163840, stream>>>((const u16*)d_in[0], (const u16*)d_in[1], (const u16*)d_in[2], (const u16*)d_in[4], (const u16*)d_in[6], (const u16*)d_in[5], (const u16*)d_in[7], (u16*)d_out, (char*)d_ws);
}
```

```cpp
#include <hip/hip_runtime.h>
#include <hip/hip_bf16.h>
#include <stdint.h>

typedef unsigned short u16;
typedef unsigned long long u64;
typedef short bf16x8 __attribute__((ext_vector_type(8)));
typedef short bf16x4 __attribute__((ext_vector_type(4)));
typedef float f32x4 __attribute__((ext_vector_type(4)));
typedef float f32x2 __attribute__((ext_vector_type(2)));
typedef unsigned u32x4 __attribute__((ext_vector_type(4)));
typedef unsigned u32x2 __attribute__((ext_vector_type(2)));
typedef __bf16 bf16v2 __attribute__((ext_vector_type(2)));
#define LAS __attribute__((address_space(3)))
#define GAS __attribute__((address_space(1)))
#define DEVI __device__ __forceinline__
typedef __attribute__((address_space(1))) unsigned gu32;
typedef __attribute__((address_space(1))) unsigned long long gu64;

constexpr int S = 2048, D = 1024, F = 3584;
constexpr float EPS = 1e-5f;

DEVI float bf2f(u16 v) { return __uint_as_float(((unsigned)v) << 16); }
DEVI float bflo(unsigned v) { return __uint_as_float(v << 16); }
DEVI float bfhi(unsigned v) { return __uint_as_float(v & 0xffff0000u); }
DEVI unsigned pk2(float lo, float hi) { bf16v2 p; p.x = (__bf16)lo; p.y = (__bf16)hi; return __builtin_bit_cast(unsigned, p); }

DEVI const void* uni_ptr(const void* p) {
  const u64 v = (u64)p;
  return (const void*)(((u64)(unsigned)__builtin_amdgcn_readfirstlane((unsigned)(v >> 32)) << 32) | (unsigned)__builtin_amdgcn_readfirstlane((unsigned)v));
}
DEVI void dma16(const void* sbase, unsigned voff, unsigned ldsaddr) {
  ldsaddr = __builtin_amdgcn_readfirstlane(ldsaddr);
  asm volatile("s_mov_b32 m0, %2\n\ts_nop 0\n\tglobal_load_lds_dwordx4 %0, %1 sc1" :: "v"(voff), "s"(sbase), "s"(ldsaddr) : "memory");
}
template <int N, int IMM> DEVI void dma_grp(const void* sbase, const unsigned* v, unsigned lb) {
  static_assert(N == 1 || N == 2 || N == 4, "");
  if constexpr (N == 4)
    asm volatile("s_add_u32 m0, %1, %2\n\ts_nop 0\n\tglobal_load_lds_dwordx4 %3, %0 sc1\n\tglobal_load_lds_dwordx4 %4, %0 offset:1024 sc1\n\t"
                 "global_load_lds_dwordx4 %5, %0 offset:2048 sc1\n\tglobal_load_lds_dwordx4 %6, %0 offset:3072 sc1"
                 :: "s"(sbase), "s"(lb), "i"(IMM), "v"(v[0]), "v"(v[1]), "v"(v[2]), "v"(v[3]) : "memory", "scc");
  else if constexpr (N == 2)
    asm volatile("s_add_u32 m0, %1, %2\n\ts_nop 0\n\tglobal_load_lds_dwordx4 %3, %0 sc1\n\tglobal_load_lds_dwordx4 %4, %0 offset:1024 sc1"
                 :: "s"(sbase), "s"(lb), "i"(IMM), "v"(v[0]), "v"(v[1]) : "memory", "scc");
  else
    asm volatile("s_add_u32 m0, %1, %2\n\ts_nop 0\n\tglobal_load_lds_dwordx4 %3, %0 sc1" :: "s"(sbase), "s"(lb), "i"(IMM), "v"(v[0]) : "memory", "scc");
}
template <int N> DEVI void wait_vm() { asm volatile("s_waitcnt vmcnt(%0)" :: "i"(N) : "memory"); }
DEVI void wait_lgkm0() { asm volatile("" ::: "memory"); __builtin_amdgcn_s_waitcnt(0xC07F); asm volatile("" ::: "memory"); }
DEVI void mfma_ip(f32x4& c, const bf16x8& a, const bf16x8& b) { asm volatile("v_mfma_f32_16x16x32_bf16 %0, %1, %2, %0" : "+v"(c) : "v"(a), "v"(b)); }
DEVI void mfma_gap() { __builtin_amdgcn_sched_barrier(0); asm volatile("s_nop 7"); __builtin_amdgcn_sched_barrier(0); }
DEVI void bar() { asm volatile("" ::: "memory"); __builtin_amdgcn_s_barrier(); asm volatile("" ::: "memory"); }
DEVI void sched0() { __builtin_amdgcn_sched_barrier(0); }

DEVI bf16x8 lds_b128(unsigned addr) { return *(const LAS bf16x8*)(uintptr_t)addr; }
DEVI bf16x4 lds_tr4(unsigned addr) { return __builtin_amdgcn_ds_read_tr16_b64_v4i16((LAS bf16x4*)(uintptr_t)addr); }

typedef __amdgpu_buffer_rsrc_t rsrc_t;
DEVI rsrc_t mkrsrc(const void* p, unsigned bytes) { return __builtin_amdgcn_make_buffer_rsrc((void*)p, 0, bytes, 0x00020000); }
DEVI void st16(rsrc_t r, unsigned off, u32x4 v) { __builtin_amdgcn_raw_buffer_store_b128(v, r, off, 0, 16); }
DEVI void ld_raw(u32x4& v, rsrc_t r, unsigned off) { asm volatile("buffer_load_dwordx4 %0, %1, %2, 0 offen sc1 ; raw load" : "=&v"(v) : "v"(off), "s"(r) : "memory"); }
DEVI void ld_again(u32x4& v, rsrc_t r, unsigned off) { asm volatile("buffer_load_dwordx4 %0, %1, %2, 0 offen sc1\n\ts_waitcnt vmcnt(0)" : "=&v"(v) : "v"(off), "s"(r) : "memory"); }
DEVI void ld_ok(u32x4& v) { asm volatile("; raw ok %0" : "+v"(v)); }
#define RLX_AGENT __ATOMIC_RELAXED, __HIP_MEMORY_SCOPE_AGENT

template <int N> DEVI float ror16(float v) { return __int_as_float(__builtin_amdgcn_update_dpp(0, __float_as_int(v), 0x120 + N, 0xf, 0xf, false)); }
DEVI float red16(float v) { v += ror16<8>(v); v += ror16<4>(v); v += ror16<2>(v); v += ror16<1>(v); return v; }
DEVI void xch32(float v, float& a, float& b) { unsigned u = __float_as_uint(v); auto r = __builtin_amdgcn_permlane32_swap(u, u, false, false); unsigned x = r[0], y = r[1]; a = __uint_as_float(x); b = __uint_as_float(y); }
DEVI void xch16(float v, float& a, float& b) { unsigned u = __float_as_uint(v); auto r = __builtin_amdgcn_permlane16_swap(u, u, false, false); unsigned x = r[0], y = r[1]; a = __uint_as_float(x); b = __uint_as_float(y); }
DEVI void swp32(float& a, float& b) { auto r = __builtin_amdgcn_permlane32_swap(__float_as_uint(a), __float_as_uint(b), false, false); unsigned x = r[0], y = r[1]; a = __uint_as_float(x); b = __uint_as_float(y); }
DEVI void swp16(float& a, float& b) { auto r = __builtin_amdgcn_permlane16_swap(__float_as_uint(a), __float_as_uint(b), false, false); unsigned x = r[0], y = r[1]; a = __uint_as_float(x); b = __uint_as_float(y); }
DEVI float colsum4(float v) { float a, b; xch32(v, a, b); v = a + b; xch16(v, a, b); return a + b; }
DEVI float vmax(float a, float b) { float r; asm("v_max_f32 %0, %1, %2" : "=v"(r) : "v"(a), "v"(b)); return r; }
DEVI float vmin3(float a, float b, float c) { float r; asm("v_min3_f32 %0, %1, %2, %3" : "=v"(r) : "v"(a), "v"(b), "v"(c)); return r; }
DEVI float vmax3(float a, float b, float c) { float r; asm("v_max3_f32 %0, %1, %2, %3" : "=v"(r) : "v"(a), "v"(b), "v"(c)); return r; }

DEVI unsigned src_kc(int c, int ld) { int row = c >> 3, ch = (c & 7) ^ ((row >> 1) & 7); return (unsigned)(row * ld * 2 + ch * 16); }
DEVI int km_f(int k) { return ((k >> 1) & 1) | (((k >> 3) & 1) << 1); }
DEVI unsigned ph(unsigned x) { return ((x >> 7) << 8) | (x & 127u); }
DEVI unsigned rd_kc(int lane, int ks) { int r = lane & 15, g = lane >> 4; return (unsigned)(r * 128 + (((ks * 4 + g) ^ (r >> 1)) << 4)); }
DEVI unsigned rd_km(int lane, int ct) {
  int g = lane >> 4, q = (lane & 15) >> 2, p = lane & 3;
  int f = ((q >> 1) & 1) | ((g & 1) << 1);
  return (unsigned)((ct >> 2) * 8192 + (8 * g + q) * 128 + (((ct & 3) ^ f) << 5) + 8 * p);
}


constexpr unsigned CODE_BYTES = 53760;
DEVI void code_warm(int part, int lane, unsigned scratch) {
  unsigned long long pc;
  asm volatile("s_getpc_b64 s[40:41]\n\ts_add_u32 s40, s40, mega_kernel@rel32@lo+4\n\ts_addc_u32 s41, s41, mega_kernel@rel32@hi+12\n\ts_mov_b64 %0, s[40:41]" : "=s"(pc) :: "s40", "s41", "scc");
  const unsigned off = (unsigned)(part * 64 + lane) * 128u;
  scratch = __builtin_amdgcn_readfirstlane(scratch);
  if (off < CODE_BYTES) asm volatile("s_nop 3\n\ts_mov_b32 m0, %2\n\ts_nop 0\n\tglobal_load_lds_dword %0, %1" :: "v"(off), "s"(pc), "s"(scratch) : "memory");
}

DEVI unsigned src_km(int lt, int ld) { const int k = lt >> 3; return (unsigned)(k * ld * 2 + (((lt & 7) ^ (km_f(k) << 1)) << 4)); }
DEVI void touch4(const void* sbase, unsigned voff, unsigned ldsaddr) {
  ldsaddr = __builtin_amdgcn_readfirstlane(ldsaddr); sbase = uni_ptr(sbase);
  asm volatile("s_nop 3\n\ts_mov_b32 m0, %2\n\ts_nop 0\n\tglobal_load_lds_dword %0, %1" :: "v"(voff), "s"(sbase), "s"(ldsaddr) : "memory");
}

template <int IMM> DEVI void spf1(const void* base, unsigned soff) { asm volatile("s_load_dword s101, %0, %1 offset:%2" :: "s"(base), "s"(soff), "i"(IMM) : "s101"); }
template <int IMM = 0> DEVI void spf2(const void* base, unsigned soff) { asm volatile("s_load_dwordx2 s[100:101], %0, %1 offset:%2" :: "s"(base), "s"(soff), "i"(IMM + 60) : "s100", "s101"); }
template <int ST> DEVI void spf2x6(const void* base, unsigned soff) {
  asm volatile("s_load_dwordx2 s[100:101], %0, %1 offset:%2\n\ts_load_dwordx2 s[100:101], %0, %1 offset:%3\n\ts_load_dwordx2 s[100:101], %0, %1 offset:%4\n\t"
               "s_load_dwordx2 s[100:101], %0, %1 offset:%5\n\ts_load_dwordx2 s[100:101], %0, %1 offset:%6\n\ts_load_dwordx2 s[100:101], %0, %1 offset:%7"
               :: "s"(base), "s"(soff), "i"(60), "i"(60 + ST), "i"(60 + 2 * ST), "i"(60 + 3 * ST), "i"(60 + 4 * ST), "i"(60 + 5 * ST) : "s100", "s101");
}
template <int ST> DEVI void spf1x12(const void* base, unsigned soff) {
  spf1<0>(base, soff); spf1<ST>(base, soff); spf1<2 * ST>(base, soff); spf1<3 * ST>(base, soff); spf1<4 * ST>(base, soff); spf1<5 * ST>(base, soff);
  spf1<6 * ST>(base, soff); spf1<7 * ST>(base, soff); spf1<8 * ST>(base, soff); spf1<9 * ST>(base, soff); spf1<10 * ST>(base, soff); spf1<11 * ST>(base, soff);
}
DEVI int lgkm_out() { int n; asm volatile("s_getreg_b32 %0, hwreg(HW_REG_IB_STS, 8, 4)" : "=s"(n)); return n; }
DEVI int slow_res(const void* w) { return (3 - ((unsigned)(uintptr_t)w >> 7)) & 7; }

constexpr int SPM = 0, WNE = 16, ANE = 24, BNE = 13, QNE = 46;
struct Plan { int n; signed char q[40], idx[40], k[40], h[40], g[40]; };
template <class O, bool PKM, bool QKM> constexpr Plan make_plan() {
  Plan p{};
  int nr = 0;
  for (int c = 0; c < O::NRO; ++c) nr += ((O::ro[c] >> 4) ? QKM : PKM) ? 4 : 2;
  int prev = 0;
  for (int c = 0; c < O::NRO; ++c) {
    const int q = O::ro[c] >> 4, i = O::ro[c] & 15;
    int av[2] = {0, 0}, k0 = -1;
    for (int m = 0; m < 2 * O::NP; ++m) {
      const int mm = (m + 2 * O::NPEND) % (2 * O::NP);
      if ((q ? O::pj[m >> 1] : O::pi[m >> 1]) == i && m < 2 * O::NPEND) av[((m >> 1) ^ m) & 1] = m;
      if ((q ? O::pj[mm >> 1] : O::pi[mm >> 1]) == i && k0 < 0) k0 = ((mm >> 1) ^ mm) & 1;
    }
    for (int kk = 0; kk < 2; ++kk)
      for (int h = 0; h < ((q ? QKM : PKM) ? 2 : 1); ++h) {
        const int k = k0 ^ kk;
        int g = p.n * O::NE / nr;
        if (g < av[k]) g = av[k];
        if (g < prev + (p.n && O::NE >= nr ? 1 : 0)) g = prev + (p.n && O::NE >= nr ? 1 : 0);
        p.q[p.n] = q; p.idx[p.n] = i; p.k[p.n] = k; p.h[p.n] = h; p.g[p.n] = g; prev = g; ++p.n;
      }
  }
  return p;
}
struct OBase { static constexpr int pidx(int i) { return i; } static constexpr int poff(int) { return 0; } static constexpr int qoff(int j) { return j * 2048; } static constexpr bool swp(int) { return false; } };
template <bool PF, int NE_> struct O44 : OBase {
  static constexpr int TP = 4, TQ = 4, NPA = 4, NP = 16, NPEND = 8, NRO = 8, NE = NE_;
  static constexpr signed char pi[16] = {2, 3, 3, 2, 2, 3, 3, 2, 0, 1, 1, 0, 0, 1, 1, 0}, pj[16] = {0, 0, 1, 1, 2, 2, 3, 3, 0, 0, 1, 1, 2, 2, 3, 3};
  static constexpr signed char ro[8] = {0, PF ? 1 : 16, PF ? 16 : 1, 17, 18, 19, 2, 3};
};
template <bool PF, int NE_> struct O42 : OBase {
  static constexpr int TP = 4, TQ = 2, NPA = 4, NP = 8, NPEND = 4, NRO = 6, NE = NE_;
  static constexpr signed char pi[8] = {2, 3, 3, 2, 0, 1, 1, 0}, pj[8] = {0, 0, 1, 1, 0, 0, 1, 1};
  static constexpr signed char ro[6] = {0, PF ? 1 : 16, PF ? 16 : 1, 17, 3, 2};
};
template <int TH> struct OQ {
  static constexpr int TP = 6, TQ = 5, NPA = 2, NP = 26, NPEND = 13, NRO = 11, NE = QNE;
  static constexpr signed char pi[26] = {3, 4, 5, 5, 4, 3, 3, 4, 5, 5, TH ? 5 : 3, 4, TH ? 3 : 4, 0, 1, 2, 2, 1, 0, 0, 1, 2, 2, TH ? 2 : 0, 1, TH ? 0 : 1};
  static constexpr signed char pj[26] = {0, 0, 0, 1, 1, 1, 2, 2, 2, TH ? 4 : 3, 3, 3, TH ? 3 : 4, 0, 0, 0, 1, 1, 1, 2, 2, 2, TH ? 4 : 3, 3, 3, TH ? 3 : 4};
  static constexpr signed char ro[11] = {0, 1, 16, 2, 17, 18, TH ? 20 : 19, TH ? 19 : 20, 3, 4, 5};
  static constexpr int pidx(int i) { return i / 3; }
  static constexpr int poff(int i) { return i % 3 * 8192; }
  static constexpr int qoff(int j) { return j == 4 ? 0 : (1 + 4 * TH + j) * 2048; }
  static constexpr bool swp(int i) { return i % 3 == 2; }
};
template <bool PKM, bool QKM, int PB, int QB, int XB, int NS, int NT, bool WISP, int BASE, int ROT, int WLD, int WSUB, int ASTEP, int RLX = 0, bool QF = false>
struct Gemm {
  static constexpr int SB = PB + QB + XB, CNT = (PB + QB) / 4096 + (XB ? 1 : 0);
  static constexpr int PO = QF ? QB : 0, QO = QF ? 0 : PB, WB = WISP ? PB : QB, AB = WISP ? QB : PB, WOFF = WISP ? PO : QO, AOFF = WISP ? QO + XB : PO;
  static constexpr int XL = XB / 64, H = NS - 1 - RLX, CA = AB / 4096 + (XB ? 1 : 0), WSTEP = 64 * WLD * 2;
  static_assert(H * CNT <= 63 && BASE + NS * SB <= 163840, "");
  struct Loader {
    static constexpr int NW = WB / 4096, NA = AB / 4096;
    const char* w; const char* ab; unsigned wv[NW], av[NA], xv, lwb, lab, lx; int rot, islot, t; bool xl;
    DEVI Loader(const void* w_, const void* ab_, int lw, int lane, int rot_ = 0)
        : w((const char*)w_ - 3072), ab((const char*)ab_ - 3072), lwb(BASE + WOFF + lw * (NW == 6 ? 2048 : WB / 4)), lab(BASE + AOFF + lw * (AB / 4)), lx(BASE + PB + lw * (XL * 16)),
          rot(rot_), islot((H + ROT) % NS), t(0), xl(lane < XL) {
      const unsigned w0 = (lane >> 3) * (WLD * 2), f0 = (lane & 7) ^ ((lane >> 3) & 2);
#pragma unroll
      for (int i = 0; i < NW; ++i) {
        const unsigned p = NW == 6 ? (i >> 1) * 8 + lw * 2 + (i & 1) : lw * NW + i;
        wv[i] = w0 + ((f0 ^ ((i & 1) << 2)) << 4) + (NW == 6 ? (i >> 1) * WSUB + (lw * 2 + (i & 1)) * (8 * WLD * 2) - (i & 1) * 1024 : (p >> 3) * WSUB + (p & 7) * (8 * WLD * 2) - i * 1024) + 3072;
        asm volatile("" : "+v"(wv[i]));
      }
      asm volatile("" : "+s"(w), "+s"(lwb));
      if (XB) asm volatile("" : "+s"(rot));
      else seta(0, 0, lw, lane);
    }
    DEVI void seta(unsigned a0, unsigned xv_, int lw, int lane) {
      xv = xv_ + 3072;
#pragma unroll
      for (int i = 0; i < NA; ++i) { const unsigned p = lw * NA + i; av[i] = (XB ? a0 + src_kc(p * 64 + lane, D) : p * 2048 + ph(lane * 16)) + 3072 - i * 1024; asm volatile("" : "+v"(av[i])); }
      asm volatile("" : "+s"(ab), "+s"(lab));
      if (XB) { asm volatile("" : "+v"(xv)); asm volatile("" : "+s"(lx)); }
    }
    DEVI int kord(int n) const { if constexpr (XB != 0) return (n + rot) & (NT - 1); else return n; }
    template <int SO> DEVI void issue_w(const char* p, unsigned so = 0) const {
      if constexpr (NW == 6) { dma_grp<2, SO>(p, wv, lwb + so); dma_grp<2, SO + 8192>(p, wv + 2, lwb + so); dma_grp<2, SO + 16384>(p, wv + 4, lwb + so); }
      else dma_grp<NW, SO>(p, wv, lwb + so);
    }
    template <int SO> DEVI void issue_a(const char* p, unsigned so = 0) const {
      if (XB) { if (xl) dma_grp<1, SO>(p, &xv, lx + so); }
      dma_grp<NA, SO>(p, av, lab + so);
    }
    template <int S0, bool W, bool A> DEVI void pre() const {
      if constexpr (S0 < H) {
        if (W) issue_w<((S0 + ROT) % NS) * SB>(w + (kord(0) + S0) * WSTEP);
        if (A) issue_a<((S0 + ROT) % NS) * SB>(ab + (kord(0) + S0) * ASTEP);
        pre<S0 + 1, W, A>();
      }
    }
    template <int T> DEVI void pre_w1() const { issue_w<((T + ROT) % NS) * SB>(w + (kord(0) + T) * WSTEP); sched0(); }
    DEVI void pre_w() const { pre<0, true, false>(); sched0(); }
    template <int T> DEVI void peel() const {
      if constexpr (T < H - 1) {
        wait_vm<(H - 1 - T) * CA + T * CNT>();
        bar();
        issue_w<((T + H + ROT) % NS) * SB>(w + (T + H) * WSTEP); issue_a<((T + H + ROT) % NS) * SB>(ab + (T + H) * ASTEP);
        peel<T + 1>();
      }
    }
    DEVI void start() {
      sched0();
      bar();
      pre<0, false, true>();
      sched0();
      if constexpr (H == 2) wait_vm<CA>();
      else { peel<0>(); islot = (2 * H - 1 + ROT) % NS; t = H - 1; }
    }
    template <class HK> DEVI void run(int tend, HK side) {
#pragma unroll 1
      for (; t < tend; ++t) {
        const char* wk = w + (t + H) * WSTEP; const char* ak = ab + (t + H) * ASTEP;
        unsigned lw_ = lwb + islot * SB, la_ = lab + islot * SB;
        islot = islot + 1 == NS ? 0 : islot + 1;
        asm volatile("" : "+s"(wk), "+s"(ak), "+s"(lw_), "+s"(la_));
        wait_vm<(H - 1) * CNT>();
        bar();
        dma_grp<NW, 0>(wk, wv, lw_);
        side(t);
        dma_grp<NA, 0>(ak, av, la_);
        sched0();
      }
    }
    template <int T, int X, class A, class B, class C> DEVI void step(A pre_, B side, C post) const {
      const int k = kord(T + H);
      const char* wk = w + k * WSTEP; const char* ak = ab + k * ASTEP;
      asm volatile("" : "+s"(wk), "+s"(ak));
      wait_vm<(H - 1) * CNT + X>();
      bar();
      pre_(BASE + ((T + ROT) % NS) * SB);
      issue_w<((T + H + ROT) % NS) * SB>(wk);
      sched0();
      if ((SPM >> T) & 1) __builtin_amdgcn_s_setprio(2);
      side();
      if ((SPM >> T) & 1) __builtin_amdgcn_s_setprio(0);
      sched0();
      issue_a<((T + H + ROT) % NS) * SB>(ak);
      post();
      sched0();
    }
    template <int I> DEVI void tail() const { if constexpr (I >= 0) { sched0(); wait_vm<I * CNT>(); bar(); tail<I - 1>(); } }
  };
  static constexpr int NWIN = (NS * SB + 65535) / 65536;
  template <class O> struct Comp {
    static constexpr int TP = O::TP, TQ = O::TQ, NM = 2 * O::NP, NPA = PKM ? O::NPA : 2, NQA = QKM ? TQ : 2;
    static constexpr Plan PL = make_plan<O, PKM, QKM>();
    unsigned prd[NWIN][NPA], qrd[NWIN][NQA];
    bf16x8 fp[TP][2], fq[TQ][2]; bf16x4 lo;
    DEVI void setwin() {
#pragma unroll
      for (int w = 1; w < NWIN; ++w) {
#pragma unroll
        for (int i = 0; i < NPA; ++i) { prd[w][i] = prd[0][i] + w * 65536; asm volatile("" : "+v"(prd[w][i])); }
#pragma unroll
        for (int i = 0; i < NQA; ++i) { qrd[w][i] = qrd[0][i] + w * 65536; asm volatile("" : "+v"(qrd[w][i])); }
      }
#pragma unroll
      for (int i = 0; i < NPA; ++i) asm volatile("" : "+v"(prd[0][i]));
#pragma unroll
      for (int i = 0; i < NQA; ++i) asm volatile("" : "+v"(qrd[0][i]));
    }
    DEVI void setup(int wp, int wq, int lane) {
      const unsigned b = BASE;
      if (PKM) {
#pragma unroll
        for (int i = 0; i < TP; ++i) prd[0][i] = b + rd_km(lane, wp * TP + i);
      } else { prd[0][0] = b + rd_kc(lane, 0) + wp * TP * 2048; prd[0][1] = b + rd_kc(lane, 1) + wp * TP * 2048; }
      if (QKM) {
#pragma unroll
        for (int j = 0; j < TQ; ++j) qrd[0][j] = b + rd_km(lane, wq * TQ + j);
      } else { qrd[0][0] = b + rd_kc(lane, 0) + wq * TQ * 2048; qrd[0][1] = b + rd_kc(lane, 1) + wq * TQ * 2048; }
    }
    template <int C, int N> DEVI static unsigned wa(const unsigned (&b)[NWIN][N], int i) { return b[C >> 16][i] + (C & 0xffff); }
    template <int SLOT, int R> DEVI void rd() {
      constexpr int q = PL.q[R], i = PL.idx[R], k = PL.k[R], h = PL.h[R];
      bf16x8& f = q ? fq[q ? i : 0][k] : fp[q ? 0 : i][k];
      if constexpr (q ? QKM : PKM) {
        unsigned a;
        if constexpr (q) a = wa<SLOT * SB + QO + k * 4096 + h * 512, NQA>(qrd, i); else a = wa<SLOT * SB + PO + O::poff(i) + k * 4096 + h * 512, NPA>(prd, O::pidx(i));
        const bf16x4 x = lds_tr4(a);
        if constexpr (h) f = __builtin_shufflevector(lo, x, 0, 1, 2, 3, 4, 5, 6, 7); else lo = x;
      } else if constexpr (q) f = lds_b128(wa<SLOT * SB + QO + O::qoff(i), 2>(qrd, k));
      else f = lds_b128(wa<SLOT * SB + PO + i * 2048, 2>(prd, k));
    }
    template <int SLOT, int M, int R> DEVI void rds() { if constexpr (R < PL.n) { if constexpr (PL.g[R] == M) rd<SLOT, R>(); if constexpr (PL.g[R] <= M) rds<SLOT, M, R + 1>(); } }
    template <int M, bool IP = false> DEVI void mm(f32x4 (&acc)[TP][TQ]) {
      constexpr int i = O::pi[M >> 1], j = O::pj[M >> 1], k = ((M >> 1) ^ M) & 1;
      if constexpr (IP) { if constexpr (O::swp(i)) mfma_ip(acc[i][j], fq[j][k], fp[i][k]); else mfma_ip(acc[i][j], fp[i][k], fq[j][k]); }
      else if constexpr (O::swp(i)) acc[i][j] = __builtin_amdgcn_mfma_f32_16x16x32_bf16(fq[j][k], fp[i][k], acc[i][j], 0, 0, 0);
      else acc[i][j] = __builtin_amdgcn_mfma_f32_16x16x32_bf16(fp[i][k], fq[j][k], acc[i][j], 0, 0, 0);
    }
    template <int A, int B> DEVI void pend(f32x4 (&acc)[TP][TQ]) { if constexpr (A < B) { mm<2 * A>(acc); mm<2 * A + 1>(acc); pend<A + 1, B>(acc); } }
    template <int SLOT, bool FIRST, int M> DEVI void body(f32x4 (&acc)[TP][TQ]) {
      if constexpr (M < NM) {
        if constexpr (!FIRST || M >= 2 * O::NPEND) { mm<M, true>(acc); sched0(); }
        rds<SLOT, M, 0>();
        sched0();
        body<SLOT, FIRST, M + 1>(acc);
      }
    }
    template <int SLOT, bool FIRST> DEVI void step(f32x4 (&acc)[TP][TQ]) { if (!RLX) wait_lgkm0(); bar(); sched0(); body<SLOT, FIRST, 0>(acc); }
    template <int I, int N, int T0, class MID> DEVI void steps(f32x4 (&acc)[TP][TQ], MID mid) {
      if constexpr (I < N) { mid(I); step<(I + T0 + ROT) % NS, false>(acc); steps<I + 1, N, T0>(acc, mid); }
    }
    DEVI void prep(f32x4 (&acc)[TP][TQ]) { setwin(); pin(acc); }
    DEVI void pin(f32x4 (&acc)[TP][TQ]) {
#pragma unroll
      for (int p = 0; p < O::NP; ++p) asm volatile("" : "+v"(acc[O::pi[p]][O::pj[p]]));
    }
    template <bool CONT, class MID> DEVI void run_steps(f32x4 (&acc)[TP][TQ], MID mid) {
      constexpr int T0 = CONT ? 0 : 1;
      if constexpr (T0) { mid((NT - T0) / NS, -1); step<ROT % NS, true>(acc); }
#pragma unroll 1
      for (int itn = (NT - T0) / NS; itn > 0; --itn) {
        steps<0, NS, T0>(acc, [&](int i) { mid(itn, i); });
      }
      steps<0, (NT - T0) % NS, T0>(acc, [](int) {});
      wait_lgkm0();
      mfma_gap();
    }
  };
};

DEVI f32x4 gelu_arg(f32x4 u) { return u * (-2.302208198f + -0.1029432397f * (u * u)); }
DEVI f32x4 gelu_tanh(f32x4 u) {
  f32x4 a = gelu_arg(u);
#pragma unroll
  for (int e = 0; e < 4; ++e) a[e] = __builtin_amdgcn_exp2f(a[e]);
  a += 1.f;
#pragma unroll
  for (int e = 0; e < 4; ++e) a[e] = __builtin_amdgcn_rcpf(a[e]);
  return u * a;
}

template <int tilesP, int tilesQ, int gp, int gq> DEVI void tile_map(unsigned bid, int& tp, int& tq) {
  const unsigned xcd = bid & 7, idx = bid >> 3;
  constexpr unsigned gP = tilesP / gp, gQ = tilesQ / gq;
  static_assert(gP * gQ == 8, "");
  tp = (xcd % gP) * gp + idx % gp;
  tq = (xcd / gP) * gq + idx / gp;
}

template <int V> struct IC { static constexpr int value = V; };
template <int A, int B, class FN> DEVI void for_c(FN f) { if constexpr (A < B) { f(IC<A>{}); for_c<A + 1, B>(f); } }
DEVI float sel(bool c, float a, float b) { return c ? a : b; }
DEVI u32x2 pk4(const f32x4& v) { return (u32x2){pk2(v[0], v[1]), pk2(v[2], v[3])}; }
DEVI u32x4 cat4(u32x2 a, u32x2 b) { return (u32x4){a.x, a.y, b.x, b.y}; }
DEVI bf16x8 cat(u32x2 a, u32x2 b) { return __builtin_bit_cast(bf16x8, cat4(a, b)); }
DEVI f32x4 mfma(const bf16x8& a, const bf16x8& b, const f32x4& c) { return __builtin_amdgcn_mfma_f32_16x16x32_bf16(a, b, c, 0, 0, 0); }
DEVI u32x4 pack_pair(const f32x4& a, const f32x4& b) {
  const unsigned ax = pk2(a[0], a[1]), ay = pk2(a[2], a[3]), bx = pk2(b[0], b[1]), by = pk2(b[2], b[3]);
  auto rx = __builtin_amdgcn_permlane16_swap(ax, bx, false, false);
  auto ry = __builtin_amdgcn_permlane16_swap(ay, by, false, false);
  const unsigned x0 = rx[0], x1 = rx[1], y0 = ry[0], y1 = ry[1];
  return (u32x4){x0, y0, x1, y1};
}
DEVI int pair_chunk(int fq) { return ((fq & 1) << 1) | (fq >> 1); }
DEVI void sel_frags(bool paired, int fr, int fq, bf16x8 (&s)[2]) {
  const unsigned one = fr & 1 ? 0x3F800000u : 0x3F80u;
#pragma unroll
  for (int i = 0; i < 2; ++i) {
    const bool on = paired ? (fq & 1) == i && (fr >> 3) == (fq >> 1) : (fr >> 2) == fq;
    const int d = paired ? (fr & 7) >> 1 : i * 2 + ((fr >> 1) & 1);
    u32x4 v;
#pragma unroll
    for (int e = 0; e < 4; ++e) v[e] = on && d == e ? one : 0u;
    s[i] = __builtin_bit_cast(bf16x8, v);
  }
}
template <int TQ> DEVI void tile_add(f32x4 (&acc)[4][TQ], const bf16x8 (&s)[2], unsigned a) {
  bf16x8 t[4];
#pragma unroll
  for (int p = 0; p < 4; ++p) t[p] = lds_b128(a + p * 1024);
#pragma unroll
  for (int p = 0; p < 4; ++p)
#pragma unroll
    for (int i = 0; i < 2; ++i) acc[(p & 1) * 2 + i][p >> 1] = mfma(s[i], t[p], acc[(p & 1) * 2 + i][p >> 1]);
}
template <int C> DEVI float dppf(float v) { return __int_as_float(__builtin_amdgcn_update_dpp(0, __float_as_int(v), C, 0xf, 0xf, false)); }
DEVI float red8(float v) { v += dppf<0xB1>(v); v += dppf<0x4E>(v); v += dppf<0x141>(v); return v; }
DEVI float red4(float v) { v += dppf<0xB1>(v); v += dppf<0x4E>(v); return v; }

constexpr unsigned SENT = 0xBF800000u;
constexpr int SPIN_MAX = 1 << 21;

constexpr unsigned MB = 1u << 20;
constexpr unsigned WS_O = 0, WS_H = 8 * MB, WS_U = 16 * MB, WS_X2 = 32 * MB, WS_XP = 40 * MB  ;
constexpr unsigned WS_ST = 120 * MB, ST_CS = 2 * S * 64, WS_XCH = 122 * MB  , WS_EPOCH = 123 * MB, WS_ABORT = 123 * MB + 256  ;
DEVI unsigned tag2(unsigned t) { return t * 0x9E3779B1u ^ 0x5BD1E995u; }
constexpr int MEDGE = 256 * 64;
constexpr unsigned WS_MAIL = 124 * MB;

constexpr int NL = 2, QBR = 4, FLS = 3, NPL = 6, SPON = 1, PC0 = 0, PGAP = 3, WSP = 3, SWW = 0, GSH = 1, GSP = 3, SSC = 1, RB = 1, SI = 1, LPR = 1, CPR = 0, GLW = 4, GL1 = 4, SW0 = 0, SW1 = 6, XE = 0, E2 = 1, LB = 2, TLQ = 0, TLW = 1, SPT = 0, SLS = 0, SW2 = 0, CP = 1, XD = 1, XS = 4, JA = 22, PS = 3, TBA = 3, TC1 = 13, NW1 = 4, EW = 12, ET = 3, SPW = 1, PA_1 = 3, PA_2 = 3;
constexpr int L_X = 0, L_TAB = 16384, L_CS = L_TAB + 1152, L_TAG = L_CS + 768, L_BIAS = L_TAG + 128, L_SCR = L_BIAS + 512  , L_QKV = 20480;
static_assert(L_SCR + 1536 <= L_QKV, "");
typedef Gemm<true, false, 24576, 16384, 2048, 3, D / 64, true, 32768, 0, 3 * D, 2 * D, 128> GQ;
typedef Gemm<true, false, 16384, 8192, 0, 5, D / 64, true, 40960, 2, D, 128, 16384, 1, true> GW;
typedef Gemm<true, true, 16384, 16384, 0, 4, S / 64, true, 32768, 1, F, 128, 32768, 0, true> G1;
typedef Gemm<false, true, 16384, 16384, 0, 3, JA, false, 65536, (3 - JA % 3) % 3, S, 1024, 32768> G2A;
typedef Gemm<false, true, 16384, 8192, 0, 4, F / 64 - 2 * JA, false, 65536, 0, S, 128, 32768> G2;

DEVI unsigned get_tag() { return __builtin_amdgcn_readfirstlane(*(volatile LAS unsigned*)(uintptr_t)L_TAG) + 1u; }
DEVI bool spin_fail(int spin, char* ws) {
  if ((spin & 255) != 255) return false;
  gu32* ab = (gu32*)(ws + WS_ABORT);
  const unsigned tag = get_tag();
  if (spin >= SPIN_MAX) { __hip_atomic_store(ab, tag, RLX_AGENT); return true; }
  return __builtin_amdgcn_readfirstlane(__hip_atomic_load(ab, RLX_AGENT)) == tag;
}
DEVI void sync_post(char* ws, int edge, int post, int wid) {
  unsigned o = post >= 0 && wid == 0 ? (post >> 6) * 256u : MB, bit = 1u << (post & 31);
  const rsrc_t r = mkrsrc(ws + WS_MAIL + edge * (MEDGE * 4), MEDGE * 4);
  asm volatile("s_waitcnt vmcnt(0) lgkmcnt(0)" : "+v"(o), "+v"(bit) : "s"(r) : "memory");
  sched0();
  bar();
  if (wid == 0) asm volatile("buffer_atomic_or %0, %1, %2, 0 offen sc1" :: "v"(bit), "v"(o), "s"(r) : "memory");
  sched0();
}
#define POLL1(r) "s_waitcnt vmcnt(2)\n\tv_readfirstlane_b32 %[seen], " r "\n\tglobal_atomic_or " r ", %[z], %[z], %[p] sc0 sc1\n\t" \
  "s_andn2_b32 %[t], %[first], %[seen]\n\ts_cbranch_scc1 3f\n\ts_cmp_eq_u32 %[first], 0\n\ts_cbranch_scc1 4f\n\ts_barrier\n\ts_mov_b32 %[first], 0\n" \
  "4:\n\ts_andn2_b32 %[t], %[second], %[seen]\n\ts_cbranch_scc0 2f\n3:\n\t"
struct Gate {
  const char* p; unsigned miss, due, a, b, c;
  DEVI Gate() : p(nullptr), miss(0), due(0) { asm volatile("v_mov_b32 %0, 0\n\tv_mov_b32 %1, 0\n\tv_mov_b32 %2, 0" : "=v"(a), "=v"(b), "=v"(c)); }
  DEVI void ask() const { asm volatile("s_mov_b64 exec, 1\n\tglobal_atomic_or %0, %1, %1, %2 sc0 sc1\n\ts_mov_b64 exec, -1" :: "v"(a), "v"(0u), "s"(p) : "memory"); }
  template <int DELAY> DEVI void wait(char* ws, int edge, int line, unsigned all, unsigned first, unsigned second, int wid) {
    sched0();
    p = ws + WS_MAIL + (edge * MEDGE + line * 64) * 4;
    second &= all;
    if (wid == 3) {
      due = 1;
      if (DELAY) __builtin_amdgcn_s_sleep(DELAY);
      int spin = 255;
#pragma unroll 1
      for (;; spin += 256) {
        asm volatile("" : "+s"(spin));
        unsigned seen, t, n;
        asm volatile("s_mov_b64 exec, 1\n\ts_waitcnt vmcnt(0)\n\tglobal_atomic_or %[a], %[z], %[z], %[p] sc0 sc1\n\ts_sleep %[gap]\n\t"
                     "global_atomic_or %[b], %[z], %[z], %[p] sc0 sc1\n\ts_sleep %[gap]\n\tglobal_atomic_or %[c], %[z], %[z], %[p] sc0 sc1\n\ts_movk_i32 %[n], 128\n"
                     "1:\n\t" POLL1("%[a]") POLL1("%[b]") POLL1("%[c]") "s_sub_u32 %[n], %[n], 1\n\ts_cbranch_scc0 1b\n"
                     "2:\n\ts_mov_b64 exec, -1 ; raw polls %[a] %[b] %[c]"
                     : [seen] "=&s"(seen), [t] "=&s"(t), [n] "=&s"(n), [first] "+&s"(first)
                     : [p] "s"(p), [second] "s"(second), [gap] "i"(PGAP), [a] "v"(a), [b] "v"(b), [c] "v"(c), [z] "v"(0u) : "memory", "scc");
        miss = ~seen & all;
        if (__builtin_expect(!(miss & second) && !first, 1)) break;
        if (spin_fail(spin, ws)) { if (first) bar(); miss = 0; break; }
      }
    } else bar();
  }
  int ev;
  DEVI void arm(int ni, int cw) { ev = miss ? ni : cw >= 0 ? 2 : 0; asm volatile("" : "+s"(ev), "+s"(miss), "+s"(due)); }
  DEVI void head(int it, int cw, int lane, int wid) {
    if (__builtin_expect(it == ev, 0)) {
      asm volatile("" : "+v"(lane)); asm volatile("" : "+s"(cw), "+s"(wid));
      if (miss) ask();
      if (it == 2) code_warm(cw, lane, L_SCR + wid * 256);
      ev = cw >= 0 && it > 2 ? 2 : 0;
      asm volatile("" : "+s"(ev));
    }
  }
  DEVI void late(char* ws) {
    if (__builtin_expect(due != 0, 0)) {
      asm volatile("" : "+s"(ws));
      asm volatile("s_waitcnt vmcnt(0) ; raw settled %0 %1 %2" :: "v"(a), "v"(b), "v"(c) : "memory");
      for (int spin = 0; miss; ++spin) {
        unsigned seen;
        asm volatile("v_readfirstlane_b32 %0, %1" : "=s"(seen) : "v"(a));
        miss &= ~seen;
        if (!miss || spin_fail(spin, ws)) break;
        ask();
        wait_vm<0>();
      }
      miss = due = 0;
      asm volatile("" : "+s"(miss), "+s"(due));
    }
  }
};
DEVI unsigned halo_off(int s0, int lw, int lane) {
  const int c = lw * 32 + (lane & 31), row = c >> 3, ch = (c & 7) ^ ((row >> 1) & 7), tok = s0 - 16 + row;
  return (unsigned)((tok < 0 ? 0 : tok) * D * 2 + ch * 16);
}
DEVI int qkv_rot(int bid) { return (bid & 3) * 4 + (bid & 4) * 2; }
DEVI unsigned x_off(int tp, int tq, int w, int fr, int fq) { return (unsigned)(((tq * 64 + (w >> 1) * 32 + fr) * D + tp * 128 + (w & 1) * 64 + pair_chunk(fq) * 8) * 2); }

DEVI void pfb_q(const u16* w, int bid, int j, int n) {
  const int r = slow_res(w);
  if ((r >> 2) == (bid & 1)) {
    const unsigned o = (unsigned)(((bid & 2) * 4 + r) * 128 + (j >> 1) * (3 * D * 2) + (j & 1) * 64 + ((n + qkv_rot(bid)) & 15) * GQ::WSTEP);
    spf1<0>(w, o); spf1<2048>(w, o); spf1<4096>(w, o);
  }
}
constexpr int PFR = NPL * 128 * 1024, PF_END = TBA * 256 + NW1 * PFR / (F * 2) / 8 * 8;
static_assert(TBA * 256 * F * 2 + NW1 * PFR <= S * F * 2 && 3 * PFR >= D * D * 2, "");
struct Pfc {
  const char* p; const char* q; int pc;
  DEVI Pfc(const u16* wo, const u16* w1, unsigned g) : p((const char*)wo + g * (NPL * 128)), q((const char*)w1 + TBA * 256 * F * 2 + g * (NPL * 128)), pc(PC0) {}
  DEVI void go() {
    unsigned t, u;
    asm volatile("s_getreg_b32 %2, hwreg(HW_REG_IB_STS, 8, 4)\n\ts_cmp_lt_u32 %1, %5\n\ts_cselect_b32 %3, 0, 1\n\ts_or_b32 %2, %2, %3\n\ts_cbranch_scc1 1f\n\t"
                 "s_load_dwordx2 s[100:101], %0, 60\n\ts_load_dwordx2 s[100:101], %0, 188\n\ts_load_dwordx2 s[100:101], %0, 316\n\t"
                 "s_load_dwordx2 s[100:101], %0, 444\n\ts_load_dwordx2 s[100:101], %0, 572\n\ts_load_dwordx2 s[100:101], %0, 700\n\t"
                 ".if %8 == 7\n\ts_load_dwordx2 s[100:101], %0, 828\n\t.endif\n\t"
                 "s_mov_b32 %2, %7\n\ts_cmp_lt_u32 %1, 2\n\ts_cselect_b32 %2, %6, %2\n\ts_mov_b64 s[40:41], %0\n\ts_add_u32 s40, s40, %2\n\ts_addc_u32 s41, s41, 0\n\t"
                 "s_cmp_eq_u32 %1, 2\n\ts_cselect_b64 %0, %4, s[40:41]\n\ts_add_u32 %1, %1, 1\n1:"
                 : "+s"(p), "+s"(pc), "=&s"(t), "=&s"(u) : "s"(q), "i"(3 + NW1), "i"((D * D * 2 - PFR) / 2), "i"(PFR), "i"(NPL) : "s40", "s41", "s100", "s101", "scc");
  }
};
template <class FN> DEVI void spare_touch(const u16* w1, const u16* w2, int bid, int wid, int lane, FN side) {
  if (!SPON) return;
  const int j = ((bid >> 3) - 28) * 8 + wid, fr = lane & 15, fq = lane >> 4;
  const unsigned scr = L_QKV + wid * 256;
  u64 due = SPT ? __builtin_amdgcn_s_memrealtime() + (j & 7) * (SPT / 8) : 0;
  auto pace = [&]() { side(); wait_vm<SPW>(); due += SPT; if (SPT) { while (__builtin_amdgcn_s_memrealtime() < due) __builtin_amdgcn_s_sleep(2); } };
#pragma unroll 1
  for (int r = PF_END + (j * 2 + ((bid >> 2) & 1)) * 4; r < S; r += 256) { if (fr < 14) touch4(w1 + (size_t)r * F + (bid & 3) * 896, fq * (F * 2) + fr * 128, scr); pace(); }
  if (SPON & 2) return;
  if (SW2 && wid < 4) __builtin_amdgcn_s_sleep(SW2);
#pragma unroll 1
  for (int r = (j * 2 + (bid & 1)) * 4; r < JA * 64; r += 256) { touch4(w2 + (size_t)(r + (bid & 2) * JA * 32) * S + (bid & 4) * 256, fq * (S * 2) + fr * 128, scr); pace(); }
#pragma unroll 1
  for (int r = JA * 128 + ((31 - j) * 2 + (bid & 1)) * 8; r < F; r += 512) { touch4(w2 + (size_t)r * S + ((bid & 7) >> 1) * 512, (lane >> 3) * (S * 2) + (lane & 7) * 128, scr); pace(); }
}

template <int TH> DEVI void qkv_attn(char* ws, int l, int h, int lane, int hp, int sb, int cw, int bid, int tp, int tq, const u16* wqkv) {
  asm volatile("; half %1" : "+v"(lane) : "i"(TH));
  const int fr = lane & 15, fq = lane >> 4;
  GQ::Comp<OQ<TH>> g;
#pragma unroll
  for (int c = 0; c < 2; ++c) { g.prd[0][c] = 32768 + rd_km(lane, h * 2 + c); g.qrd[0][c] = 32768 + rd_kc(lane, c); }
  f32x4 acc[6][5];
#pragma unroll
  for (int i = 0; i < 6; ++i)
#pragma unroll
    for (int j = 0; j < 5; ++j) acc[i][j] = (f32x4){0.f, 0.f, 0.f, 0.f};
  const int par = fr & 1, da = fr - 4 * fq - par;
  const bool ca = da >= 0, cb = da >= 2;
  const unsigned tm = L_TAB + (16 + 64 * TH) * 4, tc = L_CS + h * 128;
  unsigned am = tm + fr * 4 - 320, a4 = tm + fq * 16 - 320, aa = tm + 576 + (4 * fq + par - (ca ? 0 : 16)) * 4, ab = tm + 576 + (4 * fq + par + 2 - (cb ? 0 : 16)) * 4;
  unsigned c4 = tc + fq * 16, c1 = tc + 512 + fr * 4, xb = L_QKV + h * 3072 + lane * 16, hm = par ? 0xffff0000u : 0xffffu, m0 = ca ? 0u : hm, m1 = cb ? 0u : hm;
  unsigned oo = ph((unsigned)((((sb * 2 + TH) * 16 + hp) << 13) + fr * 128 + (((h * 4 + pair_chunk(fq)) ^ (fr >> 1)) << 4)));
  asm volatile("" : "+v"(am), "+v"(a4), "+v"(aa), "+v"(ab), "+v"(c4), "+v"(c1), "+v"(xb), "+v"(hm), "+v"(m0), "+v"(m1), "+v"(oo));
  g.prep(acc);
  if (l) {
    const int sbc = (tq >> 1) + fq;
    sync_post(ws, 3, !TH && (lane < 16 || (lane < 32 && (tq & 1) && sbc < 16)) ? (sbc * 16 + fr) * 64 + (tq - 2 * sbc + 1) * 8 + tp : -1, TH * 2 + h);
  }
  Gate gt;
  if (l) gt.wait<SW0>(ws, 3, sb * 16 + hp, sb ? 0xffffffu : 0xffff00u, (sb ? 0x10101u : 0x10100u) << ((qkv_rot(bid) & 15) >> 1), 0x30303u << ((qkv_rot(bid) & 15) >> 1), TH * 2 + h);
  else {
    unsigned eo = fr < 12 ? (unsigned)((((qkv_rot(bid) + 4 * TH + 2 * h) * 64 + (bid >> 3) * 4 + fq) & 1023) * (3 * D * 2) + (fr >> 2) * (D * 2) + ((bid & 3) * 4 + (fr & 3)) * 128) : 0u;
    asm volatile("" : "+v"(eo));
    if (!TH || !h) wait_vm<0>(); else if (ET) __builtin_amdgcn_s_sleep(ET);
    if (TH || h) touch4(wqkv, eo, L_SCR);
  }
  g.template run_steps<false>(acc, [&](int it, int i) {
    if (i == -1) gt.arm(it, cw);
    if (i == 0) gt.head(it, cw, lane, TH * 2 + h);
    if (TH && i == 1) gt.late(ws);
  });
  const rsrc_t ro = mkrsrc(ws + WS_O, S * D * 4);
  if (l == 0) bar();
  float mt[5], cv[2], rq[4], ra[4], rb[4]; f32x4 m4[5], cq[2], ck[2];
#pragma unroll
  for (int c = 0; c < 2; ++c) {
    ck[c] = *(const LAS f32x4*)(uintptr_t)(c4 + 256 + c * 64); cv[c] = *(const LAS float*)(uintptr_t)(c1 + c * 64);
    cq[c] = *(const LAS f32x4*)(uintptr_t)(c4 + c * 64);
  }
#pragma unroll
  for (int j = 4; j >= 0; --j) {
    const int o = 320 + (j == 4 ? -64 - 256 * TH : j * 64);
    if (j < 4 || !TH) mt[j] = *(const LAS float*)(uintptr_t)(am + o);
    if (j < 4 || TH) m4[j] = *(const LAS f32x4*)(uintptr_t)(a4 + o);
  }
#pragma unroll
  for (int j = 0; j < 4; ++j) { rq[j] = *(const LAS float*)(uintptr_t)(am + 896 + j * 64); ra[j] = *(const LAS float*)(uintptr_t)(aa + j * 64); rb[j] = *(const LAS float*)(uintptr_t)(ab + j * 64); }
  u32x2 kp[2][5], qp[2][4], vp[2][5];
  auto fk = [&](int j) { kp[0][(j + 1) % 5] = pk4(acc[1][j]); kp[1][(j + 1) % 5] = pk4(acc[4][j] + ck[1] * mt[j]); };
  auto fv = [&](int j) { vp[0][(j + 1) % 5] = pk4(acc[2][j]); vp[1][(j + 1) % 5] = pk4(acc[5][j] + m4[j] * cv[1]); };
  auto fqq = [&](int j) { qp[0][j] = pk4(acc[0][j]); qp[1][j] = pk4(acc[3][j] + cq[1] * mt[j]); };
  g.template pend<10, 13>(acc);
#pragma unroll
  for (int i = 0; i < 6; ++i) { __builtin_amdgcn_sched_group_barrier(0x100, 3, 0); __builtin_amdgcn_sched_group_barrier(0x8, 1, 0); }
  sched0();
  for_c<0, 20>([&](auto mc) {
    constexpr int m = mc.value, n0 = m * 52 / 20, n1 = (m + 1) * 52 / 20;
    g.template mm<(18 - (m & ~1)) + (m & 1)>(acc);
    sched0();
#pragma unroll
    for (int n = n0; n < n1; ++n) {
      const int t = 12 - (n >> 2), e = n & 3, j = t == 12 ? 4 : t / 3, i = t == 12 ? 1 + TH : t % 3;
      float x = acc[i][j][e];
      asm volatile("v_fmac_f32 %0, %1, %2" : "+v"(x) : "v"(i == 2 ? m4[j][e] : i ? ck[0][e] : cq[0][e]), "v"(i == 2 ? cv[0] : mt[j]));
      acc[i][j][e] = x;
    }
    sched0();
  });
  volatile LAS unsigned* xf = (volatile LAS unsigned*)(uintptr_t)(L_TAG + 16 + (h * 2 + TH) * 4);
  if (TH) { fv(4); *(volatile LAS bf16x8*)(uintptr_t)(xb + 2048) = cat(vp[0][0], vp[1][0]); *xf = l + 1; fk(3); }
  else { fk(3); fv(3); *(volatile LAS bf16x8*)(uintptr_t)xb = cat(kp[0][4], kp[1][4]); *(volatile LAS bf16x8*)(uintptr_t)(xb + 1024) = cat(vp[0][4], vp[1][4]); *xf = l + 1; }
  sched0();
  fqq(3); fk(2); fqq(2); fk(1);
  sched0();
  const f32x4 z = {0.f, 0.f, 0.f, 0.f};
  f32x4 sc[4][2]; float a[4], b[4], m[4];
  auto scores = [&](int j) {
#pragma unroll
    for (int u = 1; u >= 0; --u) sc[j][u] = mfma(cat(kp[0][j + u], kp[1][j + u]), cat(qp[0][j], qp[1][j]), z);
  };
  auto pick = [&](int j) {
    const f32x4 s0 = sc[j][0], s1 = sc[j][1];
    a[j] *= sel(par, sel(ca, s1[1], s0[1]), sel(ca, s1[0], s0[0]));
    b[j] *= sel(par, sel(cb, s1[3], s0[3]), sel(cb, s1[2], s0[2]));
    if (!TH && j == 0) { a[j] = sel(ca || sb, a[j], -1e9f * 1.4426950408889634f); b[j] = sel(cb || sb, b[j], -1e9f * 1.4426950408889634f); }
    m[j] = __builtin_fmaxf(a[j], b[j]);
  };
  auto expo = [&](int j) { a[j] = __builtin_amdgcn_exp2f(a[j] - m[j]); b[j] = __builtin_amdgcn_exp2f(b[j] - m[j]); m[j] = a[j] + b[j]; };
  auto pv = [&](int j) {
    const unsigned pa = pk2(a[j] * ra[j], a[j] * ra[j]), pb = pk2(b[j] * rb[j], b[j] * rb[j]);
    const bf16x8 pf = __builtin_bit_cast(bf16x8, (u32x4){pa & m0, pb & m1, pa & (m0 ^ hm), pb & (m1 ^ hm)});
#pragma unroll
    for (int c = 0; c < 2; ++c) sc[j][c] = mfma(cat(vp[c][j], vp[c][j + 1]), pf, z);
  };
  auto outp = [&](int j) { st16(ro, oo + j * 4096, pack_pair(sc[j][0] * m[j], sc[j][1] * m[j])); };
  unsigned f; u32x4 xv, xk;
  auto xrd = [&]() {
    f = xf[1 - 2 * TH]; xv = *(volatile LAS u32x4*)(uintptr_t)(xb + (TH ? 1024 : 2048));
    if (TH) xk = *(volatile LAS u32x4*)(uintptr_t)xb;
  };
  auto xget = [&]() {
    sched0();
    while (__builtin_expect(__builtin_amdgcn_readfirstlane(f) != l + 1, 0)) xrd();
    vp[0][0] = (u32x2){xv[0], xv[1]}; vp[1][0] = (u32x2){xv[2], xv[3]};
    if (TH) { kp[0][0] = (u32x2){xk[0], xk[1]}; kp[1][0] = (u32x2){xk[2], xk[3]}; }
  };
  {
    fqq(1); fk(0); fqq(0);
    if (!TH) fk(4);
    xrd();
    sched0();
    if (TH) fv(3);
    fv(2); fv(1); fv(0);
#pragma unroll
    for (int c = 0; c < 2; ++c)
#pragma unroll
      for (int j = 0; j < 4; ++j) asm volatile("" : "+v"(qp[c][j]), "+v"(kp[c][j + 1]), "+v"(vp[c][j + 1]));
    if (!TH) asm volatile("" : "+v"(kp[0][0]), "+v"(kp[1][0]));
    xget();
    scores(3); scores(2); scores(1); scores(0);
#pragma unroll
    for (int j = 0; j < 4; ++j) { const float r = rq[j] * (0.17677669529663687f * 1.4426950408889634f); a[j] = r * ra[j]; b[j] = r * rb[j]; }
#pragma unroll
    for (int i = 0; i < 8; ++i) { __builtin_amdgcn_sched_group_barrier(0x8, 1, 0); __builtin_amdgcn_sched_group_barrier(0x2, 2, 0); }
    sched0();
    pick(3); pick(2); pick(1); pick(0);
    float u, w;
    swp32(m[0], m[2]); swp32(m[1], m[3]); u = m[0]; w = m[1];
    asm volatile("v_max_f32 %0, %0, %2\n\tv_max_f32 %1, %1, %3\n\ts_nop 1" : "+v"(u), "+v"(w) : "v"(m[2]), "v"(m[3]));
    swp16(u, w); u = vmax(u, w);
    xch16(u, u, w); xch32(u, m[0], m[2]); xch32(w, m[1], m[3]);
    expo(3); expo(2); expo(1); expo(0);
    pv(3); pv(2); pv(1); pv(0);
    swp32(m[0], m[2]); swp32(m[1], m[3]); u = m[0] + m[2]; w = m[1] + m[3];
    swp16(u, w); u = __builtin_amdgcn_rcpf(u + w);
    xch16(u, u, w); xch32(u, m[0], m[2]); xch32(w, m[1], m[3]);
    outp(3); outp(2); outp(1); outp(0);
  }
}

extern "C" __global__ __launch_bounds__(512, 2) void mega_kernel(const u16* x0, const u16* wqkv, const u16* wo, const u16* w1, const u16* w2, const u16* b1, const u16* b2, u16* out, char* ws) {
  const int wid0 = __builtin_amdgcn_readfirstlane(threadIdx.x >> 6);
  asm volatile("s_getpc_b64 s[40:41]\n\ts_andn2_b32 s40, s40, 63\n\ts_load_dwordx2 s[100:101], s[40:41], %0 offset:124\n\ts_load_dwordx2 s[100:101], s[40:41], %0 offset:252"
               :: "s"(__builtin_amdgcn_readfirstlane(threadIdx.x) << 2) : "s40", "s41", "s100", "s101", "scc");
  sched0();
#define PHASE_VARS \
      int tid = threadIdx.x, bid = blockIdx.x, wid = wid0; \
      asm volatile("" : "+v"(tid)); asm volatile("" : "+s"(bid), "+s"(wid), "+s"(ws)); \
      const int lw = wid - 4, lt = tid - 256, lane = tid & 63, fr = lane & 15, fq = lane >> 4, wp = wid & 1, wq = wid >> 1; \
      const unsigned j = (bid >> 3) * 4 + lw; \
      int hp, sb; tile_map<16, 16, 4, 8>(bid, hp, sb);            \
      int tp, tq; tile_map<8, 32, 4, 8>(bid, tp, tq);             \
      int fp, fq1; tile_map<28, 8, 7, 4>(bid, fp, fq1);           \
      const int cw = bid < 16 ? wid + 4 * (bid >> 3) : -1; \
      (void)cw; (void)j; (void)lt; (void)wp; (void)wq; (void)lw; (void)fr; (void)fq; (void)hp; (void)sb; (void)tp; (void)tq; (void)fp; (void)fq1;
#define LOADER_VARS PHASE_VARS asm volatile("" : "+s"(wqkv), "+s"(wo), "+s"(w1), "+s"(w2));

  if (__builtin_expect(wid0 >= 4, 1)) {
    if (LPR) __builtin_amdgcn_s_setprio(3);
    const u16* xin = x0;
    Pfc pf(wo, w1, blockIdx.x * 4 + wid0 - 4);
#pragma unroll 1
    for (int l = 0; l < NL; ++l) {
      {
        LOADER_VARS
        const int s0 = sb * 128;
        GQ::Loader L(wqkv + hp * 64, xin, lw, lane, qkv_rot(bid));
        if (l == 0) L.pre_w1<0>();
        L.seta((unsigned)(s0 * D * 2), halo_off(s0, lw, lane), lw, lane);
        if (l == 0) { L.issue_a<0>(L.ab + L.kord(0) * 128); if (!SI) L.pre<1, true, true>(); }
        sched0();
        const rsrc_t rs = mkrsrc(ws + WS_ST, MB);
        f32x4 c = {0.f, 0.f, 0.f, 0.f}; f32x2 s1 = {0.f, 0.f}, s2 = {0.f, 0.f}, o = {0.f, 0.f}; float pv = 0.f;
        bf16x8 ones = {0x3F80, 0x3F80, 0x3F80, 0x3F80, 0x3F80, 0x3F80, 0x3F80, 0x3F80};
        const int ct = lw & 1 ? 8 + (sb & 3) : sb & 7; const bool cs = l == 0 && lw >= 2;
        int tpf = (slow_res(wqkv) >> 2) != (bid & 1) ? 0 : lw < 2 ? 10 : 9;
        unsigned ra = GQ::AOFF + ((hp & 3) * 32 + lw * 8) * 128 + lane * 16, rb = rd_km(lane, ct);
        unsigned so = lane & 7 ? MB : ph((unsigned)(((l * S + s0 + (hp & 3) * 32 + lw * 8 + (lane >> 3)) * 4 + (hp >> 2)) * 16));
        unsigned co = fr >= 2 || !cs ? MB : ph((unsigned)(ST_CS + ((hp * 96 + ct * 8 + fq * 2 + fr) * 2 + (sb >> 3)) * 16));
        asm volatile("" : "+v"(ra), "+v"(rb), "+v"(so), "+v"(co), "+v"(c), "+v"(s1), "+v"(s2), "+v"(ones), "+v"(o));
        asm volatile("" : "+s"(tpf));
        if (SI && l == 0) L.pre_w1<1>();
        sched0();
        unsigned to[3], xo = x_off(tp, tq, lw, fr, fq), q2 = ((bid & 2) * 4 + slow_res(wqkv)) * 128 + ((bid >> 3) * 2 + (lw & 1)) * (3 * D * 2);
#pragma unroll
        for (int i = 0; i < 3; ++i) {
          const int r = s0 - 16 + (i == 2 && lw ? 64 : i * 64) + (lt >> 2);
          to[i] = ph((unsigned)(((l * S + (r < 0 ? 0 : r)) * 4 + (lt & 3)) * 16));
        }
        unsigned ta = (lt & 3) < (lw ? 2 : 3) ? L_TAB + ((lt & 3) * 64 + (lt >> 2)) * 4 : L_SCR, ts = ph((unsigned)(ST_CS + (hp * 192 + (lw ? lt - 64 : lt)) * 16));
        asm volatile("" : "+v"(to[0]), "+v"(to[1]), "+v"(to[2]), "+v"(xo), "+v"(ta), "+v"(ts));
        asm volatile("" : "+s"(q2));
        if (SI == 1 && l == 0) L.pre<1, false, true>();
        sched0();
        const GW::Loader W(wo + tp * 128, ws, lw, lane);
        if (SI == 2 && l == 0) L.pre<1, false, true>();
        sched0();
        auto pfq = [&](int n) { const unsigned o = q2 + L.kord(n) * GQ::WSTEP; spf2<0>(wqkv, o); spf2<2048>(wqkv, o); spf2<4096>(wqkv, o); };
        if (l) {
          if (QBR == 3) bar();
          if (!(EW & 8)) L.pre_w();
          if (tpf) { pfq(2 + (lw >> 1)); pfq(4 + (lw >> 1)); }
          if (QBR == 4) bar();
          L.start();
        }
        if (LPR == 1) __builtin_amdgcn_s_setprio(0);
        unsigned tag, tg2; u32x4 gs, a[3];
        auto rowst = [&](int i) {
          const float m0 = __uint_as_float(a[i][0]), m = red4(m0) * 0.25f, q = red4(__uint_as_float(a[i][2]) + 256.f * (m0 - m) * (m0 - m));
          o = (f32x2){sel((lt & 3) == i, m, o.x), sel((lt & 3) == i, q, o.y)};
          asm volatile("" : "+v"(o));
        };
        for_c<0, 16 - GQ::H>([&](auto tc) {
          constexpr int t = tc.value;
          u32x4 v; unsigned tv;
          L.step<t, t == 5 || t == 6 ? 1 : t == 9 || t == 10 ? 4 : 0>([&](unsigned ls) {
            if (t < 4) v = *(const LAS u32x4*)(uintptr_t)(ls + ra);
            if (t == 3) tv = *(volatile LAS unsigned*)(uintptr_t)L_TAG;
          }, [&]() {
            if (t < 8 && cs) {
              constexpr int ls = 32768 + t % 3 * GQ::SB;
              bf16x4 x[4];
#pragma unroll
              for (int e = 0; e < 4; ++e) x[e] = lds_tr4(rb + (ls & ~0x7fff) + (ls & 0x7fff) + (e >> 1) * 4096 + (e & 1) * 512);
              if (CP) { wait_lgkm0(); sched0(); __builtin_amdgcn_s_setprio(3); sched0(); }
#pragma unroll
              for (int ks = 0; ks < 2; ++ks) c = mfma(__builtin_shufflevector(x[2 * ks], x[2 * ks + 1], 0, 1, 2, 3, 4, 5, 6, 7), ones, c);
              if (CP) { asm volatile("" : "+v"(c)); sched0(); __builtin_amdgcn_s_setprio((SPM >> t) & 1 ? 2 : 0); sched0(); }
            } else if (t >= 4) {
              if (t >= 10 || t >= tpf) { if (t < TC1) pf.go(); }
              else if (lw < 2) pfq(t + 4);
              else if (t == 8) { pfq(14); pfq(15); }
            }
            if (t < 4) {
              if (t == 0) { pv = bflo(v[0]); asm volatile("" : "+v"(pv)); }
#pragma unroll
              for (int e = 0; e < 4; ++e) {
                if (SSC) {
                  float x = bflo(v[e]), y = bfhi(v[e]), a0 = s1.x, a1 = s1.y, b0 = s2.x, b1 = s2.y;
                  asm volatile("v_sub_f32 %0, %0, %6\n\tv_sub_f32 %1, %1, %6\n\tv_add_f32 %2, %2, %0\n\tv_add_f32 %3, %3, %1\n\tv_fmac_f32 %4, %0, %0\n\tv_fmac_f32 %5, %1, %1"
                               : "+v"(x), "+v"(y), "+v"(a0), "+v"(a1), "+v"(b0), "+v"(b1) : "v"(pv));
                  s1 = (f32x2){a0, a1}; s2 = (f32x2){b0, b1};
                } else { const f32x2 d = (f32x2){bflo(v[e]), bfhi(v[e])} - pv; s1 += d; s2 += d * d; }
              }
              asm volatile("" : "+v"(s1), "+v"(s2));
            }
            if (t == 3) { tag = __builtin_amdgcn_readfirstlane(tv) + 1u; tg2 = tag2(tag); if (tpf == 10) pfq(7); if (l && tpf == 9) pfq(6); }
            if (t == 4) {
              const float d1 = (s1.x + s1.y) * (1.f / 32.f), ml = pv + d1, m = red8(ml) * 0.125f;
              const float q = red8(vmax((s2.x + s2.y) - 32.f * d1 * d1, 0.f) + 32.f * (ml - m) * (ml - m));
              gs = (u32x4){__float_as_uint(m), tag, __float_as_uint(q), tg2};
              asm volatile("" : "+v"(gs));
            }
            if (t == 8) { gs = (u32x4){__float_as_uint(sel(fr, c[2], c[0])), tag, __float_as_uint(sel(fr, c[3], c[1])), tg2}; asm volatile("" : "+v"(gs)); }
            if (t == (RB ? 12 : 11)) {
#pragma unroll
              for (int i = 0; i < 3; ++i) ld_ok(a[i]);
              for (int spin = 0;; ++spin) {
                unsigned bad = 0;
#pragma unroll
                for (int i = 0; i < 3; ++i) bad |= (a[i][1] ^ tag) | (a[i][3] ^ tg2);
                if (__builtin_expect(__all(bad == 0), 1) || spin_fail(spin, ws)) break;
                __builtin_amdgcn_s_sleep(8);
#pragma unroll
                for (int i = 0; i < 3; ++i) ld_again(a[i], rs, to[i]);
              }
              if (!RB) rowst(0);
            }
            if (t == 12 && !RB) { rowst(1); rowst(2); }
            if (t == 13 && RB == 2) rowst(0);
            if (t == 13 && !RB) { LAS float* p = (LAS float*)(uintptr_t)ta; p[0] = -o.x; p[144] = __builtin_amdgcn_rsqf(o.y * (1.f / D) + EPS); }
          }, [&]() {
            if (t == 4) st16(rs, so, gs);
            if (t == 8) {
              st16(rs, co, gs);
              sched0();
#pragma unroll
              for (int i = 0; i < 3; ++i) ld_raw(a[i], rs, to[i]);
            }
          });
        });
        if (l == 0) {
#pragma unroll
          for (int p = 0; p < 4; ++p) dma16((const char*)xin + (p >> 1) * 32768 + (p & 1) * 64, xo, L_X + lw * 4096 + p * 1024);
          ld_raw(gs, rs, ts);
          wait_vm<GQ::CNT + 5>();
        } else wait_vm<GQ::CNT>();
        bar();
        W.pre_w1<0>();
        if (RB) { if (RB == 1) rowst(0); rowst(1); rowst(2); LAS float* p = (LAS float*)(uintptr_t)ta; p[0] = -o.x; p[144] = __builtin_amdgcn_rsqf(o.y * (1.f / D) + EPS); }
        wait_lgkm0();
        if (l == 0) wait_vm<9>(); else wait_vm<4>();
        bar();
        W.pre_w1<1>(); W.pre_w1<2>();
        if (l == 0) {
          wait_vm<12>();
          ld_ok(gs);
          for (int spin = 0;; ++spin) {
            if (__builtin_expect(__all(((gs[1] ^ tag) | (gs[3] ^ tg2)) == 0), 1) || spin_fail(spin, ws)) break;
            __builtin_amdgcn_s_sleep(8);
            ld_again(gs, rs, ts);
          }
          float c0 = __uint_as_float(gs[0]), c1 = __uint_as_float(gs[2]);
          c0 += dppf<0xB1>(c0); c1 += dppf<0xB1>(c1);
          *(LAS float*)(uintptr_t)(L_CS + (lw ? lt - 64 : lt) * 4) = sel(lt & 1, c1, c0);
        }
        if (l == 0 && lw < TBA) { if (fr < 14) touch4(w1 + (size_t)(((bid >> 3) * TBA + lw) * 8 + (bid & 4)) * F + (bid & 3) * 896, fq * (F * 2) + fr * 128, L_SCR + lw * 256); }
        if (l == 0) { wait_lgkm0(); bar(); }
      }
      {
        LOADER_VARS
        GW::Loader L(wo + tp * 128, ws + WS_O + (size_t)tq * 32 * 8192, lw, lane);
        const G1::Loader W(w1 + fp * 128, ws, lw, lane);
        unsigned po = ((bid & 1) * 8 + (slow_res(wo) ^ (j & 1))) * 128 + (j >> 1) * (D * 2);
#pragma unroll
        for (int k = GW::H; k < 10; ++k) spf2(wo, po + k * GW::WSTEP);
        asm volatile("" : "+s"(po));
        if (!(LB & 1)) { sched0(); bar(); } else wait_lgkm0();
#pragma unroll
        for (int k = 10; k < 16; ++k) spf2(wo, po + k * GW::WSTEP);
#pragma unroll
        for (int r = 0; r < TLQ; ++r) { if (pf.pc < 3 + NW1) { wait_lgkm0(); pf.go(); } }
        if (LB & 1) { sched0(); bar(); }
        L.start();
        L.run(16 - GW::H, [&](int) { pf.go(); });
        pf.go();
        if (E2 && bid < 224) {
          wait_vm<2 * GW::CNT>(); bar(); W.pre_w1<1>();
          wait_vm<GW::CNT + 4>(); bar(); W.pre_w1<2>();
          wait_vm<8>(); bar();
          bar(); W.pre_w1<0>();
        } else { L.tail<GW::H - 1>(); bar(); }
      }
      {
        LOADER_VARS
        if (bid < 224) {
          G1::Loader L(w1 + fp * 128, ws + WS_H + (size_t)fq1 * 64 * 16384, lw, lane);
          if (!E2) L.pre_w();
          const G2A::Loader W((const char*)(w2 + (tq & 23) * 64) + (tq & 8 ? JA * G2::WSTEP : 0), ws, lw, lane);
          const int g0 = (bid & 3) * 14, ga = (g0 + ((slow_res(w1) - g0) & 7)) ^ (j & 1), gb = ga + 8 < g0 + 14 ? ga + 8 : ga;
          const unsigned j2 = j < 32 ? 112 + (j & 15) : j;
          unsigned pa = (j >> 1) * (F * 2) + ga * 128, pb = (j >> 1) * (F * 2) + gb * 128, pd = (j2 >> 1) * (F * 2) + (j & 16 ? gb : ga) * 128;
          asm volatile("" : "+s"(pa), "+s"(pb), "+s"(pd));
          auto pfb = [&](int k) { spf2(w1, pa + k * G1::WSTEP); spf2(w1, pb + k * G1::WSTEP); spf2(w1, pd + k * G1::WSTEP); };
#pragma unroll
          for (int k = G1::H; k < G1::H + PA_1; ++k) pfb(k);
#pragma unroll
          for (int r = 0; r < TLW; ++r) { if (pf.pc < 3 + NW1) { wait_lgkm0(); pf.go(); } }
          sched0();
          bar();
          if (!(LB & 2)) pf.go();
          L.start();
          L.run(32 - G1::H, [&](int t) {
            if (pf.pc < 3 + NW1) pf.go();
            else if (t < 32 - G1::H - PA_1) pfb(t + G1::H + PA_1);
          });
          if (EW & 4) {
            wait_vm<2 * G1::CNT>(); bar(); W.pre_w1<1>();
            wait_vm<G1::CNT + 4>(); bar();
            wait_vm<4>(); bar(); W.pre_w1<0>();
          } else L.tail<G1::H - 1>();
          bar();
        } else {
          wait_lgkm0(); pf.go();
          bar();
          if (l == 0) {
            __builtin_amdgcn_s_sleep(24);
            while (!SLS && pf.pc < 3 + NW1) { wait_lgkm0(); pf.go(); }
            spare_touch(w1, w2, bid, wid, lane, [&]() { if (SLS) { wait_lgkm0(); pf.go(); } });
          }
        }
        pf.pc = 99;
      }
      {
        LOADER_VARS
        constexpr int NB = F / 64 - 2 * JA;
        const char* ub = ws + WS_U + (size_t)tp * 112 * 16384;
        G2A::Loader A((const char*)(w2 + (tq & 23) * 64) + (tq & 8 ? JA * G2::WSTEP : 0), ub + (tq & 8 ? JA * 32768 : 0), lw, lane);
        if (!(EW & 4) || bid >= 224) A.pre_w();
        const GQ::Loader W(wqkv + hp * 64, ws, lw, lane, qkv_rot(bid));
        unsigned pb = ((bid & 4) * 4 + (j & 1) * 8 + slow_res(w2)) * 128 + (j >> 1) * (S * 2) + JA * G2::WSTEP, pa = pb - (bid & 2 ? 0 : JA * G2::WSTEP);
        asm volatile("" : "+s"(pa), "+s"(pb));
        auto side = [&](int n) { n += G2A::H + PA_2; if (n < JA + NB) spf2(w2, (n < JA ? pa : pb) + n * G2::WSTEP); };
#pragma unroll
        for (int k = 0; k < PA_2; ++k) side(k - PA_2);
        G2::Loader L((const char*)(w2 + tq * 64) + 2 * JA * G2::WSTEP, ub + 2 * JA * 32768, lw, lane);
        gu32* xf = (gu32*)(ws + WS_MAIL) + (l * 4 + 2) * MEDGE + (tq * 8 + tp) * 64 + 28 + lw;
        const char* xp = ws + WS_XP + (unsigned)(((bid ^ 2) * 4 + lw) << 14);
        unsigned fl = L_QKV + lw * 256, xv = ph(lane * 16);
        asm volatile("" : "+s"(xf), "+s"(xp), "+s"(fl)); asm volatile("" : "+v"(xv));
        sched0();
        bar();
        A.start();
        A.run(JA - G2A::H, [&](int t) { side(t); });
        auto stage = [&](auto s) { L.issue_w<s.value * G2::SB>(L.w + s.value * G2::WSTEP); L.issue_a<s.value * G2::SB>(L.ab + s.value * 32768); sched0(); };
        wait_vm<G2A::CNT>(); bar(); stage(IC<0>{}); side(JA - 2);
        wait_vm<G2::CNT>(); bar(); stage(IC<1>{}); side(JA - 1);
        wait_vm<G2::CNT>(); bar(); stage(IC<2>{}); stage(IC<3>{}); side(JA);
        L.t = 1; L.islot = 0;
        L.run(NB - G2::H, [&](int t) {
          if (t == NB - G2::H - FLS) asm volatile("s_mov_b32 m0, %2\n\ts_nop 0\n\tglobal_load_lds_dword %0, %1 sc1" :: "v"(0u), "s"(xf), "s"(fl) : "memory");
          side(JA + t);
        });
        auto xdma = [&]() {
#pragma unroll
          for (int i = 0; i < 4; ++i) dma16(xp + i * 2048, xv, L_X + lw * 4096 + i * 1024);
          sched0();
        };
        sched0(); wait_vm<2 * G2::CNT>(); bar();
        if (__builtin_amdgcn_readfirstlane(*(volatile LAS unsigned*)(uintptr_t)fl)) {
          xdma();
          if ((EW & 8) && l == 0) { W.pre_w1<0>(); wait_vm<G2::CNT + 10>(); bar(); W.pre_w1<1>(); wait_vm<16>(); bar(); wait_vm<12>(); }
          else { wait_vm<G2::CNT + 4>(); bar(); wait_vm<4>(); bar(); wait_vm<0>(); }
        } else {
          wait_vm<G2::CNT>(); bar(); wait_vm<0>(); bar();
          for (int spin = 0;; ++spin) {
            if (__builtin_amdgcn_readfirstlane(__hip_atomic_load(xf, RLX_AGENT)) || spin_fail(spin, ws)) break;
            __builtin_amdgcn_s_sleep(2);
          }
          xdma();
          if ((EW & 8) && l == 0) { W.pre_w(); wait_vm<12>(); } else wait_vm<0>();
        }
        bar();
        xin = (const u16*)(ws + WS_X2);
        if (NL == 1) bar();
      }
    }
    return;
  }

  {
    PHASE_VARS
    if (cw >= 0) code_warm(cw, lane, L_SCR + wid * 256);
    pfb_q(wqkv, bid, (bid >> 3) * 4 + wid, 0); pfb_q(wqkv, bid, (bid >> 3) * 4 + wid, 1);
    if (wid < 3) {
      if (tid == 0) asm volatile("s_mov_b32 m0, %2\n\ts_nop 0\n\tglobal_load_lds_dword %0, %1 sc1" :: "v"(WS_EPOCH), "s"(ws), "s"(L_TAG) : "memory");
      const rsrc_t rz = mkrsrc(ws + WS_XCH, 3 * MB);
      if (tid < 128) st16(rz, ph((((tid >> 6) * 256 + tq * 8 + tp) * 64 + lane) * 16), (u32x4){0u, SENT, 0u, SENT});
      else {
        const int e = (tid >> 3) & 7, line = e == 0 || e == 7 ? (2 * sb + (e & 1)) * 8 + (hp & 7) : e * 256 + bid;
        st16(rz, WS_MAIL - WS_XCH + line * 256 + (tid & 7) * 16, (u32x4){0u, 0u, 0u, 0u});
      }
      if (wid == 1 && bid < 224) touch4(b1 + fp * 128, lane * 4, L_BIAS);
      if (wid == 2) touch4(b2 + tq * 64, (lane & 31) * 4, L_BIAS + 256);
      if (wid == 0) *(LAS u32x4*)(uintptr_t)(L_TAG + 16) = (u32x4){0u, 0u, 0u, 0u};
    }
    if (CPR) __builtin_amdgcn_s_setprio(CPR);
  }
#pragma unroll 1
  for (int l = 0; l < NL; ++l) {
    {
      PHASE_VARS
      if (wid & 2) qkv_attn<1>(ws, l, wp, lane, hp, sb, cw, bid, tp, tq, wqkv);
      else qkv_attn<0>(ws, l, wp, lane, hp, sb, cw, bid, tp, tq, wqkv);
    }
    {
      PHASE_VARS
      GW::Comp<O42<false, WNE>> g; g.setup(wp, wq, lane);
      g.setwin();
      const rsrc_t rx = mkrsrc(ws + WS_XCH + l * (MB / 2), MB / 2), rh = mkrsrc(ws + WS_H, S * D * 4);
      unsigned xa = L_X + wid * 4096 + lane * 16;
      unsigned xs = fq != fr >> 2 ? MB : ph((unsigned)(((tq * 8 + tp) * 64 + wp * 32 + wq * 16 + fr) * 16)), xl = ph((unsigned)((tq * 512 + fq * 128 + wq * 16 + fr) * 16));
      const unsigned h0 = ph((unsigned)(((tp * 32 + tq) << 14) + wp * 8192 + (wq * 32 + fr) * 128 + ((pair_chunk(fq) ^ (km_f(fr) << 1)) << 4)));
      u32x4 ho = {h0, h0 ^ 64, h0 + 4096, (h0 ^ 64) + 4096};
      bf16x8 ones = {0x3F80, 0x3F80, 0x3F80, 0x3F80, 0x3F80, 0x3F80, 0x3F80, 0x3F80};
      asm volatile("" : "+v"(xa), "+v"(xs), "+v"(xl), "+v"(ho), "+v"(ones));
      Gate gt;
      auto edge = [&]() {
        sync_post(ws, l * 4, lane < 16 ? (sb * 16 + lane) * 64 + hp : -1, wid);
        gt.wait<0>(ws, l * 4, tq * 8 + tp, 0xffffu, (1u << GW::H) - 1, (1u << (GW::H + GLW)) - 1, wid);
        sched0();
      };
      if (WSP == 3) edge();
      f32x4 acc[4][2];
#pragma unroll
      for (int i = 0; i < 4; ++i)
#pragma unroll
        for (int jj = 0; jj < 2; ++jj) acc[i][jj] = (f32x4){0.f, 0.f, 0.f, 0.f};
      { bf16x8 s[2]; sel_frags(true, fr, fq, s); tile_add(acc, s, xa); }
      g.pin(acc);
      if (WSP == 2) edge(); else mfma_gap();
      g.run_steps<false>(acc, [&](int it, int i) {
        if (i == -1) gt.arm(it, cw);
        if (i == 0) gt.head(it, cw, lane, wid);
        if (i == GLW - 1) gt.late(ws);
      });
      bar();
      sched0();
      g.pend<0, 4>(acc);
      u32x4 pk[2][2]; float mp[2];
      {
        f32x4 sm[2], gm[2];
#pragma unroll
        for (int i = 0; i < 2; ++i)
#pragma unroll
          for (int jj = 0; jj < 2; ++jj) {
            pk[jj][i] = cat4(pk4(acc[2 * i][jj]), pk4(acc[2 * i + 1][jj])); asm volatile("" : "+v"(pk[jj][i]));
            const bf16x8 x = __builtin_bit_cast(bf16x8, pk[jj][i]);
            gm[jj] = mfma(x, x, i ? gm[jj] : (f32x4){0.f, 0.f, 0.f, 0.f}); sm[jj] = mfma(ones, x, i ? sm[jj] : (f32x4){0.f, 0.f, 0.f, 0.f});
          }
        u32x4 st;
#pragma unroll
        for (int jj = 0; jj < 2; ++jj) {
          const f32x4 gv = gm[jj];
          st[2 * jj] = __float_as_uint(sm[jj][0]); st[2 * jj + 1] = __float_as_uint(sel(fr & 2, sel(fr & 1, gv[3], gv[2]), sel(fr & 1, gv[1], gv[0])));
          mp[jj] = sm[jj][0] * (1.f / 64.f);
        }
        st16(rx, xs, st);
      }
      sched0();
      f32x4 gr[4];
      auto gld = [&]() {
        asm volatile("" ::: "memory");
#pragma unroll
        for (int p = 0; p < 4; ++p) gr[p] = __builtin_bit_cast(f32x4, __builtin_amdgcn_raw_buffer_load_b128(rx, xl + p * 1024, 0, 16));
        sched0();
      };
      if (XE == 1) gld();
#pragma unroll
      for (int p = 0; p < 4; ++p) *(LAS u32x4*)(uintptr_t)(xa + p * 1024) = pk[p >> 1][p & 1];
      asm volatile("" : "+v"(mp[0]), "+v"(mp[1]));
#pragma unroll
      for (int jj = 0; jj < 2; ++jj)
#pragma unroll
        for (int i = 0; i < 2; ++i) {
          const auto rx_ = __builtin_amdgcn_permlane16_swap(pk[jj][i][0], pk[jj][i][2], false, false), ry_ = __builtin_amdgcn_permlane16_swap(pk[jj][i][1], pk[jj][i][3], false, false);
          const unsigned x0 = rx_[0], x1 = rx_[1], y0 = ry_[0], y1 = ry_[1];
          acc[2 * i][jj] = (f32x4){bflo(x0), bfhi(x0), bflo(y0), bfhi(y0)} - mp[jj]; acc[2 * i + 1][jj] = (f32x4){bflo(x1), bfhi(x1), bflo(y1), bfhi(y1)} - mp[jj];
          asm volatile("" : "+v"(acc[2 * i][jj]), "+v"(acc[2 * i + 1][jj]));
          if (XE == 2 + jj * 2 + i) { sched0(); gld(); }
        }
      if (!XE) { if (XD) __builtin_amdgcn_s_sleep(XD); gld(); }
      for (int spin = 0;; ++spin) {
        const float lo = vmin3(vmin3(gr[0][1], gr[0][3], gr[1][1]), vmin3(gr[1][3], gr[2][1], gr[2][3]), vmin3(gr[3][1], gr[3][3], gr[3][3]));
        if (!__any(lo < 0.f) || spin_fail(spin, ws)) break;
        if (XS) __builtin_amdgcn_s_sleep(XS);
        gld();
      }
      {
        const f32x4 t = (gr[0] + gr[1]) + (gr[2] + gr[3]);
        float m[2] = {t[0], t[2]}, q[2] = {t[1], t[3]}, u, w, v, y;
        swp32(m[0], m[1]); swp32(q[0], q[1]);
        xch16(m[0] + m[1], u, w); xch16(q[0] + q[1], v, y);
        const float mean = (u + w) * (1.f / D), var = __builtin_fmaxf((v + y) * (1.f / D) - mean * mean, 0.f), r1 = __builtin_amdgcn_rsqf(var + EPS);
        xch32(mean, m[0], m[1]);
        xch32(r1 * __builtin_amdgcn_rsqf(var * r1 * r1 + EPS), q[0], q[1]);
#pragma unroll
        for (int jj = 0; jj < 2; ++jj) {
          const float c = (mp[jj] - m[jj]) * q[jj];
#pragma unroll
          for (int i = 0; i < 2; ++i) st16(rh, ho[jj * 2 + i], cat4(pk4(acc[2 * i][jj] * q[jj] + c), pk4(acc[2 * i + 1][jj] * q[jj] + c)));
        }
      }
    }
    {
      PHASE_VARS
      const int post = lane < 28 ? (tp * 28 + lane) * 64 + tq : -1;
      if (bid >= 224) {
        sync_post(ws, l * 4 + 1, post, wid);
        if (l == 0 && bid == 255 && wid == 0) { const unsigned tag = get_tag(); if (lane == 0) __hip_atomic_store((gu32*)(ws + WS_EPOCH), tag, RLX_AGENT); }
        if (l == 0) { __builtin_amdgcn_s_sleep(24); spare_touch(w1, w2, bid, wid, lane, []() {}); }
      } else {
        G1::Comp<O44<false, 32>> g; g.setup(wp, wq, lane);
        f32x4 acc[4][4];
#pragma unroll
        for (int i = 0; i < 4; ++i) { const u32x2 b = *(const LAS u32x2*)(uintptr_t)(L_BIAS + (wp * 4 + i) * 32 + fq * 8);
#pragma unroll
          for (int jj = 0; jj < 4; ++jj) acc[i][jj] = (f32x4){bflo(b.x), bfhi(b.x), bflo(b.y), bfhi(b.y)}; }
        const rsrc_t ro = mkrsrc(ws + WS_U, D * F * 4);
        const unsigned u0 = ph((unsigned)(((fq1 * 56 + fp * 2 + wp) << 14) + (wq * 64 + fr) * 128 + ((pair_chunk(fq) ^ (fr >> 1)) << 4)));
        u32x4 uo = {u0, u0 + 4096, u0 + 8192, u0 + 12288}, uo2 = uo ^ 64;
        asm volatile("" : "+v"(uo), "+v"(uo2));
        g.prep(acc);
        sync_post(ws, l * 4 + 1, post, wid);
        Gate gt; gt.wait<SW0>(ws, l * 4 + 1, fq1 * 28 + fp, ~0u, (1u << G1::H) - 1, (1u << (G1::H + GL1)) - 1, wid);
        g.run_steps<false>(acc, [&](int it, int i) {
          if (i == -1) gt.arm(it, cw);
        if (i == 0) gt.head(it, cw, lane, wid);
          if (i == GL1 - 1) gt.late(ws);
        });
        bar();
        sched0();
        if (GSH) {
          for_c<0, 2>([&](auto jc) {
            constexpr int jj = jc.value;
            f32x4 a[2];
#pragma unroll
            for (int i = 0; i < 2; ++i) { a[i] = gelu_arg(acc[i][jj]); asm volatile("" : "+v"(a[i])); }
            sched0();
            for_c<0, 4>([&](auto ec) { constexpr int e = ec.value; g.mm<jj * 8 + e>(acc); sched0(); a[0][e] = __builtin_amdgcn_exp2f(a[0][e]); a[1][e] = __builtin_amdgcn_exp2f(a[1][e]); asm volatile("" : "+v"(a[0]), "+v"(a[1])); sched0(); });
            a[0] += 1.f; a[1] += 1.f; asm volatile("" : "+v"(a[0]), "+v"(a[1]));
            sched0();
            for_c<0, 4>([&](auto ec) { constexpr int e = ec.value; g.mm<jj * 8 + 4 + e>(acc); sched0(); a[0][e] = __builtin_amdgcn_rcpf(a[0][e]); a[1][e] = __builtin_amdgcn_rcpf(a[1][e]); asm volatile("" : "+v"(a[0]), "+v"(a[1])); sched0(); });
            st16(ro, uo[jj], pack_pair(acc[0][jj] * a[0], acc[1][jj] * a[1]));
          });
        } else g.pend<0, 8>(acc);
#pragma unroll
        for (int n = GSH ? 2 : 0; n < 8; ++n) { const int i = n >> 2, jj = n & 3; st16(ro, (i ? uo2 : uo)[jj], pack_pair(gelu_tanh(acc[2 * i][jj]), gelu_tanh(acc[2 * i + 1][jj]))); }
      }
    }
    {
      PHASE_VARS
      asm volatile("" : "+s"(out));
      G2A::Comp<O44<true, ANE>> ga; ga.setup(wp, wq, lane);
#pragma unroll
      for (int jj = 0; jj < 4; ++jj) ga.qrd[0][jj] = 65536 + rd_km(lane, wq * 2 + (jj & 1)) + (((jj >> 1) ^ (tq >> 3)) & 1) * 8192;
      G2::Comp<O42<true, BNE>> g; g.setup(wp, wq, lane);
      g.setwin();
      ga.setwin();
      Gate gt;
      auto edge = [&]() {
        sync_post(ws, l * 4 + 2, bid < 224 && lane < 32 ? (lane * 8 + fq1) * 64 + fp : -1, wid);
        gt.wait<0>(ws, l * 4 + 2, tq * 8 + tp, 0xfffffffu, tq & 8 ? 1u << JA / 2 : 1u, tq & 8 ? 7u << JA / 2 : 7u, wid);
        sched0();
      };
      if (GSP == 3) edge();
      f32x4 ac4[4][4], acc[4][2];
#pragma unroll
      for (int jj = 0; jj < 2; ++jj) { const float b = bf2f(*(const LAS u16*)(uintptr_t)(L_BIAS + 256 + (wq * 2 + jj) * 32 + fr * 2));
#pragma unroll
        for (int i = 0; i < 4; ++i) { ac4[i][jj] = (f32x4){b, b, b, b}; ac4[i][jj + 2] = (f32x4){0.f, 0.f, 0.f, 0.f}; } }
      unsigned xa = L_X + wid * 4096 + lane * 16, xo = (unsigned)((bid * 4 + wid) << 14) + ph(lane * 16), oo = x_off(tp, tq, wid, fr, fq), xo2 = xo + 4096, oo2 = oo + 32768;
      unsigned fo = lane ? MB : (((tq ^ 8) * 8 + tp) * 64 + 28 + wid) * 4u, one = 1u;
      bf16x8 s[2]; sel_frags(false, fr, fq, s);
      if (GSP == 4) { asm volatile("" : "+v"(xa), "+v"(xo), "+v"(oo), "+v"(fo), "+v"(s[0]), "+v"(s[1])); edge(); }
      tile_add(ac4, s, xa);
      const rsrc_t rx = mkrsrc(ws + WS_XP, 16 * MB), rf = mkrsrc(ws + WS_MAIL + (l * 4 + 2) * (MEDGE * 4), MEDGE * 4), ro = mkrsrc(l ? (const void*)out : (const void*)(ws + WS_X2), S * D * 2);
      asm volatile("" : "+v"(xa), "+v"(xo), "+v"(oo), "+v"(xo2), "+v"(oo2), "+v"(fo), "+v"(one), "+v"(s[0]), "+v"(s[1]));
      ga.pin(ac4);
      static_assert(JA % 2 == 0 && G2A::H == 2, "");
      if (GSP == 2) edge(); else mfma_gap();
      ga.run_steps<false>(ac4, [&](int it, int i) {
        if (i == -1) gt.arm(it, cw);
        if (i == 0) gt.head(it, cw, lane, wid);
        if (i == 2) gt.late(ws);
      });
      ga.pend<4, 8>(ac4);
#pragma unroll
      for (int i = 0; i < 2; ++i)
#pragma unroll
        for (int jj = 0; jj < 2; ++jj) st16(rx, (jj ? xo2 : xo) + i * 2048, cat4(pk4(ac4[2 * i][jj + 2]), pk4(ac4[2 * i + 1][jj + 2])));
#pragma unroll
      for (int i = 0; i < 4; ++i) { acc[i][0] = ac4[i][0]; acc[i][1] = ac4[i][1]; }
#pragma unroll
      for (int k = 0; k < 2; ++k) { g.fp[2][k] = ga.fp[2][k]; g.fp[3][k] = ga.fp[3][k]; g.fq[0][k] = ga.fq[0][k]; g.fq[1][k] = ga.fq[1][k]; }
      sched0();
      g.run_steps<true>(acc, [&](int it, int i) {
        if (i == PS % 4 && __builtin_expect(it == (F / 64 - 2 * JA) / 4 - PS / 4, 0)) {
          wait_vm<0>();
          __builtin_amdgcn_raw_buffer_store_b32(one, rf, fo, 0, 16);
        }
      });
      bar();
      sched0();
      g.pend<0, 4>(acc);
      tile_add(acc, s, xa);
      u32x4 xr[4];
#pragma unroll
      for (int i = 0; i < 2; ++i)
#pragma unroll
        for (int jj = 0; jj < 2; ++jj) { xr[jj * 2 + i] = pack_pair(acc[2 * i][jj], acc[2 * i + 1][jj]); st16(ro, (jj ? oo2 : oo) + i * 64, xr[jj * 2 + i]); }
      if (l == 0) {
#pragma unroll
        for (int p = 0; p < 4; ++p) *(LAS u32x4*)(uintptr_t)(xa + p * 1024) = xr[p];
        if (NL == 1) sync_post(ws, 3, -1, wid);
      }
    }
  }
}

extern "C" void kernel_launch(void* const* d_in, const int* in_sizes, int n_in,
                              void* d_out, int out_size, void* d_ws, size_t ws_size,
                              hipStream_t stream) {
  (void)hipFuncSetAttribute((const void*)mega_kernel, hipFuncAttributeMaxDynamicSharedMemorySize, 163840);
  mega_kernel<<<256, 512, 163840, stream>>>((const u16*)d_in[0], (const u16*)d_in[1], (const u16*)d_in[2], (const u16*)d_in[4], (const u16*)d_in[6], (const u16*)d_in[5], (const u16*)d_in[7], (u16*)d_out, (char*)d_ws);
}
```

```cpp
#include <hip/hip_runtime.h>
#include <hip/hip_bf16.h>
#include <stdint.h>

typedef unsigned short u16;
typedef unsigned long long u64;
typedef short bf16x8 __attribute__((ext_vector_type(8)));
typedef short bf16x4 __attribute__((ext_vector_type(4)));
typedef float f32x4 __attribute__((ext_vector_type(4)));
typedef float f32x2 __attribute__((ext_vector_type(2)));
typedef unsigned u32x4 __attribute__((ext_vector_type(4)));
typedef unsigned u32x2 __attribute__((ext_vector_type(2)));
typedef __bf16 bf16v2 __attribute__((ext_vector_type(2)));
#define LAS __attribute__((address_space(3)))
#define GAS __attribute__((address_space(1)))
#define DEVI __device__ __forceinline__
typedef __attribute__((address_space(1))) unsigned gu32;
typedef __attribute__((address_space(1))) unsigned long long gu64;

constexpr int S = 2048, D = 1024, F = 3584;
constexpr float EPS = 1e-5f;

DEVI float bf2f(u16 v) { return __uint_as_float(((unsigned)v) << 16); }
DEVI float bflo(unsigned v) { return __uint_as_float(v << 16); }
DEVI float bfhi(unsigned v) { return __uint_as_float(v & 0xffff0000u); }
DEVI unsigned pk2(float lo, float hi) { bf16v2 p; p.x = (__bf16)lo; p.y = (__bf16)hi; return __builtin_bit_cast(unsigned, p); }

DEVI const void* uni_ptr(const void* p) {
  const u64 v = (u64)p;
  return (const void*)(((u64)(unsigned)__builtin_amdgcn_readfirstlane((unsigned)(v >> 32)) << 32) | (unsigned)__builtin_amdgcn_readfirstlane((unsigned)v));
}
DEVI void dma16(const void* sbase, unsigned voff, unsigned ldsaddr) {
  ldsaddr = __builtin_amdgcn_readfirstlane(ldsaddr);
  asm volatile("s_mov_b32 m0, %2\n\ts_nop 0\n\tglobal_load_lds_dwordx4 %0, %1 sc1" :: "v"(voff), "s"(sbase), "s"(ldsaddr) : "memory");
}
template <int N, int IMM> DEVI void dma_grp(const void* sbase, const unsigned* v, unsigned lb) {
  static_assert(N == 1 || N == 2 || N == 4, "");
  if constexpr (N == 4)
    asm volatile("s_add_u32 m0, %1, %2\n\ts_nop 0\n\tglobal_load_lds_dwordx4 %3, %0 sc1\n\tglobal_load_lds_dwordx4 %4, %0 offset:1024 sc1\n\t"
                 "global_load_lds_dwordx4 %5, %0 offset:2048 sc1\n\tglobal_load_lds_dwordx4 %6, %0 offset:3072 sc1"
                 :: "s"(sbase), "s"(lb), "i"(IMM), "v"(v[0]), "v"(v[1]), "v"(v[2]), "v"(v[3]) : "memory", "scc");
  else if constexpr (N == 2)
    asm volatile("s_add_u32 m0, %1, %2\n\ts_nop 0\n\tglobal_load_lds_dwordx4 %3, %0 sc1\n\tglobal_load_lds_dwordx4 %4, %0 offset:1024 sc1"
                 :: "s"(sbase), "s"(lb), "i"(IMM), "v"(v[0]), "v"(v[1]) : "memory", "scc");
  else
    asm volatile("s_add_u32 m0, %1, %2\n\ts_nop 0\n\tglobal_load_lds_dwordx4 %3, %0 sc1" :: "s"(sbase), "s"(lb), "i"(IMM), "v"(v[0]) : "memory", "scc");
}
template <int N> DEVI void wait_vm() { asm volatile("s_waitcnt vmcnt(%0)" :: "i"(N) : "memory"); }
DEVI void wait_lgkm0() { asm volatile("" ::: "memory"); __builtin_amdgcn_s_waitcnt(0xC07F); asm volatile("" ::: "memory"); }
DEVI void mfma_ip(f32x4& c, const bf16x8& a, const bf16x8& b) { asm volatile("v_mfma_f32_16x16x32_bf16 %0, %1, %2, %0" : "+v"(c) : "v"(a), "v"(b)); }
DEVI void mfma_gap() { __builtin_amdgcn_sched_barrier(0); asm volatile("s_nop 7"); __builtin_amdgcn_sched_barrier(0); }
DEVI void bar() { asm volatile("" ::: "memory"); __builtin_amdgcn_s_barrier(); asm volatile("" ::: "memory"); }
DEVI void sched0() { __builtin_amdgcn_sched_barrier(0); }

DEVI bf16x8 lds_b128(unsigned addr) { return *(const LAS bf16x8*)(uintptr_t)addr; }
DEVI bf16x4 lds_tr4(unsigned addr) { return __builtin_amdgcn_ds_read_tr16_b64_v4i16((LAS bf16x4*)(uintptr_t)addr); }

typedef __amdgpu_buffer_rsrc_t rsrc_t;
DEVI rsrc_t mkrsrc(const void* p, unsigned bytes) { return __builtin_amdgcn_make_buffer_rsrc((void*)p, 0, bytes, 0x00020000); }
DEVI void st16(rsrc_t r, unsigned off, u32x4 v) { __builtin_amdgcn_raw_buffer_store_b128(v, r, off, 0, 16); }
DEVI void ld_raw(u32x4& v, rsrc_t r, unsigned off) { asm volatile("buffer_load_dwordx4 %0, %1, %2, 0 offen sc1 ; raw load" : "=&v"(v) : "v"(off), "s"(r) : "memory"); }
DEVI void ld_again(u32x4& v, rsrc_t r, unsigned off) { asm volatile("buffer_load_dwordx4 %0, %1, %2, 0 offen sc1\n\ts_waitcnt vmcnt(0)" : "=&v"(v) : "v"(off), "s"(r) : "memory"); }
DEVI void ld_ok(u32x4& v) { asm volatile("; raw ok %0" : "+v"(v)); }
#define RLX_AGENT __ATOMIC_RELAXED, __HIP_MEMORY_SCOPE_AGENT

template <int N> DEVI float ror16(float v) { return __int_as_float(__builtin_amdgcn_update_dpp(0, __float_as_int(v), 0x120 + N, 0xf, 0xf, false)); }
DEVI float red16(float v) { v += ror16<8>(v); v += ror16<4>(v); v += ror16<2>(v); v += ror16<1>(v); return v; }
DEVI void xch32(float v, float& a, float& b) { unsigned u = __float_as_uint(v); auto r = __builtin_amdgcn_permlane32_swap(u, u, false, false); unsigned x = r[0], y = r[1]; a = __uint_as_float(x); b = __uint_as_float(y); }
DEVI void xch16(float v, float& a, float& b) { unsigned u = __float_as_uint(v); auto r = __builtin_amdgcn_permlane16_swap(u, u, false, false); unsigned x = r[0], y = r[1]; a = __uint_as_float(x); b = __uint_as_float(y); }
DEVI void swp32(float& a, float& b) { auto r = __builtin_amdgcn_permlane32_swap(__float_as_uint(a), __float_as_uint(b), false, false); unsigned x = r[0], y = r[1]; a = __uint_as_float(x); b = __uint_as_float(y); }
DEVI void swp16(float& a, float& b) { auto r = __builtin_amdgcn_permlane16_swap(__float_as_uint(a), __float_as_uint(b), false, false); unsigned x = r[0], y = r[1]; a = __uint_as_float(x); b = __uint_as_float(y); }
DEVI float colsum4(float v) { float a, b; xch32(v, a, b); v = a + b; xch16(v, a, b); return a + b; }
DEVI float vmax(float a, float b) { float r; asm("v_max_f32 %0, %1, %2" : "=v"(r) : "v"(a), "v"(b)); return r; }
DEVI float vmin3(float a, float b, float c) { float r; asm("v_min3_f32 %0, %1, %2, %3" : "=v"(r) : "v"(a), "v"(b), "v"(c)); return r; }
DEVI float vmax3(float a, float b, float c) { float r; asm("v_max3_f32 %0, %1, %2, %3" : "=v"(r) : "v"(a), "v"(b), "v"(c)); return r; }

DEVI unsigned src_kc(int c, int ld) { int row = c >> 3, ch = (c & 7) ^ ((row >> 1) & 7); return (unsigned)(row * ld * 2 + ch * 16); }
DEVI int km_f(int k) { return ((k >> 1) & 1) | (((k >> 3) & 1) << 1); }
DEVI unsigned ph(unsigned x) { return ((x >> 8) << 9) | (x & 255u); }
DEVI unsigned rd_kc(int lane, int ks) { int r = lane & 15, g = lane >> 4; return (unsigned)(r * 128 + (((ks * 4 + g) ^ (r >> 1)) << 4)); }
DEVI unsigned rd_km(int lane, int ct) {
  int g = lane >> 4, q = (lane & 15) >> 2, p = lane & 3;
  int f = ((q >> 1) & 1) | ((g & 1) << 1);
  return (unsigned)((ct >> 2) * 8192 + (8 * g + q) * 128 + (((ct & 3) ^ f) << 5) + 8 * p);
}


constexpr unsigned CODE_BYTES = 54272;
DEVI void code_warm(int part, int lane, unsigned scratch) {
  unsigned long long pc;
  asm volatile("s_getpc_b64 s[40:41]\n\ts_add_u32 s40, s40, mega_kernel@rel32@lo+4\n\ts_addc_u32 s41, s41, mega_kernel@rel32@hi+12\n\ts_mov_b64 %0, s[40:41]" : "=s"(pc) :: "s40", "s41", "scc");
  const unsigned off = (unsigned)(part * 64 + lane) * 128u;
  scratch = __builtin_amdgcn_readfirstlane(scratch);
  if (off < CODE_BYTES) asm volatile("s_nop 3\n\ts_mov_b32 m0, %2\n\ts_nop 0\n\tglobal_load_lds_dword %0, %1" :: "v"(off), "s"(pc), "s"(scratch) : "memory");
}

DEVI unsigned src_km(int lt, int ld) { const int k = lt >> 3; return (unsigned)(k * ld * 2 + (((lt & 7) ^ (km_f(k) << 1)) << 4)); }
DEVI void touch4(const void* sbase, unsigned voff, unsigned ldsaddr) {
  ldsaddr = __builtin_amdgcn_readfirstlane(ldsaddr); sbase = uni_ptr(sbase);
  asm volatile("s_nop 3\n\ts_mov_b32 m0, %2\n\ts_nop 0\n\tglobal_load_lds_dword %0, %1" :: "v"(voff), "s"(sbase), "s"(ldsaddr) : "memory");
}

template <int IMM> DEVI void spf1(const void* base, unsigned soff) { asm volatile("s_load_dword s101, %0, %1 offset:%2" :: "s"(base), "s"(soff), "i"(IMM) : "s101"); }
template <int IMM = 0> DEVI void spf2(const void* base, unsigned soff) { asm volatile("s_load_dwordx2 s[100:101], %0, %1 offset:%2" :: "s"(base), "s"(soff), "i"(IMM + 60) : "s100", "s101"); }
template <int ST> DEVI void spf2x6(const void* base, unsigned soff) {
  asm volatile("s_load_dwordx2 s[100:101], %0, %1 offset:%2\n\ts_load_dwordx2 s[100:101], %0, %1 offset:%3\n\ts_load_dwordx2 s[100:101], %0, %1 offset:%4\n\t"
               "s_load_dwordx2 s[100:101], %0, %1 offset:%5\n\ts_load_dwordx2 s[100:101], %0, %1 offset:%6\n\ts_load_dwordx2 s[100:101], %0, %1 offset:%7"
               :: "s"(base), "s"(soff), "i"(60), "i"(60 + ST), "i"(60 + 2 * ST), "i"(60 + 3 * ST), "i"(60 + 4 * ST), "i"(60 + 5 * ST) : "s100", "s101");
}
template <int ST> DEVI void spf1x12(const void* base, unsigned soff) {
  spf1<0>(base, soff); spf1<ST>(base, soff); spf1<2 * ST>(base, soff); spf1<3 * ST>(base, soff); spf1<4 * ST>(base, soff); spf1<5 * ST>(base, soff);
  spf1<6 * ST>(base, soff); spf1<7 * ST>(base, soff); spf1<8 * ST>(base, soff); spf1<9 * ST>(base, soff); spf1<10 * ST>(base, soff); spf1<11 * ST>(base, soff);
}
DEVI int lgkm_out() { int n; asm volatile("s_getreg_b32 %0, hwreg(HW_REG_IB_STS, 8, 4)" : "=s"(n)); return n; }
DEVI int slow_res(const void* w) { return (3 - ((unsigned)(uintptr_t)w >> 7)) & 7; }

constexpr int SPM = 0, WNE = 16, ANE = 24, BNE = 13, QNE = 46;
struct Plan { int n; signed char q[40], idx[40], k[40], h[40], g[40]; };
template <class O, bool PKM, bool QKM> constexpr Plan make_plan() {
  Plan p{};
  int nr = 0;
  for (int c = 0; c < O::NRO; ++c) nr += ((O::ro[c] >> 4) ? QKM : PKM) ? 4 : 2;
  int prev = 0;
  for (int c = 0; c < O::NRO; ++c) {
    const int q = O::ro[c] >> 4, i = O::ro[c] & 15;
    int av[2] = {0, 0}, k0 = -1;
    for (int m = 0; m < 2 * O::NP; ++m) {
      const int mm = (m + 2 * O::NPEND) % (2 * O::NP);
      if ((q ? O::pj[m >> 1] : O::pi[m >> 1]) == i && m < 2 * O::NPEND) av[((m >> 1) ^ m) & 1] = m;
      if ((q ? O::pj[mm >> 1] : O::pi[mm >> 1]) == i && k0 < 0) k0 = ((mm >> 1) ^ mm) & 1;
    }
    for (int kk = 0; kk < 2; ++kk)
      for (int h = 0; h < ((q ? QKM : PKM) ? 2 : 1); ++h) {
        const int k = k0 ^ kk;
        int g = p.n * O::NE / nr;
        if (g < av[k]) g = av[k];
        if (g < prev + (p.n && O::NE >= nr ? 1 : 0)) g = prev + (p.n && O::NE >= nr ? 1 : 0);
        p.q[p.n] = q; p.idx[p.n] = i; p.k[p.n] = k; p.h[p.n] = h; p.g[p.n] = g; prev = g; ++p.n;
      }
  }
  return p;
}
struct OBase { static constexpr int pidx(int i) { return i; } static constexpr int poff(int) { return 0; } static constexpr int qoff(int j) { return j * 2048; } static constexpr bool swp(int) { return false; } };
template <bool PF, int NE_> struct O44 : OBase {
  static constexpr int TP = 4, TQ = 4, NPA = 4, NP = 16, NPEND = 8, NRO = 8, NE = NE_;
  static constexpr signed char pi[16] = {2, 3, 3, 2, 2, 3, 3, 2, 0, 1, 1, 0, 0, 1, 1, 0}, pj[16] = {0, 0, 1, 1, 2, 2, 3, 3, 0, 0, 1, 1, 2, 2, 3, 3};
  static constexpr signed char ro[8] = {0, PF ? 1 : 16, PF ? 16 : 1, 17, 18, 19, 2, 3};
};
template <bool PF, int NE_> struct O42 : OBase {
  static constexpr int TP = 4, TQ = 2, NPA = 4, NP = 8, NPEND = 4, NRO = 6, NE = NE_;
  static constexpr signed char pi[8] = {2, 3, 3, 2, 0, 1, 1, 0}, pj[8] = {0, 0, 1, 1, 0, 0, 1, 1};
  static constexpr signed char ro[6] = {0, PF ? 1 : 16, PF ? 16 : 1, 17, 3, 2};
};
template <int TH> struct OQ {
  static constexpr int TP = 6, TQ = 5, NPA = 2, NP = 26, NPEND = 13, NRO = 11, NE = QNE;
  static constexpr signed char pi[26] = {3, 4, 5, 5, 4, 3, 3, 4, 5, 5, TH ? 5 : 3, 4, TH ? 3 : 4, 0, 1, 2, 2, 1, 0, 0, 1, 2, 2, TH ? 2 : 0, 1, TH ? 0 : 1};
  static constexpr signed char pj[26] = {0, 0, 0, 1, 1, 1, 2, 2, 2, TH ? 4 : 3, 3, 3, TH ? 3 : 4, 0, 0, 0, 1, 1, 1, 2, 2, 2, TH ? 4 : 3, 3, 3, TH ? 3 : 4};
  static constexpr signed char ro[11] = {0, 1, 16, 2, 17, 18, TH ? 20 : 19, TH ? 19 : 20, 3, 4, 5};
  static constexpr int pidx(int i) { return i / 3; }
  static constexpr int poff(int i) { return i % 3 * 8192; }
  static constexpr int qoff(int j) { return j == 4 ? 0 : (1 + 4 * TH + j) * 2048; }
  static constexpr bool swp(int i) { return i % 3 == 2; }
};
template <bool PKM, bool QKM, int PB, int QB, int XB, int NS, int NT, bool WISP, int BASE, int ROT, int WLD, int WSUB, int ASTEP, int RLX = 0, bool QF = false>
struct Gemm {
  static constexpr int SB = PB + QB + XB, CNT = (PB + QB) / 4096 + (XB ? 1 : 0);
  static constexpr int PO = QF ? QB : 0, QO = QF ? 0 : PB, WB = WISP ? PB : QB, AB = WISP ? QB : PB, WOFF = WISP ? PO : QO, AOFF = WISP ? QO + XB : PO;
  static constexpr int XL = XB / 64, H = NS - 1 - RLX, CA = AB / 4096 + (XB ? 1 : 0), WSTEP = 64 * WLD * 2;
  static_assert(H * CNT <= 63 && BASE + NS * SB <= 163840, "");
  struct Loader {
    static constexpr int NW = WB / 4096, NA = AB / 4096;
    const char* w; const char* ab; unsigned wv[NW], av[NA], xv, lwb, lab, lx; int rot, islot, t; bool xl;
    DEVI Loader(const void* w_, const void* ab_, int lw, int lane, int rot_ = 0)
        : w((const char*)w_ - 3072), ab((const char*)ab_ - 3072), lwb(BASE + WOFF + lw * (NW == 6 ? 2048 : WB / 4)), lab(BASE + AOFF + lw * (AB / 4)), lx(BASE + PB + lw * (XL * 16)),
          rot(rot_), islot((H + ROT) % NS), t(0), xl(lane < XL) {
      const unsigned w0 = (lane >> 3) * (WLD * 2), f0 = (lane & 7) ^ ((lane >> 3) & 2);
#pragma unroll
      for (int i = 0; i < NW; ++i) {
        const unsigned p = NW == 6 ? (i >> 1) * 8 + lw * 2 + (i & 1) : lw * NW + i;
        wv[i] = w0 + ((f0 ^ ((i & 1) << 2)) << 4) + (NW == 6 ? (i >> 1) * WSUB + (lw * 2 + (i & 1)) * (8 * WLD * 2) - (i & 1) * 1024 : (p >> 3) * WSUB + (p & 7) * (8 * WLD * 2) - i * 1024) + 3072;
        asm volatile("" : "+v"(wv[i]));
      }
      asm volatile("" : "+s"(w), "+s"(lwb));
      if (XB) asm volatile("" : "+s"(rot));
      else seta(0, 0, lw, lane);
    }
    DEVI void seta(unsigned a0, unsigned xv_, int lw, int lane) {
      xv = xv_ + 3072;
#pragma unroll
      for (int i = 0; i < NA; ++i) { const unsigned p = lw * NA + i; av[i] = (XB ? a0 + src_kc(p * 64 + lane, D) : p * 2048 + ph(lane * 16)) + 3072 - i * 1024; asm volatile("" : "+v"(av[i])); }
      asm volatile("" : "+s"(ab), "+s"(lab));
      if (XB) { asm volatile("" : "+v"(xv)); asm volatile("" : "+s"(lx)); }
    }
    DEVI int kord(int n) const { if constexpr (XB != 0) return (n + rot) & (NT - 1); else return n; }
    template <int SO> DEVI void issue_w(const char* p, unsigned so = 0) const {
      if constexpr (NW == 6) { dma_grp<2, SO>(p, wv, lwb + so); dma_grp<2, SO + 8192>(p, wv + 2, lwb + so); dma_grp<2, SO + 16384>(p, wv + 4, lwb + so); }
      else dma_grp<NW, SO>(p, wv, lwb + so);
    }
    template <int SO> DEVI void issue_a(const char* p, unsigned so = 0) const {
      if (XB) { if (xl) dma_grp<1, SO>(p, &xv, lx + so); }
      dma_grp<NA, SO>(p, av, lab + so);
    }
    template <int S0, bool W, bool A> DEVI void pre() const {
      if constexpr (S0 < H) {
        if (W) issue_w<((S0 + ROT) % NS) * SB>(w + (kord(0) + S0) * WSTEP);
        if (A) issue_a<((S0 + ROT) % NS) * SB>(ab + (kord(0) + S0) * ASTEP);
        pre<S0 + 1, W, A>();
      }
    }
    template <int T> DEVI void pre_w1() const { issue_w<((T + ROT) % NS) * SB>(w + (kord(0) + T) * WSTEP); sched0(); }
    DEVI void pre_w() const { pre<0, true, false>(); sched0(); }
    template <int T> DEVI void peel() const {
      if constexpr (T < H - 1) {
        wait_vm<(H - 1 - T) * CA + T * CNT>();
        bar();
        issue_w<((T + H + ROT) % NS) * SB>(w + (T + H) * WSTEP); issue_a<((T + H + ROT) % NS) * SB>(ab + (T + H) * ASTEP);
        peel<T + 1>();
      }
    }
    DEVI void start() {
      sched0();
      bar();
      pre<0, false, true>();
      sched0();
      if constexpr (H == 2) wait_vm<CA>();
      else { peel<0>(); islot = (2 * H - 1 + ROT) % NS; t = H - 1; }
    }
    template <class HK> DEVI void run(int tend, HK side) {
#pragma unroll 1
      for (; t < tend; ++t) {
        const char* wk = w + (t + H) * WSTEP; const char* ak = ab + (t + H) * ASTEP;
        unsigned lw_ = lwb + islot * SB, la_ = lab + islot * SB;
        islot = islot + 1 == NS ? 0 : islot + 1;
        asm volatile("" : "+s"(wk), "+s"(ak), "+s"(lw_), "+s"(la_));
        wait_vm<(H - 1) * CNT>();
        bar();
        dma_grp<NW, 0>(wk, wv, lw_);
        side(t);
        dma_grp<NA, 0>(ak, av, la_);
        sched0();
      }
    }
    template <int T, int X, class A, class B, class C> DEVI void step(A pre_, B side, C post) const {
      const int k = kord(T + H);
      const char* wk = w + k * WSTEP; const char* ak = ab + k * ASTEP;
      asm volatile("" : "+s"(wk), "+s"(ak));
      wait_vm<(H - 1) * CNT + X>();
      bar();
      pre_(BASE + ((T + ROT) % NS) * SB);
      issue_w<((T + H + ROT) % NS) * SB>(wk);
      sched0();
      if ((SPM >> T) & 1) __builtin_amdgcn_s_setprio(2);
      side();
      if ((SPM >> T) & 1) __builtin_amdgcn_s_setprio(0);
      sched0();
      issue_a<((T + H + ROT) % NS) * SB>(ak);
      post();
      sched0();
    }
    template <int I> DEVI void tail() const { if constexpr (I >= 0) { sched0(); wait_vm<I * CNT>(); bar(); tail<I - 1>(); } }
  };
  static constexpr int NWIN = (NS * SB + 65535) / 65536;
  template <class O> struct Comp {
    static constexpr int TP = O::TP, TQ = O::TQ, NM = 2 * O::NP, NPA = PKM ? O::NPA : 2, NQA = QKM ? TQ : 2;
    static constexpr Plan PL = make_plan<O, PKM, QKM>();
    unsigned prd[NWIN][NPA], qrd[NWIN][NQA];
    bf16x8 fp[TP][2], fq[TQ][2]; bf16x4 lo;
    DEVI void setwin() {
#pragma unroll
      for (int w = 1; w < NWIN; ++w) {
#pragma unroll
        for (int i = 0; i < NPA; ++i) { prd[w][i] = prd[0][i] + w * 65536; asm volatile("" : "+v"(prd[w][i])); }
#pragma unroll
        for (int i = 0; i < NQA; ++i) { qrd[w][i] = qrd[0][i] + w * 65536; asm volatile("" : "+v"(qrd[w][i])); }
      }
#pragma unroll
      for (int i = 0; i < NPA; ++i) asm volatile("" : "+v"(prd[0][i]));
#pragma unroll
      for (int i = 0; i < NQA; ++i) asm volatile("" : "+v"(qrd[0][i]));
    }
    DEVI void setup(int wp, int wq, int lane) {
      const unsigned b = BASE;
      if (PKM) {
#pragma unroll
        for (int i = 0; i < TP; ++i) prd[0][i] = b + rd_km(lane, wp * TP + i);
      } else { prd[0][0] = b + rd_kc(lane, 0) + wp * TP * 2048; prd[0][1] = b + rd_kc(lane, 1) + wp * TP * 2048; }
      if (QKM) {
#pragma unroll
        for (int j = 0; j < TQ; ++j) qrd[0][j] = b + rd_km(lane, wq * TQ + j);
      } else { qrd[0][0] = b + rd_kc(lane, 0) + wq * TQ * 2048; qrd[0][1] = b + rd_kc(lane, 1) + wq * TQ * 2048; }
    }
    template <int C, int N> DEVI static unsigned wa(const unsigned (&b)[NWIN][N], int i) { return b[C >> 16][i] + (C & 0xffff); }
    template <int SLOT, int R> DEVI void rd() {
      constexpr int q = PL.q[R], i = PL.idx[R], k = PL.k[R], h = PL.h[R];
      bf16x8& f = q ? fq[q ? i : 0][k] : fp[q ? 0 : i][k];
      if constexpr (q ? QKM : PKM) {
        unsigned a;
        if constexpr (q) a = wa<SLOT * SB + QO + k * 4096 + h * 512, NQA>(qrd, i); else a = wa<SLOT * SB + PO + O::poff(i) + k * 4096 + h * 512, NPA>(prd, O::pidx(i));
        const bf16x4 x = lds_tr4(a);
        if constexpr (h) f = __builtin_shufflevector(lo, x, 0, 1, 2, 3, 4, 5, 6, 7); else lo = x;
      } else if constexpr (q) f = lds_b128(wa<SLOT * SB + QO + O::qoff(i), 2>(qrd, k));
      else f = lds_b128(wa<SLOT * SB + PO + i * 2048, 2>(prd, k));
    }
    template <int SLOT, int M, int R> DEVI void rds() { if constexpr (R < PL.n) { if constexpr (PL.g[R] == M) rd<SLOT, R>(); if constexpr (PL.g[R] <= M) rds<SLOT, M, R + 1>(); } }
    template <int M, bool IP = false> DEVI void mm(f32x4 (&acc)[TP][TQ]) {
      constexpr int i = O::pi[M >> 1], j = O::pj[M >> 1], k = ((M >> 1) ^ M) & 1;
      if constexpr (IP) { if constexpr (O::swp(i)) mfma_ip(acc[i][j], fq[j][k], fp[i][k]); else mfma_ip(acc[i][j], fp[i][k], fq[j][k]); }
      else if constexpr (O::swp(i)) acc[i][j] = __builtin_amdgcn_mfma_f32_16x16x32_bf16(fq[j][k], fp[i][k], acc[i][j], 0, 0, 0);
      else acc[i][j] = __builtin_amdgcn_mfma_f32_16x16x32_bf16(fp[i][k], fq[j][k], acc[i][j], 0, 0, 0);
    }
    template <int A, int B> DEVI void pend(f32x4 (&acc)[TP][TQ]) { if constexpr (A < B) { mm<2 * A>(acc); mm<2 * A + 1>(acc); pend<A + 1, B>(acc); } }
    template <int SLOT, bool FIRST, int M> DEVI void body(f32x4 (&acc)[TP][TQ]) {
      if constexpr (M < NM) {
        if constexpr (!FIRST || M >= 2 * O::NPEND) { mm<M, true>(acc); sched0(); }
        rds<SLOT, M, 0>();
        sched0();
        body<SLOT, FIRST, M + 1>(acc);
      }
    }
    template <int SLOT, bool FIRST> DEVI void step(f32x4 (&acc)[TP][TQ]) { if (!RLX) wait_lgkm0(); bar(); sched0(); body<SLOT, FIRST, 0>(acc); }
    template <int I, int N, int T0, class MID> DEVI void steps(f32x4 (&acc)[TP][TQ], MID mid) {
      if constexpr (I < N) { mid(I); step<(I + T0 + ROT) % NS, false>(acc); steps<I + 1, N, T0>(acc, mid); }
    }
    DEVI void prep(f32x4 (&acc)[TP][TQ]) { setwin(); pin(acc); }
    DEVI void pin(f32x4 (&acc)[TP][TQ]) {
#pragma unroll
      for (int p = 0; p < O::NP; ++p) asm volatile("" : "+v"(acc[O::pi[p]][O::pj[p]]));
    }
    template <bool CONT, class MID> DEVI void run_steps(f32x4 (&acc)[TP][TQ], MID mid) {
      constexpr int T0 = CONT ? 0 : 1;
      if constexpr (T0) { mid((NT - T0) / NS, -1); step<ROT % NS, true>(acc); }
#pragma unroll 1
      for (int itn = (NT - T0) / NS; itn > 0; --itn) {
        steps<0, NS, T0>(acc, [&](int i) { mid(itn, i); });
      }
      steps<0, (NT - T0) % NS, T0>(acc, [](int) {});
      wait_lgkm0();
      mfma_gap();
    }
  };
};

DEVI f32x4 gelu_arg(f32x4 u) { return u * (-2.302208198f + -0.1029432397f * (u * u)); }
DEVI f32x4 gelu_tanh(f32x4 u) {
  f32x4 a = gelu_arg(u);
#pragma unroll
  for (int e = 0; e < 4; ++e) a[e] = __builtin_amdgcn_exp2f(a[e]);
  a += 1.f;
#pragma unroll
  for (int e = 0; e < 4; ++e) a[e] = __builtin_amdgcn_rcpf(a[e]);
  return u * a;
}

template <int tilesP, int tilesQ, int gp, int gq> DEVI void tile_map(unsigned bid, int& tp, int& tq) {
  const unsigned xcd = bid & 7, idx = bid >> 3;
  constexpr unsigned gP = tilesP / gp, gQ = tilesQ / gq;
  static_assert(gP * gQ == 8, "");
  tp = (xcd % gP) * gp + idx % gp;
  tq = (xcd / gP) * gq + idx / gp;
}

template <int V> struct IC { static constexpr int value = V; };
template <int A, int B, class FN> DEVI void for_c(FN f) { if constexpr (A < B) { f(IC<A>{}); for_c<A + 1, B>(f); } }
DEVI float sel(bool c, float a, float b) { return c ? a : b; }
DEVI u32x2 pk4(const f32x4& v) { return (u32x2){pk2(v[0], v[1]), pk2(v[2], v[3])}; }
DEVI u32x4 cat4(u32x2 a, u32x2 b) { return (u32x4){a.x, a.y, b.x, b.y}; }
DEVI bf16x8 cat(u32x2 a, u32x2 b) { return __builtin_bit_cast(bf16x8, cat4(a, b)); }
DEVI f32x4 mfma(const bf16x8& a, const bf16x8& b, const f32x4& c) { return __builtin_amdgcn_mfma_f32_16x16x32_bf16(a, b, c, 0, 0, 0); }
DEVI u32x4 pack_pair(const f32x4& a, const f32x4& b) {
  const unsigned ax = pk2(a[0], a[1]), ay = pk2(a[2], a[3]), bx = pk2(b[0], b[1]), by = pk2(b[2], b[3]);
  auto rx = __builtin_amdgcn_permlane16_swap(ax, bx, false, false);
  auto ry = __builtin_amdgcn_permlane16_swap(ay, by, false, false);
  const unsigned x0 = rx[0], x1 = rx[1], y0 = ry[0], y1 = ry[1];
  return (u32x4){x0, y0, x1, y1};
}
DEVI int pair_chunk(int fq) { return ((fq & 1) << 1) | (fq >> 1); }
DEVI void sel_frags(bool paired, int fr, int fq, bf16x8 (&s)[2]) {
  const unsigned one = fr & 1 ? 0x3F800000u : 0x3F80u;
#pragma unroll
  for (int i = 0; i < 2; ++i) {
    const bool on = paired ? (fq & 1) == i && (fr >> 3) == (fq >> 1) : (fr >> 2) == fq;
    const int d = paired ? (fr & 7) >> 1 : i * 2 + ((fr >> 1) & 1);
    u32x4 v;
#pragma unroll
    for (int e = 0; e < 4; ++e) v[e] = on && d == e ? one : 0u;
    s[i] = __builtin_bit_cast(bf16x8, v);
  }
}
template <int TQ> DEVI void tile_add(f32x4 (&acc)[4][TQ], const bf16x8 (&s)[2], unsigned a) {
  bf16x8 t[4];
#pragma unroll
  for (int p = 0; p < 4; ++p) t[p] = lds_b128(a + p * 1024);
#pragma unroll
  for (int p = 0; p < 4; ++p)
#pragma unroll
    for (int i = 0; i < 2; ++i) acc[(p & 1) * 2 + i][p >> 1] = mfma(s[i], t[p], acc[(p & 1) * 2 + i][p >> 1]);
}
template <int C> DEVI float dppf(float v) { return __int_as_float(__builtin_amdgcn_update_dpp(0, __float_as_int(v), C, 0xf, 0xf, false)); }
DEVI float red8(float v) { v += dppf<0xB1>(v); v += dppf<0x4E>(v); v += dppf<0x141>(v); return v; }
DEVI float red4(float v) { v += dppf<0xB1>(v); v += dppf<0x4E>(v); return v; }

constexpr unsigned SENT = 0xBF800000u;
constexpr int SPIN_MAX = 1 << 21;

constexpr unsigned MB = 1u << 20;
constexpr unsigned WS_O = 0, WS_H = 8 * MB, WS_U = 16 * MB, WS_X2 = 32 * MB, WS_XP = 40 * MB  ;
constexpr unsigned WS_ST = 120 * MB, ST_CS = 2 * S * 64, WS_XCH = 122 * MB  , WS_EPOCH = 123 * MB, WS_ABORT = 123 * MB + 256  ;
DEVI unsigned tag2(unsigned t) { return t * 0x9E3779B1u ^ 0x5BD1E995u; }
constexpr int MEDGE = 256 * 64;
constexpr unsigned WS_MAIL = 124 * MB;

constexpr int NL = 2, QBR = 4, FLS = 3, NPL = 6, SPON = 1, PC0 = 0, PGAP = 3, WSP = 3, SWW = 0, GSH = 1, GSP = 3, SSC = 1, RB = 1, SI = 1, LPR = 1, CPR = 0, GLW = 4, GL1 = 4, SW0 = 0, SW1 = 6, XE = 0, E2 = 1, LB = 2, TLQ = 0, TLW = 1, SPT = 0, SLS = 0, SW2 = 0, CP = 1, P31 = 0, Q31 = 1, CWB = 0, CWL = 1, XD = 1, XS = 4, JA = 22, PS = 3, TBA = 3, TC1 = 13, NW1 = 4, EW = 12, ET = 3, SPW = 1, PA_1 = 3, PA_2 = 3;
constexpr int L_X = 0, L_TAB = 16384, L_CS = L_TAB + 1152, L_TAG = L_CS + 768, L_BIAS = L_TAG + 128, L_SCR = L_BIAS + 512  , L_QKV = 20480;
static_assert(L_SCR + 1536 <= L_QKV, "");
typedef Gemm<true, false, 24576, 16384, 2048, 3, D / 64, true, 32768, 0, 3 * D, 2 * D, 128> GQ;
typedef Gemm<true, false, 16384, 8192, 0, 5, D / 64, true, 40960, 2, D, 128, 16384, 1, true> GW;
typedef Gemm<true, true, 16384, 16384, 0, 4, S / 64, true, 32768, 1, F, 128, 32768, 0, true> G1;
typedef Gemm<false, true, 16384, 16384, 0, 3, JA, false, 65536, (3 - JA % 3) % 3, S, 1024, 32768> G2A;
typedef Gemm<false, true, 16384, 8192, 0, 4, F / 64 - 2 * JA, false, 65536, 0, S, 128, 32768> G2;

DEVI unsigned get_tag() { return __builtin_amdgcn_readfirstlane(*(volatile LAS unsigned*)(uintptr_t)L_TAG) + 1u; }
DEVI bool spin_fail(int spin, char* ws) {
  if ((spin & 255) != 255) return false;
  gu32* ab = (gu32*)(ws + WS_ABORT);
  const unsigned tag = get_tag();
  if (spin >= SPIN_MAX) { __hip_atomic_store(ab, tag, RLX_AGENT); return true; }
  return __builtin_amdgcn_readfirstlane(__hip_atomic_load(ab, RLX_AGENT)) == tag;
}
DEVI void sync_post(char* ws, int edge, int post, int wid) {
  unsigned o = post >= 0 && wid == 0 ? (post >> 6) * 256u : MB, bit = 1u << (post & 31);
  const rsrc_t r = mkrsrc(ws + WS_MAIL + edge * (MEDGE * 4), MEDGE * 4);
  asm volatile("s_waitcnt vmcnt(0) lgkmcnt(0)" : "+v"(o), "+v"(bit) : "s"(r) : "memory");
  sched0();
  bar();
  if (wid == 0) asm volatile("buffer_atomic_or %0, %1, %2, 0 offen sc1" :: "v"(bit), "v"(o), "s"(r) : "memory");
  sched0();
}
#define POLL1(r) "s_waitcnt vmcnt(2)\n\tv_readfirstlane_b32 %[seen], " r "\n\tglobal_atomic_or " r ", %[z], %[z], %[p] sc0 sc1\n\t" \
  "s_andn2_b32 %[t], %[first], %[seen]\n\ts_cbranch_scc1 3f\n\ts_cmp_eq_u32 %[first], 0\n\ts_cbranch_scc1 4f\n\ts_barrier\n\ts_mov_b32 %[first], 0\n" \
  "4:\n\ts_andn2_b32 %[t], %[second], %[seen]\n\ts_cbranch_scc0 2f\n3:\n\t"
struct Gate {
  const char* p; unsigned miss, due, a, b, c;
  DEVI Gate() : p(nullptr), miss(0), due(0) { asm volatile("v_mov_b32 %0, 0\n\tv_mov_b32 %1, 0\n\tv_mov_b32 %2, 0" : "=v"(a), "=v"(b), "=v"(c)); }
  DEVI void ask() const { asm volatile("s_mov_b64 exec, 1\n\tglobal_atomic_or %0, %1, %1, %2 sc0 sc1\n\ts_mov_b64 exec, -1" :: "v"(a), "v"(0u), "s"(p) : "memory"); }
  template <int DELAY> DEVI void wait(char* ws, int edge, int line, unsigned all, unsigned first, unsigned second, int wid) {
    sched0();
    p = ws + WS_MAIL + (edge * MEDGE + line * 64) * 4;
    second &= all;
    if (wid == 3) {
      due = 1;
      if (DELAY) __builtin_amdgcn_s_sleep(DELAY);
      int spin = 255;
#pragma unroll 1
      for (;; spin += 256) {
        asm volatile("" : "+s"(spin));
        unsigned seen, t, n;
        asm volatile("s_mov_b64 exec, 1\n\ts_waitcnt vmcnt(0)\n\tglobal_atomic_or %[a], %[z], %[z], %[p] sc0 sc1\n\ts_sleep %[gap]\n\t"
                     "global_atomic_or %[b], %[z], %[z], %[p] sc0 sc1\n\ts_sleep %[gap]\n\tglobal_atomic_or %[c], %[z], %[z], %[p] sc0 sc1\n\ts_movk_i32 %[n], 128\n"
                     "1:\n\t" POLL1("%[a]") POLL1("%[b]") POLL1("%[c]") "s_sub_u32 %[n], %[n], 1\n\ts_cbranch_scc0 1b\n"
                     "2:\n\ts_mov_b64 exec, -1 ; raw polls %[a] %[b] %[c]"
                     : [seen] "=&s"(seen), [t] "=&s"(t), [n] "=&s"(n), [first] "+&s"(first)
                     : [p] "s"(p), [second] "s"(second), [gap] "i"(PGAP), [a] "v"(a), [b] "v"(b), [c] "v"(c), [z] "v"(0u) : "memory", "scc");
        miss = ~seen & all;
        if (__builtin_expect(!(miss & second) && !first, 1)) break;
        if (spin_fail(spin, ws)) { if (first) bar(); miss = 0; break; }
      }
    } else bar();
  }
  int ev;
  DEVI void arm(int ni, int cw) { ev = miss ? ni : cw >= 0 ? 2 : 0; asm volatile("" : "+s"(ev), "+s"(miss), "+s"(due)); }
  DEVI void head(int it, int cw, int lane, int wid) {
    if (__builtin_expect(it == ev, 0)) {
      asm volatile("" : "+v"(lane)); asm volatile("" : "+s"(cw), "+s"(wid));
      if (miss) ask();
      if (it == 2) code_warm(cw, lane, L_SCR + wid * 256);
      ev = cw >= 0 && it > 2 ? 2 : 0;
      asm volatile("" : "+s"(ev));
    }
  }
  DEVI void late(char* ws) {
    if (__builtin_expect(due != 0, 0)) {
      asm volatile("" : "+s"(ws));
      asm volatile("s_waitcnt vmcnt(0) ; raw settled %0 %1 %2" :: "v"(a), "v"(b), "v"(c) : "memory");
      for (int spin = 0; miss; ++spin) {
        unsigned seen;
        asm volatile("v_readfirstlane_b32 %0, %1" : "=s"(seen) : "v"(a));
        miss &= ~seen;
        if (!miss || spin_fail(spin, ws)) break;
        ask();
        wait_vm<0>();
      }
      miss = due = 0;
      asm volatile("" : "+s"(miss), "+s"(due));
    }
  }
};
DEVI unsigned halo_off(int s0, int lw, int lane) {
  const int c = lw * 32 + (lane & 31), row = c >> 3, ch = (c & 7) ^ ((row >> 1) & 7), tok = s0 - 16 + row;
  return (unsigned)((tok < 0 ? 0 : tok) * D * 2 + ch * 16);
}
DEVI int qkv_rot(int bid) { return (bid & 3) * 4 + (bid & 4) * 2; }
DEVI unsigned x_off(int tp, int tq, int w, int fr, int fq) { return (unsigned)(((tq * 64 + (w >> 1) * 32 + fr) * D + tp * 128 + (w & 1) * 64 + pair_chunk(fq) * 8) * 2); }

DEVI void pfb_q(const u16* w, int bid, int j, int n) {
  const int r = slow_res(w);
  if ((r >> 2) == (bid & 1)) {
    const unsigned o = (unsigned)(((bid & 2) * 4 + r) * 128 + (j >> 1) * (3 * D * 2) + (j & 1) * 64 + ((n + qkv_rot(bid)) & 15) * GQ::WSTEP);
    spf1<0>(w, o); spf1<2048>(w, o); spf1<4096>(w, o);
  }
}
constexpr int PFR = NPL * 128 * 1024, PF_END = TBA * 256 + NW1 * PFR / (F * 2) / 8 * 8;
static_assert(TBA * 256 * F * 2 + NW1 * PFR <= S * F * 2 && 3 * PFR >= D * D * 2, "");
struct Pfc {
  const char* p; const char* q; int pc;
  DEVI Pfc(const u16* wo, const u16* w1, unsigned g) : p((const char*)wo + g * (NPL * 128)), q((const char*)w1 + TBA * 256 * F * 2 + g * (NPL * 128)), pc(PC0) {}
  DEVI void go() {
    unsigned t, u;
    asm volatile("s_getreg_b32 %2, hwreg(HW_REG_IB_STS, 8, 4)\n\ts_cmp_lt_u32 %1, %5\n\ts_cselect_b32 %3, 0, 1\n\ts_or_b32 %2, %2, %3\n\ts_cbranch_scc1 1f\n\t"
                 "s_load_dwordx2 s[100:101], %0, 60\n\ts_load_dwordx2 s[100:101], %0, 188\n\ts_load_dwordx2 s[100:101], %0, 316\n\t"
                 "s_load_dwordx2 s[100:101], %0, 444\n\ts_load_dwordx2 s[100:101], %0, 572\n\ts_load_dwordx2 s[100:101], %0, 700\n\t"
                 ".if %8 == 7\n\ts_load_dwordx2 s[100:101], %0, 828\n\t.endif\n\t"
                 "s_mov_b32 %2, %7\n\ts_cmp_lt_u32 %1, 2\n\ts_cselect_b32 %2, %6, %2\n\ts_mov_b64 s[40:41], %0\n\ts_add_u32 s40, s40, %2\n\ts_addc_u32 s41, s41, 0\n\t"
                 "s_cmp_eq_u32 %1, 2\n\ts_cselect_b64 %0, %4, s[40:41]\n\ts_add_u32 %1, %1, 1\n1:"
                 : "+s"(p), "+s"(pc), "=&s"(t), "=&s"(u) : "s"(q), "i"(3 + NW1), "i"((D * D * 2 - PFR) / 2), "i"(PFR), "i"(NPL) : "s40", "s41", "s100", "s101", "scc");
  }
};
template <class FN> DEVI void spare_touch(const u16* w1, const u16* w2, int bid, int wid, int lane, FN side) {
  if (!SPON) return;
  const int j = ((bid >> 3) - 28) * 8 + wid, fr = lane & 15, fq = lane >> 4;
  const unsigned scr = L_QKV + wid * 256;
  u64 due = SPT ? __builtin_amdgcn_s_memrealtime() + (j & 7) * (SPT / 8) : 0;
  auto pace = [&]() { side(); wait_vm<SPW>(); due += SPT; if (SPT) { while (__builtin_amdgcn_s_memrealtime() < due) __builtin_amdgcn_s_sleep(2); } };
#pragma unroll 1
  for (int r = PF_END + (j * 2 + ((bid >> 2) & 1)) * 4; r < S; r += 256) { if (fr < 14) touch4(w1 + (size_t)r * F + (bid & 3) * 896, fq * (F * 2) + fr * 128, scr); pace(); }
  if (SPON & 2) return;
  if (SW2 && wid < 4) __builtin_amdgcn_s_sleep(SW2);
#pragma unroll 1
  for (int r = (j * 2 + (bid & 1)) * 4; r < JA * 64; r += 256) { touch4(w2 + (size_t)(r + (bid & 2) * JA * 32) * S + (bid & 4) * 256, fq * (S * 2) + fr * 128, scr); pace(); }
#pragma unroll 1
  for (int r = JA * 128 + ((31 - j) * 2 + (bid & 1)) * 8; r < F; r += 512) { touch4(w2 + (size_t)r * S + ((bid & 7) >> 1) * 512, (lane >> 3) * (S * 2) + (lane & 7) * 128, scr); pace(); }
}

template <int TH> DEVI void qkv_attn(char* ws, int l, int h, int lane, int hp, int sb, int cw, int bid, int tp, int tq, const u16* wqkv) {
  asm volatile("; half %1" : "+v"(lane) : "i"(TH));
  const int fr = lane & 15, fq = lane >> 4;
  GQ::Comp<OQ<TH>> g;
#pragma unroll
  for (int c = 0; c < 2; ++c) { g.prd[0][c] = 32768 + rd_km(lane, h * 2 + c); g.qrd[0][c] = 32768 + rd_kc(lane, c); }
  f32x4 acc[6][5];
#pragma unroll
  for (int i = 0; i < 6; ++i)
#pragma unroll
    for (int j = 0; j < 5; ++j) acc[i][j] = (f32x4){0.f, 0.f, 0.f, 0.f};
  const int par = fr & 1, da = fr - 4 * fq - par;
  const bool ca = da >= 0, cb = da >= 2;
  const unsigned tm = L_TAB + (16 + 64 * TH) * 4, tc = L_CS + h * 128;
  unsigned am = tm + fr * 4 - 320, a4 = tm + fq * 16 - 320, aa = tm + 576 + (4 * fq + par - (ca ? 0 : 16)) * 4, ab = tm + 576 + (4 * fq + par + 2 - (cb ? 0 : 16)) * 4;
  unsigned c4 = tc + fq * 16, c1 = tc + 512 + fr * 4, xb = L_QKV + h * 3072 + lane * 16, hm = par ? 0xffff0000u : 0xffffu, m0 = ca ? 0u : hm, m1 = cb ? 0u : hm;
  unsigned oo = ph((unsigned)((((sb * 2 + TH) * 16 + hp) << 13) + fr * 128 + (((h * 4 + pair_chunk(fq)) ^ (fr >> 1)) << 4)));
  asm volatile("" : "+v"(am), "+v"(a4), "+v"(aa), "+v"(ab), "+v"(c4), "+v"(c1), "+v"(xb), "+v"(hm), "+v"(m0), "+v"(m1), "+v"(oo));
  g.prep(acc);
  if (l) {
    const int sbc = (tq >> 1) + fq;
    sync_post(ws, 3, !TH && (lane < 16 || (lane < 32 && (tq & 1) && sbc < 16)) ? (sbc * 16 + fr) * 64 + (tq - 2 * sbc + 1) * 8 + tp : -1, TH * 2 + h);
  }
  Gate gt;
  if (l) gt.wait<SW0>(ws, 3, sb * 16 + hp, sb ? 0xffffffu : 0xffff00u, (sb ? 0x10101u : 0x10100u) << ((qkv_rot(bid) & 15) >> 1), 0x30303u << ((qkv_rot(bid) & 15) >> 1), TH * 2 + h);
  else {
    unsigned eo = fr < 12 ? (unsigned)((((qkv_rot(bid) + 4 * TH + 2 * h) * 64 + (bid >> 3) * 4 + fq) & 1023) * (3 * D * 2) + (fr >> 2) * (D * 2) + ((bid & 3) * 4 + (fr & 3)) * 128) : 0u;
    asm volatile("" : "+v"(eo));
    if (!TH || !h) wait_vm<0>(); else if (ET) __builtin_amdgcn_s_sleep(ET);
    if (TH || h) touch4(wqkv, eo, L_SCR);
  }
  g.template run_steps<false>(acc, [&](int it, int i) {
    if (i == -1) gt.arm(it, l && CWL ? -1 : cw);
    if (i == 0) gt.head(it, l && CWL ? -1 : cw, lane, TH * 2 + h);
    if (TH && i == 1) gt.late(ws);
  });
  const rsrc_t ro = mkrsrc(ws + WS_O, S * D * 4);
  if (l == 0) bar();
  float mt[5], cv[2], rq[4], ra[4], rb[4]; f32x4 m4[5], cq[2], ck[2];
#pragma unroll
  for (int c = 0; c < 2; ++c) {
    ck[c] = *(const LAS f32x4*)(uintptr_t)(c4 + 256 + c * 64); cv[c] = *(const LAS float*)(uintptr_t)(c1 + c * 64);
    cq[c] = *(const LAS f32x4*)(uintptr_t)(c4 + c * 64);
  }
#pragma unroll
  for (int j = 4; j >= 0; --j) {
    const int o = 320 + (j == 4 ? -64 - 256 * TH : j * 64);
    if (j < 4 || !TH) mt[j] = *(const LAS float*)(uintptr_t)(am + o);
    if (j < 4 || TH) m4[j] = *(const LAS f32x4*)(uintptr_t)(a4 + o);
  }
#pragma unroll
  for (int j = 0; j < 4; ++j) { rq[j] = *(const LAS float*)(uintptr_t)(am + 896 + j * 64); ra[j] = *(const LAS float*)(uintptr_t)(aa + j * 64); rb[j] = *(const LAS float*)(uintptr_t)(ab + j * 64); }
  u32x2 kp[2][5], qp[2][4], vp[2][5];
  auto fk = [&](int j) { kp[0][(j + 1) % 5] = pk4(acc[1][j]); kp[1][(j + 1) % 5] = pk4(acc[4][j] + ck[1] * mt[j]); };
  auto fv = [&](int j) { vp[0][(j + 1) % 5] = pk4(acc[2][j]); vp[1][(j + 1) % 5] = pk4(acc[5][j] + m4[j] * cv[1]); };
  auto fqq = [&](int j) { qp[0][j] = pk4(acc[0][j]); qp[1][j] = pk4(acc[3][j] + cq[1] * mt[j]); };
  g.template pend<10, 13>(acc);
#pragma unroll
  for (int i = 0; i < 6; ++i) { __builtin_amdgcn_sched_group_barrier(0x100, 3, 0); __builtin_amdgcn_sched_group_barrier(0x8, 1, 0); }
  sched0();
  for_c<0, 20>([&](auto mc) {
    constexpr int m = mc.value, n0 = m * 52 / 20, n1 = (m + 1) * 52 / 20;
    g.template mm<(18 - (m & ~1)) + (m & 1)>(acc);
    sched0();
#pragma unroll
    for (int n = n0; n < n1; ++n) {
      const int t = 12 - (n >> 2), e = n & 3, j = t == 12 ? 4 : t / 3, i = t == 12 ? 1 + TH : t % 3;
      float x = acc[i][j][e];
      asm volatile("v_fmac_f32 %0, %1, %2" : "+v"(x) : "v"(i == 2 ? m4[j][e] : i ? ck[0][e] : cq[0][e]), "v"(i == 2 ? cv[0] : mt[j]));
      acc[i][j][e] = x;
    }
    sched0();
  });
  volatile LAS unsigned* xf = (volatile LAS unsigned*)(uintptr_t)(L_TAG + 16 + (h * 2 + TH) * 4);
  if (TH) { fv(4); *(volatile LAS bf16x8*)(uintptr_t)(xb + 2048) = cat(vp[0][0], vp[1][0]); *xf = l + 1; fk(3); }
  else { fk(3); fv(3); *(volatile LAS bf16x8*)(uintptr_t)xb = cat(kp[0][4], kp[1][4]); *(volatile LAS bf16x8*)(uintptr_t)(xb + 1024) = cat(vp[0][4], vp[1][4]); *xf = l + 1; }
  sched0();
  fqq(3); fk(2); fqq(2); fk(1);
  sched0();
  const f32x4 z = {0.f, 0.f, 0.f, 0.f};
  f32x4 sc[4][2]; float a[4], b[4], m[4];
  auto scores = [&](int j) {
#pragma unroll
    for (int u = 1; u >= 0; --u) sc[j][u] = mfma(cat(kp[0][j + u], kp[1][j + u]), cat(qp[0][j], qp[1][j]), z);
  };
  auto pick = [&](int j) {
    const f32x4 s0 = sc[j][0], s1 = sc[j][1];
    a[j] *= sel(par, sel(ca, s1[1], s0[1]), sel(ca, s1[0], s0[0]));
    b[j] *= sel(par, sel(cb, s1[3], s0[3]), sel(cb, s1[2], s0[2]));
    if (!TH && j == 0) { a[j] = sel(ca || sb, a[j], -1e9f * 1.4426950408889634f); b[j] = sel(cb || sb, b[j], -1e9f * 1.4426950408889634f); }
    m[j] = __builtin_fmaxf(a[j], b[j]);
  };
  auto expo = [&](int j) { a[j] = __builtin_amdgcn_exp2f(a[j] - m[j]); b[j] = __builtin_amdgcn_exp2f(b[j] - m[j]); m[j] = a[j] + b[j]; };
  auto pv = [&](int j) {
    const unsigned pa = pk2(a[j] * ra[j], a[j] * ra[j]), pb = pk2(b[j] * rb[j], b[j] * rb[j]);
    const bf16x8 pf = __builtin_bit_cast(bf16x8, (u32x4){pa & m0, pb & m1, pa & (m0 ^ hm), pb & (m1 ^ hm)});
#pragma unroll
    for (int c = 0; c < 2; ++c) sc[j][c] = mfma(cat(vp[c][j], vp[c][j + 1]), pf, z);
  };
  auto outp = [&](int j) { st16(ro, oo + j * 4096, pack_pair(sc[j][0] * m[j], sc[j][1] * m[j])); };
  unsigned f; u32x4 xv, xk;
  auto xrd = [&]() {
    f = xf[1 - 2 * TH]; xv = *(volatile LAS u32x4*)(uintptr_t)(xb + (TH ? 1024 : 2048));
    if (TH) xk = *(volatile LAS u32x4*)(uintptr_t)xb;
  };
  auto xget = [&]() {
    sched0();
    while (__builtin_expect(__builtin_amdgcn_readfirstlane(f) != l + 1, 0)) xrd();
    vp[0][0] = (u32x2){xv[0], xv[1]}; vp[1][0] = (u32x2){xv[2], xv[3]};
    if (TH) { kp[0][0] = (u32x2){xk[0], xk[1]}; kp[1][0] = (u32x2){xk[2], xk[3]}; }
  };
  {
    fqq(1); fk(0); fqq(0);
    if (!TH) fk(4);
    xrd();
    sched0();
    if (TH) fv(3);
    fv(2); fv(1); fv(0);
#pragma unroll
    for (int c = 0; c < 2; ++c)
#pragma unroll
      for (int j = 0; j < 4; ++j) asm volatile("" : "+v"(qp[c][j]), "+v"(kp[c][j + 1]), "+v"(vp[c][j + 1]));
    if (!TH) asm volatile("" : "+v"(kp[0][0]), "+v"(kp[1][0]));
    xget();
    scores(3); scores(2); scores(1); scores(0);
#pragma unroll
    for (int j = 0; j < 4; ++j) { const float r = rq[j] * (0.17677669529663687f * 1.4426950408889634f); a[j] = r * ra[j]; b[j] = r * rb[j]; }
#pragma unroll
    for (int i = 0; i < 8; ++i) { __builtin_amdgcn_sched_group_barrier(0x8, 1, 0); __builtin_amdgcn_sched_group_barrier(0x2, 2, 0); }
    sched0();
    pick(3); pick(2); pick(1); pick(0);
    float u, w;
    swp32(m[0], m[2]); swp32(m[1], m[3]); u = m[0]; w = m[1];
    asm volatile("v_max_f32 %0, %0, %2\n\tv_max_f32 %1, %1, %3\n\ts_nop 1" : "+v"(u), "+v"(w) : "v"(m[2]), "v"(m[3]));
    swp16(u, w); u = vmax(u, w);
    xch16(u, u, w); xch32(u, m[0], m[2]); xch32(w, m[1], m[3]);
    expo(3); expo(2); expo(1); expo(0);
    pv(3); pv(2); pv(1); pv(0);
    swp32(m[0], m[2]); swp32(m[1], m[3]); u = m[0] + m[2]; w = m[1] + m[3];
    swp16(u, w); u = __builtin_amdgcn_rcpf(u + w);
    xch16(u, u, w); xch32(u, m[0], m[2]); xch32(w, m[1], m[3]);
    outp(3); outp(2); outp(1); outp(0);
  }
}

extern "C" __global__ __launch_bounds__(512, 2) void mega_kernel(const u16* x0, const u16* wqkv, const u16* wo, const u16* w1, const u16* w2, const u16* b1, const u16* b2, u16* out, char* ws) {
  const int wid0 = __builtin_amdgcn_readfirstlane(threadIdx.x >> 6);
  asm volatile("s_getpc_b64 s[40:41]\n\ts_andn2_b32 s40, s40, 63\n\ts_load_dwordx2 s[100:101], s[40:41], %0 offset:124\n\ts_load_dwordx2 s[100:101], s[40:41], %0 offset:252"
               :: "s"(__builtin_amdgcn_readfirstlane(threadIdx.x) << 2) : "s40", "s41", "s100", "s101", "scc");
  sched0();
#define PHASE_VARS \
      int tid = threadIdx.x, bid = blockIdx.x, wid = wid0; \
      asm volatile("" : "+v"(tid)); asm volatile("" : "+s"(bid), "+s"(wid), "+s"(ws)); \
      const int lw = wid - 4, lt = tid - 256, lane = tid & 63, fr = lane & 15, fq = lane >> 4, wp = wid & 1, wq = wid >> 1; \
      const unsigned j = (bid >> 3) * 4 + lw; \
      int hp, sb; tile_map<16, 16, 4, 8>(bid, hp, sb);            \
      int tp, tq; tile_map<8, 32, 4, 8>(bid, tp, tq);             \
      int fp, fq1; tile_map<28, 8, 7, 4>(bid, fp, fq1);           \
      const int cw = (bid >> 4) == CWB ? wid + 4 * ((bid >> 3) & 1) : -1; \
      (void)cw; (void)j; (void)lt; (void)wp; (void)wq; (void)lw; (void)fr; (void)fq; (void)hp; (void)sb; (void)tp; (void)tq; (void)fp; (void)fq1;
#define LOADER_VARS PHASE_VARS asm volatile("" : "+s"(wqkv), "+s"(wo), "+s"(w1), "+s"(w2));

  if (__builtin_expect(wid0 >= 4, 1)) {
    if (LPR) __builtin_amdgcn_s_setprio(3);
    const u16* xin = x0;
    Pfc pf(wo, w1, blockIdx.x * 4 + wid0 - 4);
#pragma unroll 1
    for (int l = 0; l < NL; ++l) {
      {
        LOADER_VARS
        const int s0 = sb * 128;
        GQ::Loader L(wqkv + hp * 64, xin, lw, lane, qkv_rot(bid));
        if (l == 0) L.pre_w1<0>();
        L.seta((unsigned)(s0 * D * 2), halo_off(s0, lw, lane), lw, lane);
        if (l == 0) { L.issue_a<0>(L.ab + L.kord(0) * 128); if (!SI) L.pre<1, true, true>(); }
        sched0();
        const rsrc_t rs = mkrsrc(ws + WS_ST, MB);
        f32x4 c = {0.f, 0.f, 0.f, 0.f}; f32x2 s1 = {0.f, 0.f}, s2 = {0.f, 0.f}, o = {0.f, 0.f}; float pv = 0.f;
        bf16x8 ones = {0x3F80, 0x3F80, 0x3F80, 0x3F80, 0x3F80, 0x3F80, 0x3F80, 0x3F80};
        const int ct = lw & 1 ? 8 + (sb & 3) : sb & 7; const bool cs = l == 0 && lw >= 2;
        int tpf = (slow_res(wqkv) >> 2) != (bid & 1) ? 0 : lw < 2 ? 10 : 9;
        unsigned ra = GQ::AOFF + ((hp & 3) * 32 + lw * 8) * 128 + lane * 16, rb = rd_km(lane, ct);
        unsigned so = lane & 7 ? MB : ph((unsigned)(((l * S + s0 + (hp & 3) * 32 + lw * 8 + (lane >> 3)) * 4 + (hp >> 2)) * 16));
        unsigned co = fr >= 2 || !cs ? MB : ph((unsigned)(ST_CS + ((hp * 96 + ct * 8 + fq * 2 + fr) * 2 + (sb >> 3)) * 16));
        asm volatile("" : "+v"(ra), "+v"(rb), "+v"(so), "+v"(co), "+v"(c), "+v"(s1), "+v"(s2), "+v"(ones), "+v"(o));
        asm volatile("" : "+s"(tpf));
        if (SI && l == 0) L.pre_w1<1>();
        sched0();
        unsigned to[3], xo = x_off(tp, tq, lw, fr, fq), q2 = ((bid & 2) * 4 + slow_res(wqkv)) * 128 + ((bid >> 3) * 2 + (lw & 1)) * (3 * D * 2);
#pragma unroll
        for (int i = 0; i < 3; ++i) {
          const int r = s0 - 16 + (i == 2 && lw ? 64 : i * 64) + (lt >> 2);
          to[i] = ph((unsigned)(((l * S + (r < 0 ? 0 : r)) * 4 + (lt & 3)) * 16));
        }
        unsigned ta = (lt & 3) < (lw ? 2 : 3) ? L_TAB + ((lt & 3) * 64 + (lt >> 2)) * 4 : L_SCR, ts = ph((unsigned)(ST_CS + (hp * 192 + (lw ? lt - 64 : lt)) * 16));
        asm volatile("" : "+v"(to[0]), "+v"(to[1]), "+v"(to[2]), "+v"(xo), "+v"(ta), "+v"(ts));
        asm volatile("" : "+s"(q2));
        if (SI == 1 && l == 0) L.pre<1, false, true>();
        sched0();
        const GW::Loader W(wo + tp * 128, ws, lw, lane);
        if (SI == 2 && l == 0) L.pre<1, false, true>();
        sched0();
        auto pfq = [&](int n) { const unsigned o = q2 + L.kord(n) * GQ::WSTEP; spf2<0>(wqkv, o); spf2<2048>(wqkv, o); spf2<4096>(wqkv, o); };
        if (l) {
          if (QBR == 3) bar();
          if (!(EW & 8)) L.pre_w();
          if (tpf) { pfq(2 + (lw >> 1)); pfq(4 + (lw >> 1)); }
          if (QBR == 4) bar();
          L.start();
        }
        if (LPR == 1) __builtin_amdgcn_s_setprio(0);
        unsigned tag, tg2; u32x4 gs, a[3];
        auto rowst = [&](int i) {
          const float m0 = __uint_as_float(a[i][0]), m = red4(m0) * 0.25f, q = red4(__uint_as_float(a[i][2]) + 256.f * (m0 - m) * (m0 - m));
          o = (f32x2){sel((lt & 3) == i, m, o.x), sel((lt & 3) == i, q, o.y)};
          asm volatile("" : "+v"(o));
        };
        for_c<0, 16 - GQ::H>([&](auto tc) {
          constexpr int t = tc.value;
          u32x4 v; unsigned tv;
          L.step<t, t == 5 || t == 6 ? 1 : t == 9 || t == 10 ? 4 : 0>([&](unsigned ls) {
            if (t < 4) v = *(const LAS u32x4*)(uintptr_t)(ls + ra);
            if (t == 3) tv = *(volatile LAS unsigned*)(uintptr_t)L_TAG;
          }, [&]() {
            if (t < 8 && cs) {
              constexpr int ls = 32768 + t % 3 * GQ::SB;
              bf16x4 x[4];
#pragma unroll
              for (int e = 0; e < 4; ++e) x[e] = lds_tr4(rb + (ls & ~0x7fff) + (ls & 0x7fff) + (e >> 1) * 4096 + (e & 1) * 512);
              if (CP) { wait_lgkm0(); sched0(); __builtin_amdgcn_s_setprio(3); sched0(); }
#pragma unroll
              for (int ks = 0; ks < 2; ++ks) c = mfma(__builtin_shufflevector(x[2 * ks], x[2 * ks + 1], 0, 1, 2, 3, 4, 5, 6, 7), ones, c);
              if (CP) { asm volatile("" : "+v"(c)); sched0(); __builtin_amdgcn_s_setprio((SPM >> t) & 1 ? 2 : 0); sched0(); }
            } else if (t >= 4) {
              if (t >= 10 || t >= tpf) { if (t < TC1) pf.go(); }
              else if (lw < 2) pfq(t + 4);
              else if (t == 8) { pfq(14); pfq(15); }
            }
            if (t < 4) {
              if (t == 0) { pv = bflo(v[0]); asm volatile("" : "+v"(pv)); }
#pragma unroll
              for (int e = 0; e < 4; ++e) {
                if (SSC) {
                  float x = bflo(v[e]), y = bfhi(v[e]), a0 = s1.x, a1 = s1.y, b0 = s2.x, b1 = s2.y;
                  asm volatile("v_sub_f32 %0, %0, %6\n\tv_sub_f32 %1, %1, %6\n\tv_add_f32 %2, %2, %0\n\tv_add_f32 %3, %3, %1\n\tv_fmac_f32 %4, %0, %0\n\tv_fmac_f32 %5, %1, %1"
                               : "+v"(x), "+v"(y), "+v"(a0), "+v"(a1), "+v"(b0), "+v"(b1) : "v"(pv));
                  s1 = (f32x2){a0, a1}; s2 = (f32x2){b0, b1};
                } else { const f32x2 d = (f32x2){bflo(v[e]), bfhi(v[e])} - pv; s1 += d; s2 += d * d; }
              }
              asm volatile("" : "+v"(s1), "+v"(s2));
            }
            if (t == 3) { tag = __builtin_amdgcn_readfirstlane(tv) + 1u; tg2 = tag2(tag); if (tpf == 10) pfq(7); if (l && tpf == 9) pfq(6); }
            if (t == 4) {
              const float d1 = (s1.x + s1.y) * (1.f / 32.f), ml = pv + d1, m = red8(ml) * 0.125f;
              const float q = red8(vmax((s2.x + s2.y) - 32.f * d1 * d1, 0.f) + 32.f * (ml - m) * (ml - m));
              gs = (u32x4){__float_as_uint(m), tag, __float_as_uint(q), tg2};
              asm volatile("" : "+v"(gs));
            }
            if (t == 8) { gs = (u32x4){__float_as_uint(sel(fr, c[2], c[0])), tag, __float_as_uint(sel(fr, c[3], c[1])), tg2}; asm volatile("" : "+v"(gs)); }
            if (t == (RB ? 12 : 11)) {
#pragma unroll
              for (int i = 0; i < 3; ++i) ld_ok(a[i]);
              for (int spin = 0;; ++spin) {
                unsigned bad = 0;
#pragma unroll
                for (int i = 0; i < 3; ++i) bad |= (a[i][1] ^ tag) | (a[i][3] ^ tg2);
                if (__builtin_expect(__all(bad == 0), 1) || spin_fail(spin, ws)) break;
                __builtin_amdgcn_s_sleep(8);
#pragma unroll
                for (int i = 0; i < 3; ++i) ld_again(a[i], rs, to[i]);
              }
              if (!RB) rowst(0);
            }
            if (t == 12 && !RB) { rowst(1); rowst(2); }
            if (t == 13 && RB == 2) rowst(0);
            if (t == 13 && !RB) { LAS float* p = (LAS float*)(uintptr_t)ta; p[0] = -o.x; p[144] = __builtin_amdgcn_rsqf(o.y * (1.f / D) + EPS); }
          }, [&]() {
            if (t == 4) st16(rs, so, gs);
            if (t == 8) {
              st16(rs, co, gs);
              sched0();
#pragma unroll
              for (int i = 0; i < 3; ++i) ld_raw(a[i], rs, to[i]);
            }
          });
        });
        if (l == 0) {
#pragma unroll
          for (int p = 0; p < 4; ++p) dma16((const char*)xin + (p >> 1) * 32768 + (p & 1) * 64, xo, L_X + lw * 4096 + p * 1024);
          ld_raw(gs, rs, ts);
          wait_vm<GQ::CNT + 5>();
        } else wait_vm<GQ::CNT>();
        bar();
        W.pre_w1<0>();
        if (RB) { if (RB == 1) rowst(0); rowst(1); rowst(2); LAS float* p = (LAS float*)(uintptr_t)ta; p[0] = -o.x; p[144] = __builtin_amdgcn_rsqf(o.y * (1.f / D) + EPS); }
        wait_lgkm0();
        if (l == 0) wait_vm<9>(); else wait_vm<4>();
        bar();
        W.pre_w1<1>(); W.pre_w1<2>();
        if (l == 0) {
          wait_vm<12>();
          ld_ok(gs);
          for (int spin = 0;; ++spin) {
            if (__builtin_expect(__all(((gs[1] ^ tag) | (gs[3] ^ tg2)) == 0), 1) || spin_fail(spin, ws)) break;
            __builtin_amdgcn_s_sleep(8);
            ld_again(gs, rs, ts);
          }
          float c0 = __uint_as_float(gs[0]), c1 = __uint_as_float(gs[2]);
          c0 += dppf<0xB1>(c0); c1 += dppf<0xB1>(c1);
          *(LAS float*)(uintptr_t)(L_CS + (lw ? lt - 64 : lt) * 4) = sel(lt & 1, c1, c0);
        }
        if (l == 0 && lw < TBA) { if (fr < 14) touch4(w1 + (size_t)(((bid >> 3) * TBA + lw) * 8 + (bid & 4)) * F + (bid & 3) * 896, fq * (F * 2) + fr * 128, L_SCR + lw * 256); }
        if (l == 0) { wait_lgkm0(); bar(); }
      }
      {
        LOADER_VARS
        GW::Loader L(wo + tp * 128, ws + WS_O + (size_t)tq * 32 * 8192, lw, lane);
        const G1::Loader W(w1 + fp * 128, ws, lw, lane);
        unsigned po = ((bid & 1) * 8 + (slow_res(wo) ^ (j & 1))) * 128 + (j >> 1) * (D * 2);
#pragma unroll
        for (int k = GW::H; k < 10; ++k) spf2(wo, po + k * GW::WSTEP);
        asm volatile("" : "+s"(po));
        if (!(LB & 1)) { sched0(); bar(); } else wait_lgkm0();
#pragma unroll
        for (int k = 10; k < 16; ++k) spf2(wo, po + k * GW::WSTEP);
#pragma unroll
        for (int r = 0; r < TLQ; ++r) { if (pf.pc < 3 + NW1) { wait_lgkm0(); pf.go(); } }
        if (LB & 1) { sched0(); bar(); }
        L.start();
        L.run(16 - GW::H, [&](int) { pf.go(); });
        pf.go();
        if (E2 && bid < 224) {
          wait_vm<2 * GW::CNT>(); bar(); W.pre_w1<1>();
          wait_vm<GW::CNT + 4>(); bar(); W.pre_w1<2>();
          wait_vm<8>(); bar();
          bar(); W.pre_w1<0>();
        } else { L.tail<GW::H - 1>(); bar(); }
      }
      {
        LOADER_VARS
        if (bid < 224) {
          G1::Loader L(w1 + fp * 128, ws + WS_H + (size_t)fq1 * 64 * 16384, lw, lane);
          if (!E2) L.pre_w();
          const G2A::Loader W((const char*)(w2 + (tq & 23) * 64) + (tq & 8 ? JA * G2::WSTEP : 0), ws, lw, lane);
          const int g0 = (bid & 3) * 14, ga = (g0 + ((slow_res(w1) - g0) & 7)) ^ (j & 1), gb = ga + 8 < g0 + 14 ? ga + 8 : ga;
          const unsigned j2 = j < 32 ? 112 + (j & 15) : j;
          unsigned pa = (j >> 1) * (F * 2) + ga * 128, pb = (j >> 1) * (F * 2) + gb * 128, pd = (j2 >> 1) * (F * 2) + (j & 16 ? gb : ga) * 128;
          if (Q31 && j >= 32 && j < 64) {
            const int g7 = g0 + ((slow_res(w1) + 4 - g0) & 7), g = (j & 16) && g7 + 8 < g0 + 14 ? g7 + 8 : g7;
            pd = ((j & 15) * 4 + ((((((unsigned)(uintptr_t)w1 >> 7) + g) >> 3) + 1) & 3)) * (F * 2) + g * 128;
          }
          asm volatile("" : "+s"(pa), "+s"(pb), "+s"(pd));
          auto pfb = [&](int k) { spf2(w1, pa + k * G1::WSTEP); spf2(w1, pb + k * G1::WSTEP); spf2(w1, pd + k * G1::WSTEP); };
#pragma unroll
          for (int k = G1::H; k < G1::H + PA_1; ++k) pfb(k);
#pragma unroll
          for (int r = 0; r < TLW; ++r) { if (pf.pc < 3 + NW1) { wait_lgkm0(); pf.go(); } }
          sched0();
          bar();
          if (!(LB & 2)) pf.go();
          L.start();
          L.run(32 - G1::H, [&](int t) {
            if (pf.pc < 3 + NW1) pf.go();
            else if (t < 32 - G1::H - PA_1) pfb(t + G1::H + PA_1);
          });
          if (EW & 4) {
            wait_vm<2 * G1::CNT>(); bar(); W.pre_w1<1>();
            wait_vm<G1::CNT + 4>(); bar();
            wait_vm<4>(); bar(); W.pre_w1<0>();
          } else L.tail<G1::H - 1>();
          bar();
        } else {
          wait_lgkm0(); pf.go();
          bar();
          if (l == 0) {
            __builtin_amdgcn_s_sleep(24);
            while (!SLS && pf.pc < 3 + NW1) { wait_lgkm0(); pf.go(); }
            spare_touch(w1, w2, bid, wid, lane, [&]() { if (SLS) { wait_lgkm0(); pf.go(); } });
          }
        }
        pf.pc = 99;
      }
      {
        LOADER_VARS
        constexpr int NB = F / 64 - 2 * JA;
        const char* ub = ws + WS_U + (size_t)tp * 112 * 16384;
        G2A::Loader A((const char*)(w2 + (tq & 23) * 64) + (tq & 8 ? JA * G2::WSTEP : 0), ub + (tq & 8 ? JA * 32768 : 0), lw, lane);
        if (!(EW & 4) || bid >= 224) A.pre_w();
        const GQ::Loader W(wqkv + hp * 64, ws, lw, lane, qkv_rot(bid));
        unsigned pb = ((bid & 4) * 4 + (j & 1) * 8 + slow_res(w2)) * 128 + (j >> 1) * (S * 2) + JA * G2::WSTEP, pa = pb - (bid & 2 ? 0 : JA * G2::WSTEP);
        asm volatile("" : "+s"(pa), "+s"(pb));
        const unsigned g31 = (31 - ((unsigned)(uintptr_t)w2 >> 7)) & 31;
        int d31 = P31 && (g31 >> 4) == ((bid >> 2) & 1) && ((g31 >> 3) & 1) == (j & 1) ? (int)((g31 & 7) - slow_res(w2)) * 128 : 0;
        asm volatile("" : "+s"(d31));
        auto side = [&](int n) {
          n += G2A::H + PA_2;
          if (n < JA + NB) { const unsigned o = (n < JA ? pa : pb) + n * G2::WSTEP; spf2(w2, o); if (d31) spf2(w2, o + d31); }
        };
#pragma unroll
        for (int k = 0; k < PA_2; ++k) side(k - PA_2);
        G2::Loader L((const char*)(w2 + tq * 64) + 2 * JA * G2::WSTEP, ub + 2 * JA * 32768, lw, lane);
        gu32* xf = (gu32*)(ws + WS_MAIL) + (l * 4 + 2) * MEDGE + (tq * 8 + tp) * 64 + 28 + lw;
        const char* xp = ws + WS_XP + (unsigned)(((bid ^ 2) * 4 + lw) << 14);
        unsigned fl = L_QKV + lw * 256, xv = ph(lane * 16);
        asm volatile("" : "+s"(xf), "+s"(xp), "+s"(fl)); asm volatile("" : "+v"(xv));
        sched0();
        bar();
        A.start();
        A.run(JA - G2A::H, [&](int t) { side(t); });
        auto stage = [&](auto s) { L.issue_w<s.value * G2::SB>(L.w + s.value * G2::WSTEP); L.issue_a<s.value * G2::SB>(L.ab + s.value * 32768); sched0(); };
        wait_vm<G2A::CNT>(); bar(); stage(IC<0>{}); side(JA - 2);
        wait_vm<G2::CNT>(); bar(); stage(IC<1>{}); side(JA - 1);
        wait_vm<G2::CNT>(); bar(); stage(IC<2>{}); stage(IC<3>{}); side(JA);
        L.t = 1; L.islot = 0;
        L.run(NB - G2::H, [&](int t) {
          if (t == NB - G2::H - FLS) asm volatile("s_mov_b32 m0, %2\n\ts_nop 0\n\tglobal_load_lds_dword %0, %1 sc1" :: "v"(0u), "s"(xf), "s"(fl) : "memory");
          side(JA + t);
        });
        auto xdma = [&]() {
#pragma unroll
          for (int i = 0; i < 4; ++i) dma16(xp + i * 2048, xv, L_X + lw * 4096 + i * 1024);
          sched0();
        };
        sched0(); wait_vm<2 * G2::CNT>(); bar();
        if (__builtin_amdgcn_readfirstlane(*(volatile LAS unsigned*)(uintptr_t)fl)) {
          xdma();
          if ((EW & 8) && l == 0) { W.pre_w1<0>(); wait_vm<G2::CNT + 10>(); bar(); W.pre_w1<1>(); wait_vm<16>(); bar(); wait_vm<12>(); }
          else { wait_vm<G2::CNT + 4>(); bar(); wait_vm<4>(); bar(); wait_vm<0>(); }
        } else {
          wait_vm<G2::CNT>(); bar(); wait_vm<0>(); bar();
          for (int spin = 0;; ++spin) {
            if (__builtin_amdgcn_readfirstlane(__hip_atomic_load(xf, RLX_AGENT)) || spin_fail(spin, ws)) break;
            __builtin_amdgcn_s_sleep(2);
          }
          xdma();
          if ((EW & 8) && l == 0) { W.pre_w(); wait_vm<12>(); } else wait_vm<0>();
        }
        bar();
        xin = (const u16*)(ws + WS_X2);
        if (NL == 1) bar();
      }
    }
    return;
  }

  {
    PHASE_VARS
    if (bid < 16) code_warm(wid + 4 * (bid >> 3), lane, L_SCR + wid * 256);
    pfb_q(wqkv, bid, (bid >> 3) * 4 + wid, 0); pfb_q(wqkv, bid, (bid >> 3) * 4 + wid, 1);
    if (wid < 3) {
      if (tid == 0) asm volatile("s_mov_b32 m0, %2\n\ts_nop 0\n\tglobal_load_lds_dword %0, %1 sc1" :: "v"(WS_EPOCH), "s"(ws), "s"(L_TAG) : "memory");
      const rsrc_t rz = mkrsrc(ws + WS_XCH, 3 * MB);
      if (tid < 128) st16(rz, ph((((tid >> 6) * 256 + tq * 8 + tp) * 64 + lane) * 16), (u32x4){0u, SENT, 0u, SENT});
      else {
        const int e = (tid >> 3) & 7, line = e == 0 || e == 7 ? (2 * sb + (e & 1)) * 8 + (hp & 7) : e * 256 + bid;
        st16(rz, WS_MAIL - WS_XCH + line * 256 + (tid & 7) * 16, (u32x4){0u, 0u, 0u, 0u});
      }
      if (wid == 1 && bid < 224) touch4(b1 + fp * 128, lane * 4, L_BIAS);
      if (wid == 2) touch4(b2 + tq * 64, (lane & 31) * 4, L_BIAS + 256);
      if (wid == 0) *(LAS u32x4*)(uintptr_t)(L_TAG + 16) = (u32x4){0u, 0u, 0u, 0u};
    }
    if (CPR) __builtin_amdgcn_s_setprio(CPR);
  }
#pragma unroll 1
  for (int l = 0; l < NL; ++l) {
    {
      PHASE_VARS
      if (wid & 2) qkv_attn<1>(ws, l, wp, lane, hp, sb, cw, bid, tp, tq, wqkv);
      else qkv_attn<0>(ws, l, wp, lane, hp, sb, cw, bid, tp, tq, wqkv);
    }
    {
      PHASE_VARS
      GW::Comp<O42<false, WNE>> g; g.setup(wp, wq, lane);
      g.setwin();
      const rsrc_t rx = mkrsrc(ws + WS_XCH + l * (MB / 2), MB / 2), rh = mkrsrc(ws + WS_H, S * D * 4);
      unsigned xa = L_X + wid * 4096 + lane * 16;
      unsigned xs = fq != fr >> 2 ? MB : ph((unsigned)(((tq * 8 + tp) * 64 + wp * 32 + wq * 16 + fr) * 16)), xl = ph((unsigned)((tq * 512 + fq * 128 + wq * 16 + fr) * 16));
      const unsigned h0 = ph((unsigned)(((tp * 32 + tq) << 14) + wp * 8192 + (wq * 32 + fr) * 128 + ((pair_chunk(fq) ^ (km_f(fr) << 1)) << 4)));
      u32x4 ho = {h0, h0 ^ 64, h0 + 4096, (h0 ^ 64) + 4096};
      bf16x8 ones = {0x3F80, 0x3F80, 0x3F80, 0x3F80, 0x3F80, 0x3F80, 0x3F80, 0x3F80};
      asm volatile("" : "+v"(xa), "+v"(xs), "+v"(xl), "+v"(ho), "+v"(ones));
      Gate gt;
      auto edge = [&]() {
        sync_post(ws, l * 4, lane < 16 ? (sb * 16 + lane) * 64 + hp : -1, wid);
        gt.wait<0>(ws, l * 4, tq * 8 + tp, 0xffffu, (1u << GW::H) - 1, (1u << (GW::H + GLW)) - 1, wid);
        sched0();
      };
      if (WSP == 3) edge();
      f32x4 acc[4][2];
#pragma unroll
      for (int i = 0; i < 4; ++i)
#pragma unroll
        for (int jj = 0; jj < 2; ++jj) acc[i][jj] = (f32x4){0.f, 0.f, 0.f, 0.f};
      { bf16x8 s[2]; sel_frags(true, fr, fq, s); tile_add(acc, s, xa); }
      g.pin(acc);
      if (WSP == 2) edge(); else mfma_gap();
      g.run_steps<false>(acc, [&](int it, int i) {
        if (i == -1) gt.arm(it, l && CWL ? -1 : cw);
        if (i == 0) gt.head(it, l && CWL ? -1 : cw, lane, wid);
        if (i == GLW - 1) gt.late(ws);
      });
      bar();
      sched0();
      g.pend<0, 4>(acc);
      u32x4 pk[2][2]; float mp[2];
      {
        f32x4 sm[2], gm[2];
#pragma unroll
        for (int i = 0; i < 2; ++i)
#pragma unroll
          for (int jj = 0; jj < 2; ++jj) {
            pk[jj][i] = cat4(pk4(acc[2 * i][jj]), pk4(acc[2 * i + 1][jj])); asm volatile("" : "+v"(pk[jj][i]));
            const bf16x8 x = __builtin_bit_cast(bf16x8, pk[jj][i]);
            gm[jj] = mfma(x, x, i ? gm[jj] : (f32x4){0.f, 0.f, 0.f, 0.f}); sm[jj] = mfma(ones, x, i ? sm[jj] : (f32x4){0.f, 0.f, 0.f, 0.f});
          }
        u32x4 st;
#pragma unroll
        for (int jj = 0; jj < 2; ++jj) {
          const f32x4 gv = gm[jj];
          st[2 * jj] = __float_as_uint(sm[jj][0]); st[2 * jj + 1] = __float_as_uint(sel(fr & 2, sel(fr & 1, gv[3], gv[2]), sel(fr & 1, gv[1], gv[0])));
          mp[jj] = sm[jj][0] * (1.f / 64.f);
        }
        st16(rx, xs, st);
      }
      sched0();
      f32x4 gr[4];
      auto gld = [&]() {
        asm volatile("" ::: "memory");
#pragma unroll
        for (int p = 0; p < 4; ++p) gr[p] = __builtin_bit_cast(f32x4, __builtin_amdgcn_raw_buffer_load_b128(rx, xl + p * 1024, 0, 16));
        sched0();
      };
      if (XE == 1) gld();
#pragma unroll
      for (int p = 0; p < 4; ++p) *(LAS u32x4*)(uintptr_t)(xa + p * 1024) = pk[p >> 1][p & 1];
      asm volatile("" : "+v"(mp[0]), "+v"(mp[1]));
#pragma unroll
      for (int jj = 0; jj < 2; ++jj)
#pragma unroll
        for (int i = 0; i < 2; ++i) {
          const auto rx_ = __builtin_amdgcn_permlane16_swap(pk[jj][i][0], pk[jj][i][2], false, false), ry_ = __builtin_amdgcn_permlane16_swap(pk[jj][i][1], pk[jj][i][3], false, false);
          const unsigned x0 = rx_[0], x1 = rx_[1], y0 = ry_[0], y1 = ry_[1];
          acc[2 * i][jj] = (f32x4){bflo(x0), bfhi(x0), bflo(y0), bfhi(y0)} - mp[jj]; acc[2 * i + 1][jj] = (f32x4){bflo(x1), bfhi(x1), bflo(y1), bfhi(y1)} - mp[jj];
          asm volatile("" : "+v"(acc[2 * i][jj]), "+v"(acc[2 * i + 1][jj]));
          if (XE == 2 + jj * 2 + i) { sched0(); gld(); }
        }
      if (!XE) { if (XD) __builtin_amdgcn_s_sleep(XD); gld(); }
      for (int spin = 0;; ++spin) {
        const float lo = vmin3(vmin3(gr[0][1], gr[0][3], gr[1][1]), vmin3(gr[1][3], gr[2][1], gr[2][3]), vmin3(gr[3][1], gr[3][3], gr[3][3]));
        if (!__any(lo < 0.f) || spin_fail(spin, ws)) break;
        if (XS) __builtin_amdgcn_s_sleep(XS);
        gld();
      }
      {
        const f32x4 t = (gr[0] + gr[1]) + (gr[2] + gr[3]);
        float m[2] = {t[0], t[2]}, q[2] = {t[1], t[3]}, u, w, v, y;
        swp32(m[0], m[1]); swp32(q[0], q[1]);
        xch16(m[0] + m[1], u, w); xch16(q[0] + q[1], v, y);
        const float mean = (u + w) * (1.f / D), var = __builtin_fmaxf((v + y) * (1.f / D) - mean * mean, 0.f), r1 = __builtin_amdgcn_rsqf(var + EPS);
        xch32(mean, m[0], m[1]);
        xch32(r1 * __builtin_amdgcn_rsqf(var * r1 * r1 + EPS), q[0], q[1]);
#pragma unroll
        for (int jj = 0; jj < 2; ++jj) {
          const float c = (mp[jj] - m[jj]) * q[jj];
#pragma unroll
          for (int i = 0; i < 2; ++i) st16(rh, ho[jj * 2 + i], cat4(pk4(acc[2 * i][jj] * q[jj] + c), pk4(acc[2 * i + 1][jj] * q[jj] + c)));
        }
      }
    }
    {
      PHASE_VARS
      const int post = lane < 28 ? (tp * 28 + lane) * 64 + tq : -1;
      if (bid >= 224) {
        sync_post(ws, l * 4 + 1, post, wid);
        if (l == 0 && bid == 255 && wid == 0) { const unsigned tag = get_tag(); if (lane == 0) __hip_atomic_store((gu32*)(ws + WS_EPOCH), tag, RLX_AGENT); }
        if (l == 0) { __builtin_amdgcn_s_sleep(24); spare_touch(w1, w2, bid, wid, lane, []() {}); if (cw >= 0) code_warm(cw, lane, L_SCR + wid * 256); }
      } else {
        G1::Comp<O44<false, 32>> g; g.setup(wp, wq, lane);
        f32x4 acc[4][4];
#pragma unroll
        for (int i = 0; i < 4; ++i) { const u32x2 b = *(const LAS u32x2*)(uintptr_t)(L_BIAS + (wp * 4 + i) * 32 + fq * 8);
#pragma unroll
          for (int jj = 0; jj < 4; ++jj) acc[i][jj] = (f32x4){bflo(b.x), bfhi(b.x), bflo(b.y), bfhi(b.y)}; }
        const rsrc_t ro = mkrsrc(ws + WS_U, D * F * 4);
        const unsigned u0 = ph((unsigned)(((fq1 * 56 + fp * 2 + wp) << 14) + (wq * 64 + fr) * 128 + ((pair_chunk(fq) ^ (fr >> 1)) << 4)));
        u32x4 uo = {u0, u0 + 4096, u0 + 8192, u0 + 12288}, uo2 = uo ^ 64;
        asm volatile("" : "+v"(uo), "+v"(uo2));
        g.prep(acc);
        sync_post(ws, l * 4 + 1, post, wid);
        Gate gt; gt.wait<SW0>(ws, l * 4 + 1, fq1 * 28 + fp, ~0u, (1u << G1::H) - 1, (1u << (G1::H + GL1)) - 1, wid);
        g.run_steps<false>(acc, [&](int it, int i) {
          if (i == -1) gt.arm(it, l && CWL ? -1 : cw);
        if (i == 0) gt.head(it, l && CWL ? -1 : cw, lane, wid);
          if (i == GL1 - 1) gt.late(ws);
        });
        bar();
        sched0();
        if (GSH) {
          for_c<0, 2>([&](auto jc) {
            constexpr int jj = jc.value;
            f32x4 a[2];
#pragma unroll
            for (int i = 0; i < 2; ++i) { a[i] = gelu_arg(acc[i][jj]); asm volatile("" : "+v"(a[i])); }
            sched0();
            for_c<0, 4>([&](auto ec) { constexpr int e = ec.value; g.mm<jj * 8 + e>(acc); sched0(); a[0][e] = __builtin_amdgcn_exp2f(a[0][e]); a[1][e] = __builtin_amdgcn_exp2f(a[1][e]); asm volatile("" : "+v"(a[0]), "+v"(a[1])); sched0(); });
            a[0] += 1.f; a[1] += 1.f; asm volatile("" : "+v"(a[0]), "+v"(a[1]));
            sched0();
            for_c<0, 4>([&](auto ec) { constexpr int e = ec.value; g.mm<jj * 8 + 4 + e>(acc); sched0(); a[0][e] = __builtin_amdgcn_rcpf(a[0][e]); a[1][e] = __builtin_amdgcn_rcpf(a[1][e]); asm volatile("" : "+v"(a[0]), "+v"(a[1])); sched0(); });
            st16(ro, uo[jj], pack_pair(acc[0][jj] * a[0], acc[1][jj] * a[1]));
          });
        } else g.pend<0, 8>(acc);
#pragma unroll
        for (int n = GSH ? 2 : 0; n < 8; ++n) { const int i = n >> 2, jj = n & 3; st16(ro, (i ? uo2 : uo)[jj], pack_pair(gelu_tanh(acc[2 * i][jj]), gelu_tanh(acc[2 * i + 1][jj]))); }
      }
    }
    {
      PHASE_VARS
      asm volatile("" : "+s"(out));
      G2A::Comp<O44<true, ANE>> ga; ga.setup(wp, wq, lane);
#pragma unroll
      for (int jj = 0; jj < 4; ++jj) ga.qrd[0][jj] = 65536 + rd_km(lane, wq * 2 + (jj & 1)) + (((jj >> 1) ^ (tq >> 3)) & 1) * 8192;
      G2::Comp<O42<true, BNE>> g; g.setup(wp, wq, lane);
      g.setwin();
      ga.setwin();
      Gate gt;
      auto edge = [&]() {
        sync_post(ws, l * 4 + 2, bid < 224 && lane < 32 ? (lane * 8 + fq1) * 64 + fp : -1, wid);
        gt.wait<0>(ws, l * 4 + 2, tq * 8 + tp, 0xfffffffu, tq & 8 ? 1u << JA / 2 : 1u, tq & 8 ? 7u << JA / 2 : 7u, wid);
        sched0();
      };
      if (GSP == 3) edge();
      f32x4 ac4[4][4], acc[4][2];
#pragma unroll
      for (int jj = 0; jj < 2; ++jj) { const float b = bf2f(*(const LAS u16*)(uintptr_t)(L_BIAS + 256 + (wq * 2 + jj) * 32 + fr * 2));
#pragma unroll
        for (int i = 0; i < 4; ++i) { ac4[i][jj] = (f32x4){b, b, b, b}; ac4[i][jj + 2] = (f32x4){0.f, 0.f, 0.f, 0.f}; } }
      unsigned xa = L_X + wid * 4096 + lane * 16, xo = (unsigned)((bid * 4 + wid) << 14) + ph(lane * 16), oo = x_off(tp, tq, wid, fr, fq), xo2 = xo + 4096, oo2 = oo + 32768;
      unsigned fo = lane ? MB : (((tq ^ 8) * 8 + tp) * 64 + 28 + wid) * 4u, one = 1u;
      bf16x8 s[2]; sel_frags(false, fr, fq, s);
      if (GSP == 4) { asm volatile("" : "+v"(xa), "+v"(xo), "+v"(oo), "+v"(fo), "+v"(s[0]), "+v"(s[1])); edge(); }
      tile_add(ac4, s, xa);
      const rsrc_t rx = mkrsrc(ws + WS_XP, 16 * MB), rf = mkrsrc(ws + WS_MAIL + (l * 4 + 2) * (MEDGE * 4), MEDGE * 4), ro = mkrsrc(l ? (const void*)out : (const void*)(ws + WS_X2), S * D * 2);
      asm volatile("" : "+v"(xa), "+v"(xo), "+v"(oo), "+v"(xo2), "+v"(oo2), "+v"(fo), "+v"(one), "+v"(s[0]), "+v"(s[1]));
      ga.pin(ac4);
      static_assert(JA % 2 == 0 && G2A::H == 2, "");
      if (GSP == 2) edge(); else mfma_gap();
      ga.run_steps<false>(ac4, [&](int it, int i) {
        if (i == -1) gt.arm(it, l && CWL ? -1 : cw);
        if (i == 0) gt.head(it, l && CWL ? -1 : cw, lane, wid);
        if (i == 2) gt.late(ws);
      });
      ga.pend<4, 8>(ac4);
#pragma unroll
      for (int i = 0; i < 2; ++i)
#pragma unroll
        for (int jj = 0; jj < 2; ++jj) st16(rx, (jj ? xo2 : xo) + i * 2048, cat4(pk4(ac4[2 * i][jj + 2]), pk4(ac4[2 * i + 1][jj + 2])));
#pragma unroll
      for (int i = 0; i < 4; ++i) { acc[i][0] = ac4[i][0]; acc[i][1] = ac4[i][1]; }
#pragma unroll
      for (int k = 0; k < 2; ++k) { g.fp[2][k] = ga.fp[2][k]; g.fp[3][k] = ga.fp[3][k]; g.fq[0][k] = ga.fq[0][k]; g.fq[1][k] = ga.fq[1][k]; }
      sched0();
      g.run_steps<true>(acc, [&](int it, int i) {
        if (i == PS % 4 && __builtin_expect(it == (F / 64 - 2 * JA) / 4 - PS / 4, 0)) {
          wait_vm<0>();
          __builtin_amdgcn_raw_buffer_store_b32(one, rf, fo, 0, 16);
        }
      });
      bar();
      sched0();
      g.pend<0, 4>(acc);
      tile_add(acc, s, xa);
      u32x4 xr[4];
#pragma unroll
      for (int i = 0; i < 2; ++i)
#pragma unroll
        for (int jj = 0; jj < 2; ++jj) { xr[jj * 2 + i] = pack_pair(acc[2 * i][jj], acc[2 * i + 1][jj]); st16(ro, (jj ? oo2 : oo) + i * 64, xr[jj * 2 + i]); }
      if (l == 0) {
#pragma unroll
        for (int p = 0; p < 4; ++p) *(LAS u32x4*)(uintptr_t)(xa + p * 1024) = xr[p];
        if (NL == 1) sync_post(ws, 3, -1, wid);
      }
    }
  }
}

extern "C" void kernel_launch(void* const* d_in, const int* in_sizes, int n_in,
                              void* d_out, int out_size, void* d_ws, size_t ws_size,
                              hipStream_t stream) {
  (void)hipFuncSetAttribute((const void*)mega_kernel, hipFuncAttributeMaxDynamicSharedMemorySize, 163840);
  mega_kernel<<<256, 512, 163840, stream>>>((const u16*)d_in[0], (const u16*)d_in[1], (const u16*)d_in[2], (const u16*)d_in[4], (const u16*)d_in[6], (const u16*)d_in[5], (const u16*)d_in[7], (u16*)d_out, (char*)d_ws);
}
```
